```python
import torch

def _softmax(x, axis=-1):
    return torch.softmax(x, dim=axis)
def _gelu(x):
    return torch.nn.functional.gelu(x, approximate="tanh")
def _silu(x):
    return torch.nn.functional.silu(x)
def _sigmoid(x):
    return torch.sigmoid(x)
def _layernorm(x, axis=-1, eps=1e-5):
    m = x.mean(dim=axis, keepdim=True)
    v = x.var(dim=axis, keepdim=True, unbiased=False)
    return (x - m) / torch.sqrt(v + eps)
def _scatter_add(tgt, idx, src, axis=0):
    return tgt.clone().index_add_(axis, idx, src)
def _segment_sum(src, seg, n):
    out = torch.zeros((n,) + src.shape[1:], dtype=src.dtype)
    return out.index_add_(0, seg, src)
def _pad(x, pw):
    flat = tuple(v for pair in reversed(pw) for v in pair)
    return torch.nn.functional.pad(x, flat)

kernel_name = 'interleave_layers__sliding_local_dilated__glu_gated_geglu__prompt'

def setup_inputs():

    _w = (lambda a: (lambda t: t.to(torch.bfloat16) if t.is_floating_point() else t)(torch.as_tensor(a)))
    def _A_setup():
        B, S, D = 1, 2048, 576
        return {
            "x": _w((lambda *s: torch.randn(*s, dtype=torch.bfloat16) * 0.05)(B, S, D)),
            "wqkv": _w((lambda *s: torch.randn(*s, dtype=torch.bfloat16) * 0.05)(D, 3 * D)),
            "wo": _w((lambda *s: torch.randn(*s, dtype=torch.bfloat16) * 0.05)(D, D)),
        }
    _A_in = _A_setup()

    def _B_setup():
        B, S, Dm, Df = 1, 2048, 576, 4608
        return {
            "x": _w((lambda *s: torch.randn(*s, dtype=torch.bfloat16) * 0.05)(B, S, Dm)),
            "w_gate": _w((lambda *s: torch.randn(*s, dtype=torch.bfloat16) * 0.8)(Dm, Df) * (Dm ** -0.5)),
            "w_up": _w((lambda *s: torch.randn(*s, dtype=torch.bfloat16) * 0.8)(Dm, Df) * (Dm ** -0.5)),
            "w_down": _w((lambda *s: torch.randn(*s, dtype=torch.bfloat16) * 0.8)(Df, Dm) * (Df ** -0.5)),
        }
    _B_in = _B_setup()

    return {
        "A_x": _A_in["x"],
        "A_wqkv": _A_in["wqkv"],
        "A_wo": _A_in["wo"],
        "B_x": _B_in["x"],
        "B_w_gate": _B_in["w_gate"],
        "B_w_up": _B_in["w_up"],
        "B_w_down": _B_in["w_down"],
    }

def reference(A_x, A_wqkv, A_wo, B_x, B_w_gate, B_w_up, B_w_down):
    def _A_ref(x, wqkv, wo):
        B, S, D = x.shape
        H = 32; Dh = D // H
        W, dil = 8, 2
        qkv = (lambda x, *d: x.permute(*d))((x @ wqkv).reshape(B, S, 3, H, Dh), 2, 0, 3, 1, 4)
        q, k, v = qkv[0], qkv[1], qkv[2]
        scale = Dh ** -0.5
        pos = torch.arange(S)
        off = torch.arange(W) * dil
        idx = pos[:, None] - off[None, :]
        mask = idx >= 0
        idx_c = torch.where(mask, idx, 0).reshape(-1)
        kg = k[:, :, idx_c, :].reshape(B, H, S, W, Dh)
        vg = v[:, :, idx_c, :].reshape(B, H, S, W, Dh)
        logits = torch.einsum("bhsd,bhswd->bhsw", q, kg) * scale
        logits = torch.where(mask[None, None, :, :], logits, -1e9)
        attn = _softmax(logits, axis=-1)
        o = torch.einsum("bhsw,bhswd->bhsd", attn, vg)
        return (lambda x, *d: x.permute(*d))(o, 0, 2, 1, 3).reshape(B, S, D) @ wo

    def _B_ref(x, w_gate, w_up, w_down):
        g = torch.einsum("bsd,df->bsf", x, w_gate)
        u = torch.einsum("bsd,df->bsf", x, w_up)
        h = _gelu(g) * u
        return torch.einsum("bsf,fd->bsd", h, w_down)

    x = A_x
    x = x + _A_ref(_layernorm(x, axis=-1), A_wqkv, A_wo)
    x = x + _B_ref(_layernorm(x, axis=-1), B_w_gate, B_w_up, B_w_down)
    x = x + _A_ref(_layernorm(x, axis=-1), A_wqkv, A_wo)
    x = x + _B_ref(_layernorm(x, axis=-1), B_w_gate, B_w_up, B_w_down)
    return x
```

```cpp
#include <hip/hip_runtime.h>
#include <stdint.h>
#define SPDBG 0
constexpr size_t XS = 20480, WS_XB = 0, WS_O = 5283840, WS_X1 = 10526720, HS = 147456, WS_H = 15851520, PSS = 110592, WS_PS = 53600256, WS_FL = 81911808, WS_FX = 82042880, WS_EP = 82439168, WS_FW = 82439680, WS_END = 82506240;
typedef unsigned short u16;
constexpr int SP_LDS = 160 * 1024;

extern "C" __global__ __launch_bounds__(256, 1) void k_sp(const u16* X, const u16* Wqkv, const u16* Wo, const u16* Wg, const u16* Wu,
                                                          const u16* Wd, char* ws, u16* out
) {
  unsigned tid = threadIdx.x;
  asm volatile(
      ".Lbeg:\n"
      "s_load_dwordx16 s[8:23], %2, 0\n"
      "s_mov_b32 s34, %1\n"
      "v_lshrrev_b32 v3, 6, %0\n"
      "v_and_b32 v1, 63, %0\n"
      "s_nop 0\n"
      "v_readfirstlane_b32 s28, v3\n"
      "v_lshlrev_b32 v2, 4, v1\n"
      "s_and_b32 s48, s34, 7\n"
      "s_lshr_b32 s49, s34, 3\n"
      "s_and_b32 s50, s48, 1\n"
      "s_mul_i32 s50, s50, 10\n"
      "s_add_u32 s49, s49, s50\n"
      "s_cmp_lt_u32 s49, 8\n"
      "s_cbranch_scc1 .Lqkv\n"
      "s_cmp_lt_u32 s49, 19\n"
      "s_cbranch_scc0 .Lhelp\n"
      "s_cmp_lt_u32 s49, 10\n"
      "s_cbranch_scc0 .Ldn\n"
      "s_getpc_b64 vcc\n"
      "s_add_u32 vcc_lo, vcc_lo, .Lbeg-.\n"
      "s_addc_u32 vcc_hi, vcc_hi, -1\n"
      "s_lshl_b32 s50, s28, 13\n"
      "v_lshlrev_b32 v20, 7, v1\n"
      "v_add_u32 v20, s50, v20\n"
      "v_add_u32 v21, 0, v20\n"
      "v_min_u32 v21, .Lfin-.Lbeg-4, v21\n"
      "global_load_dword v249, v21, vcc\n"
      "v_add_u32 v21, 32768, v20\n"
      "v_min_u32 v21, .Lfin-.Lbeg-4, v21\n"
      "global_load_dword v250, v21, vcc\n"
      "v_add_u32 v21, 65536, v20\n"
      "v_min_u32 v21, .Lfin-.Lbeg-4, v21\n"
      "global_load_dword v251, v21, vcc\n"
      "s_branch .Lwo\n"
      ".Lhelp:\n"
      "s_bitcmp1_b32 s48, 0\n"
      "s_cbranch_scc1 .Lhelp1\n"
      "s_sub_u32 s53, s49, 19\n"
      "s_lshl_b32 s53, s53, 2\n"
      "s_add_u32 s53, s53, s28\n"
      "s_cmp_lt_u32 s53, 48\n"
      "s_cbranch_scc0 .Lgu\n"
      "v_min_u32 v62, 26, v1\n"
      "v_mul_u32_u24 v61, 57, v62\n"
      "v_lshrrev_b32 v61, 9, v61\n"
      "v_mul_u32_u24 v63, 9, v61\n"
      "v_sub_u32 v62, v62, v63\n"
      "v_mul_u32_u24 v63, 43, v62\n"
      "v_lshrrev_b32 v63, 7, v63\n"
      "v_mul_u32_u24 v64, 3, v63\n"
      "v_sub_u32 v64, v62, v64\n"
      "v_lshlrev_b32 v64, 7, v64\n"
      "v_add_u32 v64, 28, v64\n"
      "v_min_u32 v64, 284, v64\n"
      "v_mul_u32_u24 v63, 1152, v63\n"
      "s_mul_i32 s54, s53, 3\n"
      "v_add_u32 v61, s54, v61\n"
      "v_mul_u32_u24 v60, 3456, v61\n"
      "v_add3_u32 v60, v60, v63, v64\n"
      "s_mul_i32 s54, s48, 144\n"
      "s_mul_i32 s55, s53, 24\n"
      "s_mul_i32 s52, s53, 5462\n"
      "s_lshr_b32 s52, s52, 16\n"
      "s_mul_i32 s52, s52, 552960\n"
      "s_lshl_b32 s55, s55, 7\n"
      "s_add_u32 s52, s52, s55\n"
      "s_add_u32 s52, s52, 552960\n"
      "v_min_u32 v65, 23, v1\n"
      "v_lshlrev_b32 v65, 7, v65\n"
      "s_mul_i32 s55, s53, 6912\n"
      "v_min_u32 v59, 53, v1\n"
      "v_lshlrev_b32 v59, 7, v59\n"
      "s_mov_b32 s46, 0x240000\n"
      "s_mov_b32 s47, 0x20000\n"
      "s_mov_b32 s42, 0x1e6000\n"
      "s_mov_b32 s43, 0x20000\n"
      "s_waitcnt lgkmcnt(0)\n"
      "s_mov_b32 s44, s8\n"
      "s_and_b32 s45, s9, 0xffff\n"
      "buffer_load_dword v52, v65, s[44:47], s52 offen\n"
      "s_mov_b32 s40, s10\n"
      "s_and_b32 s41, s11, 0xffff\n"
      "s_mov_b32 s44, s12\n"
      "s_and_b32 s45, s13, 0xffff\n"
      "s_mov_b32 s46, 0xa2000\n"
      "buffer_load_dword v53, v60, s[40:43], s54 offen\n"
      "s_add_u32 s54, s54, 497664\n"
      "buffer_load_dword v54, v60, s[40:43], s54 offen\n"
      "s_add_u32 s54, s54, 497664\n"
      "buffer_load_dword v57, v59, s[44:47], s55 offen\n"
      "s_add_u32 s55, s55, 331776\n"
      "buffer_load_dword v55, v60, s[40:43], s54 offen\n"
      "s_add_u32 s54, s54, 497664\n"
      "buffer_load_dword v56, v60, s[40:43], s54 offen\n"
      "s_add_u32 s54, s54, 497664\n"
      "buffer_load_dword v58, v59, s[44:47], s55 offen\n"
      "s_sleep 8\n"
      "s_sleep 8\n"
      "s_sleep 8\n"
      "s_sleep 8\n"
      "s_branch .Lgu\n"
      ".Lhelp1:\n"
      "s_cmp_eq_u32 s49, 19\n"
      "s_cbranch_scc0 .Lgu\n"
      "s_getpc_b64 vcc\n"
      "s_add_u32 vcc_lo, vcc_lo, .Lbeg-.\n"
      "s_addc_u32 vcc_hi, vcc_hi, -1\n"
      "s_lshl_b32 s50, s28, 13\n"
      "v_lshlrev_b32 v60, 7, v1\n"
      "v_add_u32 v60, s50, v60\n"
      "v_add_u32 v61, 0, v60\n"
      "v_min_u32 v61, .Lfin-.Lbeg-4, v61\n"
      "global_load_dword v52, v61, vcc\n"
      "v_add_u32 v61, 32768, v60\n"
      "v_min_u32 v61, .Lfin-.Lbeg-4, v61\n"
      "global_load_dword v53, v61, vcc\n"
      "v_add_u32 v61, 65536, v60\n"
      "v_min_u32 v61, .Lfin-.Lbeg-4, v61\n"
      "global_load_dword v54, v61, vcc\n"
      "s_branch .Lgu\n"
      ".Lqkv:\n"
      "s_and_b32 s29, s49, 3\n"
      "s_lshr_b32 s30, s49, 2\n"
      "s_add_u32 s30, s30, s48\n"
      "s_and_b32 s31, s28, 1\n"
      "v_lshrrev_b32 v168, 4, v1\n"
      "v_and_b32 v169, 15, v1\n"
      "v_mov_b32 v172, 0x40000000\n"
      "v_cmp_le_u32 vcc, 36, v1\n"
      "v_subrev_u32 v170, 36, v1\n"
      "s_nop 0\n"
      "v_cndmask_b32 v170, v1, v170, vcc\n"
      "v_mov_b32 v171, 3456\n"
      "v_cndmask_b32 v171, 0, v171, vcc\n"
      "v_mul_u32_u24 v173, 57, v170\n"
      "v_lshrrev_b32 v173, 9, v173\n"
      "v_mul_u32_u24 v173, 1008, v173\n"
      "v_lshl_add_u32 v171, v170, 4, v171\n"
      "v_add_u32 v171, v171, v173\n"
      "v_cmp_gt_u32 vcc, 27, v170\n"
      "s_mul_i32 s82, s30, 144\n"
      "s_mul_i32 s50, s28, 27648\n"
      "v_cndmask_b32 v96, v172, v171, vcc\n"
      "s_add_u32 s82, s82, s50\n"
      "s_mul_i32 s77, s28, 4224\n"
      "s_add_u32 s77, s77, 61440\n"
      "v_lshrrev_b32 v170, 3, v1\n"
      "v_and_b32 v171, 7, v1\n"
      "v_xor_b32 v171, v171, v170\n"
      "v_mul_u32_u24 v101, 1152, v170\n"
      "v_lshl_add_u32 v101, v171, 4, v101\n"
      "s_lshl_b32 s84, s29, 5\n"
      "s_add_u32 s84, s84, 31\n"
      "s_sub_u32 s84, s84, s31\n"
      "s_mul_i32 s84, s84, 18432\n"
      "s_lshr_b32 s50, s28, 1\n"
      "s_mul_i32 s51, s50, 9216\n"
      "s_add_u32 s84, s84, s51\n"
      "s_mul_i32 s85, s31, 20480\n"
      "s_lshl_b32 s50, s50, 10\n"
      "s_add_u32 s85, s85, s50\n"
      "s_waitcnt lgkmcnt(0)\n"
      "s_mov_b32 s36, s20\n"
      "s_and_b32 s37, s21, 0xffff\n"
      "s_mov_b32 s38, 0x4eaf200\n"
      "s_mov_b32 s39, 0x20000\n"
      "s_mov_b32 s98, 82439168\n"
      "buffer_load_dwordx2 v[252:253], off, s[36:39], s98 sc1\n"
      "s_mov_b32 s40, s10\n"
      "s_and_b32 s41, s11, 0xffff\n"
      "s_mov_b32 s42, 0x1e6000\n"
      "s_mov_b32 s43, 0x20000\n"
      "s_mov_b32 s44, s8\n"
      "s_and_b32 s45, s9, 0xffff\n"
      "s_mov_b32 s46, 0x240000\n"
      "s_mov_b32 s47, 0x20000\n"
      "s_add_u32 m0, s85, 0\n"
      "s_add_u32 s50, s84, 0\n"
      "buffer_load_dwordx4 v101, s[44:47], s50 offen lds\n"
      "s_add_u32 m0, s85, 2048\n"
      "s_add_u32 s50, s84, 128\n"
      "buffer_load_dwordx4 v101, s[44:47], s50 offen lds\n"
      "s_add_u32 m0, s85, 4096\n"
      "s_add_u32 s50, s84, 256\n"
      "buffer_load_dwordx4 v101, s[44:47], s50 offen lds\n"
      "s_add_u32 m0, s85, 6144\n"
      "s_add_u32 s50, s84, 384\n"
      "buffer_load_dwordx4 v101, s[44:47], s50 offen lds\n"
      "s_add_u32 m0, s85, 8192\n"
      "s_add_u32 s50, s84, 512\n"
      "buffer_load_dwordx4 v101, s[44:47], s50 offen lds\n"
      "s_add_u32 m0, s85, 10240\n"
      "s_add_u32 s50, s84, 640\n"
      "buffer_load_dwordx4 v101, s[44:47], s50 offen lds\n"
      "s_add_u32 m0, s85, 12288\n"
      "s_add_u32 s50, s84, 768\n"
      "buffer_load_dwordx4 v101, s[44:47], s50 offen lds\n"
      "s_add_u32 m0, s85, 14336\n"
      "s_add_u32 s50, s84, 896\n"
      "buffer_load_dwordx4 v101, s[44:47], s50 offen lds\n"
      "s_add_u32 m0, s85, 16384\n"
      "s_add_u32 s50, s84, 1024\n"
      "buffer_load_dwordx4 v101, s[44:47], s50 offen lds\n"
      "s_add_u32 m0, s77, 0\n"
      "s_add_u32 s50, s82, 0\n"
      "buffer_load_dwordx4 v96, s[40:43], s50 offen lds\n"
      "s_add_u32 m0, s77, 1056\n"
      "s_add_u32 s50, s82, 6912\n"
      "buffer_load_dwordx4 v96, s[40:43], s50 offen lds\n"
      "s_add_u32 m0, s77, 2112\n"
      "s_add_u32 s50, s82, 13824\n"
      "buffer_load_dwordx4 v96, s[40:43], s50 offen lds\n"
      "s_add_u32 m0, s77, 3168\n"
      "s_add_u32 s50, s82, 20736\n"
      "buffer_load_dwordx4 v96, s[40:43], s50 offen lds\n"
      "s_add_u32 m0, s77, 16896\n"
      "s_add_u32 s50, s82, 110592\n"
      "buffer_load_dwordx4 v96, s[40:43], s50 offen lds\n"
      "s_add_u32 m0, s77, 17952\n"
      "s_add_u32 s50, s82, 117504\n"
      "buffer_load_dwordx4 v96, s[40:43], s50 offen lds\n"
      "s_add_u32 m0, s77, 19008\n"
      "s_add_u32 s50, s82, 124416\n"
      "buffer_load_dwordx4 v96, s[40:43], s50 offen lds\n"
      "s_add_u32 m0, s77, 20064\n"
      "s_add_u32 s50, s82, 131328\n"
      "buffer_load_dwordx4 v96, s[40:43], s50 offen lds\n"
      "v_and_b32 v170, 7, v1\n"
      "v_xor_b32 v170, v170, v168\n"
      "v_lshlrev_b32 v171, 7, v169\n"
      "v_lshl_add_u32 v101, v170, 4, v171\n"
      "v_xor_b32 v103, 64, v101\n"
      "v_bfe_u32 v170, v1, 3, 1\n"
      "v_lshl_add_u32 v170, v168, 2, v170\n"
      "v_mul_u32_u24 v170, 1056, v170\n"
      "v_bfe_u32 v171, v1, 2, 1\n"
      "v_mul_u32_u24 v171, 576, v171\n"
      "v_and_b32 v173, 3, v1\n"
      "v_lshl_add_u32 v171, v173, 3, v171\n"
      "s_mul_i32 s51, s28, 36\n"
      "s_lshl_b32 s52, s31, 2\n"
      "s_add_u32 s52, s52, s51\n"
      "s_add_u32 s52, s52, 61440\n"
      "v_add3_u32 v97, v170, v171, s52\n"
      "v_add_u32 v98, 50688, v97\n"
      "v_and_b32 v170, 12, v1\n"
      "v_bfe_u32 v171, v1, 1, 1\n"
      "v_add_u32 v170, v170, v171\n"
      "v_mul_u32_u24 v170, 1056, v170\n"
      "v_and_b32 v171, 1, v1\n"
      "v_mul_u32_u24 v171, 576, v171\n"
      "v_add_u32 v170, v170, v171\n"
      "s_lshl_b32 s52, s31, 5\n"
      "s_sub_u32 s52, s51, s52\n"
      "s_add_u32 s52, s52, 32\n"
      "v_mul_u32_u24 v171, 144, v168\n"
      "v_add_u32 v171, s52, v171\n"
      "v_min_u32 v171, 432, v171\n"
      "v_add_u32 v170, v170, v171\n"
      "v_add_u32 v99, 61440, v170\n"
      "v_add_u32 v100, 112128, v170\n"
      "v_mov_b32 v152, 0\n"
      "v_mov_b32 v153, 0\n"
      "v_mov_b32 v155, 0\n"
      "v_mov_b32 v156, 0\n"
      "v_mov_b32 v157, 0\n"
      "v_mov_b32 v145, 0\n"
      "v_mov_b32 v147, 0\n"
      "v_mov_b32 v149, 0\n"
      "v_mov_b32 v151, 0\n"
      "v_mov_b32 v170, 0x3f80\n"
      "v_mov_b32 v171, 0x3f800000\n"
      "s_mov_b32 vcc_lo, 0x20001\n"
      "s_mov_b32 vcc_hi, 0x4\n"
      "v_cndmask_b32 v154, v152, v170, vcc\n"
      "s_mov_b32 vcc_lo, 0x200010\n"
      "s_mov_b32 vcc_hi, 0x40\n"
      "v_cndmask_b32 v154, v154, v171, vcc\n"
      "v_mov_b32 v164, 0x3f803f80\n"
      "v_mov_b32 v165, 0x3f803f80\n"
      "v_mov_b32 v166, 0x3f803f80\n"
      "v_mov_b32 v167, 0x3f803f80\n"
      "s_mov_b32 s66, 1\n"
      "s_mov_b32 s79, 40960\n"
      "s_lshl_b32 s83, s28, 12\n"
      "s_lshl_b32 s73, s29, 5\n"
      "s_sub_u32 s73, 96, s73\n"
      "s_mul_i32 s71, s73, 1536\n"
      "s_add_u32 s71, s71, 82042880\n"
      "s_mul_i32 s69, s73, 20480\n"
      "s_add_u32 s69, s69, s83\n"
      "s_add_u32 s69, s69, 0\n"
      "s_add_u32 s67, s73, 161\n"
      "s_min_u32 s67, s67, 256\n"
      "s_lshl_b32 s78, s28, 10\n"
      "s_add_u32 s78, s78, 16384\n"
      "v_min_u32 v5, 11, v169\n"
      "v_lshlrev_b32 v5, 7, v5\n"
      "v_lshrrev_b32 v170, 5, v1\n"
      "v_lshl_add_u32 v5, v170, 2, v5\n"
      "v_add_u32 v5, 8, v5\n"
      "v_min_u32 v102, 59, v1\n"
      "v_lshlrev_b32 v102, 7, v102\n"
      "s_sub_u32 s51, s78, s83\n"
      "v_add_u32 v4, s51, v2\n"
      "s_cmp_lt_u32 s28, 3\n"
      "s_cselect_b32 s51, 64, 32\n"
      "v_cmp_gt_u32 vcc, s51, v1\n"
      "s_nop 1\n"
      "v_cndmask_b32 v4, v172, v4, vcc\n"
      "s_mul_i32 s74, s29, 1310720\n"
      "s_add_u32 s74, s74, 5283840\n"
      "s_mov_b32 s76, 0\n"
      "s_lshl_b32 s51, s30, 2\n"
      "s_add_u32 s51, s51, s28\n"
      "v_lshrrev_b32 v170, 1, v168\n"
      "v_lshl_add_u32 v170, v170, 4, v169\n"
      "v_lshlrev_b32 v170, 4, v170\n"
      "v_and_b32 v171, 1, v168\n"
      "v_lshl_add_u32 v170, v171, 3, v170\n"
      "s_lshl_b32 s52, s51, 9\n"
      "v_add_u32 v11, s52, v170\n"
      "s_lshl_b32 s52, s51, 7\n"
      "s_add_u32 s52, s52, 16384\n"
      "v_lshlrev_b32 v170, 2, v1\n"
      "v_add_u32 v6, s52, v170\n"
      "v_cmp_gt_u32 vcc, 32, v1\n"
      "s_nop 1\n"
      "v_cndmask_b32 v6, v172, v6, vcc\n"
      "s_lshl_b32 s51, s29, 3\n"
      "s_add_u32 s51, s51, s30\n"
      "s_lshl_b32 s51, s51, 2\n"
      "s_add_u32 s51, s51, s28\n"
      "s_lshl_b32 s51, s51, 7\n"
      "s_add_u32 s51, s51, 81911808\n"
      "v_lshlrev_b32 v7, 2, v1\n"
      "v_lshrrev_b32 v170, 5, v1\n"
      "v_lshl_or_b32 v7, v170, 30, v7\n"
      "v_add_u32 v7, s51, v7\n"
      "v_lshlrev_b32 v10, 3, v1\n"
      "v_add_u32 v9, 40960, v10\n"
      "v_add_u32 v3, 40960, v2\n"
      "v_mov_b32 v17, 0x3ae38e39\n"
      "s_cmp_eq_u32 s29, 0\n"
      "s_cselect_b32 s80, 32, 1000\n"
      "s_mov_b32 s81, 1\n"
      "v_mov_b32 v87, 0\n"
      "v_mov_b32 v91, 0\n"
      "v_mov_b32 v95, 0\n"
      "v_mov_b32 v163, 0\n"
      "v_mov_b32 v250, 0\n"
      "v_mov_b32 v251, 0\n"
      "v_mov_b32 v12, 0\n"
      "v_mov_b32 v13, 0\n"
      "v_mov_b32 v15, 0\n"
      "v_mov_b32 v170, 0x3f80\n"
      "v_mov_b32 v171, 0x3f800000\n"
      "s_mov_b32 vcc_lo, 0x100001\n"
      "s_mov_b32 vcc_hi, 0x10000100\n"
      "v_cndmask_b32 v248, v95, v170, vcc\n"
      "s_mov_b32 vcc_lo, 0x200002\n"
      "s_mov_b32 vcc_hi, 0x20000200\n"
      "v_cndmask_b32 v248, v248, v171, vcc\n"
      "s_mov_b32 vcc_lo, 0x400004\n"
      "s_mov_b32 vcc_hi, 0x40000400\n"
      "v_cndmask_b32 v249, v95, v170, vcc\n"
      "s_mov_b32 vcc_lo, 0x800008\n"
      "s_mov_b32 vcc_hi, 0x80000800\n"
      "v_cndmask_b32 v249, v249, v171, vcc\n"
      "s_mov_b32 vcc_lo, 0x20001\n"
      "s_mov_b32 vcc_hi, 0x80004\n"
      "v_cndmask_b32 v14, v95, v170, vcc\n"
      "v_mov_b32 v170, 0xff61b1e6\n"
      "v_mov_b32 v112, v170\n"
      "v_mov_b32 v171, 0xffff\n"
      "v_mov_b32 v116, 0\n"
      "v_mov_b32 v118, 0\n"
      "s_mov_b32 vcc_lo, 0x55505555\n"
      "s_mov_b32 vcc_hi, 0x50005500\n"
      "v_cndmask_b32 v104, v170, v95, vcc\n"
      "v_cndmask_b32 v116, v116, v171, vcc\n"
      "s_mov_b32 vcc_lo, 0x50000\n"
      "s_mov_b32 vcc_hi, 0x5550055\n"
      "v_cndmask_b32 v108, v170, v95, vcc\n"
      "v_cndmask_b32 v118, v118, v171, vcc\n"
      "v_mov_b32 v170, 0xff61b1e6\n"
      "v_mov_b32 v113, v170\n"
      "v_mov_b32 v171, 0xffff0000\n"
      "s_mov_b32 vcc_lo, 0xaaa0aaaa\n"
      "s_mov_b32 vcc_hi, 0xa000aa00\n"
      "v_cndmask_b32 v105, v170, v95, vcc\n"
      "v_cndmask_b32 v116, v116, v171, vcc\n"
      "s_mov_b32 vcc_lo, 0xa0000\n"
      "s_mov_b32 vcc_hi, 0xaaa00aa\n"
      "v_cndmask_b32 v109, v170, v95, vcc\n"
      "v_cndmask_b32 v118, v118, v171, vcc\n"
      "v_mov_b32 v170, 0xff61b1e6\n"
      "v_mov_b32 v114, v170\n"
      "v_mov_b32 v171, 0xffff\n"
      "v_mov_b32 v117, 0\n"
      "v_mov_b32 v119, 0\n"
      "s_mov_b32 vcc_lo, 0x55405554\n"
      "s_mov_b32 vcc_hi, 0x40005400\n"
      "v_cndmask_b32 v106, v170, v95, vcc\n"
      "v_cndmask_b32 v117, v117, v171, vcc\n"
      "s_mov_b32 vcc_lo, 0x150001\n"
      "s_mov_b32 vcc_hi, 0x15550155\n"
      "v_cndmask_b32 v110, v170, v95, vcc\n"
      "v_cndmask_b32 v119, v119, v171, vcc\n"
      "v_mov_b32 v170, 0xff61b1e6\n"
      "v_mov_b32 v115, v170\n"
      "v_mov_b32 v171, 0xffff0000\n"
      "s_mov_b32 vcc_lo, 0xaa80aaa8\n"
      "s_mov_b32 vcc_hi, 0x8000a800\n"
      "v_cndmask_b32 v107, v170, v95, vcc\n"
      "v_cndmask_b32 v117, v117, v171, vcc\n"
      "s_mov_b32 vcc_lo, 0x2a0002\n"
      "s_mov_b32 vcc_hi, 0x2aaa02aa\n"
      "v_cndmask_b32 v111, v170, v95, vcc\n"
      "v_cndmask_b32 v119, v119, v171, vcc\n"
      "s_add_u32 m0, s77, 33792\n"
      "s_add_u32 s50, s82, 221184\n"
      "buffer_load_dwordx4 v96, s[40:43], s50 offen lds\n"
      "s_add_u32 m0, s77, 34848\n"
      "s_add_u32 s50, s82, 228096\n"
      "buffer_load_dwordx4 v96, s[40:43], s50 offen lds\n"
      "s_add_u32 m0, s77, 35904\n"
      "s_add_u32 s50, s82, 235008\n"
      "buffer_load_dwordx4 v96, s[40:43], s50 offen lds\n"
      "s_add_u32 m0, s77, 36960\n"
      "s_add_u32 s50, s82, 241920\n"
      "buffer_load_dwordx4 v96, s[40:43], s50 offen lds\n"
      "s_waitcnt vmcnt(21)\n"
      "v_readfirstlane_b32 s98, v252\n"
      "v_readfirstlane_b32 s99, v253\n"
      "v_mov_b32 v8, 0\n"
      "s_add_u32 s98, s98, 1\n"
      "s_addc_u32 s99, s99, 0\n"
      "v_writelane_b32 v8, s98, 2\n"
      "v_writelane_b32 v8, s99, 3\n"
      "buffer_store_dword v8, v7, s[36:39], 0 offen sc1\n"
      "s_cmp_eq_u32 s28, 0\n"
      "s_cselect_b32 s50, 0, 0x40000000\n"
      "v_or_b32 v7, s50, v7\n"
      "v_mov_b32 v16, s98\n"
      "v_mov_b32 v170, s99\n"
      "s_mov_b32 vcc_lo, 0x0\n"
      "s_mov_b32 vcc_hi, 0xffffffff\n"
      "v_cndmask_b32 v16, v16, v170, vcc\n"
      "s_waitcnt vmcnt(9)\n"
      "s_barrier\n"
      "ds_read_b32 v144, v99 offset:0\n"
      "ds_read_b32 v146, v99 offset:2112\n"
      "ds_read_b64_tr_b16 a[0:1], v97 offset:0\n"
      "ds_read_b64_tr_b16 a[2:3], v97 offset:2112\n"
      "ds_read_b64_tr_b16 a[4:5], v97 offset:144\n"
      "ds_read_b64_tr_b16 a[6:7], v97 offset:2256\n"
      "ds_read_b64_tr_b16 a[8:9], v97 offset:288\n"
      "ds_read_b64_tr_b16 a[10:11], v97 offset:2400\n"
      "s_add_u32 m0, s77, 50688\n"
      "s_add_u32 s50, s82, 331776\n"
      "buffer_load_dwordx4 v96, s[40:43], s50 offen lds\n"
      "s_add_u32 m0, s77, 51744\n"
      "s_add_u32 s50, s82, 338688\n"
      "buffer_load_dwordx4 v96, s[40:43], s50 offen lds\n"
      "s_add_u32 m0, s77, 52800\n"
      "s_add_u32 s50, s82, 345600\n"
      "buffer_load_dwordx4 v96, s[40:43], s50 offen lds\n"
      "s_add_u32 m0, s77, 53856\n"
      "s_add_u32 s50, s82, 352512\n"
      "buffer_load_dwordx4 v96, s[40:43], s50 offen lds\n"
      "s_add_u32 m0, s77, 67584\n"
      "s_add_u32 s50, s82, 442368\n"
      "buffer_load_dwordx4 v96, s[40:43], s50 offen lds\n"
      "s_add_u32 m0, s77, 68640\n"
      "s_add_u32 s50, s82, 449280\n"
      "buffer_load_dwordx4 v96, s[40:43], s50 offen lds\n"
      "s_add_u32 m0, s77, 69696\n"
      "s_add_u32 s50, s82, 456192\n"
      "buffer_load_dwordx4 v96, s[40:43], s50 offen lds\n"
      "s_add_u32 m0, s77, 70752\n"
      "s_add_u32 s50, s82, 463104\n"
      "buffer_load_dwordx4 v96, s[40:43], s50 offen lds\n"
      "s_waitcnt vmcnt(13) lgkmcnt(0)\n"
      "s_barrier\n"
      "v_mfma_f32_16x16x32_bf16 v[128:131], v[144:147], v[154:157], 0\n"
      "ds_read_b32 v148, v99 offset:16896\n"
      "ds_read_b32 v150, v99 offset:19008\n"
      "ds_read_b64_tr_b16 a[12:13], v97 offset:16896\n"
      "ds_read_b64_tr_b16 a[14:15], v97 offset:19008\n"
      "ds_read_b64_tr_b16 a[16:17], v97 offset:17040\n"
      "ds_read_b64_tr_b16 a[18:19], v97 offset:19152\n"
      "ds_read_b64_tr_b16 a[20:21], v97 offset:17184\n"
      "ds_read_b64_tr_b16 a[22:23], v97 offset:19296\n"
      "ds_read_b128 v[120:123], v101 offset:0\n"
      "ds_read_b128 v[28:31], v101 offset:20480\n"
      "s_add_u32 m0, s77, 84480\n"
      "s_add_u32 s50, s82, 552960\n"
      "buffer_load_dwordx4 v96, s[40:43], s50 offen lds\n"
      "s_add_u32 m0, s77, 85536\n"
      "s_add_u32 s50, s82, 559872\n"
      "buffer_load_dwordx4 v96, s[40:43], s50 offen lds\n"
      "s_add_u32 m0, s77, 86592\n"
      "v_mfma_f32_16x16x32_bf16 v[132:135], v[144:147], v[152:155], 0\n"
      "s_add_u32 s50, s82, 566784\n"
      "buffer_load_dwordx4 v96, s[40:43], s50 offen lds\n"
      "s_add_u32 m0, s77, 87648\n"
      "s_add_u32 s50, s82, 573696\n"
      "buffer_load_dwordx4 v96, s[40:43], s50 offen lds\n"
      "s_add_u32 m0, s77, 0\n"
      "s_add_u32 s50, s82, 663552\n"
      "buffer_load_dwordx4 v96, s[40:43], s50 offen lds\n"
      "s_add_u32 m0, s77, 1056\n"
      "s_add_u32 s50, s82, 670464\n"
      "buffer_load_dwordx4 v96, s[40:43], s50 offen lds\n"
      "s_add_u32 m0, s77, 2112\n"
      "s_add_u32 s50, s82, 677376\n"
      "buffer_load_dwordx4 v96, s[40:43], s50 offen lds\n"
      "s_add_u32 m0, s77, 3168\n"
      "s_add_u32 s50, s82, 684288\n"
      "buffer_load_dwordx4 v96, s[40:43], s50 offen lds\n"
      "s_waitcnt vmcnt(17) lgkmcnt(0)\n"
      "s_barrier\n"
      "v_cvt_pk_bf16_f32 v176, v128, v129\n"
      "v_cvt_pk_bf16_f32 v177, v130, v131\n"
      "v_cvt_pk_bf16_f32 v178, v132, v133\n"
      "v_cvt_pk_bf16_f32 v179, v134, v135\n"
      "v_mfma_f32_16x16x32_bf16 v[136:139], v[148:151], v[154:157], 0\n"
      "ds_read_b32 v144, v99 offset:33792\n"
      "ds_read_b32 v146, v99 offset:35904\n"
      "v_mfma_f32_16x16x32_bf16 v[140:143], v[148:151], v[152:155], 0\n"
      "ds_read_b64_tr_b16 a[24:25], v97 offset:33792\n"
      "ds_read_b64_tr_b16 a[26:27], v97 offset:35904\n"
      "v_mfma_f32_16x16x32_bf16 v[36:39], a[4:7], v[120:123], 0\n"
      "ds_read_b64_tr_b16 a[28:29], v97 offset:33936\n"
      "ds_read_b64_tr_b16 a[30:31], v97 offset:36048\n"
      "v_mfma_f32_16x16x32_bf16 v[40:43], v[176:179], v[120:123], 0\n"
      "ds_read_b64_tr_b16 a[32:33], v97 offset:34080\n"
      "ds_read_b64_tr_b16 a[34:35], v97 offset:36192\n"
      "v_mfma_f32_16x16x32_bf16 v[44:47], a[8:11], v[120:123], 0\n"
      "ds_read_b128 v[124:127], v103 offset:0\n"
      "ds_read_b128 v[32:35], v103 offset:20480\n"
      "v_mfma_f32_16x16x32_bf16 v[48:51], a[0:3], v[120:123], 0\n"
      "s_add_u32 m0, s77, 16896\n"
      "s_add_u32 s50, s82, 774144\n"
      "v_mfma_f32_16x16x32_bf16 v[68:71], a[4:7], v[164:167], 0\n"
      "buffer_load_dwordx4 v96, s[40:43], s50 offen lds\n"
      "s_add_u32 m0, s77, 17952\n"
      "v_mfma_f32_16x16x32_bf16 v[72:75], v[176:179], v[164:167], 0\n"
      "s_add_u32 s50, s82, 781056\n"
      "buffer_load_dwordx4 v96, s[40:43], s50 offen lds\n"
      "v_mfma_f32_16x16x32_bf16 v[76:79], a[8:11], v[164:167], 0\n"
      "s_add_u32 m0, s77, 19008\n"
      "s_add_u32 s50, s82, 787968\n"
      "v_mfma_f32_16x16x32_bf16 v[80:83], a[0:3], v[164:167], 0\n"
      "buffer_load_dwordx4 v96, s[40:43], s50 offen lds\n"
      "s_add_u32 m0, s77, 20064\n"
      "v_mfma_f32_16x16x32_bf16 v[52:55], a[4:7], v[28:31], 0\n"
      "s_add_u32 s50, s82, 794880\n"
      "buffer_load_dwordx4 v96, s[40:43], s50 offen lds\n"
      "v_mfma_f32_16x16x32_bf16 v[56:59], v[176:179], v[28:31], 0\n"
      "v_mfma_f32_16x16x32_bf16 v[60:63], a[8:11], v[28:31], 0\n"
      "v_mfma_f32_16x16x32_bf16 v[64:67], a[0:3], v[28:31], 0\n"
      "s_waitcnt vmcnt(16) lgkmcnt(0)\n"
      "s_barrier\n"
      "v_cvt_pk_bf16_f32 v180, v136, v137\n"
      "v_cvt_pk_bf16_f32 v181, v138, v139\n"
      "v_cvt_pk_bf16_f32 v182, v140, v141\n"
      "v_cvt_pk_bf16_f32 v183, v142, v143\n"
      "v_mfma_f32_16x16x32_bf16 v[128:131], v[144:147], v[154:157], 0\n"
      "ds_read_b32 v148, v100 offset:0\n"
      "ds_read_b32 v150, v100 offset:2112\n"
      "v_mfma_f32_16x16x32_bf16 v[132:135], v[144:147], v[152:155], 0\n"
      "ds_read_b64_tr_b16 a[36:37], v98 offset:0\n"
      "ds_read_b64_tr_b16 a[38:39], v98 offset:2112\n"
      "v_mfma_f32_16x16x32_bf16 v[36:39], a[16:19], v[124:127], v[36:39]\n"
      "ds_read_b64_tr_b16 a[40:41], v98 offset:144\n"
      "ds_read_b64_tr_b16 a[42:43], v98 offset:2256\n"
      "v_mfma_f32_16x16x32_bf16 v[40:43], v[180:183], v[124:127], v[40:43]\n"
      "ds_read_b64_tr_b16 a[44:45], v98 offset:288\n"
      "ds_read_b64_tr_b16 a[46:47], v98 offset:2400\n"
      "v_mfma_f32_16x16x32_bf16 v[44:47], a[20:23], v[124:127], v[44:47]\n"
      "ds_read_b128 v[120:123], v101 offset:2048\n"
      "ds_read_b128 v[20:23], v101 offset:22528\n"
      "v_mfma_f32_16x16x32_bf16 v[48:51], a[12:15], v[124:127], v[48:51]\n"
      "s_add_u32 m0, s77, 33792\n"
      "s_add_u32 s50, s82, 884736\n"
      "v_mfma_f32_16x16x32_bf16 v[68:71], a[16:19], v[164:167], v[68:71]\n"
      "buffer_load_dwordx4 v96, s[40:43], s50 offen lds\n"
      "s_add_u32 m0, s77, 34848\n"
      "v_mfma_f32_16x16x32_bf16 v[72:75], v[180:183], v[164:167], v[72:75]\n"
      "s_add_u32 s50, s82, 891648\n"
      "buffer_load_dwordx4 v96, s[40:43], s50 offen lds\n"
      "v_mfma_f32_16x16x32_bf16 v[76:79], a[20:23], v[164:167], v[76:79]\n"
      "s_add_u32 m0, s77, 35904\n"
      "s_add_u32 s50, s82, 898560\n"
      "v_mfma_f32_16x16x32_bf16 v[80:83], a[12:15], v[164:167], v[80:83]\n"
      "buffer_load_dwordx4 v96, s[40:43], s50 offen lds\n"
      "s_add_u32 m0, s77, 36960\n"
      "v_mfma_f32_16x16x32_bf16 v[52:55], a[16:19], v[32:35], v[52:55]\n"
      "s_add_u32 s50, s82, 905472\n"
      "buffer_load_dwordx4 v96, s[40:43], s50 offen lds\n"
      "v_mfma_f32_16x16x32_bf16 v[56:59], v[180:183], v[32:35], v[56:59]\n"
      "v_mfma_f32_16x16x32_bf16 v[60:63], a[20:23], v[32:35], v[60:63]\n"
      "v_mfma_f32_16x16x32_bf16 v[64:67], a[12:15], v[32:35], v[64:67]\n"
      "s_waitcnt vmcnt(16) lgkmcnt(0)\n"
      "s_barrier\n"
      "v_cvt_pk_bf16_f32 v184, v128, v129\n"
      "v_cvt_pk_bf16_f32 v185, v130, v131\n"
      "v_cvt_pk_bf16_f32 v186, v132, v133\n"
      "v_cvt_pk_bf16_f32 v187, v134, v135\n"
      "v_mfma_f32_16x16x32_bf16 v[136:139], v[148:151], v[154:157], 0\n"
      "ds_read_b32 v144, v100 offset:16896\n"
      "ds_read_b32 v146, v100 offset:19008\n"
      "v_mfma_f32_16x16x32_bf16 v[140:143], v[148:151], v[152:155], 0\n"
      "ds_read_b64_tr_b16 a[48:49], v98 offset:16896\n"
      "ds_read_b64_tr_b16 a[50:51], v98 offset:19008\n"
      "v_mfma_f32_16x16x32_bf16 v[36:39], a[28:31], v[120:123], v[36:39]\n"
      "ds_read_b64_tr_b16 a[52:53], v98 offset:17040\n"
      "ds_read_b64_tr_b16 a[54:55], v98 offset:19152\n"
      "v_mfma_f32_16x16x32_bf16 v[40:43], v[184:187], v[120:123], v[40:43]\n"
      "ds_read_b64_tr_b16 a[56:57], v98 offset:17184\n"
      "ds_read_b64_tr_b16 a[58:59], v98 offset:19296\n"
      "v_mfma_f32_16x16x32_bf16 v[44:47], a[32:35], v[120:123], v[44:47]\n"
      "ds_read_b128 v[124:127], v103 offset:2048\n"
      "ds_read_b128 v[24:27], v103 offset:22528\n"
      "v_mfma_f32_16x16x32_bf16 v[48:51], a[24:27], v[120:123], v[48:51]\n"
      "s_add_u32 m0, s77, 50688\n"
      "s_add_u32 s50, s82, 995328\n"
      "v_mfma_f32_16x16x32_bf16 v[68:71], a[28:31], v[164:167], v[68:71]\n"
      "buffer_load_dwordx4 v96, s[40:43], s50 offen lds\n"
      "s_add_u32 m0, s77, 51744\n"
      "v_mfma_f32_16x16x32_bf16 v[72:75], v[184:187], v[164:167], v[72:75]\n"
      "s_add_u32 s50, s82, 1002240\n"
      "buffer_load_dwordx4 v96, s[40:43], s50 offen lds\n"
      "v_mfma_f32_16x16x32_bf16 v[76:79], a[32:35], v[164:167], v[76:79]\n"
      "s_add_u32 m0, s77, 52800\n"
      "s_add_u32 s50, s82, 1009152\n"
      "v_mfma_f32_16x16x32_bf16 v[80:83], a[24:27], v[164:167], v[80:83]\n"
      "buffer_load_dwordx4 v96, s[40:43], s50 offen lds\n"
      "s_add_u32 m0, s77, 53856\n"
      "v_mfma_f32_16x16x32_bf16 v[52:55], a[28:31], v[20:23], v[52:55]\n"
      "s_add_u32 s50, s82, 1016064\n"
      "buffer_load_dwordx4 v96, s[40:43], s50 offen lds\n"
      "v_mfma_f32_16x16x32_bf16 v[56:59], v[184:187], v[20:23], v[56:59]\n"
      "v_mfma_f32_16x16x32_bf16 v[60:63], a[32:35], v[20:23], v[60:63]\n"
      "v_mfma_f32_16x16x32_bf16 v[64:67], a[24:27], v[20:23], v[64:67]\n"
      "s_waitcnt vmcnt(16) lgkmcnt(0)\n"
      "s_barrier\n"
      "v_cvt_pk_bf16_f32 v188, v136, v137\n"
      "v_cvt_pk_bf16_f32 v189, v138, v139\n"
      "v_cvt_pk_bf16_f32 v190, v140, v141\n"
      "v_cvt_pk_bf16_f32 v191, v142, v143\n"
      "v_mfma_f32_16x16x32_bf16 v[128:131], v[144:147], v[154:157], 0\n"
      "ds_read_b32 v148, v100 offset:33792\n"
      "ds_read_b32 v150, v100 offset:35904\n"
      "v_mfma_f32_16x16x32_bf16 v[132:135], v[144:147], v[152:155], 0\n"
      "ds_read_b64_tr_b16 a[60:61], v98 offset:33792\n"
      "ds_read_b64_tr_b16 a[62:63], v98 offset:35904\n"
      "v_mfma_f32_16x16x32_bf16 v[36:39], a[40:43], v[124:127], v[36:39]\n"
      "ds_read_b64_tr_b16 a[64:65], v98 offset:33936\n"
      "ds_read_b64_tr_b16 a[66:67], v98 offset:36048\n"
      "v_mfma_f32_16x16x32_bf16 v[40:43], v[188:191], v[124:127], v[40:43]\n"
      "ds_read_b64_tr_b16 a[68:69], v98 offset:34080\n"
      "ds_read_b64_tr_b16 a[70:71], v98 offset:36192\n"
      "v_mfma_f32_16x16x32_bf16 v[44:47], a[44:47], v[124:127], v[44:47]\n"
      "ds_read_b128 v[120:123], v101 offset:4096\n"
      "ds_read_b128 v[28:31], v101 offset:24576\n"
      "v_mfma_f32_16x16x32_bf16 v[48:51], a[36:39], v[124:127], v[48:51]\n"
      "s_add_u32 m0, s77, 67584\n"
      "s_add_u32 s50, s82, 1105920\n"
      "v_mfma_f32_16x16x32_bf16 v[68:71], a[40:43], v[164:167], v[68:71]\n"
      "buffer_load_dwordx4 v96, s[40:43], s50 offen lds\n"
      "s_add_u32 m0, s77, 68640\n"
      "v_mfma_f32_16x16x32_bf16 v[72:75], v[188:191], v[164:167], v[72:75]\n"
      "s_add_u32 s50, s82, 1112832\n"
      "buffer_load_dwordx4 v96, s[40:43], s50 offen lds\n"
      "v_mfma_f32_16x16x32_bf16 v[76:79], a[44:47], v[164:167], v[76:79]\n"
      "s_add_u32 m0, s77, 69696\n"
      "s_add_u32 s50, s82, 1119744\n"
      "v_mfma_f32_16x16x32_bf16 v[80:83], a[36:39], v[164:167], v[80:83]\n"
      "buffer_load_dwordx4 v96, s[40:43], s50 offen lds\n"
      "s_add_u32 m0, s77, 70752\n"
      "v_mfma_f32_16x16x32_bf16 v[52:55], a[40:43], v[24:27], v[52:55]\n"
      "s_add_u32 s50, s82, 1126656\n"
      "buffer_load_dwordx4 v96, s[40:43], s50 offen lds\n"
      "v_mfma_f32_16x16x32_bf16 v[56:59], v[188:191], v[24:27], v[56:59]\n"
      "v_mfma_f32_16x16x32_bf16 v[60:63], a[44:47], v[24:27], v[60:63]\n"
      "v_mfma_f32_16x16x32_bf16 v[64:67], a[36:39], v[24:27], v[64:67]\n"
      "s_waitcnt vmcnt(16) lgkmcnt(0)\n"
      "s_barrier\n"
      "v_cvt_pk_bf16_f32 v192, v128, v129\n"
      "v_cvt_pk_bf16_f32 v193, v130, v131\n"
      "v_cvt_pk_bf16_f32 v194, v132, v133\n"
      "v_cvt_pk_bf16_f32 v195, v134, v135\n"
      "v_mfma_f32_16x16x32_bf16 v[136:139], v[148:151], v[154:157], 0\n"
      "ds_read_b32 v144, v99 offset:0\n"
      "ds_read_b32 v146, v99 offset:2112\n"
      "v_mfma_f32_16x16x32_bf16 v[140:143], v[148:151], v[152:155], 0\n"
      "ds_read_b64_tr_b16 a[72:73], v97 offset:0\n"
      "ds_read_b64_tr_b16 a[74:75], v97 offset:2112\n"
      "v_mfma_f32_16x16x32_bf16 v[36:39], a[52:55], v[120:123], v[36:39]\n"
      "ds_read_b64_tr_b16 a[76:77], v97 offset:144\n"
      "ds_read_b64_tr_b16 a[78:79], v97 offset:2256\n"
      "v_mfma_f32_16x16x32_bf16 v[40:43], v[192:195], v[120:123], v[40:43]\n"
      "ds_read_b64_tr_b16 a[80:81], v97 offset:288\n"
      "ds_read_b64_tr_b16 a[82:83], v97 offset:2400\n"
      "v_mfma_f32_16x16x32_bf16 v[44:47], a[56:59], v[120:123], v[44:47]\n"
      "ds_read_b128 v[124:127], v103 offset:4096\n"
      "ds_read_b128 v[32:35], v103 offset:24576\n"
      "v_mfma_f32_16x16x32_bf16 v[48:51], a[48:51], v[120:123], v[48:51]\n"
      "s_add_u32 m0, s77, 84480\n"
      "s_add_u32 s50, s82, 1216512\n"
      "v_mfma_f32_16x16x32_bf16 v[68:71], a[52:55], v[164:167], v[68:71]\n"
      "buffer_load_dwordx4 v96, s[40:43], s50 offen lds\n"
      "s_add_u32 m0, s77, 85536\n"
      "v_mfma_f32_16x16x32_bf16 v[72:75], v[192:195], v[164:167], v[72:75]\n"
      "s_add_u32 s50, s82, 1223424\n"
      "buffer_load_dwordx4 v96, s[40:43], s50 offen lds\n"
      "v_mfma_f32_16x16x32_bf16 v[76:79], a[56:59], v[164:167], v[76:79]\n"
      "s_add_u32 m0, s77, 86592\n"
      "s_add_u32 s50, s82, 1230336\n"
      "v_mfma_f32_16x16x32_bf16 v[80:83], a[48:51], v[164:167], v[80:83]\n"
      "buffer_load_dwordx4 v96, s[40:43], s50 offen lds\n"
      "s_add_u32 m0, s77, 87648\n"
      "v_mfma_f32_16x16x32_bf16 v[52:55], a[52:55], v[28:31], v[52:55]\n"
      "s_add_u32 s50, s82, 1237248\n"
      "buffer_load_dwordx4 v96, s[40:43], s50 offen lds\n"
      "v_mfma_f32_16x16x32_bf16 v[56:59], v[192:195], v[28:31], v[56:59]\n"
      "v_mfma_f32_16x16x32_bf16 v[60:63], a[56:59], v[28:31], v[60:63]\n"
      "v_mfma_f32_16x16x32_bf16 v[64:67], a[48:51], v[28:31], v[64:67]\n"
      "s_waitcnt vmcnt(16) lgkmcnt(0)\n"
      "s_barrier\n"
      "v_cvt_pk_bf16_f32 v196, v136, v137\n"
      "v_cvt_pk_bf16_f32 v197, v138, v139\n"
      "v_cvt_pk_bf16_f32 v198, v140, v141\n"
      "v_cvt_pk_bf16_f32 v199, v142, v143\n"
      "v_mfma_f32_16x16x32_bf16 v[128:131], v[144:147], v[154:157], 0\n"
      "ds_read_b32 v148, v99 offset:16896\n"
      "ds_read_b32 v150, v99 offset:19008\n"
      "v_mfma_f32_16x16x32_bf16 v[132:135], v[144:147], v[152:155], 0\n"
      "ds_read_b64_tr_b16 a[84:85], v97 offset:16896\n"
      "ds_read_b64_tr_b16 a[86:87], v97 offset:19008\n"
      "v_mfma_f32_16x16x32_bf16 v[36:39], a[64:67], v[124:127], v[36:39]\n"
      "ds_read_b64_tr_b16 a[88:89], v97 offset:17040\n"
      "ds_read_b64_tr_b16 a[90:91], v97 offset:19152\n"
      "v_mfma_f32_16x16x32_bf16 v[40:43], v[196:199], v[124:127], v[40:43]\n"
      "ds_read_b64_tr_b16 a[92:93], v97 offset:17184\n"
      "ds_read_b64_tr_b16 a[94:95], v97 offset:19296\n"
      "v_mfma_f32_16x16x32_bf16 v[44:47], a[68:71], v[124:127], v[44:47]\n"
      "ds_read_b128 v[120:123], v101 offset:6144\n"
      "ds_read_b128 v[20:23], v101 offset:26624\n"
      "v_mfma_f32_16x16x32_bf16 v[48:51], a[60:63], v[124:127], v[48:51]\n"
      "s_add_u32 m0, s77, 0\n"
      "s_add_u32 s50, s82, 1327104\n"
      "v_mfma_f32_16x16x32_bf16 v[68:71], a[64:67], v[164:167], v[68:71]\n"
      "buffer_load_dwordx4 v96, s[40:43], s50 offen lds\n"
      "s_add_u32 m0, s77, 1056\n"
      "v_mfma_f32_16x16x32_bf16 v[72:75], v[196:199], v[164:167], v[72:75]\n"
      "s_add_u32 s50, s82, 1334016\n"
      "buffer_load_dwordx4 v96, s[40:43], s50 offen lds\n"
      "v_mfma_f32_16x16x32_bf16 v[76:79], a[68:71], v[164:167], v[76:79]\n"
      "s_add_u32 m0, s77, 2112\n"
      "s_add_u32 s50, s82, 1340928\n"
      "v_mfma_f32_16x16x32_bf16 v[80:83], a[60:63], v[164:167], v[80:83]\n"
      "buffer_load_dwordx4 v96, s[40:43], s50 offen lds\n"
      "s_add_u32 m0, s77, 3168\n"
      "v_mfma_f32_16x16x32_bf16 v[52:55], a[64:67], v[32:35], v[52:55]\n"
      "s_add_u32 s50, s82, 1347840\n"
      "buffer_load_dwordx4 v96, s[40:43], s50 offen lds\n"
      "v_mfma_f32_16x16x32_bf16 v[56:59], v[196:199], v[32:35], v[56:59]\n"
      "v_mfma_f32_16x16x32_bf16 v[60:63], a[68:71], v[32:35], v[60:63]\n"
      "v_mfma_f32_16x16x32_bf16 v[64:67], a[60:63], v[32:35], v[64:67]\n"
      "s_waitcnt vmcnt(16) lgkmcnt(0)\n"
      "s_barrier\n"
      "v_cvt_pk_bf16_f32 v200, v128, v129\n"
      "v_cvt_pk_bf16_f32 v201, v130, v131\n"
      "v_cvt_pk_bf16_f32 v202, v132, v133\n"
      "v_cvt_pk_bf16_f32 v203, v134, v135\n"
      "v_mfma_f32_16x16x32_bf16 v[136:139], v[148:151], v[154:157], 0\n"
      "ds_read_b32 v144, v99 offset:33792\n"
      "ds_read_b32 v146, v99 offset:35904\n"
      "v_mfma_f32_16x16x32_bf16 v[140:143], v[148:151], v[152:155], 0\n"
      "ds_read_b64_tr_b16 a[96:97], v97 offset:33792\n"
      "ds_read_b64_tr_b16 a[98:99], v97 offset:35904\n"
      "v_mfma_f32_16x16x32_bf16 v[36:39], a[76:79], v[120:123], v[36:39]\n"
      "ds_read_b64_tr_b16 a[100:101], v97 offset:33936\n"
      "ds_read_b64_tr_b16 a[102:103], v97 offset:36048\n"
      "v_mfma_f32_16x16x32_bf16 v[40:43], v[200:203], v[120:123], v[40:43]\n"
      "ds_read_b64_tr_b16 a[104:105], v97 offset:34080\n"
      "ds_read_b64_tr_b16 a[106:107], v97 offset:36192\n"
      "v_mfma_f32_16x16x32_bf16 v[44:47], a[80:83], v[120:123], v[44:47]\n"
      "ds_read_b128 v[124:127], v103 offset:6144\n"
      "ds_read_b128 v[24:27], v103 offset:26624\n"
      "v_mfma_f32_16x16x32_bf16 v[48:51], a[72:75], v[120:123], v[48:51]\n"
      "s_add_u32 m0, s77, 16896\n"
      "s_add_u32 s50, s82, 1437696\n"
      "v_mfma_f32_16x16x32_bf16 v[68:71], a[76:79], v[164:167], v[68:71]\n"
      "buffer_load_dwordx4 v96, s[40:43], s50 offen lds\n"
      "s_add_u32 m0, s77, 17952\n"
      "v_mfma_f32_16x16x32_bf16 v[72:75], v[200:203], v[164:167], v[72:75]\n"
      "s_add_u32 s50, s82, 1444608\n"
      "buffer_load_dwordx4 v96, s[40:43], s50 offen lds\n"
      "v_mfma_f32_16x16x32_bf16 v[76:79], a[80:83], v[164:167], v[76:79]\n"
      "s_add_u32 m0, s77, 19008\n"
      "s_add_u32 s50, s82, 1451520\n"
      "v_mfma_f32_16x16x32_bf16 v[80:83], a[72:75], v[164:167], v[80:83]\n"
      "buffer_load_dwordx4 v96, s[40:43], s50 offen lds\n"
      "s_add_u32 m0, s77, 20064\n"
      "v_mfma_f32_16x16x32_bf16 v[52:55], a[76:79], v[20:23], v[52:55]\n"
      "s_add_u32 s50, s82, 1458432\n"
      "buffer_load_dwordx4 v96, s[40:43], s50 offen lds\n"
      "v_mfma_f32_16x16x32_bf16 v[56:59], v[200:203], v[20:23], v[56:59]\n"
      "v_mfma_f32_16x16x32_bf16 v[60:63], a[80:83], v[20:23], v[60:63]\n"
      "v_mfma_f32_16x16x32_bf16 v[64:67], a[72:75], v[20:23], v[64:67]\n"
      "s_waitcnt vmcnt(16) lgkmcnt(0)\n"
      "s_barrier\n"
      "v_cvt_pk_bf16_f32 v204, v136, v137\n"
      "v_cvt_pk_bf16_f32 v205, v138, v139\n"
      "v_cvt_pk_bf16_f32 v206, v140, v141\n"
      "v_cvt_pk_bf16_f32 v207, v142, v143\n"
      "v_mfma_f32_16x16x32_bf16 v[128:131], v[144:147], v[154:157], 0\n"
      "ds_read_b32 v148, v100 offset:0\n"
      "ds_read_b32 v150, v100 offset:2112\n"
      "v_mfma_f32_16x16x32_bf16 v[132:135], v[144:147], v[152:155], 0\n"
      "ds_read_b64_tr_b16 a[108:109], v98 offset:0\n"
      "ds_read_b64_tr_b16 a[110:111], v98 offset:2112\n"
      "v_mfma_f32_16x16x32_bf16 v[36:39], a[88:91], v[124:127], v[36:39]\n"
      "ds_read_b64_tr_b16 a[112:113], v98 offset:144\n"
      "ds_read_b64_tr_b16 a[114:115], v98 offset:2256\n"
      "v_mfma_f32_16x16x32_bf16 v[40:43], v[204:207], v[124:127], v[40:43]\n"
      "ds_read_b64_tr_b16 a[116:117], v98 offset:288\n"
      "ds_read_b64_tr_b16 a[118:119], v98 offset:2400\n"
      "v_mfma_f32_16x16x32_bf16 v[44:47], a[92:95], v[124:127], v[44:47]\n"
      "ds_read_b128 v[120:123], v101 offset:8192\n"
      "ds_read_b128 v[28:31], v101 offset:28672\n"
      "v_mfma_f32_16x16x32_bf16 v[48:51], a[84:87], v[124:127], v[48:51]\n"
      "s_add_u32 m0, s77, 33792\n"
      "s_add_u32 s50, s82, 1548288\n"
      "v_mfma_f32_16x16x32_bf16 v[68:71], a[88:91], v[164:167], v[68:71]\n"
      "buffer_load_dwordx4 v96, s[40:43], s50 offen lds\n"
      "s_add_u32 m0, s77, 34848\n"
      "v_mfma_f32_16x16x32_bf16 v[72:75], v[204:207], v[164:167], v[72:75]\n"
      "s_add_u32 s50, s82, 1555200\n"
      "buffer_load_dwordx4 v96, s[40:43], s50 offen lds\n"
      "v_mfma_f32_16x16x32_bf16 v[76:79], a[92:95], v[164:167], v[76:79]\n"
      "s_add_u32 m0, s77, 35904\n"
      "s_add_u32 s50, s82, 1562112\n"
      "v_mfma_f32_16x16x32_bf16 v[80:83], a[84:87], v[164:167], v[80:83]\n"
      "buffer_load_dwordx4 v96, s[40:43], s50 offen lds\n"
      "s_add_u32 m0, s77, 36960\n"
      "v_mfma_f32_16x16x32_bf16 v[52:55], a[88:91], v[24:27], v[52:55]\n"
      "s_add_u32 s50, s82, 1569024\n"
      "buffer_load_dwordx4 v96, s[40:43], s50 offen lds\n"
      "v_mfma_f32_16x16x32_bf16 v[56:59], v[204:207], v[24:27], v[56:59]\n"
      "v_mfma_f32_16x16x32_bf16 v[60:63], a[92:95], v[24:27], v[60:63]\n"
      "v_mfma_f32_16x16x32_bf16 v[64:67], a[84:87], v[24:27], v[64:67]\n"
      "s_waitcnt vmcnt(16) lgkmcnt(0)\n"
      "s_barrier\n"
      "v_cvt_pk_bf16_f32 v208, v128, v129\n"
      "v_cvt_pk_bf16_f32 v209, v130, v131\n"
      "v_cvt_pk_bf16_f32 v210, v132, v133\n"
      "v_cvt_pk_bf16_f32 v211, v134, v135\n"
      "v_mfma_f32_16x16x32_bf16 v[136:139], v[148:151], v[154:157], 0\n"
      "ds_read_b32 v144, v100 offset:16896\n"
      "ds_read_b32 v146, v100 offset:19008\n"
      "v_mfma_f32_16x16x32_bf16 v[140:143], v[148:151], v[152:155], 0\n"
      "ds_read_b64_tr_b16 a[120:121], v98 offset:16896\n"
      "ds_read_b64_tr_b16 a[122:123], v98 offset:19008\n"
      "v_mfma_f32_16x16x32_bf16 v[36:39], a[100:103], v[120:123], v[36:39]\n"
      "ds_read_b64_tr_b16 a[124:125], v98 offset:17040\n"
      "ds_read_b64_tr_b16 a[126:127], v98 offset:19152\n"
      "v_mfma_f32_16x16x32_bf16 v[40:43], v[208:211], v[120:123], v[40:43]\n"
      "ds_read_b64_tr_b16 a[128:129], v98 offset:17184\n"
      "ds_read_b64_tr_b16 a[130:131], v98 offset:19296\n"
      "v_mfma_f32_16x16x32_bf16 v[44:47], a[104:107], v[120:123], v[44:47]\n"
      "ds_read_b128 v[124:127], v103 offset:8192\n"
      "ds_read_b128 v[32:35], v103 offset:28672\n"
      "v_mfma_f32_16x16x32_bf16 v[48:51], a[96:99], v[120:123], v[48:51]\n"
      "s_add_u32 m0, s77, 50688\n"
      "s_add_u32 s50, s82, 1658880\n"
      "v_mfma_f32_16x16x32_bf16 v[68:71], a[100:103], v[164:167], v[68:71]\n"
      "buffer_load_dwordx4 v96, s[40:43], s50 offen lds\n"
      "s_add_u32 m0, s77, 51744\n"
      "v_mfma_f32_16x16x32_bf16 v[72:75], v[208:211], v[164:167], v[72:75]\n"
      "s_add_u32 s50, s82, 1665792\n"
      "buffer_load_dwordx4 v96, s[40:43], s50 offen lds\n"
      "v_mfma_f32_16x16x32_bf16 v[76:79], a[104:107], v[164:167], v[76:79]\n"
      "s_add_u32 m0, s77, 52800\n"
      "s_add_u32 s50, s82, 1672704\n"
      "v_mfma_f32_16x16x32_bf16 v[80:83], a[96:99], v[164:167], v[80:83]\n"
      "buffer_load_dwordx4 v96, s[40:43], s50 offen lds\n"
      "s_add_u32 m0, s77, 53856\n"
      "v_mfma_f32_16x16x32_bf16 v[52:55], a[100:103], v[28:31], v[52:55]\n"
      "s_add_u32 s50, s82, 1679616\n"
      "buffer_load_dwordx4 v96, s[40:43], s50 offen lds\n"
      "v_mfma_f32_16x16x32_bf16 v[56:59], v[208:211], v[28:31], v[56:59]\n"
      "v_mfma_f32_16x16x32_bf16 v[60:63], a[104:107], v[28:31], v[60:63]\n"
      "v_mfma_f32_16x16x32_bf16 v[64:67], a[96:99], v[28:31], v[64:67]\n"
      "s_waitcnt vmcnt(16) lgkmcnt(0)\n"
      "s_barrier\n"
      "v_cvt_pk_bf16_f32 v212, v136, v137\n"
      "v_cvt_pk_bf16_f32 v213, v138, v139\n"
      "v_cvt_pk_bf16_f32 v214, v140, v141\n"
      "v_cvt_pk_bf16_f32 v215, v142, v143\n"
      "v_mfma_f32_16x16x32_bf16 v[128:131], v[144:147], v[154:157], 0\n"
      "ds_read_b32 v148, v100 offset:33792\n"
      "ds_read_b32 v150, v100 offset:35904\n"
      "v_mfma_f32_16x16x32_bf16 v[132:135], v[144:147], v[152:155], 0\n"
      "ds_read_b64_tr_b16 a[132:133], v98 offset:33792\n"
      "ds_read_b64_tr_b16 a[134:135], v98 offset:35904\n"
      "v_mfma_f32_16x16x32_bf16 v[36:39], a[112:115], v[124:127], v[36:39]\n"
      "ds_read_b64_tr_b16 a[136:137], v98 offset:33936\n"
      "ds_read_b64_tr_b16 a[138:139], v98 offset:36048\n"
      "v_mfma_f32_16x16x32_bf16 v[40:43], v[212:215], v[124:127], v[40:43]\n"
      "ds_read_b64_tr_b16 a[140:141], v98 offset:34080\n"
      "ds_read_b64_tr_b16 a[142:143], v98 offset:36192\n"
      "v_mfma_f32_16x16x32_bf16 v[44:47], a[116:119], v[124:127], v[44:47]\n"
      "ds_read_b128 v[120:123], v101 offset:10240\n"
      "ds_read_b128 v[20:23], v101 offset:30720\n"
      "v_mfma_f32_16x16x32_bf16 v[48:51], a[108:111], v[124:127], v[48:51]\n"
      "s_add_u32 m0, s77, 67584\n"
      "s_add_u32 s50, s82, 1769472\n"
      "v_mfma_f32_16x16x32_bf16 v[68:71], a[112:115], v[164:167], v[68:71]\n"
      "buffer_load_dwordx4 v96, s[40:43], s50 offen lds\n"
      "s_add_u32 m0, s77, 68640\n"
      "v_mfma_f32_16x16x32_bf16 v[72:75], v[212:215], v[164:167], v[72:75]\n"
      "s_add_u32 s50, s82, 1776384\n"
      "buffer_load_dwordx4 v96, s[40:43], s50 offen lds\n"
      "v_mfma_f32_16x16x32_bf16 v[76:79], a[116:119], v[164:167], v[76:79]\n"
      "s_add_u32 m0, s77, 69696\n"
      "s_add_u32 s50, s82, 1783296\n"
      "v_mfma_f32_16x16x32_bf16 v[80:83], a[108:111], v[164:167], v[80:83]\n"
      "buffer_load_dwordx4 v96, s[40:43], s50 offen lds\n"
      "s_add_u32 m0, s77, 70752\n"
      "v_mfma_f32_16x16x32_bf16 v[52:55], a[112:115], v[32:35], v[52:55]\n"
      "s_add_u32 s50, s82, 1790208\n"
      "buffer_load_dwordx4 v96, s[40:43], s50 offen lds\n"
      "v_mfma_f32_16x16x32_bf16 v[56:59], v[212:215], v[32:35], v[56:59]\n"
      "v_mfma_f32_16x16x32_bf16 v[60:63], a[116:119], v[32:35], v[60:63]\n"
      "v_mfma_f32_16x16x32_bf16 v[64:67], a[108:111], v[32:35], v[64:67]\n"
      "s_waitcnt vmcnt(16) lgkmcnt(0)\n"
      "s_barrier\n"
      "v_cvt_pk_bf16_f32 v216, v128, v129\n"
      "v_cvt_pk_bf16_f32 v217, v130, v131\n"
      "v_cvt_pk_bf16_f32 v218, v132, v133\n"
      "v_cvt_pk_bf16_f32 v219, v134, v135\n"
      "v_mfma_f32_16x16x32_bf16 v[136:139], v[148:151], v[154:157], 0\n"
      "ds_read_b32 v144, v99 offset:0\n"
      "ds_read_b32 v146, v99 offset:2112\n"
      "v_mfma_f32_16x16x32_bf16 v[140:143], v[148:151], v[152:155], 0\n"
      "ds_read_b64_tr_b16 a[144:145], v97 offset:0\n"
      "ds_read_b64_tr_b16 a[146:147], v97 offset:2112\n"
      "v_mfma_f32_16x16x32_bf16 v[36:39], a[124:127], v[120:123], v[36:39]\n"
      "ds_read_b64_tr_b16 a[148:149], v97 offset:144\n"
      "ds_read_b64_tr_b16 a[150:151], v97 offset:2256\n"
      "v_mfma_f32_16x16x32_bf16 v[40:43], v[216:219], v[120:123], v[40:43]\n"
      "ds_read_b64_tr_b16 a[152:153], v97 offset:288\n"
      "ds_read_b64_tr_b16 a[154:155], v97 offset:2400\n"
      "v_mfma_f32_16x16x32_bf16 v[44:47], a[128:131], v[120:123], v[44:47]\n"
      "ds_read_b128 v[124:127], v103 offset:10240\n"
      "ds_read_b128 v[24:27], v103 offset:30720\n"
      "v_mfma_f32_16x16x32_bf16 v[48:51], a[120:123], v[120:123], v[48:51]\n"
      "s_add_u32 m0, s77, 84480\n"
      "s_add_u32 s50, s82, 1880064\n"
      "v_mfma_f32_16x16x32_bf16 v[68:71], a[124:127], v[164:167], v[68:71]\n"
      "buffer_load_dwordx4 v96, s[40:43], s50 offen lds\n"
      "s_add_u32 m0, s77, 85536\n"
      "v_mfma_f32_16x16x32_bf16 v[72:75], v[216:219], v[164:167], v[72:75]\n"
      "s_add_u32 s50, s82, 1886976\n"
      "buffer_load_dwordx4 v96, s[40:43], s50 offen lds\n"
      "v_mfma_f32_16x16x32_bf16 v[76:79], a[128:131], v[164:167], v[76:79]\n"
      "s_add_u32 m0, s77, 86592\n"
      "s_add_u32 s50, s82, 1893888\n"
      "v_mfma_f32_16x16x32_bf16 v[80:83], a[120:123], v[164:167], v[80:83]\n"
      "buffer_load_dwordx4 v96, s[40:43], s50 offen lds\n"
      "s_add_u32 m0, s77, 87648\n"
      "v_mfma_f32_16x16x32_bf16 v[52:55], a[124:127], v[20:23], v[52:55]\n"
      "s_add_u32 s50, s82, 1900800\n"
      "buffer_load_dwordx4 v96, s[40:43], s50 offen lds\n"
      "v_mfma_f32_16x16x32_bf16 v[56:59], v[216:219], v[20:23], v[56:59]\n"
      "v_mfma_f32_16x16x32_bf16 v[60:63], a[128:131], v[20:23], v[60:63]\n"
      "v_mfma_f32_16x16x32_bf16 v[64:67], a[120:123], v[20:23], v[64:67]\n"
      "s_waitcnt vmcnt(16) lgkmcnt(0)\n"
      "s_barrier\n"
      "v_cvt_pk_bf16_f32 v220, v136, v137\n"
      "v_cvt_pk_bf16_f32 v221, v138, v139\n"
      "v_cvt_pk_bf16_f32 v222, v140, v141\n"
      "v_cvt_pk_bf16_f32 v223, v142, v143\n"
      "v_mfma_f32_16x16x32_bf16 v[128:131], v[144:147], v[154:157], 0\n"
      "ds_read_b32 v148, v99 offset:16896\n"
      "ds_read_b32 v150, v99 offset:19008\n"
      "v_mfma_f32_16x16x32_bf16 v[132:135], v[144:147], v[152:155], 0\n"
      "ds_read_b64_tr_b16 a[156:157], v97 offset:16896\n"
      "ds_read_b64_tr_b16 a[158:159], v97 offset:19008\n"
      "v_mfma_f32_16x16x32_bf16 v[36:39], a[136:139], v[124:127], v[36:39]\n"
      "ds_read_b64_tr_b16 a[160:161], v97 offset:17040\n"
      "ds_read_b64_tr_b16 a[162:163], v97 offset:19152\n"
      "v_mfma_f32_16x16x32_bf16 v[40:43], v[220:223], v[124:127], v[40:43]\n"
      "ds_read_b64_tr_b16 a[164:165], v97 offset:17184\n"
      "ds_read_b64_tr_b16 a[166:167], v97 offset:19296\n"
      "v_mfma_f32_16x16x32_bf16 v[44:47], a[140:143], v[124:127], v[44:47]\n"
      "ds_read_b128 v[120:123], v101 offset:12288\n"
      "ds_read_b128 v[28:31], v101 offset:32768\n"
      "v_mfma_f32_16x16x32_bf16 v[48:51], a[132:135], v[124:127], v[48:51]\n"
      "buffer_load_dwordx2 v[18:19], v102, s[36:39], s71 offen offset:8 sc1\n"
      "v_mfma_f32_16x16x32_bf16 v[68:71], a[136:139], v[164:167], v[68:71]\n"
      "v_mfma_f32_16x16x32_bf16 v[72:75], v[220:223], v[164:167], v[72:75]\n"
      "v_mfma_f32_16x16x32_bf16 v[76:79], a[140:143], v[164:167], v[76:79]\n"
      "v_mfma_f32_16x16x32_bf16 v[80:83], a[132:135], v[164:167], v[80:83]\n"
      "v_mfma_f32_16x16x32_bf16 v[52:55], a[136:139], v[24:27], v[52:55]\n"
      "v_mfma_f32_16x16x32_bf16 v[56:59], v[220:223], v[24:27], v[56:59]\n"
      "v_mfma_f32_16x16x32_bf16 v[60:63], a[140:143], v[24:27], v[60:63]\n"
      "v_mfma_f32_16x16x32_bf16 v[64:67], a[132:135], v[24:27], v[64:67]\n"
      "s_waitcnt vmcnt(13) lgkmcnt(0)\n"
      "s_barrier\n"
      "v_cvt_pk_bf16_f32 v224, v128, v129\n"
      "v_cvt_pk_bf16_f32 v225, v130, v131\n"
      "v_cvt_pk_bf16_f32 v226, v132, v133\n"
      "v_cvt_pk_bf16_f32 v227, v134, v135\n"
      "v_mfma_f32_16x16x32_bf16 v[136:139], v[148:151], v[154:157], 0\n"
      "ds_read_b32 v144, v99 offset:33792\n"
      "ds_read_b32 v146, v99 offset:35904\n"
      "v_mfma_f32_16x16x32_bf16 v[140:143], v[148:151], v[152:155], 0\n"
      "ds_read_b64_tr_b16 a[168:169], v97 offset:33792\n"
      "ds_read_b64_tr_b16 a[170:171], v97 offset:35904\n"
      "v_mfma_f32_16x16x32_bf16 v[36:39], a[148:151], v[120:123], v[36:39]\n"
      "ds_read_b64_tr_b16 a[172:173], v97 offset:33936\n"
      "ds_read_b64_tr_b16 a[174:175], v97 offset:36048\n"
      "v_mfma_f32_16x16x32_bf16 v[40:43], v[224:227], v[120:123], v[40:43]\n"
      "ds_read_b64_tr_b16 a[176:177], v97 offset:34080\n"
      "ds_read_b64_tr_b16 a[178:179], v97 offset:36192\n"
      "v_mfma_f32_16x16x32_bf16 v[44:47], a[152:155], v[120:123], v[44:47]\n"
      "ds_read_b128 v[124:127], v103 offset:12288\n"
      "ds_read_b128 v[32:35], v103 offset:32768\n"
      "v_mfma_f32_16x16x32_bf16 v[48:51], a[144:147], v[120:123], v[48:51]\n"
      "v_mfma_f32_16x16x32_bf16 v[68:71], a[148:151], v[164:167], v[68:71]\n"
      "v_mfma_f32_16x16x32_bf16 v[72:75], v[224:227], v[164:167], v[72:75]\n"
      "v_mfma_f32_16x16x32_bf16 v[76:79], a[152:155], v[164:167], v[76:79]\n"
      "v_mfma_f32_16x16x32_bf16 v[80:83], a[144:147], v[164:167], v[80:83]\n"
      "v_mfma_f32_16x16x32_bf16 v[52:55], a[148:151], v[28:31], v[52:55]\n"
      "v_mfma_f32_16x16x32_bf16 v[56:59], v[224:227], v[28:31], v[56:59]\n"
      "v_mfma_f32_16x16x32_bf16 v[60:63], a[152:155], v[28:31], v[60:63]\n"
      "v_mfma_f32_16x16x32_bf16 v[64:67], a[144:147], v[28:31], v[64:67]\n"
      "s_waitcnt vmcnt(9) lgkmcnt(0)\n"
      "s_barrier\n"
      "v_cvt_pk_bf16_f32 v228, v136, v137\n"
      "v_cvt_pk_bf16_f32 v229, v138, v139\n"
      "v_cvt_pk_bf16_f32 v230, v140, v141\n"
      "v_cvt_pk_bf16_f32 v231, v142, v143\n"
      "v_mfma_f32_16x16x32_bf16 v[128:131], v[144:147], v[154:157], 0\n"
      "ds_read_b32 v148, v100 offset:0\n"
      "ds_read_b32 v150, v100 offset:2112\n"
      "ds_read_b64_tr_b16 a[180:181], v98 offset:0\n"
      "v_mfma_f32_16x16x32_bf16 v[132:135], v[144:147], v[152:155], 0\n"
      "ds_read_b64_tr_b16 a[182:183], v98 offset:2112\n"
      "ds_read_b64_tr_b16 a[184:185], v98 offset:144\n"
      "ds_read_b64_tr_b16 a[186:187], v98 offset:2256\n"
      "v_mfma_f32_16x16x32_bf16 v[36:39], a[160:163], v[124:127], v[36:39]\n"
      "ds_read_b64_tr_b16 a[188:189], v98 offset:288\n"
      "ds_read_b64_tr_b16 a[190:191], v98 offset:2400\n"
      "ds_read_b128 v[120:123], v101 offset:14336\n"
      "v_mfma_f32_16x16x32_bf16 v[40:43], v[228:231], v[124:127], v[40:43]\n"
      "ds_read_b128 v[20:23], v101 offset:34816\n"
      "s_waitcnt vmcnt(0)\n"
      "v_cmp_ne_u64 vcc, s[98:99], v[18:19]\n"
      "s_cbranch_vccnz .Lsl0\n"
      ".Lbk0:\n"
      "v_mfma_f32_16x16x32_bf16 v[44:47], a[164:167], v[124:127], v[44:47]\n"
      "s_cmp_lt_u32 s28, 2\n"
      "s_cselect_b32 s51, 162816, s78\n"
      "s_cselect_b32 s52, 0, 20480\n"
      "v_mfma_f32_16x16x32_bf16 v[48:51], a[156:159], v[124:127], v[48:51]\n"
      "s_mov_b32 m0, s51\n"
      "s_nop 0\n"
      "buffer_load_dwordx4 v4, s[36:39], s69 offen sc1 lds\n"
      "v_mfma_f32_16x16x32_bf16 v[68:71], a[160:163], v[164:167], v[68:71]\n"
      "s_add_u32 s69, s69, 20480\n"
      "s_add_u32 m0, s51, s52\n"
      "s_nop 0\n"
      "v_mfma_f32_16x16x32_bf16 v[72:75], v[228:231], v[164:167], v[72:75]\n"
      "buffer_load_dwordx4 v4, s[36:39], s69 offen sc1 lds\n"
      "s_add_u32 s69, s69, 20480\n"
      "s_add_u32 m0, s83, 40960\n"
      "v_mfma_f32_16x16x32_bf16 v[76:79], a[164:167], v[164:167], v[76:79]\n"
      "s_nop 0\n"
      "buffer_load_dwordx4 v2, s[36:39], s69 offen sc1 lds\n"
      "buffer_load_dwordx4 v2, s[36:39], s69 offen offset:1024 sc1 lds\n"
      "v_mfma_f32_16x16x32_bf16 v[80:83], a[156:159], v[164:167], v[80:83]\n"
      "buffer_load_dwordx4 v2, s[36:39], s69 offen offset:2048 sc1 lds\n"
      "buffer_load_dwordx4 v2, s[36:39], s69 offen offset:3072 sc1 lds\n"
      "s_add_u32 m0, s78, 40960\n"
      "v_mfma_f32_16x16x32_bf16 v[52:55], a[160:163], v[32:35], v[52:55]\n"
      "s_nop 0\n"
      "buffer_load_dwordx4 v4, s[36:39], s69 offen sc1 lds\n"
      "s_add_u32 s69, s69, 20480\n"
      "v_mfma_f32_16x16x32_bf16 v[56:59], v[228:231], v[32:35], v[56:59]\n"
      "v_mfma_f32_16x16x32_bf16 v[60:63], a[164:167], v[32:35], v[60:63]\n"
      "v_mfma_f32_16x16x32_bf16 v[64:67], a[156:159], v[32:35], v[64:67]\n"
      "s_waitcnt lgkmcnt(0)\n"
      "s_barrier\n"
      "v_cvt_pk_bf16_f32 v232, v128, v129\n"
      "v_cvt_pk_bf16_f32 v233, v130, v131\n"
      "v_cvt_pk_bf16_f32 v234, v132, v133\n"
      "v_cvt_pk_bf16_f32 v235, v134, v135\n"
      "v_mfma_f32_16x16x32_bf16 v[136:139], v[148:151], v[154:157], 0\n"
      "ds_read_b32 v144, v100 offset:16896\n"
      "ds_read_b32 v146, v100 offset:19008\n"
      "ds_read_b64_tr_b16 a[192:193], v98 offset:16896\n"
      "v_mfma_f32_16x16x32_bf16 v[140:143], v[148:151], v[152:155], 0\n"
      "ds_read_b64_tr_b16 a[194:195], v98 offset:19008\n"
      "ds_read_b64_tr_b16 a[196:197], v98 offset:17040\n"
      "ds_read_b64_tr_b16 a[198:199], v98 offset:19152\n"
      "v_mfma_f32_16x16x32_bf16 v[36:39], a[172:175], v[120:123], v[36:39]\n"
      "ds_read_b64_tr_b16 a[200:201], v98 offset:17184\n"
      "ds_read_b64_tr_b16 a[202:203], v98 offset:19296\n"
      "ds_read_b128 v[124:127], v103 offset:14336\n"
      "v_mfma_f32_16x16x32_bf16 v[40:43], v[232:235], v[120:123], v[40:43]\n"
      "ds_read_b128 v[24:27], v103 offset:34816\n"
      "s_add_u32 m0, s83, 61440\n"
      "s_nop 0\n"
      "v_mfma_f32_16x16x32_bf16 v[44:47], a[176:179], v[120:123], v[44:47]\n"
      "buffer_load_dwordx4 v2, s[36:39], s69 offen sc1 lds\n"
      "buffer_load_dwordx4 v2, s[36:39], s69 offen offset:1024 sc1 lds\n"
      "buffer_load_dwordx4 v2, s[36:39], s69 offen offset:2048 sc1 lds\n"
      "v_mfma_f32_16x16x32_bf16 v[48:51], a[168:171], v[120:123], v[48:51]\n"
      "buffer_load_dwordx4 v2, s[36:39], s69 offen offset:3072 sc1 lds\n"
      "s_add_u32 m0, s78, 61440\n"
      "s_nop 0\n"
      "v_mfma_f32_16x16x32_bf16 v[68:71], a[172:175], v[164:167], v[68:71]\n"
      "buffer_load_dwordx4 v4, s[36:39], s69 offen sc1 lds\n"
      "s_add_u32 s69, s69, 20480\n"
      "s_add_u32 s71, s71, 6144\n"
      "v_mfma_f32_16x16x32_bf16 v[72:75], v[232:235], v[164:167], v[72:75]\n"
      "s_add_u32 s73, s73, 6\n"
      "s_add_u32 s72, s71, 1536\n"
      "s_add_u32 s70, s69, 20480\n"
      "v_mfma_f32_16x16x32_bf16 v[76:79], a[176:179], v[164:167], v[76:79]\n"
      "v_mov_b32 v18, v16\n"
      "v_mfma_f32_16x16x32_bf16 v[80:83], a[168:171], v[164:167], v[80:83]\n"
      "v_mfma_f32_16x16x32_bf16 v[52:55], a[172:175], v[20:23], v[52:55]\n"
      "v_mfma_f32_16x16x32_bf16 v[56:59], v[232:235], v[20:23], v[56:59]\n"
      "s_waitcnt lgkmcnt(0)\n"
      "s_barrier\n"
      "v_cvt_pk_bf16_f32 v236, v136, v137\n"
      "v_cvt_pk_bf16_f32 v237, v138, v139\n"
      "v_cvt_pk_bf16_f32 v238, v140, v141\n"
      "v_cvt_pk_bf16_f32 v239, v142, v143\n"
      "v_mfma_f32_16x16x32_bf16 v[128:131], v[144:147], v[154:157], 0\n"
      "ds_read_b32 v148, v100 offset:33792\n"
      "ds_read_b32 v150, v100 offset:35904\n"
      "v_mfma_f32_16x16x32_bf16 v[132:135], v[144:147], v[152:155], 0\n"
      "ds_read_b64_tr_b16 a[204:205], v98 offset:33792\n"
      "ds_read_b64_tr_b16 a[206:207], v98 offset:35904\n"
      "v_mfma_f32_16x16x32_bf16 v[36:39], a[184:187], v[124:127], v[36:39]\n"
      "ds_read_b64_tr_b16 a[208:209], v98 offset:33936\n"
      "ds_read_b64_tr_b16 a[210:211], v98 offset:36048\n"
      "v_mfma_f32_16x16x32_bf16 v[40:43], v[236:239], v[124:127], v[40:43]\n"
      "ds_read_b64_tr_b16 a[212:213], v98 offset:34080\n"
      "ds_read_b64_tr_b16 a[214:215], v98 offset:36192\n"
      "v_mfma_f32_16x16x32_bf16 v[44:47], a[188:191], v[124:127], v[44:47]\n"
      "ds_read_b128 v[120:123], v101 offset:16384\n"
      "ds_read_b128 v[28:31], v101 offset:36864\n"
      "v_mfma_f32_16x16x32_bf16 v[48:51], a[180:183], v[124:127], v[48:51]\n"
      "v_mfma_f32_16x16x32_bf16 v[68:71], a[184:187], v[164:167], v[68:71]\n"
      "v_mfma_f32_16x16x32_bf16 v[72:75], v[236:239], v[164:167], v[72:75]\n"
      "v_mfma_f32_16x16x32_bf16 v[76:79], a[188:191], v[164:167], v[76:79]\n"
      "v_mfma_f32_16x16x32_bf16 v[80:83], a[180:183], v[164:167], v[80:83]\n"
      "v_mfma_f32_16x16x32_bf16 v[52:55], a[184:187], v[24:27], v[52:55]\n"
      "v_mfma_f32_16x16x32_bf16 v[56:59], v[236:239], v[24:27], v[56:59]\n"
      "s_waitcnt vmcnt(5) lgkmcnt(0)\n"
      "s_barrier\n"
      "v_cvt_pk_bf16_f32 v240, v128, v129\n"
      "v_cvt_pk_bf16_f32 v241, v130, v131\n"
      "v_cvt_pk_bf16_f32 v242, v132, v133\n"
      "v_cvt_pk_bf16_f32 v243, v134, v135\n"
      "v_mfma_f32_16x16x32_bf16 v[136:139], v[148:151], v[154:157], 0\n"
      "ds_read_b128 v[124:127], v103 offset:16384\n"
      "ds_read_b128 v[32:35], v103 offset:36864\n"
      "ds_read_b64 v[158:159], v10 offset:18432\n"
      "v_mfma_f32_16x16x32_bf16 v[140:143], v[148:151], v[152:155], 0\n"
      "ds_read_b64 v[160:161], v10 offset:18944\n"
      "ds_read_b64 v[162:163], v10 offset:19456\n"
      "s_waitcnt lgkmcnt(0)\n"
      "v_add_f32 v158, v158, v160\n"
      "v_mfma_f32_16x16x32_bf16 v[36:39], a[196:199], v[120:123], v[36:39]\n"
      "v_add_f32 v159, v159, v161\n"
      "v_add_f32 v158, v158, v162\n"
      "v_add_f32 v159, v159, v163\n"
      "v_mfma_f32_16x16x32_bf16 v[40:43], v[240:243], v[120:123], v[40:43]\n"
      "s_nop 0\n"
      "v_permlane32_swap_b32 v158, v159\n"
      "v_add_f32 v158, v158, v159\n"
      "v_mov_b32 v160, v158\n"
      "v_mfma_f32_16x16x32_bf16 v[44:47], a[200:203], v[120:123], v[44:47]\n"
      "s_nop 0\n"
      "v_permlane16_swap_b32 v158, v160\n"
      "v_add_f32 v158, v158, v160\n"
      "v_mov_b32 v252, v158\n"
      "v_mfma_f32_16x16x32_bf16 v[48:51], a[192:195], v[120:123], v[48:51]\n"
      "s_nop 0\n"
      "v_permlane32_swap_b32 v252, v158\n"
      "v_mul_f32 v252, 0xbae38e39, v252\n"
      "v_fmaak_f32 v158, v17, v158, 0x3727c5ac\n"
      "v_mfma_f32_16x16x32_bf16 v[68:71], a[196:199], v[164:167], v[68:71]\n"
      "v_fma_f32 v158, -v252, v252, v158\n"
      "v_max_f32 v158, 0x3727c5ac, v158\n"
      "v_rsq_f32 v174, v158\n"
      "v_mfma_f32_16x16x32_bf16 v[72:75], v[240:243], v[164:167], v[72:75]\n"
      "v_mfma_f32_16x16x32_bf16 v[76:79], a[200:203], v[164:167], v[76:79]\n"
      "v_mfma_f32_16x16x32_bf16 v[80:83], a[192:195], v[164:167], v[80:83]\n"
      "s_barrier\n"
      "v_cvt_pk_bf16_f32 v244, v136, v137\n"
      "v_cvt_pk_bf16_f32 v245, v138, v139\n"
      "v_cvt_pk_bf16_f32 v246, v140, v141\n"
      "v_cvt_pk_bf16_f32 v247, v142, v143\n"
      "v_mfma_f32_16x16x32_bf16 v[36:39], a[208:211], v[124:127], v[36:39]\n"
      "ds_read_b64 v[158:159], v10 offset:38912\n"
      "ds_read_b64 v[160:161], v10 offset:39424\n"
      "ds_read_b64 v[162:163], v10 offset:39936\n"
      "v_mfma_f32_16x16x32_bf16 v[40:43], v[244:247], v[124:127], v[40:43]\n"
      "s_waitcnt lgkmcnt(0)\n"
      "v_add_f32 v158, v158, v160\n"
      "v_add_f32 v159, v159, v161\n"
      "v_add_f32 v158, v158, v162\n"
      "v_mfma_f32_16x16x32_bf16 v[44:47], a[212:215], v[124:127], v[44:47]\n"
      "v_add_f32 v159, v159, v163\n"
      "s_nop 1\n"
      "v_permlane32_swap_b32 v158, v159\n"
      "v_add_f32 v158, v158, v159\n"
      "v_mfma_f32_16x16x32_bf16 v[48:51], a[204:207], v[124:127], v[48:51]\n"
      "v_mov_b32 v160, v158\n"
      "s_nop 1\n"
      "v_permlane16_swap_b32 v158, v160\n"
      "v_add_f32 v158, v158, v160\n"
      "v_mfma_f32_16x16x32_bf16 v[68:71], a[208:211], v[164:167], v[68:71]\n"
      "v_mov_b32 v253, v158\n"
      "s_nop 1\n"
      "v_permlane32_swap_b32 v253, v158\n"
      "v_mul_f32 v253, 0xbae38e39, v253\n"
      "v_mfma_f32_16x16x32_bf16 v[72:75], v[244:247], v[164:167], v[72:75]\n"
      "v_fmaak_f32 v158, v17, v158, 0x3727c5ac\n"
      "v_fma_f32 v158, -v253, v253, v158\n"
      "v_max_f32 v158, 0x3727c5ac, v158\n"
      "v_mfma_f32_16x16x32_bf16 v[76:79], a[212:215], v[164:167], v[76:79]\n"
      "v_rsq_f32 v175, v158\n"
      "v_mfma_f32_16x16x32_bf16 v[80:83], a[204:207], v[164:167], v[80:83]\n"
      "s_mov_b32 s77, s83\n"
      "v_mov_b32 v163, 0\n"
      "v_fmac_f32 v36, v252, v68\n"
      "v_fmac_f32 v37, v252, v69\n"
      "v_fmac_f32 v38, v252, v70\n"
      "v_fmac_f32 v39, v252, v71\n"
      "v_mul_f32 v36, v174, v36\n"
      "v_mul_f32 v37, v174, v37\n"
      "v_mul_f32 v38, v174, v38\n"
      "v_mul_f32 v39, v174, v39\n"
      "v_fmac_f32 v41, v252, v73\n"
      "v_cvt_pk_bf16_f32 v92, v36, v37\n"
      "v_cvt_pk_bf16_f32 v93, v38, v39\n"
      "v_fmac_f32 v44, v252, v76\n"
      "v_mul_f32 v41, v174, v41\n"
      "v_mul_f32 v44, v174, v44\n"
      "v_fmac_f32 v45, v252, v77\n"
      "v_fmac_f32 v46, v252, v78\n"
      "v_cvt_pk_bf16_f32 v94, v41, 0\n"
      "v_mul_f32 v45, v174, v45\n"
      "v_mul_f32 v46, v174, v46\n"
      "v_fmac_f32 v47, v252, v79\n"
      "v_mul_f32 v47, v174, v47\n"
      "v_fmac_f32 v42, v252, v74\n"
      "v_cvt_pk_bf16_f32 v160, v44, v45\n"
      "v_fmac_f32 v48, v252, v80\n"
      "v_mul_f32 v42, v174, v42\n"
      "v_cvt_pk_bf16_f32 v161, v46, v47\n"
      "v_fmac_f32 v49, v252, v81\n"
      "v_fmac_f32 v50, v252, v82\n"
      "v_cvt_pk_bf16_f32 v162, v42, 0\n"
      "v_fmac_f32 v51, v252, v83\n"
      "v_fmac_f32 v40, v252, v72\n"
      "v_cvt_pk_bf16_f32 v84, v48, v49\n"
      "v_cvt_pk_bf16_f32 v85, v50, v51\n"
      "v_cvt_pk_bf16_f32 v86, v40, 0\n"
      "v_mul_f32 v150, 0x3eae1a93, v174\n"
      "v_mfma_f32_16x16x32_bf16 v[132:135], v[160:163], v[248:251], 0\n"
      "v_mfma_f32_16x16x32_bf16 v[136:139], v[160:163], v[12:15], 0\n"
      "v_mfma_f32_16x16x32_bf16 v[120:123], v[92:95], v[84:87], v[104:107]\n"
      "s_nop 5\n"
      "v_cvt_pk_bf16_f32 v96, v132, v133\n"
      "v_cvt_pk_bf16_f32 v97, v134, v135\n"
      "v_cvt_pk_bf16_f32 v100, v136, v137\n"
      "v_cvt_pk_bf16_f32 v101, v138, v139\n"
      "v_max_f32 v152, v120, v121\n"
      "v_max_f32 v153, v122, v123\n"
      ".Lqkv_loop0:\n"
      "v_mfma_f32_16x16x32_bf16 v[60:63], a[176:179], v[20:23], v[60:63]\n"
      "ds_read_b128 v[146:149], v3 offset:0\n"
      "v_writelane_b32 v8, s76, 0\n"
      "v_mfma_f32_16x16x32_bf16 v[60:63], a[188:191], v[24:27], v[60:63]\n"
      "s_waitcnt vmcnt(8)\n"
      "v_cmp_ne_u32 vcc, v16, v18\n"
      "ds_read_b64 v[140:141], v9 offset:18432\n"
      "v_mfma_f32_16x16x32_bf16 v[64:67], a[168:171], v[20:23], v[64:67]\n"
      "s_add_u32 m0, s77, 0\n"
      "ds_read_b64 v[142:143], v9 offset:18944\n"
      "ds_read_b64 v[144:145], v9 offset:19456\n"
      "v_mfma_f32_16x16x32_bf16 v[64:67], a[180:183], v[24:27], v[64:67]\n"
      "s_waitcnt lgkmcnt(5)\n"
      "s_cbranch_vccnz .Lsl1\n"
      ".Lbk1:\n"
      "v_mul_f32 v151, 0x3eae1a93, v175\n"
      "v_mfma_f32_16x16x32_bf16 v[52:55], a[196:199], v[28:31], v[52:55]\n"
      "s_waitcnt lgkmcnt(4)\n"
      "ds_read_b128 v[168:171], v3 offset:1024\n"
      "v_mfma_f32_16x16x32_bf16 v[52:55], a[208:211], v[32:35], v[52:55]\n"
      "s_cmp_eq_u32 s66, s80\n"
      "buffer_load_dword v18, v5, s[36:39], s72 offen sc1\n"
      "v_mfma_f32_16x16x32_bf16 v[56:59], v[240:243], v[28:31], v[56:59]\n"
      "s_cbranch_scc1 .Ltg0\n"
      ".Ltb0:\n"
      "v_mfma_f32_16x16x32_bf16 v[56:59], v[244:247], v[32:35], v[56:59]\n"
      "buffer_load_dwordx4 v2, s[36:39], s69 offen sc1 lds\n"
      "v_mfma_f32_16x16x32_bf16 v[60:63], a[200:203], v[28:31], v[60:63]\n"
      "ds_read_b128 v[20:23], v3 offset:2048\n"
      "v_fmac_f32 v52, v253, v68\n"
      "v_mfma_f32_16x16x32_bf16 v[60:63], a[212:215], v[32:35], v[60:63]\n"
      "v_mul_f32 v52, v175, v52\n"
      "v_fmac_f32 v53, v253, v69\n"
      "v_mfma_f32_16x16x32_bf16 v[64:67], a[192:195], v[28:31], v[64:67]\n"
      "v_mul_f32 v53, v175, v53\n"
      "v_fmac_f32 v54, v253, v70\n"
      "v_mfma_f32_16x16x32_bf16 v[64:67], a[204:207], v[32:35], v[64:67]\n"
      "s_waitcnt lgkmcnt(5)\n"
      "buffer_load_dwordx4 v2, s[36:39], s69 offen offset:1024 sc1 lds\n"
      "v_mfma_f32_16x16x32_bf16 v[36:39], a[4:7], v[146:149], 0\n"
      "s_waitcnt lgkmcnt(1)\n"
      "ds_read_b128 v[24:27], v3 offset:3072\n"
      "v_mul_f32 v54, v175, v54\n"
      "v_mfma_f32_16x16x32_bf16 v[36:39], a[16:19], v[168:171], v[36:39]\n"
      "v_fmac_f32 v55, v253, v71\n"
      "v_fmac_f32 v57, v253, v73\n"
      "v_mfma_f32_16x16x32_bf16 v[40:43], v[176:179], v[146:149], 0\n"
      "v_mul_f32 v55, v175, v55\n"
      "v_mul_f32 v57, v175, v57\n"
      "v_mfma_f32_16x16x32_bf16 v[40:43], v[180:183], v[168:171], v[40:43]\n"
      "buffer_load_dwordx4 v2, s[36:39], s69 offen offset:2048 sc1 lds\n"
      "v_mfma_f32_16x16x32_bf16 v[44:47], a[8:11], v[146:149], 0\n"
      "ds_read_b128 v[28:31], v3 offset:4096\n"
      "v_cvt_pk_bf16_f32 v92, v52, v53\n"
      "v_mfma_f32_16x16x32_bf16 v[44:47], a[20:23], v[168:171], v[44:47]\n"
      "v_cvt_pk_bf16_f32 v93, v54, v55\n"
      "v_cvt_pk_bf16_f32 v94, v57, 0\n"
      "v_mfma_f32_16x16x32_bf16 v[48:51], a[0:3], v[146:149], 0\n"
      "v_fmac_f32 v60, v253, v76\n"
      "v_fmac_f32 v61, v253, v77\n"
      "v_mfma_f32_16x16x32_bf16 v[48:51], a[12:15], v[168:171], v[48:51]\n"
      "s_waitcnt lgkmcnt(2)\n"
      "buffer_load_dwordx4 v2, s[36:39], s69 offen offset:3072 sc1 lds\n"
      "v_mfma_f32_16x16x32_bf16 v[36:39], a[28:31], v[20:23], v[36:39]\n"
      "s_waitcnt lgkmcnt(1)\n"
      "ds_read_b128 v[32:35], v3 offset:5120\n"
      "v_mul_f32 v60, v175, v60\n"
      "v_mfma_f32_16x16x32_bf16 v[36:39], a[40:43], v[24:27], v[36:39]\n"
      "s_add_u32 m0, s78, 0\n"
      "v_mul_f32 v61, v175, v61\n"
      "v_fmac_f32 v62, v253, v78\n"
      "v_mfma_f32_16x16x32_bf16 v[40:43], v[184:187], v[20:23], v[40:43]\n"
      "v_mul_f32 v62, v175, v62\n"
      "v_fmac_f32 v63, v253, v79\n"
      "v_mfma_f32_16x16x32_bf16 v[124:127], v[92:95], v[84:87], v[108:111]\n"
      "buffer_load_dwordx4 v4, s[36:39], s69 offen sc1 lds\n"
      "v_mfma_f32_16x16x32_bf16 v[40:43], v[188:191], v[24:27], v[40:43]\n"
      "s_mov_b32 s71, s72\n"
      "v_mul_f32 v63, v175, v63\n"
      "v_fmac_f32 v58, v253, v74\n"
      "v_mfma_f32_16x16x32_bf16 v[44:47], a[32:35], v[20:23], v[44:47]\n"
      "s_cmp_le_i32 s66, 61\n"
      "ds_read_b128 v[146:149], v3 offset:6144\n"
      "v_mul_f32 v58, v175, v58\n"
      "v_mfma_f32_16x16x32_bf16 v[44:47], a[44:47], v[24:27], v[44:47]\n"
      "s_cselect_b32 s69, s70, 0x40000000\n"
      "v_max3_f32 v154, v152, v124, v125\n"
      "v_max3_f32 v155, v153, v126, v127\n"
      "v_mfma_f32_16x16x32_bf16 v[48:51], a[24:27], v[20:23], v[48:51]\n"
      "s_sub_u32 s48, s66, 1\n"
      "v_max_f32 v156, v154, v155\n"
      "v_mov_b32 v157, v156\n"
      "v_mfma_f32_16x16x32_bf16 v[48:51], a[36:39], v[24:27], v[48:51]\n"
      "s_waitcnt lgkmcnt(2)\n"
      "v_permlane32_swap_b32 v156, v157\n"
      "v_cvt_pk_bf16_f32 v160, v60, v61\n"
      "v_mfma_f32_16x16x32_bf16 v[36:39], a[52:55], v[28:31], v[36:39]\n"
      "s_waitcnt lgkmcnt(1)\n"
      "ds_read_b128 v[168:171], v3 offset:7168\n"
      "v_max_f32 v156, v156, v157\n"
      "v_mfma_f32_16x16x32_bf16 v[36:39], a[64:67], v[32:35], v[36:39]\n"
      "s_cmp_eq_u32 s66, 33\n"
      "v_mov_b32 v157, v156\n"
      "v_cvt_pk_bf16_f32 v161, v62, v63\n"
      "v_mfma_f32_16x16x32_bf16 v[40:43], v[192:195], v[28:31], v[40:43]\n"
      "s_cselect_b32 s48, 99, s48\n"
      "v_permlane16_swap_b32 v156, v157\n"
      "v_cvt_pk_bf16_f32 v162, v58, 0\n"
      "v_mfma_f32_16x16x32_bf16 v[40:43], v[196:199], v[32:35], v[40:43]\n"
      "s_cmp_lt_u32 s48, 65\n"
      "v_max_f32 v156, v156, v157\n"
      "v_mul_f32 v156, v156, v150\n"
      "v_mfma_f32_16x16x32_bf16 v[44:47], a[56:59], v[28:31], v[44:47]\n"
      "s_cselect_b32 s75, s74, 0x40000000\n"
      "ds_read_b128 v[20:23], v3 offset:8192\n"
      "v_fma_f32 v154, v154, v150, -v156\n"
      "v_mfma_f32_16x16x32_bf16 v[44:47], a[68:71], v[32:35], v[44:47]\n"
      "s_cselect_b32 s48, 20480, 0\n"
      "v_fma_f32 v155, v155, v150, -v156\n"
      "v_fmac_f32 v64, v253, v80\n"
      "v_mfma_f32_16x16x32_bf16 v[48:51], a[48:51], v[28:31], v[48:51]\n"
      "s_addc_u32 s76, s76, 0\n"
      "v_exp_f32 v154, v154\n"
      "v_mfma_f32_16x16x32_bf16 v[48:51], a[60:63], v[32:35], v[48:51]\n"
      "s_waitcnt lgkmcnt(2)\n"
      "v_exp_f32 v155, v155\n"
      "v_mfma_f32_16x16x32_bf16 v[36:39], a[76:79], v[146:149], v[36:39]\n"
      "s_waitcnt lgkmcnt(1)\n"
      "ds_read_b128 v[24:27], v3 offset:9216\n"
      "v_add_f32 v156, v154, v155\n"
      "v_mfma_f32_16x16x32_bf16 v[36:39], a[88:91], v[168:171], v[36:39]\n"
      "s_add_u32 s74, s74, s48\n"
      "v_mov_b32 v157, v156\n"
      "v_fmac_f32 v65, v253, v81\n"
      "v_mfma_f32_16x16x32_bf16 v[40:43], v[200:203], v[146:149], v[40:43]\n"
      "s_waitcnt vmcnt(6)\n"
      "v_permlane32_swap_b32 v156, v157\n"
      "s_barrier\n"
      "v_mfma_f32_16x16x32_bf16 v[40:43], v[204:207], v[168:171], v[40:43]\n"
      "buffer_store_dword v8, v7, s[36:39], 0 offen sc1\n"
      "v_mfma_f32_16x16x32_bf16 v[44:47], a[80:83], v[146:149], v[44:47]\n"
      "s_cmp_eq_u32 s73, 128\n"
      "ds_read_b128 v[28:31], v3 offset:10240\n"
      "v_add_f32 v156, v156, v157\n"
      "v_mfma_f32_16x16x32_bf16 v[44:47], a[92:95], v[168:171], v[44:47]\n"
      "s_subb_u32 s48, s73, 0\n"
      "v_mov_b32 v157, v156\n"
      "v_fmac_f32 v66, v253, v82\n"
      "v_mfma_f32_16x16x32_bf16 v[48:51], a[72:75], v[146:149], v[48:51]\n"
      "s_min_u32 s48, s48, s67\n"
      "v_permlane16_swap_b32 v156, v157\n"
      "v_fmac_f32 v67, v253, v83\n"
      "v_mfma_f32_16x16x32_bf16 v[48:51], a[84:87], v[168:171], v[48:51]\n"
      "s_mul_i32 s72, s48, 1536\n"
      "v_add_f32 v156, v156, v157\n"
      "v_fmac_f32 v56, v253, v72\n"
      "v_mfma_f32_16x16x32_bf16 v[132:135], v[160:163], v[248:251], 0\n"
      "s_add_u32 s72, s72, 82042880\n"
      "v_rcp_f32 v156, v156\n"
      "v_mfma_f32_16x16x32_bf16 v[136:139], v[160:163], v[12:15], 0\n"
      "s_waitcnt lgkmcnt(2)\n"
      "v_mul_f32 v154, v154, v156\n"
      "v_mul_f32 v155, v155, v156\n"
      "v_mfma_f32_16x16x32_bf16 v[36:39], a[100:103], v[20:23], v[36:39]\n"
      "s_waitcnt lgkmcnt(1)\n"
      "ds_read_b128 v[32:35], v3 offset:11264\n"
      "v_cvt_pk_bf16_f32 v154, v154, v154\n"
      "v_mfma_f32_16x16x32_bf16 v[36:39], a[112:115], v[24:27], v[36:39]\n"
      "s_mul_i32 s70, s48, 20480\n"
      "v_cvt_pk_bf16_f32 v155, v155, v155\n"
      "v_and_b32 v128, v154, v116\n"
      "v_mfma_f32_16x16x32_bf16 v[40:43], v[208:211], v[20:23], v[40:43]\n"
      "s_add_u32 s70, s70, s83\n"
      "v_and_b32 v129, v155, v117\n"
      "v_and_b32 v130, v154, v118\n"
      "v_mfma_f32_16x16x32_bf16 v[40:43], v[212:215], v[24:27], v[40:43]\n"
      "s_cmp_eq_u32 s66, 27\n"
      "v_and_b32 v131, v155, v119\n"
      "v_cvt_pk_bf16_f32 v98, v132, v133\n"
      "v_mfma_f32_16x16x32_bf16 v[44:47], a[104:107], v[20:23], v[44:47]\n"
      "s_cselect_b32 s48, 97, 1\n"
      "ds_read_b128 v[146:149], v3 offset:12288\n"
      "v_cvt_pk_bf16_f32 v99, v134, v135\n"
      "v_mfma_f32_16x16x32_bf16 v[44:47], a[116:119], v[24:27], v[44:47]\n"
      "s_add_u32 s73, s73, s48\n"
      "v_cvt_pk_bf16_f32 v102, v136, v137\n"
      "v_cvt_pk_bf16_f32 v103, v138, v139\n"
      "v_mfma_f32_16x16x32_bf16 v[48:51], a[96:99], v[20:23], v[48:51]\n"
      "s_add_u32 s66, s66, 1\n"
      "v_cvt_pk_bf16_f32 v88, v64, v65\n"
      "v_cvt_pk_bf16_f32 v89, v66, v67\n"
      "v_mfma_f32_16x16x32_bf16 v[48:51], a[108:111], v[24:27], v[48:51]\n"
      "v_cvt_pk_bf16_f32 v90, v56, 0\n"
      "v_add_f32 v140, v140, v142\n"
      "v_mfma_f32_16x16x32_bf16 v[132:135], v[96:99], v[128:131], 0\n"
      "v_add_f32 v141, v141, v143\n"
      "v_add_f32 v140, v140, v144\n"
      "v_mfma_f32_16x16x32_bf16 v[136:139], v[100:103], v[128:131], 0\n"
      "s_waitcnt lgkmcnt(2)\n"
      "v_add_f32 v141, v141, v145\n"
      "v_mfma_f32_16x16x32_bf16 v[36:39], a[124:127], v[28:31], v[36:39]\n"
      "s_waitcnt lgkmcnt(1)\n"
      "ds_read_b128 v[168:171], v3 offset:13312\n"
      "v_permlane32_swap_b32 v140, v141\n"
      "v_mfma_f32_16x16x32_bf16 v[36:39], a[136:139], v[32:35], v[36:39]\n"
      "v_cvt_pk_bf16_f32 v164, v132, v133\n"
      "v_cvt_pk_bf16_f32 v165, v134, v135\n"
      "v_mfma_f32_16x16x32_bf16 v[40:43], v[216:219], v[28:31], v[40:43]\n"
      "buffer_store_dwordx2 v[164:165], v11, s[36:39], s75 offen sc1\n"
      "v_mfma_f32_16x16x32_bf16 v[40:43], v[220:223], v[32:35], v[40:43]\n"
      "v_cvt_pk_bf16_f32 v166, v136, v137\n"
      "v_add_f32 v140, v140, v141\n"
      "v_mfma_f32_16x16x32_bf16 v[44:47], a[128:131], v[28:31], v[44:47]\n"
      "ds_read_b128 v[20:23], v3 offset:14336\n"
      "v_mov_b32 v142, v140\n"
      "v_mfma_f32_16x16x32_bf16 v[44:47], a[140:143], v[32:35], v[44:47]\n"
      "s_nop 0\n"
      "v_permlane16_swap_b32 v140, v142\n"
      "v_mfma_f32_16x16x32_bf16 v[48:51], a[120:123], v[28:31], v[48:51]\n"
      "v_add_f32 v140, v140, v142\n"
      "v_mfma_f32_16x16x32_bf16 v[48:51], a[132:135], v[32:35], v[48:51]\n"
      "s_waitcnt lgkmcnt(2)\n"
      "buffer_store_dword v166, v6, s[36:39], s75 offen sc1\n"
      "v_mfma_f32_16x16x32_bf16 v[36:39], a[148:151], v[146:149], v[36:39]\n"
      "s_waitcnt lgkmcnt(1)\n"
      "ds_read_b128 v[24:27], v3 offset:15360\n"
      "v_mov_b32 v252, v140\n"
      "v_mfma_f32_16x16x32_bf16 v[36:39], a[160:163], v[168:171], v[36:39]\n"
      "s_nop 0\n"
      "v_permlane32_swap_b32 v252, v140\n"
      "v_mfma_f32_16x16x32_bf16 v[120:123], v[92:95], v[88:91], v[104:107]\n"
      "v_mul_f32 v252, 0xbae38e39, v252\n"
      "v_mfma_f32_16x16x32_bf16 v[40:43], v[224:227], v[146:149], v[40:43]\n"
      "v_fmaak_f32 v140, v17, v140, 0x3727c5ac\n"
      "v_mfma_f32_16x16x32_bf16 v[40:43], v[228:231], v[168:171], v[40:43]\n"
      "v_fma_f32 v140, -v252, v252, v140\n"
      "v_mfma_f32_16x16x32_bf16 v[44:47], a[152:155], v[146:149], v[44:47]\n"
      "ds_read_b128 v[28:31], v3 offset:16384\n"
      "s_nop 0\n"
      "v_max_f32 v152, v120, v121\n"
      "v_mfma_f32_16x16x32_bf16 v[44:47], a[164:167], v[168:171], v[44:47]\n"
      "v_max_f32 v153, v122, v123\n"
      "v_max_f32 v140, 0x3727c5ac, v140\n"
      "v_mfma_f32_16x16x32_bf16 v[48:51], a[144:147], v[146:149], v[48:51]\n"
      "v_rsq_f32 v174, v140\n"
      "v_mfma_f32_16x16x32_bf16 v[48:51], a[156:159], v[168:171], v[48:51]\n"
      "s_waitcnt lgkmcnt(2)\n"
      "v_mfma_f32_16x16x32_bf16 v[36:39], a[172:175], v[20:23], v[36:39]\n"
      "s_waitcnt lgkmcnt(1)\n"
      "ds_read_b128 v[32:35], v3 offset:17408\n"
      "v_mfma_f32_16x16x32_bf16 v[36:39], a[184:187], v[24:27], v[36:39]\n"
      "v_mfma_f32_16x16x32_bf16 v[40:43], v[232:235], v[20:23], v[40:43]\n"
      "v_mfma_f32_16x16x32_bf16 v[40:43], v[236:239], v[24:27], v[40:43]\n"
      ".Lmq00:\n"
      "v_mfma_f32_16x16x32_bf16 v[44:47], a[176:179], v[20:23], v[44:47]\n"
      "s_add_u32 m0, s77, 20480\n"
      "ds_read_b128 v[146:149], v3 offset:20480\n"
      "v_writelane_b32 v8, s76, 0\n"
      "v_mfma_f32_16x16x32_bf16 v[44:47], a[188:191], v[24:27], v[44:47]\n"
      "s_waitcnt vmcnt(8)\n"
      "v_cmp_ne_u32 vcc, v16, v18\n"
      "ds_read_b64 v[140:141], v9 offset:38912\n"
      "v_mfma_f32_16x16x32_bf16 v[48:51], a[168:171], v[20:23], v[48:51]\n"
      "s_cbranch_vccnz .Lsl2\n"
      ".Lbk2:\n"
      "buffer_load_dword v18, v5, s[36:39], s72 offen sc1\n"
      "v_mfma_f32_16x16x32_bf16 v[48:51], a[180:183], v[24:27], v[48:51]\n"
      "s_waitcnt lgkmcnt(3)\n"
      "ds_read_b64 v[142:143], v9 offset:39424\n"
      "ds_read_b64 v[144:145], v9 offset:39936\n"
      "v_mfma_f32_16x16x32_bf16 v[36:39], a[196:199], v[28:31], v[36:39]\n"
      "s_waitcnt lgkmcnt(4)\n"
      "ds_read_b128 v[168:171], v3 offset:21504\n"
      "v_mul_f32 v150, 0x3eae1a93, v174\n"
      "v_mfma_f32_16x16x32_bf16 v[36:39], a[208:211], v[32:35], v[36:39]\n"
      "s_cmp_eq_u32 s66, s80\n"
      "buffer_load_dwordx4 v2, s[36:39], s69 offen sc1 lds\n"
      "v_mfma_f32_16x16x32_bf16 v[40:43], v[240:243], v[28:31], v[40:43]\n"
      "s_cbranch_scc1 .Ltg1\n"
      ".Ltb1:\n"
      "v_mfma_f32_16x16x32_bf16 v[40:43], v[244:247], v[32:35], v[40:43]\n"
      "v_mfma_f32_16x16x32_bf16 v[44:47], a[200:203], v[28:31], v[44:47]\n"
      "ds_read_b128 v[20:23], v3 offset:22528\n"
      "s_nop 0\n"
      "v_fmac_f32 v36, v252, v68\n"
      "v_mfma_f32_16x16x32_bf16 v[44:47], a[212:215], v[32:35], v[44:47]\n"
      "buffer_load_dwordx4 v2, s[36:39], s69 offen offset:1024 sc1 lds\n"
      "v_mfma_f32_16x16x32_bf16 v[48:51], a[192:195], v[28:31], v[48:51]\n"
      "v_mul_f32 v36, v174, v36\n"
      "v_fmac_f32 v37, v252, v69\n"
      "v_mfma_f32_16x16x32_bf16 v[48:51], a[204:207], v[32:35], v[48:51]\n"
      "s_waitcnt lgkmcnt(5)\n"
      "v_mul_f32 v37, v174, v37\n"
      "v_fmac_f32 v38, v252, v70\n"
      "v_mfma_f32_16x16x32_bf16 v[52:55], a[4:7], v[146:149], 0\n"
      "s_waitcnt lgkmcnt(1)\n"
      "ds_read_b128 v[24:27], v3 offset:23552\n"
      "v_mul_f32 v38, v174, v38\n"
      "v_mfma_f32_16x16x32_bf16 v[52:55], a[16:19], v[168:171], v[52:55]\n"
      "buffer_load_dwordx4 v2, s[36:39], s69 offen offset:2048 sc1 lds\n"
      "v_mfma_f32_16x16x32_bf16 v[56:59], v[176:179], v[146:149], 0\n"
      "v_fmac_f32 v39, v252, v71\n"
      "v_fmac_f32 v41, v252, v73\n"
      "v_mfma_f32_16x16x32_bf16 v[56:59], v[180:183], v[168:171], v[56:59]\n"
      "v_mul_f32 v39, v174, v39\n"
      "v_mul_f32 v41, v174, v41\n"
      "v_mfma_f32_16x16x32_bf16 v[60:63], a[8:11], v[146:149], 0\n"
      "ds_read_b128 v[28:31], v3 offset:24576\n"
      "v_cvt_pk_bf16_f32 v92, v36, v37\n"
      "v_mfma_f32_16x16x32_bf16 v[60:63], a[20:23], v[168:171], v[60:63]\n"
      "buffer_load_dwordx4 v2, s[36:39], s69 offen offset:3072 sc1 lds\n"
      "v_mfma_f32_16x16x32_bf16 v[64:67], a[0:3], v[146:149], 0\n"
      "s_add_u32 m0, s78, 20480\n"
      "v_cvt_pk_bf16_f32 v93, v38, v39\n"
      "v_cvt_pk_bf16_f32 v94, v41, 0\n"
      "v_mfma_f32_16x16x32_bf16 v[64:67], a[12:15], v[168:171], v[64:67]\n"
      "s_waitcnt lgkmcnt(2)\n"
      "v_fmac_f32 v44, v252, v76\n"
      "v_fmac_f32 v45, v252, v77\n"
      "v_mfma_f32_16x16x32_bf16 v[52:55], a[28:31], v[20:23], v[52:55]\n"
      "s_waitcnt lgkmcnt(1)\n"
      "ds_read_b128 v[32:35], v3 offset:25600\n"
      "v_mul_f32 v44, v174, v44\n"
      "v_mfma_f32_16x16x32_bf16 v[52:55], a[40:43], v[24:27], v[52:55]\n"
      "buffer_load_dwordx4 v4, s[36:39], s69 offen sc1 lds\n"
      "v_mfma_f32_16x16x32_bf16 v[56:59], v[184:187], v[20:23], v[56:59]\n"
      "s_mov_b32 s71, s72\n"
      "v_mul_f32 v45, v174, v45\n"
      "v_fmac_f32 v46, v252, v78\n"
      "v_mfma_f32_16x16x32_bf16 v[124:127], v[92:95], v[88:91], v[108:111]\n"
      "s_cmp_le_i32 s66, 61\n"
      "v_mul_f32 v46, v174, v46\n"
      "v_fmac_f32 v47, v252, v79\n"
      "v_mfma_f32_16x16x32_bf16 v[56:59], v[188:191], v[24:27], v[56:59]\n"
      "s_cselect_b32 s69, s70, 0x40000000\n"
      "v_mul_f32 v47, v174, v47\n"
      "v_fmac_f32 v42, v252, v74\n"
      "v_mfma_f32_16x16x32_bf16 v[60:63], a[32:35], v[20:23], v[60:63]\n"
      "ds_read_b128 v[146:149], v3 offset:26624\n"
      "v_mul_f32 v42, v174, v42\n"
      "v_mfma_f32_16x16x32_bf16 v[60:63], a[44:47], v[24:27], v[60:63]\n"
      "v_max3_f32 v154, v152, v124, v125\n"
      "v_max3_f32 v155, v153, v126, v127\n"
      "v_mfma_f32_16x16x32_bf16 v[64:67], a[24:27], v[20:23], v[64:67]\n"
      "v_max_f32 v156, v154, v155\n"
      "v_mov_b32 v157, v156\n"
      "v_mfma_f32_16x16x32_bf16 v[64:67], a[36:39], v[24:27], v[64:67]\n"
      "s_waitcnt lgkmcnt(2)\n"
      "v_permlane32_swap_b32 v156, v157\n"
      "v_cvt_pk_bf16_f32 v160, v44, v45\n"
      "v_mfma_f32_16x16x32_bf16 v[52:55], a[52:55], v[28:31], v[52:55]\n"
      "s_waitcnt lgkmcnt(1)\n"
      "ds_read_b128 v[168:171], v3 offset:27648\n"
      "v_max_f32 v156, v156, v157\n"
      "v_mfma_f32_16x16x32_bf16 v[52:55], a[64:67], v[32:35], v[52:55]\n"
      "v_mov_b32 v157, v156\n"
      "v_cvt_pk_bf16_f32 v161, v46, v47\n"
      "v_mfma_f32_16x16x32_bf16 v[56:59], v[192:195], v[28:31], v[56:59]\n"
      "v_permlane16_swap_b32 v156, v157\n"
      "v_cvt_pk_bf16_f32 v162, v42, 0\n"
      "v_mfma_f32_16x16x32_bf16 v[56:59], v[196:199], v[32:35], v[56:59]\n"
      "v_max_f32 v156, v156, v157\n"
      "v_mul_f32 v156, v156, v151\n"
      "v_mfma_f32_16x16x32_bf16 v[60:63], a[56:59], v[28:31], v[60:63]\n"
      "ds_read_b128 v[20:23], v3 offset:28672\n"
      "v_fma_f32 v154, v154, v151, -v156\n"
      "v_mfma_f32_16x16x32_bf16 v[60:63], a[68:71], v[32:35], v[60:63]\n"
      "v_fma_f32 v155, v155, v151, -v156\n"
      "v_fmac_f32 v48, v252, v80\n"
      "v_mfma_f32_16x16x32_bf16 v[64:67], a[48:51], v[28:31], v[64:67]\n"
      "v_exp_f32 v154, v154\n"
      "v_mfma_f32_16x16x32_bf16 v[64:67], a[60:63], v[32:35], v[64:67]\n"
      "s_waitcnt lgkmcnt(2)\n"
      "v_exp_f32 v155, v155\n"
      "v_mfma_f32_16x16x32_bf16 v[52:55], a[76:79], v[146:149], v[52:55]\n"
      "s_waitcnt lgkmcnt(1)\n"
      "ds_read_b128 v[24:27], v3 offset:29696\n"
      "v_add_f32 v156, v154, v155\n"
      "v_mfma_f32_16x16x32_bf16 v[52:55], a[88:91], v[168:171], v[52:55]\n"
      "v_mov_b32 v157, v156\n"
      "v_fmac_f32 v49, v252, v81\n"
      "v_mfma_f32_16x16x32_bf16 v[56:59], v[200:203], v[146:149], v[56:59]\n"
      "s_waitcnt vmcnt(6)\n"
      "v_permlane32_swap_b32 v156, v157\n"
      "s_barrier\n"
      "v_mfma_f32_16x16x32_bf16 v[56:59], v[204:207], v[168:171], v[56:59]\n"
      "buffer_store_dword v8, v7, s[36:39], 0 offen sc1\n"
      "v_mfma_f32_16x16x32_bf16 v[60:63], a[80:83], v[146:149], v[60:63]\n"
      "s_cmp_ge_i32 s66, 66\n"
      "ds_read_b128 v[28:31], v3 offset:30720\n"
      "v_add_f32 v156, v156, v157\n"
      "v_mfma_f32_16x16x32_bf16 v[60:63], a[92:95], v[168:171], v[60:63]\n"
      "s_cbranch_scc1 .Lqkv_x\n"
      "v_mov_b32 v157, v156\n"
      "v_fmac_f32 v50, v252, v82\n"
      "v_mfma_f32_16x16x32_bf16 v[64:67], a[72:75], v[146:149], v[64:67]\n"
      "s_sub_u32 s48, s66, 1\n"
      "v_permlane16_swap_b32 v156, v157\n"
      "v_fmac_f32 v51, v252, v83\n"
      "v_mfma_f32_16x16x32_bf16 v[64:67], a[84:87], v[168:171], v[64:67]\n"
      "s_cmp_eq_u32 s66, 33\n"
      "v_add_f32 v156, v156, v157\n"
      "v_fmac_f32 v40, v252, v72\n"
      "v_mfma_f32_16x16x32_bf16 v[132:135], v[160:163], v[248:251], 0\n"
      "s_cselect_b32 s48, 99, s48\n"
      "v_rcp_f32 v156, v156\n"
      "v_mfma_f32_16x16x32_bf16 v[136:139], v[160:163], v[12:15], 0\n"
      "s_waitcnt lgkmcnt(2)\n"
      "v_mul_f32 v154, v154, v156\n"
      "v_mul_f32 v155, v155, v156\n"
      "v_mfma_f32_16x16x32_bf16 v[52:55], a[100:103], v[20:23], v[52:55]\n"
      "s_waitcnt lgkmcnt(1)\n"
      "ds_read_b128 v[32:35], v3 offset:31744\n"
      "v_cvt_pk_bf16_f32 v154, v154, v154\n"
      "v_mfma_f32_16x16x32_bf16 v[52:55], a[112:115], v[24:27], v[52:55]\n"
      "s_cmp_lt_u32 s48, 65\n"
      "v_cvt_pk_bf16_f32 v155, v155, v155\n"
      "v_and_b32 v128, v154, v118\n"
      "v_mfma_f32_16x16x32_bf16 v[56:59], v[208:211], v[20:23], v[56:59]\n"
      "s_cselect_b32 s75, s74, 0x40000000\n"
      "v_and_b32 v129, v155, v119\n"
      "v_and_b32 v130, v154, v116\n"
      "v_mfma_f32_16x16x32_bf16 v[56:59], v[212:215], v[24:27], v[56:59]\n"
      "s_cselect_b32 s48, 20480, 0\n"
      "v_and_b32 v131, v155, v117\n"
      "v_cvt_pk_bf16_f32 v96, v132, v133\n"
      "v_mfma_f32_16x16x32_bf16 v[60:63], a[104:107], v[20:23], v[60:63]\n"
      "s_addc_u32 s76, s76, 0\n"
      "ds_read_b128 v[146:149], v3 offset:32768\n"
      "v_cvt_pk_bf16_f32 v97, v134, v135\n"
      "v_mfma_f32_16x16x32_bf16 v[60:63], a[116:119], v[24:27], v[60:63]\n"
      "s_add_u32 s74, s74, s48\n"
      "v_cvt_pk_bf16_f32 v100, v136, v137\n"
      "v_cvt_pk_bf16_f32 v101, v138, v139\n"
      "v_mfma_f32_16x16x32_bf16 v[64:67], a[96:99], v[20:23], v[64:67]\n"
      "s_cmp_eq_u32 s73, 128\n"
      "v_cvt_pk_bf16_f32 v84, v48, v49\n"
      "v_cvt_pk_bf16_f32 v85, v50, v51\n"
      "v_mfma_f32_16x16x32_bf16 v[64:67], a[108:111], v[24:27], v[64:67]\n"
      "s_subb_u32 s48, s73, 0\n"
      "v_cvt_pk_bf16_f32 v86, v40, 0\n"
      "v_mfma_f32_16x16x32_bf16 v[132:135], v[96:99], v[128:131], 0\n"
      "s_min_u32 s48, s48, s67\n"
      "v_add_f32 v140, v140, v142\n"
      "v_mfma_f32_16x16x32_bf16 v[136:139], v[100:103], v[128:131], 0\n"
      "s_waitcnt lgkmcnt(2)\n"
      "v_add_f32 v141, v141, v143\n"
      "v_add_f32 v140, v140, v144\n"
      "v_mfma_f32_16x16x32_bf16 v[52:55], a[124:127], v[28:31], v[52:55]\n"
      "s_waitcnt lgkmcnt(1)\n"
      "ds_read_b128 v[168:171], v3 offset:33792\n"
      "v_add_f32 v141, v141, v145\n"
      "v_mfma_f32_16x16x32_bf16 v[52:55], a[136:139], v[32:35], v[52:55]\n"
      "s_mul_i32 s72, s48, 1536\n"
      "v_cvt_pk_bf16_f32 v164, v132, v133\n"
      "v_cvt_pk_bf16_f32 v165, v134, v135\n"
      "v_mfma_f32_16x16x32_bf16 v[56:59], v[216:219], v[28:31], v[56:59]\n"
      "s_add_u32 s72, s72, 82042880\n"
      "buffer_store_dwordx2 v[164:165], v11, s[36:39], s75 offen sc1\n"
      "v_mfma_f32_16x16x32_bf16 v[56:59], v[220:223], v[32:35], v[56:59]\n"
      "s_mul_i32 s70, s48, 20480\n"
      "v_cvt_pk_bf16_f32 v166, v136, v137\n"
      "v_permlane32_swap_b32 v140, v141\n"
      "v_mfma_f32_16x16x32_bf16 v[60:63], a[128:131], v[28:31], v[60:63]\n"
      "s_add_u32 s70, s70, s83\n"
      "ds_read_b128 v[20:23], v3 offset:34816\n"
      "v_add_f32 v140, v140, v141\n"
      "v_mfma_f32_16x16x32_bf16 v[60:63], a[140:143], v[32:35], v[60:63]\n"
      "s_cmp_eq_u32 s66, 27\n"
      "v_mov_b32 v142, v140\n"
      "v_mfma_f32_16x16x32_bf16 v[64:67], a[120:123], v[28:31], v[64:67]\n"
      "s_cselect_b32 s48, 97, 1\n"
      "v_permlane16_swap_b32 v140, v142\n"
      "v_mfma_f32_16x16x32_bf16 v[64:67], a[132:135], v[32:35], v[64:67]\n"
      "s_waitcnt lgkmcnt(2)\n"
      "buffer_store_dword v166, v6, s[36:39], s75 offen sc1\n"
      "v_mfma_f32_16x16x32_bf16 v[52:55], a[148:151], v[146:149], v[52:55]\n"
      "s_waitcnt lgkmcnt(1)\n"
      "ds_read_b128 v[24:27], v3 offset:35840\n"
      "v_add_f32 v140, v140, v142\n"
      "v_mfma_f32_16x16x32_bf16 v[52:55], a[160:163], v[168:171], v[52:55]\n"
      "s_add_u32 s73, s73, s48\n"
      "v_mov_b32 v253, v140\n"
      "v_mfma_f32_16x16x32_bf16 v[120:123], v[92:95], v[84:87], v[104:107]\n"
      "s_add_u32 s66, s66, 1\n"
      "v_permlane32_swap_b32 v253, v140\n"
      "v_mfma_f32_16x16x32_bf16 v[56:59], v[224:227], v[146:149], v[56:59]\n"
      "s_xor_b32 s48, s79, 40960\n"
      "v_mul_f32 v253, 0xbae38e39, v253\n"
      "v_mfma_f32_16x16x32_bf16 v[56:59], v[228:231], v[168:171], v[56:59]\n"
      "s_sub_u32 s49, s79, s48\n"
      "v_fmaak_f32 v140, v17, v140, 0x3727c5ac\n"
      "v_mfma_f32_16x16x32_bf16 v[60:63], a[152:155], v[146:149], v[60:63]\n"
      "s_mov_b32 s79, s48\n"
      "ds_read_b128 v[28:31], v3 offset:36864\n"
      "v_max_f32 v152, v120, v121\n"
      "v_mfma_f32_16x16x32_bf16 v[60:63], a[164:167], v[168:171], v[60:63]\n"
      "s_add_u32 s77, s77, s49\n"
      "v_max_f32 v153, v122, v123\n"
      "v_fma_f32 v140, -v253, v253, v140\n"
      "v_mfma_f32_16x16x32_bf16 v[64:67], a[144:147], v[146:149], v[64:67]\n"
      "s_add_u32 s78, s78, s49\n"
      "v_max_f32 v140, 0x3727c5ac, v140\n"
      "v_mfma_f32_16x16x32_bf16 v[64:67], a[156:159], v[168:171], v[64:67]\n"
      "s_waitcnt lgkmcnt(2)\n"
      "v_add_u32 v9, s79, v10\n"
      "v_mfma_f32_16x16x32_bf16 v[52:55], a[172:175], v[20:23], v[52:55]\n"
      "s_waitcnt lgkmcnt(1)\n"
      "ds_read_b128 v[32:35], v3 offset:37888\n"
      "v_mfma_f32_16x16x32_bf16 v[52:55], a[184:187], v[24:27], v[52:55]\n"
      "v_add_u32 v3, s79, v2\n"
      "v_mfma_f32_16x16x32_bf16 v[56:59], v[232:235], v[20:23], v[56:59]\n"
      "v_rsq_f32 v175, v140\n"
      "v_mfma_f32_16x16x32_bf16 v[56:59], v[236:239], v[24:27], v[56:59]\n"
      ".Lmq10:\n"
      "s_branch .Lqkv_loop0\n"
      ".Lqkv_x:\n"
      "s_cmp_lt_u32 s34, 8\n"
      "s_cbranch_scc0 .Lexit\n"
      "s_getpc_b64 vcc\n"
      "s_add_u32 vcc_lo, vcc_lo, .Lbeg-.\n"
      "s_addc_u32 vcc_hi, vcc_hi, -1\n"
      "s_lshl_b32 s50, s28, 13\n"
      "v_lshlrev_b32 v60, 7, v1\n"
      "v_add_u32 v60, s50, v60\n"
      "v_add_u32 v61, 0, v60\n"
      "v_min_u32 v61, .Lfin-.Lbeg-4, v61\n"
      "global_load_dword v62, v61, vcc\n"
      "v_add_u32 v61, 32768, v60\n"
      "v_min_u32 v61, .Lfin-.Lbeg-4, v61\n"
      "global_load_dword v63, v61, vcc\n"
      "v_add_u32 v61, 65536, v60\n"
      "v_min_u32 v61, .Lfin-.Lbeg-4, v61\n"
      "global_load_dword v64, v61, vcc\n"
      "s_or_b32 s48, s34, s28\n"
      "s_cmp_lg_u32 s48, 0\n"
      "s_cbranch_scc1 .Lexit\n"
      "s_waitcnt vmcnt(0)\n"
      "v_lshlrev_b32 v20, 7, v1\n"
      "s_mov_b32 s56, 0x4000\n"
      ".Lqkv_y:\n"
      "s_mov_b32 s48, 81911808\n"
      "buffer_load_dwordx2 v[24:25], v20, s[36:39], s48 offen offset:8 sc1\n"
      "s_mov_b32 s48, 81920000\n"
      "buffer_load_dwordx2 v[26:27], v20, s[36:39], s48 offen offset:8 sc1\n"
      "s_mov_b32 s48, 81928192\n"
      "buffer_load_dwordx2 v[28:29], v20, s[36:39], s48 offen offset:8 sc1\n"
      "s_mov_b32 s48, 81936384\n"
      "buffer_load_dwordx2 v[30:31], v20, s[36:39], s48 offen offset:8 sc1\n"
      "s_mov_b32 s48, 81944576\n"
      "buffer_load_dwordx2 v[32:33], v20, s[36:39], s48 offen offset:8 sc1\n"
      "s_mov_b32 s48, 81952768\n"
      "buffer_load_dwordx2 v[34:35], v20, s[36:39], s48 offen offset:8 sc1\n"
      "s_mov_b32 s48, 81960960\n"
      "buffer_load_dwordx2 v[36:37], v20, s[36:39], s48 offen offset:8 sc1\n"
      "s_mov_b32 s48, 81969152\n"
      "buffer_load_dwordx2 v[38:39], v20, s[36:39], s48 offen offset:8 sc1\n"
      "s_mov_b32 s48, 81977344\n"
      "buffer_load_dwordx2 v[40:41], v20, s[36:39], s48 offen offset:8 sc1\n"
      "s_mov_b32 s48, 81985536\n"
      "buffer_load_dwordx2 v[42:43], v20, s[36:39], s48 offen offset:8 sc1\n"
      "s_mov_b32 s48, 81993728\n"
      "buffer_load_dwordx2 v[44:45], v20, s[36:39], s48 offen offset:8 sc1\n"
      "s_mov_b32 s48, 82001920\n"
      "buffer_load_dwordx2 v[46:47], v20, s[36:39], s48 offen offset:8 sc1\n"
      "s_mov_b32 s48, 82010112\n"
      "buffer_load_dwordx2 v[48:49], v20, s[36:39], s48 offen offset:8 sc1\n"
      "s_mov_b32 s48, 82018304\n"
      "buffer_load_dwordx2 v[50:51], v20, s[36:39], s48 offen offset:8 sc1\n"
      "s_mov_b32 s48, 82026496\n"
      "buffer_load_dwordx2 v[52:53], v20, s[36:39], s48 offen offset:8 sc1\n"
      "s_mov_b32 s48, 82034688\n"
      "buffer_load_dwordx2 v[54:55], v20, s[36:39], s48 offen offset:8 sc1\n"
      "s_mov_b64 s[50:51], 0\n"
      "s_waitcnt vmcnt(0)\n"
      "v_cmp_ne_u64 vcc, s[98:99], v[24:25]\n"
      "s_nop 0\n"
      "s_or_b64 s[50:51], s[50:51], vcc\n"
      "v_cmp_ne_u64 vcc, s[98:99], v[26:27]\n"
      "s_nop 0\n"
      "s_or_b64 s[50:51], s[50:51], vcc\n"
      "v_cmp_ne_u64 vcc, s[98:99], v[28:29]\n"
      "s_nop 0\n"
      "s_or_b64 s[50:51], s[50:51], vcc\n"
      "v_cmp_ne_u64 vcc, s[98:99], v[30:31]\n"
      "s_nop 0\n"
      "s_or_b64 s[50:51], s[50:51], vcc\n"
      "v_cmp_ne_u64 vcc, s[98:99], v[32:33]\n"
      "s_nop 0\n"
      "s_or_b64 s[50:51], s[50:51], vcc\n"
      "v_cmp_ne_u64 vcc, s[98:99], v[34:35]\n"
      "s_nop 0\n"
      "s_or_b64 s[50:51], s[50:51], vcc\n"
      "v_cmp_ne_u64 vcc, s[98:99], v[36:37]\n"
      "s_nop 0\n"
      "s_or_b64 s[50:51], s[50:51], vcc\n"
      "v_cmp_ne_u64 vcc, s[98:99], v[38:39]\n"
      "s_nop 0\n"
      "s_or_b64 s[50:51], s[50:51], vcc\n"
      "v_cmp_ne_u64 vcc, s[98:99], v[40:41]\n"
      "s_nop 0\n"
      "s_or_b64 s[50:51], s[50:51], vcc\n"
      "v_cmp_ne_u64 vcc, s[98:99], v[42:43]\n"
      "s_nop 0\n"
      "s_or_b64 s[50:51], s[50:51], vcc\n"
      "v_cmp_ne_u64 vcc, s[98:99], v[44:45]\n"
      "s_nop 0\n"
      "s_or_b64 s[50:51], s[50:51], vcc\n"
      "v_cmp_ne_u64 vcc, s[98:99], v[46:47]\n"
      "s_nop 0\n"
      "s_or_b64 s[50:51], s[50:51], vcc\n"
      "v_cmp_ne_u64 vcc, s[98:99], v[48:49]\n"
      "s_nop 0\n"
      "s_or_b64 s[50:51], s[50:51], vcc\n"
      "v_cmp_ne_u64 vcc, s[98:99], v[50:51]\n"
      "s_nop 0\n"
      "s_or_b64 s[50:51], s[50:51], vcc\n"
      "v_cmp_ne_u64 vcc, s[98:99], v[52:53]\n"
      "s_nop 0\n"
      "s_or_b64 s[50:51], s[50:51], vcc\n"
      "v_cmp_ne_u64 vcc, s[98:99], v[54:55]\n"
      "s_nop 0\n"
      "s_or_b64 s[50:51], s[50:51], vcc\n"
      "s_cbranch_scc0 .Lqkv_z\n"
      "s_sub_u32 s56, s56, 1\n"
      "s_cbranch_scc0 .Lqkv_y\n"
      ".Lqkv_z:\n"
      "v_mov_b32 v22, s98\n"
      "v_mov_b32 v23, s99\n"
      "s_mov_b32 s48, 82439168\n"
      "buffer_store_dwordx2 v[22:23], off, s[36:39], s48 sc1\n"
      "s_branch .Lexit\n"
      ".Ldn:\n"
      "s_sub_u32 s49, s49, 10\n"
      "s_mul_i32 s50, s49, 43\n"
      "s_lshr_b32 s50, s50, 7\n"
      "s_mul_i32 s51, s50, 3\n"
      "s_sub_u32 s31, s49, s51\n"
      "s_mul_i32 s51, s48, 3\n"
      "s_add_u32 s50, s50, s51\n"
      "s_lshr_b32 s30, s50, 2\n"
      "s_and_b32 s29, s50, 3\n"
      "v_lshrrev_b32 v160, 4, v1\n"
      "v_and_b32 v161, 15, v1\n"
      "s_mul_i32 s80, s31, 192\n"
      "s_mul_i32 s50, s28, 48\n"
      "s_add_u32 s80, s80, s50\n"
      "s_and_b32 s52, s28, 1\n"
      "s_lshl_b32 s50, s52, 4\n"
      "s_add_u32 s50, s50, s80\n"
      "s_lshl_b32 s51, s52, 5\n"
      "s_sub_u32 s51, s80, s51\n"
      "s_add_u32 s51, s51, 32\n"
      "v_mul_u32_u24 v162, 1152, v161\n"
      "v_lshl_add_u32 v13, v160, 4, v162\n"
      "v_lshl_add_u32 v14, v160, 3, v162\n"
      "s_lshl_b32 s53, s50, 1\n"
      "v_add_u32 v13, s53, v13\n"
      "s_lshl_b32 s53, s51, 1\n"
      "v_add_u32 v14, s53, v14\n"
      "s_lshl_b32 s53, s50, 5\n"
      "v_add_u32 v12, s53, v2\n"
      "v_lshrrev_b32 v162, 1, v160\n"
      "v_lshl_add_u32 v162, v162, 4, v161\n"
      "v_lshlrev_b32 v162, 4, v162\n"
      "v_and_b32 v163, 1, v160\n"
      "v_lshl_add_u32 v162, v163, 3, v162\n"
      "s_lshl_b32 s53, s51, 5\n"
      "v_add_u32 v11, s53, v162\n"
      "s_lshl_b32 s53, s31, 2\n"
      "s_add_u32 s53, s53, s28\n"
      "s_lshl_b32 s53, s53, 7\n"
      "s_add_u32 s53, s53, 18432\n"
      "v_lshrrev_b32 v162, 5, v1\n"
      "v_lshlrev_b32 v162, 2, v162\n"
      "v_lshl_add_u32 v162, v161, 3, v162\n"
      "v_add_u32 v15, s53, v162\n"
      "s_lshl_b32 s81, s29, 5\n"
      "s_sub_u32 s81, 96, s81\n"
      "s_mul_i32 s50, s29, 18\n"
      "s_mul_i32 s51, s31, 6\n"
      "s_add_u32 s50, s50, s51\n"
      "s_add_u32 s50, s50, s30\n"
      "s_add_u32 s50, s50, 184\n"
      "s_lshl_b32 s50, s50, 2\n"
      "s_add_u32 s50, s50, s28\n"
      "s_lshl_b32 s50, s50, 7\n"
      "s_add_u32 s50, s50, 81911808\n"
      "v_lshlrev_b32 v7, 2, v1\n"
      "v_lshrrev_b32 v162, 5, v1\n"
      "v_lshl_or_b32 v7, v162, 30, v7\n"
      "v_add_u32 v7, s50, v7\n"
      "s_waitcnt lgkmcnt(0)\n"
      "s_mov_b32 s36, s20\n"
      "s_and_b32 s37, s21, 0xffff\n"
      "s_mov_b32 s38, 0x4eaf200\n"
      "s_mov_b32 s39, 0x20000\n"
      "s_mov_b32 s98, 82439168\n"
      "buffer_load_dwordx2 v[252:253], off, s[36:39], s98 sc1\n"
      "s_mov_b32 s40, s8\n"
      "s_and_b32 s41, s9, 0xffff\n"
      "s_mov_b32 s42, 0x240000\n"
      "s_mov_b32 s43, 0x20000\n"
      "s_add_u32 s50, s81, s30\n"
      "s_sub_u32 s51, 127, s50\n"
      "s_mul_i32 s51, s51, 18432\n"
      "s_mul_i32 s50, s50, 20480\n"
      "s_add_u32 s50, s50, 0\n"
      "s_mov_b32 s54, 0x3f803f80\n"
      "buffer_load_dwordx4 v[20:23], v13, s[40:43], s51 offen\n"
      "buffer_load_dwordx2 v[24:25], v14, s[40:43], s51 offen\n"
      "s_sub_u32 s51, s51, 110592\n"
      "buffer_load_dwordx4 v[26:29], v13, s[40:43], s51 offen\n"
      "buffer_load_dwordx2 v[30:31], v14, s[40:43], s51 offen\n"
      "s_sub_u32 s51, s51, 110592\n"
      "buffer_load_dwordx4 v[32:35], v13, s[40:43], s51 offen\n"
      "buffer_load_dwordx2 v[36:37], v14, s[40:43], s51 offen\n"
      "s_sub_u32 s51, s51, 110592\n"
      "buffer_load_dwordx4 v[38:41], v13, s[40:43], s51 offen\n"
      "buffer_load_dwordx2 v[42:43], v14, s[40:43], s51 offen\n"
      "s_sub_u32 s51, s51, 110592\n"
      "buffer_load_dwordx4 v[44:47], v13, s[40:43], s51 offen\n"
      "buffer_load_dwordx2 v[48:49], v14, s[40:43], s51 offen\n"
      "s_sub_u32 s51, s51, 110592\n"
      "s_cmp_lt_u32 s30, 2\n"
      "s_cselect_b32 s51, s51, 0x40000000\n"
      "buffer_load_dwordx4 v[50:53], v13, s[40:43], s51 offen\n"
      "buffer_load_dwordx2 v[54:55], v14, s[40:43], s51 offen\n"
      "s_sub_u32 s51, s51, 110592\n"
      "v_lshrrev_b32 v162, 3, v1\n"
      "v_mul_u32_u24 v162, 9216, v162\n"
      "v_and_b32 v163, 7, v1\n"
      "v_lshl_add_u32 v162, v163, 4, v162\n"
      "s_cmp_lt_u32 s30, 2\n"
      "s_cselect_b32 s53, 48, 40\n"
      "v_mov_b32 v164, 0x40000000\n"
      "v_cmp_gt_u32 vcc, s53, v1\n"
      "s_nop 1\n"
      "v_cndmask_b32 v162, v164, v162, vcc\n"
      "s_add_u32 s55, s81, s30\n"
      "s_mul_i32 s55, s55, 1536\n"
      "s_lshl_b32 s65, s31, 2\n"
      "s_add_u32 s65, s65, s28\n"
      "s_lshl_b32 s65, s65, 7\n"
      "s_add_u32 s65, s65, 82042880\n"
      "s_add_u32 s55, s55, s65\n"
      "v_cmp_gt_u32 vcc, 8, v1\n"
      "s_nop 1\n"
      "v_cndmask_b32 v163, v164, v162, vcc\n"
      "v_cndmask_b32 v162, v162, v164, vcc\n"
      "s_waitcnt vmcnt(12)\n"
      "v_readfirstlane_b32 s98, v252\n"
      "v_readfirstlane_b32 s99, v253\n"
      "v_mov_b32 v8, 0\n"
      "s_add_u32 s98, s98, 1\n"
      "s_addc_u32 s99, s99, 0\n"
      "v_writelane_b32 v8, s98, 2\n"
      "v_writelane_b32 v8, s99, 3\n"
      "buffer_store_dword v8, v7, s[36:39], 0 offen sc1\n"
      "v_mov_b32 v100, s98\n"
      "v_mov_b32 v101, s99\n"
      "v_mov_b32 v102, s98\n"
      "v_mov_b32 v103, s99\n"
      "s_waitcnt vmcnt(11)\n"
      "buffer_store_dwordx4 v[20:23], v12, s[36:39], s50 offen sc1\n"
      "buffer_store_dwordx2 v[24:25], v11, s[36:39], s50 offen sc1\n"
      "v_dot2_f32_bf16 v60, v20, s54, 0\n"
      "v_dot2_f32_bf16 v61, v20, v20, 0\n"
      "v_dot2_f32_bf16 v60, v21, s54, v60\n"
      "v_dot2_f32_bf16 v61, v21, v21, v61\n"
      "v_dot2_f32_bf16 v60, v22, s54, v60\n"
      "v_dot2_f32_bf16 v61, v22, v22, v61\n"
      "v_dot2_f32_bf16 v60, v23, s54, v60\n"
      "v_dot2_f32_bf16 v61, v23, v23, v61\n"
      "v_dot2_f32_bf16 v60, v24, s54, v60\n"
      "v_dot2_f32_bf16 v61, v24, v24, v61\n"
      "v_dot2_f32_bf16 v60, v25, s54, v60\n"
      "v_dot2_f32_bf16 v61, v25, v25, v61\n"
      "s_nop 2\n"
      "v_permlane32_swap_b32 v60, v61\n"
      "v_add_f32 v60, v60, v61\n"
      "v_mov_b32 v62, v60\n"
      "s_nop 1\n"
      "v_permlane16_swap_b32 v60, v62\n"
      "v_add_f32 v60, v60, v62\n"
      "buffer_store_dword v60, v15, s[36:39], s50 offen sc1\n"
      "s_add_u32 s50, s50, 122880\n"
      "s_waitcnt vmcnt(12)\n"
      "buffer_store_dwordx4 v[26:29], v12, s[36:39], s50 offen sc1\n"
      "buffer_store_dwordx2 v[30:31], v11, s[36:39], s50 offen sc1\n"
      "v_dot2_f32_bf16 v64, v26, s54, 0\n"
      "v_dot2_f32_bf16 v65, v26, v26, 0\n"
      "v_dot2_f32_bf16 v64, v27, s54, v64\n"
      "v_dot2_f32_bf16 v65, v27, v27, v65\n"
      "v_dot2_f32_bf16 v64, v28, s54, v64\n"
      "v_dot2_f32_bf16 v65, v28, v28, v65\n"
      "v_dot2_f32_bf16 v64, v29, s54, v64\n"
      "v_dot2_f32_bf16 v65, v29, v29, v65\n"
      "v_dot2_f32_bf16 v64, v30, s54, v64\n"
      "v_dot2_f32_bf16 v65, v30, v30, v65\n"
      "v_dot2_f32_bf16 v64, v31, s54, v64\n"
      "v_dot2_f32_bf16 v65, v31, v31, v65\n"
      "s_nop 2\n"
      "v_permlane32_swap_b32 v64, v65\n"
      "v_add_f32 v64, v64, v65\n"
      "v_mov_b32 v66, v64\n"
      "s_nop 1\n"
      "v_permlane16_swap_b32 v64, v66\n"
      "v_add_f32 v64, v64, v66\n"
      "buffer_store_dword v64, v15, s[36:39], s50 offen sc1\n"
      "s_add_u32 s50, s50, 122880\n"
      "s_waitcnt vmcnt(13)\n"
      "buffer_store_dwordx4 v[32:35], v12, s[36:39], s50 offen sc1\n"
      "buffer_store_dwordx2 v[36:37], v11, s[36:39], s50 offen sc1\n"
      "v_dot2_f32_bf16 v68, v32, s54, 0\n"
      "v_dot2_f32_bf16 v69, v32, v32, 0\n"
      "v_dot2_f32_bf16 v68, v33, s54, v68\n"
      "v_dot2_f32_bf16 v69, v33, v33, v69\n"
      "v_dot2_f32_bf16 v68, v34, s54, v68\n"
      "v_dot2_f32_bf16 v69, v34, v34, v69\n"
      "v_dot2_f32_bf16 v68, v35, s54, v68\n"
      "v_dot2_f32_bf16 v69, v35, v35, v69\n"
      "v_dot2_f32_bf16 v68, v36, s54, v68\n"
      "v_dot2_f32_bf16 v69, v36, v36, v69\n"
      "v_dot2_f32_bf16 v68, v37, s54, v68\n"
      "v_dot2_f32_bf16 v69, v37, v37, v69\n"
      "s_nop 2\n"
      "v_permlane32_swap_b32 v68, v69\n"
      "v_add_f32 v68, v68, v69\n"
      "v_mov_b32 v70, v68\n"
      "s_nop 1\n"
      "v_permlane16_swap_b32 v68, v70\n"
      "v_add_f32 v68, v68, v70\n"
      "buffer_store_dword v68, v15, s[36:39], s50 offen sc1\n"
      "s_add_u32 s50, s50, 122880\n"
      "s_waitcnt vmcnt(6)\n"
      "buffer_store_dwordx4 v[100:103], v163, s[36:39], s55 offen sc1\n"
      "buffer_store_dwordx4 v[38:41], v12, s[36:39], s50 offen sc1\n"
      "buffer_store_dwordx2 v[42:43], v11, s[36:39], s50 offen sc1\n"
      "v_dot2_f32_bf16 v72, v38, s54, 0\n"
      "v_dot2_f32_bf16 v73, v38, v38, 0\n"
      "v_dot2_f32_bf16 v72, v39, s54, v72\n"
      "v_dot2_f32_bf16 v73, v39, v39, v73\n"
      "v_dot2_f32_bf16 v72, v40, s54, v72\n"
      "v_dot2_f32_bf16 v73, v40, v40, v73\n"
      "v_dot2_f32_bf16 v72, v41, s54, v72\n"
      "v_dot2_f32_bf16 v73, v41, v41, v73\n"
      "v_dot2_f32_bf16 v72, v42, s54, v72\n"
      "v_dot2_f32_bf16 v73, v42, v42, v73\n"
      "v_dot2_f32_bf16 v72, v43, s54, v72\n"
      "v_dot2_f32_bf16 v73, v43, v43, v73\n"
      "s_nop 2\n"
      "v_permlane32_swap_b32 v72, v73\n"
      "v_add_f32 v72, v72, v73\n"
      "v_mov_b32 v74, v72\n"
      "s_nop 1\n"
      "v_permlane16_swap_b32 v72, v74\n"
      "v_add_f32 v72, v72, v74\n"
      "buffer_store_dword v72, v15, s[36:39], s50 offen sc1\n"
      "s_add_u32 s50, s50, 122880\n"
      "buffer_store_dwordx4 v[44:47], v12, s[36:39], s50 offen sc1\n"
      "buffer_store_dwordx2 v[48:49], v11, s[36:39], s50 offen sc1\n"
      "v_dot2_f32_bf16 v76, v44, s54, 0\n"
      "v_dot2_f32_bf16 v77, v44, v44, 0\n"
      "v_dot2_f32_bf16 v76, v45, s54, v76\n"
      "v_dot2_f32_bf16 v77, v45, v45, v77\n"
      "v_dot2_f32_bf16 v76, v46, s54, v76\n"
      "v_dot2_f32_bf16 v77, v46, v46, v77\n"
      "v_dot2_f32_bf16 v76, v47, s54, v76\n"
      "v_dot2_f32_bf16 v77, v47, v47, v77\n"
      "v_dot2_f32_bf16 v76, v48, s54, v76\n"
      "v_dot2_f32_bf16 v77, v48, v48, v77\n"
      "v_dot2_f32_bf16 v76, v49, s54, v76\n"
      "v_dot2_f32_bf16 v77, v49, v49, v77\n"
      "s_nop 2\n"
      "v_permlane32_swap_b32 v76, v77\n"
      "v_add_f32 v76, v76, v77\n"
      "v_mov_b32 v78, v76\n"
      "s_nop 1\n"
      "v_permlane16_swap_b32 v76, v78\n"
      "v_add_f32 v76, v76, v78\n"
      "buffer_store_dword v76, v15, s[36:39], s50 offen sc1\n"
      "s_add_u32 s50, s50, 122880\n"
      "s_cmp_lt_u32 s30, 2\n"
      "s_cselect_b32 s50, s50, 0x40000000\n"
      "buffer_store_dwordx4 v[50:53], v12, s[36:39], s50 offen sc1\n"
      "buffer_store_dwordx2 v[54:55], v11, s[36:39], s50 offen sc1\n"
      "v_dot2_f32_bf16 v80, v50, s54, 0\n"
      "v_dot2_f32_bf16 v81, v50, v50, 0\n"
      "v_dot2_f32_bf16 v80, v51, s54, v80\n"
      "v_dot2_f32_bf16 v81, v51, v51, v81\n"
      "v_dot2_f32_bf16 v80, v52, s54, v80\n"
      "v_dot2_f32_bf16 v81, v52, v52, v81\n"
      "v_dot2_f32_bf16 v80, v53, s54, v80\n"
      "v_dot2_f32_bf16 v81, v53, v53, v81\n"
      "v_dot2_f32_bf16 v80, v54, s54, v80\n"
      "v_dot2_f32_bf16 v81, v54, v54, v81\n"
      "v_dot2_f32_bf16 v80, v55, s54, v80\n"
      "v_dot2_f32_bf16 v81, v55, v55, v81\n"
      "s_nop 2\n"
      "v_permlane32_swap_b32 v80, v81\n"
      "v_add_f32 v80, v80, v81\n"
      "v_mov_b32 v82, v80\n"
      "s_nop 1\n"
      "v_permlane16_swap_b32 v80, v82\n"
      "v_add_f32 v80, v80, v82\n"
      "buffer_store_dword v80, v15, s[36:39], s50 offen sc1\n"
      "s_add_u32 s50, s50, 122880\n"
      "s_waitcnt vmcnt(0) lgkmcnt(0)\n"
      "buffer_store_dwordx4 v[100:103], v162, s[36:39], s55 offen sc1\n"
      "s_mov_b32 s40, s14\n"
      "s_and_b32 s41, s15, 0xffff\n"
      "s_mov_b32 s42, 0x510000\n"
      "s_mov_b32 s43, 0x20000\n"
      "s_mov_b32 s44, s16\n"
      "s_and_b32 s45, s17, 0xffff\n"
      "s_mov_b32 s46, 0x510000\n"
      "s_mov_b32 s47, 0x20000\n"
      "s_mov_b32 s44, s18\n"
      "s_and_b32 s45, s19, 0xffff\n"
      "s_mov_b32 s46, 0x510000\n"
      "s_mov_b32 s47, 0x20000\n"
      "v_add_u32 v162, 0, v1\n"
      "v_mul_u32_u24 v163, 171, v162\n"
      "v_lshrrev_b32 v163, 12, v163\n"
      "v_mul_u32_u24 v163, 768, v163\n"
      "v_lshl_add_u32 v20, v1, 4, v163\n"
      "v_add_u32 v162, 64, v1\n"
      "v_mul_u32_u24 v163, 171, v162\n"
      "v_lshrrev_b32 v163, 12, v163\n"
      "v_mul_u32_u24 v163, 768, v163\n"
      "v_lshl_add_u32 v21, v1, 4, v163\n"
      "v_add_u32 v162, 128, v1\n"
      "v_mul_u32_u24 v163, 171, v162\n"
      "v_lshrrev_b32 v163, 12, v163\n"
      "v_mul_u32_u24 v163, 768, v163\n"
      "v_lshl_add_u32 v22, v1, 4, v163\n"
      "v_bfe_u32 v162, v1, 2, 2\n"
      "v_lshl_add_u32 v162, v160, 3, v162\n"
      "v_mul_u32_u24 v162, 384, v162\n"
      "s_mul_i32 s50, s28, 96\n"
      "v_add_u32 v162, s50, v162\n"
      "v_and_b32 v163, 3, v1\n"
      "v_lshl_add_u32 v23, v163, 4, v162\n"
      "v_lshl_add_u32 v24, v163, 3, v162\n"
      "s_lshl_b32 s50, s52, 5\n"
      "v_add_u32 v23, s50, v23\n"
      "s_lshl_b32 s50, s52, 6\n"
      "s_sub_u32 s50, 64, s50\n"
      "v_add_u32 v24, s50, v24\n"
      "v_add_u32 v25, 0x10000, v23\n"
      "v_add_u32 v26, 0x10000, v24\n"
      "v_add_u32 v27, 0x20000, v23\n"
      "v_add_u32 v28, 0x20000, v24\n"
      "s_mul_i32 s73, s28, 3072\n"
      "s_mul_i32 s51, s30, 884736\n"
      "s_mul_i32 s50, s31, 384\n"
      "s_add_u32 s51, s51, s50\n"
      "s_mul_i32 s50, s28, 9216\n"
      "s_add_u32 s51, s51, s50\n"
      "s_sleep 16\n"
      "s_sleep 16\n"
      "s_sleep 16\n"
      "s_sleep 16\n"
      "s_sleep 16\n"
      "s_sleep 16\n"
      "s_add_u32 m0, s73, 0\n"
      "s_add_u32 s50, s51, 0\n"
      "buffer_load_dwordx4 v20, s[44:47], s50 offen lds\n"
      "buffer_load_dwordx4 v21, s[44:47], s50 offen offset:1024 lds\n"
      "buffer_load_dwordx4 v22, s[44:47], s50 offen offset:2048 lds\n"
      "s_add_u32 m0, s73, 12288\n"
      "s_add_u32 s50, s51, 36864\n"
      "buffer_load_dwordx4 v20, s[44:47], s50 offen lds\n"
      "buffer_load_dwordx4 v21, s[44:47], s50 offen offset:1024 lds\n"
      "buffer_load_dwordx4 v22, s[44:47], s50 offen offset:2048 lds\n"
      "s_add_u32 m0, s73, 24576\n"
      "s_add_u32 s50, s51, 73728\n"
      "buffer_load_dwordx4 v20, s[44:47], s50 offen lds\n"
      "buffer_load_dwordx4 v21, s[44:47], s50 offen offset:1024 lds\n"
      "buffer_load_dwordx4 v22, s[44:47], s50 offen offset:2048 lds\n"
      "s_add_u32 m0, s73, 36864\n"
      "s_add_u32 s50, s51, 110592\n"
      "buffer_load_dwordx4 v20, s[44:47], s50 offen lds\n"
      "buffer_load_dwordx4 v21, s[44:47], s50 offen offset:1024 lds\n"
      "buffer_load_dwordx4 v22, s[44:47], s50 offen offset:2048 lds\n"
      "s_add_u32 m0, s73, 49152\n"
      "s_add_u32 s50, s51, 147456\n"
      "buffer_load_dwordx4 v20, s[44:47], s50 offen lds\n"
      "buffer_load_dwordx4 v21, s[44:47], s50 offen offset:1024 lds\n"
      "buffer_load_dwordx4 v22, s[44:47], s50 offen offset:2048 lds\n"
      "s_add_u32 m0, s73, 61440\n"
      "s_add_u32 s50, s51, 184320\n"
      "buffer_load_dwordx4 v20, s[44:47], s50 offen lds\n"
      "buffer_load_dwordx4 v21, s[44:47], s50 offen offset:1024 lds\n"
      "buffer_load_dwordx4 v22, s[44:47], s50 offen offset:2048 lds\n"
      "s_add_u32 m0, s73, 73728\n"
      "s_add_u32 s50, s51, 221184\n"
      "buffer_load_dwordx4 v20, s[44:47], s50 offen lds\n"
      "buffer_load_dwordx4 v21, s[44:47], s50 offen offset:1024 lds\n"
      "buffer_load_dwordx4 v22, s[44:47], s50 offen offset:2048 lds\n"
      "s_add_u32 m0, s73, 86016\n"
      "s_add_u32 s50, s51, 258048\n"
      "buffer_load_dwordx4 v20, s[44:47], s50 offen lds\n"
      "buffer_load_dwordx4 v21, s[44:47], s50 offen offset:1024 lds\n"
      "buffer_load_dwordx4 v22, s[44:47], s50 offen offset:2048 lds\n"
      "s_add_u32 m0, s73, 98304\n"
      "s_add_u32 s50, s51, 294912\n"
      "buffer_load_dwordx4 v20, s[44:47], s50 offen lds\n"
      "buffer_load_dwordx4 v21, s[44:47], s50 offen offset:1024 lds\n"
      "buffer_load_dwordx4 v22, s[44:47], s50 offen offset:2048 lds\n"
      "s_add_u32 m0, s73, 110592\n"
      "s_add_u32 s50, s51, 331776\n"
      "buffer_load_dwordx4 v20, s[44:47], s50 offen lds\n"
      "buffer_load_dwordx4 v21, s[44:47], s50 offen offset:1024 lds\n"
      "buffer_load_dwordx4 v22, s[44:47], s50 offen offset:2048 lds\n"
      "s_add_u32 m0, s73, 122880\n"
      "s_add_u32 s50, s51, 368640\n"
      "buffer_load_dwordx4 v20, s[44:47], s50 offen lds\n"
      "buffer_load_dwordx4 v21, s[44:47], s50 offen offset:1024 lds\n"
      "buffer_load_dwordx4 v22, s[44:47], s50 offen offset:2048 lds\n"
      "s_add_u32 m0, s73, 135168\n"
      "s_add_u32 s50, s51, 405504\n"
      "buffer_load_dwordx4 v20, s[44:47], s50 offen lds\n"
      "buffer_load_dwordx4 v21, s[44:47], s50 offen offset:1024 lds\n"
      "buffer_load_dwordx4 v22, s[44:47], s50 offen offset:2048 lds\n"
      "s_waitcnt vmcnt(30)\n"
      "s_barrier\n"
      "ds_read_b64_tr_b16 a[0:1], v23 offset:0\n"
      "ds_read_b64_tr_b16 a[2:3], v23 offset:1536\n"
      "ds_read_b64_tr_b16 a[4:5], v23 offset:8\n"
      "ds_read_b64_tr_b16 a[6:7], v23 offset:1544\n"
      "ds_read_b64_tr_b16 a[8:9], v24 offset:0\n"
      "ds_read_b64_tr_b16 a[10:11], v24 offset:1536\n"
      "ds_read_b64_tr_b16 a[12:13], v23 offset:12288\n"
      "ds_read_b64_tr_b16 a[14:15], v23 offset:13824\n"
      "ds_read_b64_tr_b16 a[16:17], v23 offset:12296\n"
      "ds_read_b64_tr_b16 a[18:19], v23 offset:13832\n"
      "ds_read_b64_tr_b16 a[20:21], v24 offset:12288\n"
      "ds_read_b64_tr_b16 a[22:23], v24 offset:13824\n"
      "s_waitcnt vmcnt(24) lgkmcnt(0)\n"
      "s_barrier\n"
      "s_add_u32 m0, s73, 0\n"
      "s_add_u32 s50, s51, 442368\n"
      "buffer_load_dwordx4 v20, s[44:47], s50 offen lds\n"
      "buffer_load_dwordx4 v21, s[44:47], s50 offen offset:1024 lds\n"
      "buffer_load_dwordx4 v22, s[44:47], s50 offen offset:2048 lds\n"
      "s_add_u32 m0, s73, 12288\n"
      "s_add_u32 s50, s51, 479232\n"
      "buffer_load_dwordx4 v20, s[44:47], s50 offen lds\n"
      "buffer_load_dwordx4 v21, s[44:47], s50 offen offset:1024 lds\n"
      "buffer_load_dwordx4 v22, s[44:47], s50 offen offset:2048 lds\n"
      "ds_read_b64_tr_b16 a[24:25], v23 offset:24576\n"
      "ds_read_b64_tr_b16 a[26:27], v23 offset:26112\n"
      "ds_read_b64_tr_b16 a[28:29], v23 offset:24584\n"
      "ds_read_b64_tr_b16 a[30:31], v23 offset:26120\n"
      "ds_read_b64_tr_b16 a[32:33], v24 offset:24576\n"
      "ds_read_b64_tr_b16 a[34:35], v24 offset:26112\n"
      "ds_read_b64_tr_b16 a[36:37], v23 offset:36864\n"
      "ds_read_b64_tr_b16 a[38:39], v23 offset:38400\n"
      "ds_read_b64_tr_b16 a[40:41], v23 offset:36872\n"
      "ds_read_b64_tr_b16 a[42:43], v23 offset:38408\n"
      "ds_read_b64_tr_b16 a[44:45], v24 offset:36864\n"
      "ds_read_b64_tr_b16 a[46:47], v24 offset:38400\n"
      "s_waitcnt vmcnt(24) lgkmcnt(0)\n"
      "s_barrier\n"
      "s_add_u32 m0, s73, 24576\n"
      "s_add_u32 s50, s51, 516096\n"
      "buffer_load_dwordx4 v20, s[44:47], s50 offen lds\n"
      "buffer_load_dwordx4 v21, s[44:47], s50 offen offset:1024 lds\n"
      "buffer_load_dwordx4 v22, s[44:47], s50 offen offset:2048 lds\n"
      "s_add_u32 m0, s73, 36864\n"
      "s_add_u32 s50, s51, 552960\n"
      "buffer_load_dwordx4 v20, s[44:47], s50 offen lds\n"
      "buffer_load_dwordx4 v21, s[44:47], s50 offen offset:1024 lds\n"
      "buffer_load_dwordx4 v22, s[44:47], s50 offen offset:2048 lds\n"
      "ds_read_b64_tr_b16 a[48:49], v23 offset:49152\n"
      "ds_read_b64_tr_b16 a[50:51], v23 offset:50688\n"
      "ds_read_b64_tr_b16 a[52:53], v23 offset:49160\n"
      "ds_read_b64_tr_b16 a[54:55], v23 offset:50696\n"
      "ds_read_b64_tr_b16 a[56:57], v24 offset:49152\n"
      "ds_read_b64_tr_b16 a[58:59], v24 offset:50688\n"
      "ds_read_b64_tr_b16 a[60:61], v23 offset:61440\n"
      "ds_read_b64_tr_b16 a[62:63], v23 offset:62976\n"
      "ds_read_b64_tr_b16 a[64:65], v23 offset:61448\n"
      "ds_read_b64_tr_b16 a[66:67], v23 offset:62984\n"
      "ds_read_b64_tr_b16 a[68:69], v24 offset:61440\n"
      "ds_read_b64_tr_b16 a[70:71], v24 offset:62976\n"
      "s_waitcnt vmcnt(24) lgkmcnt(0)\n"
      "s_barrier\n"
      "s_add_u32 m0, s73, 49152\n"
      "s_add_u32 s50, s51, 589824\n"
      "buffer_load_dwordx4 v20, s[44:47], s50 offen lds\n"
      "buffer_load_dwordx4 v21, s[44:47], s50 offen offset:1024 lds\n"
      "buffer_load_dwordx4 v22, s[44:47], s50 offen offset:2048 lds\n"
      "s_add_u32 m0, s73, 61440\n"
      "s_add_u32 s50, s51, 626688\n"
      "buffer_load_dwordx4 v20, s[44:47], s50 offen lds\n"
      "buffer_load_dwordx4 v21, s[44:47], s50 offen offset:1024 lds\n"
      "buffer_load_dwordx4 v22, s[44:47], s50 offen offset:2048 lds\n"
      "ds_read_b64_tr_b16 a[72:73], v25 offset:8192\n"
      "ds_read_b64_tr_b16 a[74:75], v25 offset:9728\n"
      "ds_read_b64_tr_b16 a[76:77], v25 offset:8200\n"
      "ds_read_b64_tr_b16 a[78:79], v25 offset:9736\n"
      "ds_read_b64_tr_b16 a[80:81], v26 offset:8192\n"
      "ds_read_b64_tr_b16 a[82:83], v26 offset:9728\n"
      "ds_read_b64_tr_b16 a[84:85], v25 offset:20480\n"
      "ds_read_b64_tr_b16 a[86:87], v25 offset:22016\n"
      "ds_read_b64_tr_b16 a[88:89], v25 offset:20488\n"
      "ds_read_b64_tr_b16 a[90:91], v25 offset:22024\n"
      "ds_read_b64_tr_b16 a[92:93], v26 offset:20480\n"
      "ds_read_b64_tr_b16 a[94:95], v26 offset:22016\n"
      "s_waitcnt vmcnt(24) lgkmcnt(0)\n"
      "s_barrier\n"
      "s_add_u32 m0, s73, 73728\n"
      "s_add_u32 s50, s51, 663552\n"
      "buffer_load_dwordx4 v20, s[44:47], s50 offen lds\n"
      "buffer_load_dwordx4 v21, s[44:47], s50 offen offset:1024 lds\n"
      "buffer_load_dwordx4 v22, s[44:47], s50 offen offset:2048 lds\n"
      "s_add_u32 m0, s73, 86016\n"
      "s_add_u32 s50, s51, 700416\n"
      "buffer_load_dwordx4 v20, s[44:47], s50 offen lds\n"
      "buffer_load_dwordx4 v21, s[44:47], s50 offen offset:1024 lds\n"
      "buffer_load_dwordx4 v22, s[44:47], s50 offen offset:2048 lds\n"
      "ds_read_b64_tr_b16 a[96:97], v25 offset:32768\n"
      "ds_read_b64_tr_b16 a[98:99], v25 offset:34304\n"
      "ds_read_b64_tr_b16 a[100:101], v25 offset:32776\n"
      "ds_read_b64_tr_b16 a[102:103], v25 offset:34312\n"
      "ds_read_b64_tr_b16 a[104:105], v26 offset:32768\n"
      "ds_read_b64_tr_b16 a[106:107], v26 offset:34304\n"
      "ds_read_b64_tr_b16 a[108:109], v25 offset:45056\n"
      "ds_read_b64_tr_b16 a[110:111], v25 offset:46592\n"
      "ds_read_b64_tr_b16 a[112:113], v25 offset:45064\n"
      "ds_read_b64_tr_b16 a[114:115], v25 offset:46600\n"
      "ds_read_b64_tr_b16 a[116:117], v26 offset:45056\n"
      "ds_read_b64_tr_b16 a[118:119], v26 offset:46592\n"
      "s_waitcnt vmcnt(24) lgkmcnt(0)\n"
      "s_barrier\n"
      "s_add_u32 m0, s73, 98304\n"
      "s_add_u32 s50, s51, 737280\n"
      "buffer_load_dwordx4 v20, s[44:47], s50 offen lds\n"
      "buffer_load_dwordx4 v21, s[44:47], s50 offen offset:1024 lds\n"
      "buffer_load_dwordx4 v22, s[44:47], s50 offen offset:2048 lds\n"
      "s_add_u32 m0, s73, 110592\n"
      "s_add_u32 s50, s51, 774144\n"
      "buffer_load_dwordx4 v20, s[44:47], s50 offen lds\n"
      "buffer_load_dwordx4 v21, s[44:47], s50 offen offset:1024 lds\n"
      "buffer_load_dwordx4 v22, s[44:47], s50 offen offset:2048 lds\n"
      "ds_read_b64_tr_b16 a[120:121], v25 offset:57344\n"
      "ds_read_b64_tr_b16 a[122:123], v25 offset:58880\n"
      "ds_read_b64_tr_b16 a[124:125], v25 offset:57352\n"
      "ds_read_b64_tr_b16 a[126:127], v25 offset:58888\n"
      "ds_read_b64_tr_b16 a[128:129], v26 offset:57344\n"
      "ds_read_b64_tr_b16 a[130:131], v26 offset:58880\n"
      "ds_read_b64_tr_b16 a[132:133], v27 offset:4096\n"
      "ds_read_b64_tr_b16 a[134:135], v27 offset:5632\n"
      "ds_read_b64_tr_b16 a[136:137], v27 offset:4104\n"
      "ds_read_b64_tr_b16 a[138:139], v27 offset:5640\n"
      "ds_read_b64_tr_b16 a[140:141], v28 offset:4096\n"
      "ds_read_b64_tr_b16 a[142:143], v28 offset:5632\n"
      "s_waitcnt vmcnt(24) lgkmcnt(0)\n"
      "s_barrier\n"
      "s_add_u32 m0, s73, 122880\n"
      "s_add_u32 s50, s51, 811008\n"
      "buffer_load_dwordx4 v20, s[44:47], s50 offen lds\n"
      "buffer_load_dwordx4 v21, s[44:47], s50 offen offset:1024 lds\n"
      "buffer_load_dwordx4 v22, s[44:47], s50 offen offset:2048 lds\n"
      "s_add_u32 m0, s73, 135168\n"
      "s_add_u32 s50, s51, 847872\n"
      "buffer_load_dwordx4 v20, s[44:47], s50 offen lds\n"
      "buffer_load_dwordx4 v21, s[44:47], s50 offen offset:1024 lds\n"
      "buffer_load_dwordx4 v22, s[44:47], s50 offen offset:2048 lds\n"
      "ds_read_b64_tr_b16 a[144:145], v23 offset:0\n"
      "ds_read_b64_tr_b16 a[146:147], v23 offset:1536\n"
      "ds_read_b64_tr_b16 a[148:149], v23 offset:8\n"
      "ds_read_b64_tr_b16 a[150:151], v23 offset:1544\n"
      "ds_read_b64_tr_b16 a[152:153], v24 offset:0\n"
      "ds_read_b64_tr_b16 a[154:155], v24 offset:1536\n"
      "ds_read_b64_tr_b16 a[156:157], v23 offset:12288\n"
      "ds_read_b64_tr_b16 a[158:159], v23 offset:13824\n"
      "ds_read_b64_tr_b16 a[160:161], v23 offset:12296\n"
      "ds_read_b64_tr_b16 a[162:163], v23 offset:13832\n"
      "ds_read_b64_tr_b16 a[164:165], v24 offset:12288\n"
      "ds_read_b64_tr_b16 a[166:167], v24 offset:13824\n"
      "s_waitcnt vmcnt(24) lgkmcnt(0)\n"
      "s_barrier\n"
      "ds_read_b64_tr_b16 a[168:169], v23 offset:24576\n"
      "ds_read_b64_tr_b16 a[170:171], v23 offset:26112\n"
      "ds_read_b64_tr_b16 a[172:173], v23 offset:24584\n"
      "ds_read_b64_tr_b16 a[174:175], v23 offset:26120\n"
      "ds_read_b64_tr_b16 a[176:177], v24 offset:24576\n"
      "ds_read_b64_tr_b16 a[178:179], v24 offset:26112\n"
      "ds_read_b64_tr_b16 a[180:181], v23 offset:36864\n"
      "ds_read_b64_tr_b16 a[182:183], v23 offset:38400\n"
      "ds_read_b64_tr_b16 a[184:185], v23 offset:36872\n"
      "ds_read_b64_tr_b16 a[186:187], v23 offset:38408\n"
      "ds_read_b64_tr_b16 a[188:189], v24 offset:36864\n"
      "ds_read_b64_tr_b16 a[190:191], v24 offset:38400\n"
      "s_waitcnt vmcnt(18) lgkmcnt(0)\n"
      "s_barrier\n"
      "ds_read_b64_tr_b16 a[192:193], v23 offset:49152\n"
      "ds_read_b64_tr_b16 a[194:195], v23 offset:50688\n"
      "ds_read_b64_tr_b16 a[196:197], v23 offset:49160\n"
      "ds_read_b64_tr_b16 a[198:199], v23 offset:50696\n"
      "ds_read_b64_tr_b16 a[200:201], v24 offset:49152\n"
      "ds_read_b64_tr_b16 a[202:203], v24 offset:50688\n"
      "ds_read_b64_tr_b16 a[204:205], v23 offset:61440\n"
      "ds_read_b64_tr_b16 a[206:207], v23 offset:62976\n"
      "ds_read_b64_tr_b16 a[208:209], v23 offset:61448\n"
      "ds_read_b64_tr_b16 a[210:211], v23 offset:62984\n"
      "ds_read_b64_tr_b16 a[212:213], v24 offset:61440\n"
      "ds_read_b64_tr_b16 a[214:215], v24 offset:62976\n"
      "s_waitcnt vmcnt(12) lgkmcnt(0)\n"
      "s_barrier\n"
      "ds_read_b64_tr_b16 a[216:217], v25 offset:8192\n"
      "ds_read_b64_tr_b16 a[218:219], v25 offset:9728\n"
      "ds_read_b64_tr_b16 a[220:221], v25 offset:8200\n"
      "ds_read_b64_tr_b16 a[222:223], v25 offset:9736\n"
      "ds_read_b64_tr_b16 a[224:225], v26 offset:8192\n"
      "ds_read_b64_tr_b16 a[226:227], v26 offset:9728\n"
      "ds_read_b64_tr_b16 a[228:229], v25 offset:20480\n"
      "ds_read_b64_tr_b16 a[230:231], v25 offset:22016\n"
      "ds_read_b64_tr_b16 a[232:233], v25 offset:20488\n"
      "ds_read_b64_tr_b16 a[234:235], v25 offset:22024\n"
      "ds_read_b64_tr_b16 a[236:237], v26 offset:20480\n"
      "ds_read_b64_tr_b16 a[238:239], v26 offset:22016\n"
      "s_waitcnt vmcnt(6) lgkmcnt(0)\n"
      "s_barrier\n"
      "ds_read_b64_tr_b16 a[240:241], v25 offset:32768\n"
      "ds_read_b64_tr_b16 a[242:243], v25 offset:34304\n"
      "ds_read_b64_tr_b16 a[244:245], v25 offset:32776\n"
      "ds_read_b64_tr_b16 a[246:247], v25 offset:34312\n"
      "ds_read_b64_tr_b16 a[248:249], v26 offset:32768\n"
      "ds_read_b64_tr_b16 a[250:251], v26 offset:34304\n"
      "ds_read_b64_tr_b16 a[252:253], v25 offset:45056\n"
      "ds_read_b64_tr_b16 a[254:255], v25 offset:46592\n"
      "ds_read_b64_tr_b16 v[216:217], v25 offset:45064\n"
      "ds_read_b64_tr_b16 v[218:219], v25 offset:46600\n"
      "ds_read_b64_tr_b16 v[220:221], v26 offset:45056\n"
      "ds_read_b64_tr_b16 v[222:223], v26 offset:46592\n"
      "s_waitcnt vmcnt(0) lgkmcnt(0)\n"
      "s_barrier\n"
      "ds_read_b64_tr_b16 v[224:225], v25 offset:57344\n"
      "ds_read_b64_tr_b16 v[226:227], v25 offset:58880\n"
      "ds_read_b64_tr_b16 v[228:229], v25 offset:57352\n"
      "ds_read_b64_tr_b16 v[230:231], v25 offset:58888\n"
      "ds_read_b64_tr_b16 v[232:233], v26 offset:57344\n"
      "ds_read_b64_tr_b16 v[234:235], v26 offset:58880\n"
      "ds_read_b64_tr_b16 v[236:237], v27 offset:4096\n"
      "ds_read_b64_tr_b16 v[238:239], v27 offset:5632\n"
      "ds_read_b64_tr_b16 v[240:241], v27 offset:4104\n"
      "ds_read_b64_tr_b16 v[242:243], v27 offset:5640\n"
      "ds_read_b64_tr_b16 v[244:245], v28 offset:4096\n"
      "ds_read_b64_tr_b16 v[246:247], v28 offset:5632\n"
      "s_mov_b32 s40, s22\n"
      "s_and_b32 s41, s23, 0xffff\n"
      "s_mov_b32 s42, 0x240000\n"
      "s_mov_b32 s43, 0x20000\n"
      "s_mov_b32 s66, -1\n"
      "s_mul_i32 s68, s29, 9437184\n"
      "s_mul_i32 s50, s30, 24576\n"
      "s_mul_i32 s73, s28, 6144\n"
      "s_add_u32 s68, s68, s50\n"
      "s_add_u32 s68, s68, s73\n"
      "s_add_u32 s68, s68, 15851520\n"
      "s_mul_i32 s70, s29, 7077888\n"
      "s_mul_i32 s50, s31, 36864\n"
      "s_add_u32 s70, s70, s50\n"
      "s_mul_i32 s50, s30, 6144\n"
      "s_add_u32 s70, s70, s50\n"
      "s_add_u32 s70, s70, 53600256\n"
      "s_mul_i32 s50, s28, 1536\n"
      "v_add_u32 v18, s50, v2\n"
      "v_lshlrev_b32 v118, 3, v1\n"
      "v_add_u32 v118, s50, v118\n"
      "s_mov_b32 s50, -30720\n"
      "s_cmp_lt_u32 s30, 5\n"
      "s_cselect_b32 s82, 6144, s50\n"
      "s_mov_b32 s50, -24576\n"
      "s_cmp_lt_u32 s30, 4\n"
      "s_cselect_b32 s83, 12288, s50\n"
      "s_mov_b32 s50, -18432\n"
      "s_cmp_lt_u32 s30, 3\n"
      "s_cselect_b32 s84, 18432, s50\n"
      "s_mov_b32 s50, -12288\n"
      "s_cmp_lt_u32 s30, 2\n"
      "s_cselect_b32 s85, 24576, s50\n"
      "s_mov_b32 s50, -6144\n"
      "s_cmp_lt_u32 s30, 1\n"
      "s_cselect_b32 s86, 30720, s50\n"
      "v_mov_b32 v3, v2\n"
      "v_add_u32 v4, 49152, v2\n"
      "v_add_u32 v119, 98304, v2\n"
      "v_add_u32 v19, 4096, v2\n"
      "v_min_u32 v162, 5, v1\n"
      "v_lshlrev_b32 v162, 7, v162\n"
      "s_mul_i32 s50, s29, 36\n"
      "s_mul_i32 s51, s30, 6\n"
      "s_add_u32 s50, s50, s51\n"
      "s_lshl_b32 s50, s50, 9\n"
      "s_mul_i32 s51, s28, 768\n"
      "s_add_u32 s50, s50, s51\n"
      "s_add_u32 s50, s50, 81932288\n"
      "v_add_u32 v5, s50, v162\n"
      "v_and_b32 v5, 0xfffffe7f, v5\n"
      "s_mul_i32 s50, s29, 18\n"
      "s_mul_i32 s51, s31, 6\n"
      "s_add_u32 s50, s50, s51\n"
      "s_lshl_b32 s50, s50, 2\n"
      "s_add_u32 s50, s50, s28\n"
      "s_lshl_b32 s50, s50, 7\n"
      "s_add_u32 s50, s50, 82006016\n"
      "v_min_u32 v162, 4, v1\n"
      "v_cmp_le_u32 vcc, s30, v162\n"
      "s_nop 1\n"
      "v_addc_co_u32 v162, vcc, 0, v162, vcc\n"
      "v_lshlrev_b32 v162, 9, v162\n"
      "v_add_u32 v9, s50, v162\n"
      "v_lshlrev_b32 v162, 2, v1\n"
      "v_cmp_gt_u32 vcc, 32, v1\n"
      "s_nop 1\n"
      "v_cndmask_b32 v16, v164, v162, vcc\n"
      "v_mov_b32 v6, 0\n"
      "v_mov_b32 v10, 0\n"
      "v_mov_b32 v17, 0\n"
      "s_mov_b32 s74, 0\n"
      "s_mov_b32 s88, -1\n"
      "s_mov_b32 s75, 0x40000000\n"
      "s_mov_b32 s76, 0x40000000\n"
      "s_mov_b32 s77, 0x40000000\n"
      "s_mov_b32 s78, 0x40000000\n"
      "s_mov_b32 s79, 0x40000000\n"
      "s_add_u32 s65, s65, 198144\n"
      "s_sub_u32 s50, 8, s30\n"
      "s_cmp_ge_u32 s50, 6\n"
      "s_cselect_b32 s51, 6, 0\n"
      "s_sub_u32 s50, s50, s51\n"
      "s_cmp_lt_u32 s50, 2\n"
      "s_cselect_b32 s51, 48, 40\n"
      "v_lshrrev_b32 v162, 3, v1\n"
      "v_mul_u32_u24 v162, 9216, v162\n"
      "v_and_b32 v163, 7, v1\n"
      "v_lshl_add_u32 v162, v163, 4, v162\n"
      "v_cmp_gt_u32 vcc, s51, v1\n"
      "s_add_u32 s50, s50, s81\n"
      "s_mul_i32 s50, s50, 1536\n"
      "v_cndmask_b32 v162, v164, v162, vcc\n"
      "s_add_u32 s50, s50, s65\n"
      "s_not_b32 s51, s98\n"
      "v_mov_b32 v100, s51\n"
      "v_mov_b32 v101, s51\n"
      "v_mov_b32 v102, s51\n"
      "v_mov_b32 v103, s51\n"
      "buffer_store_dwordx4 v[100:103], v162, s[36:39], s50 offen sc1\n"
      "s_mov_b32 s56, 0x20000\n"
      ".Lsp3:\n"
      "buffer_load_dwordx2 v[20:21], v5, s[36:39], 0 offen offset:8 sc1\n"
      "s_waitcnt vmcnt(0)\n"
      "v_cmp_ne_u64 vcc, s[98:99], v[20:21]\n"
      "s_cbranch_vccz .Lbk3\n"
      "s_sleep 4\n"
      "s_sub_u32 s56, s56, 1\n"
      "s_cbranch_scc0 .Lsp3\n"
      ".Lbk3:\n"
      "s_mov_b32 s56, 0x20000\n"
      ".Lsp4:\n"
      "buffer_load_dwordx2 v[20:21], v9, s[36:39], 0 offen offset:8 sc1\n"
      "s_waitcnt vmcnt(0)\n"
      "v_cmp_ne_u64 vcc, s[98:99], v[20:21]\n"
      "s_cbranch_vccz .Lbk4\n"
      "s_sleep 4\n"
      "s_sub_u32 s56, s56, 1\n"
      "s_cbranch_scc0 .Lsp4\n"
      ".Lbk4:\n"
      "s_waitcnt vmcnt(0) lgkmcnt(0)\n"
      "s_barrier\n"
      "s_mov_b32 s71, 0x40000000\n"
      "s_add_u32 s50, s30, 1\n"
      "s_mov_b32 s56, 0x20000\n"
      ".Lsp5:\n"
      "v_cmp_gt_u32 vcc, 1, v6\n"
      "s_cbranch_vccz .Lbk5\n"
      "s_sleep 4\n"
      "buffer_load_dword v6, v5, s[36:39], 0 offen sc1\n"
      "s_waitcnt vmcnt(0)\n"
      "s_sub_u32 s56, s56, 1\n"
      "s_cbranch_scc0 .Lsp5\n"
      ".Lbk5:\n"
      "s_cmp_ge_u32 s50, 6\n"
      "s_cselect_b32 s51, 6, 0\n"
      "s_sub_u32 s51, s50, s51\n"
      "s_mul_i32 s51, s51, 24576\n"
      "v_add_u32 v162, s51, v2\n"
      "s_add_u32 m0, s73, s51\n"
      "s_add_u32 s50, s50, 1\n"
      "buffer_load_dwordx4 v2, s[36:39], s68 offen sc1 lds\n"
      "buffer_load_dwordx4 v2, s[36:39], s68 offen offset:1024 sc1 lds\n"
      "buffer_load_dwordx4 v2, s[36:39], s68 offen offset:2048 sc1 lds\n"
      "buffer_load_dwordx4 v2, s[36:39], s68 offen offset:3072 sc1 lds\n"
      "s_add_u32 m0, m0, 4096\n"
      "s_nop 0\n"
      "buffer_load_dwordx4 v19, s[36:39], s68 offen sc1 lds\n"
      "buffer_load_dwordx4 v19, s[36:39], s68 offen offset:1024 sc1 lds\n"
      "s_mov_b32 s56, 0x20000\n"
      ".Lsp6:\n"
      "v_cmp_gt_u32 vcc, 2, v6\n"
      "s_cbranch_vccz .Lbk6\n"
      "s_sleep 4\n"
      "buffer_load_dword v6, v5, s[36:39], 0 offen sc1\n"
      "s_waitcnt vmcnt(0)\n"
      "s_sub_u32 s56, s56, 1\n"
      "s_cbranch_scc0 .Lsp6\n"
      ".Lbk6:\n"
      "s_cmp_ge_u32 s50, 6\n"
      "s_cselect_b32 s51, 6, 0\n"
      "s_sub_u32 s51, s50, s51\n"
      "s_mul_i32 s51, s51, 24576\n"
      "s_add_u32 s68, s68, 147456\n"
      "s_add_u32 m0, s73, s51\n"
      "s_add_u32 s50, s50, 1\n"
      "buffer_load_dwordx4 v2, s[36:39], s68 offen sc1 lds\n"
      "buffer_load_dwordx4 v2, s[36:39], s68 offen offset:1024 sc1 lds\n"
      "buffer_load_dwordx4 v2, s[36:39], s68 offen offset:2048 sc1 lds\n"
      "buffer_load_dwordx4 v2, s[36:39], s68 offen offset:3072 sc1 lds\n"
      "s_add_u32 m0, m0, 4096\n"
      "s_nop 0\n"
      "buffer_load_dwordx4 v19, s[36:39], s68 offen sc1 lds\n"
      "buffer_load_dwordx4 v19, s[36:39], s68 offen offset:1024 sc1 lds\n"
      "s_waitcnt vmcnt(6)\n"
      "s_barrier\n"
      "ds_read_b128 v[20:23], v162 offset:0\n"
      "ds_read_b128 v[24:27], v162 offset:1024\n"
      "ds_read_b128 v[28:31], v162 offset:2048\n"
      "ds_read_b128 v[32:35], v162 offset:3072\n"
      "buffer_load_dword v6, v5, s[36:39], 0 offen sc1\n"
      "s_mov_b32 s87, 0x40000000\n"
      "buffer_load_dword v163, off, s[36:39], s87\n"
      "buffer_load_dword v164, off, s[36:39], s87\n"
      "buffer_load_dword v165, off, s[36:39], s87\n"
      "buffer_load_dword v166, off, s[36:39], s87\n"
      "s_waitcnt lgkmcnt(0)\n"
      "s_cmp_eq_u32 s30, 1\n"
      "s_cbranch_scc1 .Ldn_e1\n"
      "s_cmp_eq_u32 s30, 2\n"
      "s_cbranch_scc1 .Ldn_e2\n"
      "s_cmp_eq_u32 s30, 3\n"
      "s_cbranch_scc1 .Ldn_e3\n"
      "s_cmp_eq_u32 s30, 4\n"
      "s_cbranch_scc1 .Ldn_e4\n"
      "s_cmp_eq_u32 s30, 5\n"
      "s_cbranch_scc1 .Ldn_e5\n"
      "s_branch .Ldn_e0\n"
      ".Ldn_loop:\n"
      "v_mfma_f32_16x16x32_bf16 v[48:51], a[232:235], v[24:27], v[48:51]\n"
      "ds_read_b128 v[40:43], v3 offset:23552\n"
      "v_mfma_f32_16x16x32_bf16 v[52:55], a[224:227], v[20:23], v[52:55]\n"
      "s_barrier\n"
      "v_mfma_f32_16x16x32_bf16 v[52:55], a[236:239], v[24:27], v[52:55]\n"
      "s_waitcnt vmcnt(7) lgkmcnt(3)\n"
      "buffer_store_dword v8, v7, s[36:39], 0 offen sc1\n"
      "v_mfma_f32_16x16x32_bf16 v[44:47], a[240:243], v[28:31], v[44:47]\n"
      "s_waitcnt lgkmcnt(2)\n"
      "ds_read_b128 v[20:23], v3 offset:24576\n"
      "v_mfma_f32_16x16x32_bf16 v[44:47], a[252:255], v[32:35], v[44:47]\n"
      "s_add_u32 s87, s75, s82\n"
      "v_mfma_f32_16x16x32_bf16 v[48:51], a[244:247], v[28:31], v[48:51]\n"
      "s_cmp_lt_u32 s66, 64\n"
      "buffer_load_dwordx4 v[80:83], v18, s[36:39], s87 offen sc1\n"
      "v_mfma_f32_16x16x32_bf16 v[48:51], v[216:219], v[32:35], v[48:51]\n"
      "s_cselect_b32 s71, s70, 0x40000000\n"
      "ds_read_b128 v[24:27], v3 offset:25600\n"
      "v_mfma_f32_16x16x32_bf16 v[52:55], a[248:251], v[28:31], v[52:55]\n"
      "s_add_u32 s70, s70, 110592\n"
      "v_mfma_f32_16x16x32_bf16 v[52:55], v[220:223], v[32:35], v[52:55]\n"
      "s_waitcnt lgkmcnt(3)\n"
      "v_mfma_f32_16x16x32_bf16 v[44:47], v[224:227], v[36:39], v[44:47]\n"
      "s_waitcnt lgkmcnt(2)\n"
      "ds_read_b128 v[28:31], v3 offset:26624\n"
      "v_mfma_f32_16x16x32_bf16 v[44:47], v[236:239], v[40:43], v[44:47]\n"
      "buffer_load_dwordx2 v[84:85], v118, s[36:39], s87 offen offset:1024 sc1\n"
      "v_mfma_f32_16x16x32_bf16 v[48:51], v[228:231], v[36:39], v[48:51]\n"
      "v_mfma_f32_16x16x32_bf16 v[48:51], v[240:243], v[40:43], v[48:51]\n"
      "ds_read_b128 v[32:35], v3 offset:27648\n"
      "v_mfma_f32_16x16x32_bf16 v[52:55], v[232:235], v[36:39], v[52:55]\n"
      "v_mfma_f32_16x16x32_bf16 v[52:55], v[244:247], v[40:43], v[52:55]\n"
      "s_waitcnt lgkmcnt(3)\n"
      ".Ldn_e0:\n"
      "v_mfma_f32_16x16x32_bf16 v[56:59], a[0:3], v[20:23], 0\n"
      "s_waitcnt lgkmcnt(2)\n"
      "ds_read_b128 v[36:39], v3 offset:28672\n"
      "v_cvt_pk_bf16_f32 v112, v44, v45\n"
      "v_mfma_f32_16x16x32_bf16 v[56:59], a[12:15], v[24:27], v[56:59]\n"
      "s_add_u32 s67, s66, 4\n"
      "v_cvt_pk_bf16_f32 v113, v46, v47\n"
      "v_mfma_f32_16x16x32_bf16 v[60:63], a[4:7], v[20:23], 0\n"
      "s_min_i32 s67, s67, 64\n"
      "v_cvt_pk_bf16_f32 v114, v48, v49\n"
      "v_cvt_pk_bf16_f32 v115, v50, v51\n"
      "v_mfma_f32_16x16x32_bf16 v[60:63], a[16:19], v[24:27], v[60:63]\n"
      "s_max_i32 s67, s67, 0\n"
      "ds_read_b128 v[40:43], v3 offset:29696\n"
      "v_mfma_f32_16x16x32_bf16 v[64:67], a[8:11], v[20:23], 0\n"
      "s_add_u32 s68, s68, 147456\n"
      "buffer_store_dwordx4 v[112:115], v18, s[36:39], s71 offen sc1\n"
      "v_mfma_f32_16x16x32_bf16 v[64:67], a[20:23], v[24:27], v[64:67]\n"
      "s_waitcnt lgkmcnt(3)\n"
      "v_cvt_pk_bf16_f32 v116, v52, v53\n"
      "v_cvt_pk_bf16_f32 v117, v54, v55\n"
      "v_mfma_f32_16x16x32_bf16 v[56:59], a[24:27], v[28:31], v[56:59]\n"
      "s_waitcnt lgkmcnt(2)\n"
      "ds_read_b128 v[20:23], v3 offset:30720\n"
      "v_mfma_f32_16x16x32_bf16 v[56:59], a[36:39], v[32:35], v[56:59]\n"
      "s_add_u32 s48, s66, 3\n"
      "v_mfma_f32_16x16x32_bf16 v[60:63], a[28:31], v[28:31], v[60:63]\n"
      "s_cmp_lt_u32 s48, 64\n"
      "v_mfma_f32_16x16x32_bf16 v[60:63], a[40:43], v[32:35], v[60:63]\n"
      "s_cselect_b32 s69, s68, 0x40000000\n"
      "ds_read_b128 v[24:27], v3 offset:31744\n"
      "v_mfma_f32_16x16x32_bf16 v[64:67], a[32:35], v[28:31], v[64:67]\n"
      "s_add_u32 s66, s66, 1\n"
      "buffer_store_dwordx2 v[116:117], v118, s[36:39], s71 offen offset:1024 sc1\n"
      "v_mfma_f32_16x16x32_bf16 v[64:67], a[44:47], v[32:35], v[64:67]\n"
      "s_waitcnt lgkmcnt(3)\n"
      "v_mfma_f32_16x16x32_bf16 v[56:59], a[48:51], v[36:39], v[56:59]\n"
      "s_waitcnt lgkmcnt(2)\n"
      "ds_read_b128 v[28:31], v3 offset:32768\n"
      "v_mfma_f32_16x16x32_bf16 v[56:59], a[60:63], v[40:43], v[56:59]\n"
      "s_max_i32 s72, s66, 0\n"
      "v_mfma_f32_16x16x32_bf16 v[60:63], a[52:55], v[36:39], v[60:63]\n"
      "s_add_u32 s87, s75, s83\n"
      "v_writelane_b32 v8, s72, 0\n"
      "v_mfma_f32_16x16x32_bf16 v[60:63], a[64:67], v[40:43], v[60:63]\n"
      "ds_read_b128 v[32:35], v3 offset:33792\n"
      "v_mfma_f32_16x16x32_bf16 v[64:67], a[56:59], v[36:39], v[64:67]\n"
      "buffer_load_dwordx4 v[86:89], v18, s[36:39], s87 offen sc1\n"
      "v_mfma_f32_16x16x32_bf16 v[64:67], a[68:71], v[40:43], v[64:67]\n"
      "s_waitcnt lgkmcnt(3)\n"
      "v_mfma_f32_16x16x32_bf16 v[56:59], a[72:75], v[20:23], v[56:59]\n"
      "s_waitcnt lgkmcnt(2)\n"
      "ds_read_b128 v[36:39], v3 offset:34816\n"
      "v_mfma_f32_16x16x32_bf16 v[56:59], a[84:87], v[24:27], v[56:59]\n"
      "v_mfma_f32_16x16x32_bf16 v[60:63], a[76:79], v[20:23], v[60:63]\n"
      "buffer_load_dwordx2 v[90:91], v118, s[36:39], s87 offen offset:1024 sc1\n"
      "v_mfma_f32_16x16x32_bf16 v[60:63], a[88:91], v[24:27], v[60:63]\n"
      "s_add_u32 s87, s75, s84\n"
      "ds_read_b128 v[40:43], v3 offset:35840\n"
      "v_mfma_f32_16x16x32_bf16 v[64:67], a[80:83], v[20:23], v[64:67]\n"
      "s_add_u32 m0, s73, 73728\n"
      "v_mfma_f32_16x16x32_bf16 v[64:67], a[92:95], v[24:27], v[64:67]\n"
      "s_waitcnt lgkmcnt(3)\n"
      "v_mfma_f32_16x16x32_bf16 v[56:59], a[96:99], v[28:31], v[56:59]\n"
      "s_waitcnt lgkmcnt(2)\n"
      "ds_read_b128 v[20:23], v3 offset:36864\n"
      "v_mfma_f32_16x16x32_bf16 v[56:59], a[108:111], v[32:35], v[56:59]\n"
      "buffer_load_dwordx4 v[92:95], v18, s[36:39], s87 offen sc1\n"
      "v_mfma_f32_16x16x32_bf16 v[60:63], a[100:103], v[28:31], v[60:63]\n"
      "v_mfma_f32_16x16x32_bf16 v[60:63], a[112:115], v[32:35], v[60:63]\n"
      "ds_read_b128 v[24:27], v3 offset:37888\n"
      "v_mfma_f32_16x16x32_bf16 v[64:67], a[104:107], v[28:31], v[64:67]\n"
      "v_mfma_f32_16x16x32_bf16 v[64:67], a[116:119], v[32:35], v[64:67]\n"
      "s_waitcnt lgkmcnt(3)\n"
      "v_mfma_f32_16x16x32_bf16 v[56:59], a[120:123], v[36:39], v[56:59]\n"
      "s_waitcnt lgkmcnt(2)\n"
      "ds_read_b128 v[28:31], v3 offset:38912\n"
      "v_mfma_f32_16x16x32_bf16 v[56:59], a[132:135], v[40:43], v[56:59]\n"
      "v_mfma_f32_16x16x32_bf16 v[60:63], a[124:127], v[36:39], v[60:63]\n"
      "v_mfma_f32_16x16x32_bf16 v[60:63], a[136:139], v[40:43], v[60:63]\n"
      "ds_read_b128 v[32:35], v3 offset:39936\n"
      "v_mfma_f32_16x16x32_bf16 v[64:67], a[128:131], v[36:39], v[64:67]\n"
      "v_mfma_f32_16x16x32_bf16 v[64:67], a[140:143], v[40:43], v[64:67]\n"
      "s_waitcnt vmcnt(14) lgkmcnt(3)\n"
      "v_cmp_gt_u32 vcc, s67, v6\n"
      "v_mfma_f32_16x16x32_bf16 v[56:59], a[144:147], v[20:23], v[56:59]\n"
      "s_waitcnt lgkmcnt(2)\n"
      "ds_read_b128 v[36:39], v3 offset:40960\n"
      "v_mfma_f32_16x16x32_bf16 v[56:59], a[156:159], v[24:27], v[56:59]\n"
      "s_cbranch_vccnz .Lsl7\n"
      ".Lbk7:\n"
      "buffer_load_dword v6, v5, s[36:39], 0 offen sc1\n"
      "v_mfma_f32_16x16x32_bf16 v[60:63], a[148:151], v[20:23], v[60:63]\n"
      "v_mfma_f32_16x16x32_bf16 v[60:63], a[160:163], v[24:27], v[60:63]\n"
      "ds_read_b128 v[40:43], v3 offset:41984\n"
      "v_mfma_f32_16x16x32_bf16 v[64:67], a[152:155], v[20:23], v[64:67]\n"
      "buffer_load_dwordx4 v2, s[36:39], s69 offen sc1 lds\n"
      "v_mfma_f32_16x16x32_bf16 v[64:67], a[164:167], v[24:27], v[64:67]\n"
      "s_waitcnt lgkmcnt(3)\n"
      "v_mfma_f32_16x16x32_bf16 v[56:59], a[168:171], v[28:31], v[56:59]\n"
      "s_waitcnt lgkmcnt(2)\n"
      "ds_read_b128 v[20:23], v3 offset:43008\n"
      "v_mfma_f32_16x16x32_bf16 v[56:59], a[180:183], v[32:35], v[56:59]\n"
      "buffer_load_dwordx4 v2, s[36:39], s69 offen offset:1024 sc1 lds\n"
      "v_mfma_f32_16x16x32_bf16 v[60:63], a[172:175], v[28:31], v[60:63]\n"
      "v_mfma_f32_16x16x32_bf16 v[60:63], a[184:187], v[32:35], v[60:63]\n"
      "ds_read_b128 v[24:27], v3 offset:44032\n"
      "v_mfma_f32_16x16x32_bf16 v[64:67], a[176:179], v[28:31], v[64:67]\n"
      "buffer_load_dwordx4 v2, s[36:39], s69 offen offset:2048 sc1 lds\n"
      "v_mfma_f32_16x16x32_bf16 v[64:67], a[188:191], v[32:35], v[64:67]\n"
      "s_waitcnt lgkmcnt(3)\n"
      "v_mfma_f32_16x16x32_bf16 v[56:59], a[192:195], v[36:39], v[56:59]\n"
      "s_waitcnt lgkmcnt(2)\n"
      "ds_read_b128 v[28:31], v3 offset:45056\n"
      "v_mfma_f32_16x16x32_bf16 v[56:59], a[204:207], v[40:43], v[56:59]\n"
      "buffer_load_dwordx4 v2, s[36:39], s69 offen offset:3072 sc1 lds\n"
      "v_mfma_f32_16x16x32_bf16 v[60:63], a[196:199], v[36:39], v[60:63]\n"
      "s_waitcnt vmcnt(11)\n"
      "s_add_u32 m0, s73, 77824\n"
      "v_lshlrev_b32 v120, 16, v80\n"
      "v_mfma_f32_16x16x32_bf16 v[60:63], a[208:211], v[40:43], v[60:63]\n"
      "s_cmp_lt_i32 s66, 63\n"
      "ds_read_b128 v[32:35], v3 offset:46080\n"
      "v_add_f32 v168, v168, v120\n"
      "v_mfma_f32_16x16x32_bf16 v[64:67], a[200:203], v[36:39], v[64:67]\n"
      "buffer_load_dwordx4 v19, s[36:39], s69 offen sc1 lds\n"
      "v_mfma_f32_16x16x32_bf16 v[64:67], a[212:215], v[40:43], v[64:67]\n"
      "s_waitcnt lgkmcnt(3)\n"
      "v_and_b32 v121, 0xffff0000, v80\n"
      "v_lshlrev_b32 v122, 16, v81\n"
      "v_mfma_f32_16x16x32_bf16 v[56:59], a[216:219], v[20:23], v[56:59]\n"
      "s_waitcnt lgkmcnt(2)\n"
      "ds_read_b128 v[36:39], v3 offset:47104\n"
      "v_add_f32 v169, v169, v121\n"
      "v_mfma_f32_16x16x32_bf16 v[56:59], a[228:231], v[24:27], v[56:59]\n"
      "buffer_load_dwordx4 v19, s[36:39], s69 offen offset:1024 sc1 lds\n"
      "v_mfma_f32_16x16x32_bf16 v[60:63], a[220:223], v[20:23], v[60:63]\n"
      "v_add_f32 v170, v170, v122\n"
      "v_and_b32 v123, 0xffff0000, v81\n"
      ".Lmd0:\n"
      "s_cbranch_scc0 .Ldn_t1\n"
      "v_mfma_f32_16x16x32_bf16 v[60:63], a[232:235], v[24:27], v[60:63]\n"
      "ds_read_b128 v[40:43], v3 offset:48128\n"
      "v_add_f32 v171, v171, v123\n"
      "v_mfma_f32_16x16x32_bf16 v[64:67], a[224:227], v[20:23], v[64:67]\n"
      "v_lshlrev_b32 v120, 16, v82\n"
      "s_barrier\n"
      "v_mfma_f32_16x16x32_bf16 v[64:67], a[236:239], v[24:27], v[64:67]\n"
      "s_waitcnt vmcnt(10) lgkmcnt(3)\n"
      "buffer_store_dword v8, v7, s[36:39], 0 offen sc1\n"
      "v_mfma_f32_16x16x32_bf16 v[56:59], a[240:243], v[28:31], v[56:59]\n"
      "s_waitcnt lgkmcnt(2)\n"
      "ds_read_b128 v[20:23], v4 offset:0\n"
      "v_add_f32 v172, v172, v120\n"
      "v_mfma_f32_16x16x32_bf16 v[56:59], a[252:255], v[32:35], v[56:59]\n"
      "buffer_load_dwordx2 v[96:97], v118, s[36:39], s87 offen offset:1024 sc1\n"
      "v_mfma_f32_16x16x32_bf16 v[60:63], a[244:247], v[28:31], v[60:63]\n"
      "v_and_b32 v121, 0xffff0000, v82\n"
      "v_lshlrev_b32 v122, 16, v83\n"
      "v_mfma_f32_16x16x32_bf16 v[60:63], v[216:219], v[32:35], v[60:63]\n"
      "ds_read_b128 v[24:27], v4 offset:1024\n"
      "v_add_f32 v173, v173, v121\n"
      "v_mfma_f32_16x16x32_bf16 v[64:67], a[248:251], v[28:31], v[64:67]\n"
      "v_add_f32 v174, v174, v122\n"
      "v_and_b32 v123, 0xffff0000, v83\n"
      "v_mfma_f32_16x16x32_bf16 v[64:67], v[220:223], v[32:35], v[64:67]\n"
      "s_waitcnt lgkmcnt(3)\n"
      "v_add_f32 v175, v175, v123\n"
      "v_lshlrev_b32 v120, 16, v84\n"
      "v_mfma_f32_16x16x32_bf16 v[56:59], v[224:227], v[36:39], v[56:59]\n"
      "s_waitcnt lgkmcnt(2)\n"
      "ds_read_b128 v[28:31], v4 offset:2048\n"
      "v_add_f32 v176, v176, v120\n"
      "v_mfma_f32_16x16x32_bf16 v[56:59], v[236:239], v[40:43], v[56:59]\n"
      "s_add_u32 s87, s75, s85\n"
      "v_and_b32 v121, 0xffff0000, v84\n"
      "v_lshlrev_b32 v122, 16, v85\n"
      "v_mfma_f32_16x16x32_bf16 v[60:63], v[228:231], v[36:39], v[60:63]\n"
      "s_cmp_lt_u32 s66, 64\n"
      "buffer_load_dwordx4 v[98:101], v18, s[36:39], s87 offen sc1\n"
      "v_mfma_f32_16x16x32_bf16 v[60:63], v[240:243], v[40:43], v[60:63]\n"
      "s_cselect_b32 s71, s70, 0x40000000\n"
      "ds_read_b128 v[32:35], v4 offset:3072\n"
      "v_add_f32 v177, v177, v121\n"
      "v_mfma_f32_16x16x32_bf16 v[64:67], v[232:235], v[36:39], v[64:67]\n"
      "s_add_u32 s70, s70, 110592\n"
      "v_add_f32 v178, v178, v122\n"
      "v_and_b32 v123, 0xffff0000, v85\n"
      "v_mfma_f32_16x16x32_bf16 v[64:67], v[244:247], v[40:43], v[64:67]\n"
      "s_waitcnt lgkmcnt(3)\n"
      "v_add_f32 v179, v179, v123\n"
      ".Ldn_e1:\n"
      "v_mfma_f32_16x16x32_bf16 v[44:47], a[0:3], v[20:23], 0\n"
      "s_waitcnt lgkmcnt(2)\n"
      "ds_read_b128 v[36:39], v4 offset:4096\n"
      "v_cvt_pk_bf16_f32 v112, v56, v57\n"
      "v_mfma_f32_16x16x32_bf16 v[44:47], a[12:15], v[24:27], v[44:47]\n"
      "s_add_u32 s67, s66, 4\n"
      "v_cvt_pk_bf16_f32 v113, v58, v59\n"
      "v_mfma_f32_16x16x32_bf16 v[48:51], a[4:7], v[20:23], 0\n"
      "s_min_i32 s67, s67, 64\n"
      "v_cvt_pk_bf16_f32 v114, v60, v61\n"
      "v_cvt_pk_bf16_f32 v115, v62, v63\n"
      "v_mfma_f32_16x16x32_bf16 v[48:51], a[16:19], v[24:27], v[48:51]\n"
      "s_max_i32 s67, s67, 0\n"
      "ds_read_b128 v[40:43], v4 offset:5120\n"
      "v_mfma_f32_16x16x32_bf16 v[52:55], a[8:11], v[20:23], 0\n"
      "s_add_u32 s68, s68, 147456\n"
      "buffer_store_dwordx4 v[112:115], v18, s[36:39], s71 offen sc1\n"
      "v_mfma_f32_16x16x32_bf16 v[52:55], a[20:23], v[24:27], v[52:55]\n"
      "s_waitcnt lgkmcnt(3)\n"
      "v_cvt_pk_bf16_f32 v116, v64, v65\n"
      "v_cvt_pk_bf16_f32 v117, v66, v67\n"
      "v_mfma_f32_16x16x32_bf16 v[44:47], a[24:27], v[28:31], v[44:47]\n"
      "s_waitcnt vmcnt(13) lgkmcnt(2)\n"
      "ds_read_b128 v[20:23], v4 offset:6144\n"
      "v_lshlrev_b32 v120, 16, v86\n"
      "v_mfma_f32_16x16x32_bf16 v[44:47], a[36:39], v[32:35], v[44:47]\n"
      "s_add_u32 s48, s66, 3\n"
      "v_add_f32 v168, v168, v120\n"
      "v_and_b32 v121, 0xffff0000, v86\n"
      "v_mfma_f32_16x16x32_bf16 v[48:51], a[28:31], v[28:31], v[48:51]\n"
      "s_cmp_lt_u32 s48, 64\n"
      "v_add_f32 v169, v169, v121\n"
      "v_lshlrev_b32 v122, 16, v87\n"
      "v_mfma_f32_16x16x32_bf16 v[48:51], a[40:43], v[32:35], v[48:51]\n"
      "s_cselect_b32 s69, s68, 0x40000000\n"
      "ds_read_b128 v[24:27], v4 offset:7168\n"
      "v_add_f32 v170, v170, v122\n"
      "v_mfma_f32_16x16x32_bf16 v[52:55], a[32:35], v[28:31], v[52:55]\n"
      "s_add_u32 s66, s66, 1\n"
      "buffer_store_dwordx2 v[116:117], v118, s[36:39], s71 offen offset:1024 sc1\n"
      "v_mfma_f32_16x16x32_bf16 v[52:55], a[44:47], v[32:35], v[52:55]\n"
      "s_waitcnt lgkmcnt(3)\n"
      "v_and_b32 v123, 0xffff0000, v87\n"
      "v_lshlrev_b32 v120, 16, v88\n"
      "v_mfma_f32_16x16x32_bf16 v[44:47], a[48:51], v[36:39], v[44:47]\n"
      "s_waitcnt lgkmcnt(2)\n"
      "ds_read_b128 v[28:31], v4 offset:8192\n"
      "v_add_f32 v171, v171, v123\n"
      "v_mfma_f32_16x16x32_bf16 v[44:47], a[60:63], v[40:43], v[44:47]\n"
      "s_max_i32 s72, s66, 0\n"
      "v_add_f32 v172, v172, v120\n"
      "v_and_b32 v121, 0xffff0000, v88\n"
      "v_mfma_f32_16x16x32_bf16 v[48:51], a[52:55], v[36:39], v[48:51]\n"
      "v_writelane_b32 v8, s72, 0\n"
      "v_add_f32 v173, v173, v121\n"
      "v_mfma_f32_16x16x32_bf16 v[48:51], a[64:67], v[40:43], v[48:51]\n"
      "ds_read_b128 v[32:35], v4 offset:9216\n"
      "v_lshlrev_b32 v122, 16, v89\n"
      "v_mfma_f32_16x16x32_bf16 v[52:55], a[56:59], v[36:39], v[52:55]\n"
      "buffer_load_dwordx2 v[102:103], v118, s[36:39], s87 offen offset:1024 sc1\n"
      "v_mfma_f32_16x16x32_bf16 v[52:55], a[68:71], v[40:43], v[52:55]\n"
      "s_waitcnt lgkmcnt(3)\n"
      "v_add_f32 v174, v174, v122\n"
      "v_and_b32 v123, 0xffff0000, v89\n"
      "v_mfma_f32_16x16x32_bf16 v[44:47], a[72:75], v[20:23], v[44:47]\n"
      "s_waitcnt lgkmcnt(2)\n"
      "ds_read_b128 v[36:39], v4 offset:10240\n"
      "v_add_f32 v175, v175, v123\n"
      "v_mfma_f32_16x16x32_bf16 v[44:47], a[84:87], v[24:27], v[44:47]\n"
      "s_waitcnt vmcnt(14)\n"
      "s_add_u32 s87, s75, s86\n"
      "v_lshlrev_b32 v120, 16, v90\n"
      "v_mfma_f32_16x16x32_bf16 v[48:51], a[76:79], v[20:23], v[48:51]\n"
      "s_add_u32 m0, s73, 98304\n"
      "buffer_load_dwordx4 v[104:107], v18, s[36:39], s87 offen sc1\n"
      "v_mfma_f32_16x16x32_bf16 v[48:51], a[88:91], v[24:27], v[48:51]\n"
      "ds_read_b128 v[40:43], v4 offset:11264\n"
      "v_add_f32 v176, v176, v120\n"
      "v_mfma_f32_16x16x32_bf16 v[52:55], a[80:83], v[20:23], v[52:55]\n"
      "v_and_b32 v121, 0xffff0000, v90\n"
      "v_lshlrev_b32 v122, 16, v91\n"
      "v_mfma_f32_16x16x32_bf16 v[52:55], a[92:95], v[24:27], v[52:55]\n"
      "s_waitcnt lgkmcnt(3)\n"
      "v_add_f32 v177, v177, v121\n"
      "v_add_f32 v178, v178, v122\n"
      "v_mfma_f32_16x16x32_bf16 v[44:47], a[96:99], v[28:31], v[44:47]\n"
      "s_waitcnt lgkmcnt(2)\n"
      "ds_read_b128 v[20:23], v4 offset:12288\n"
      "v_and_b32 v123, 0xffff0000, v91\n"
      "v_mfma_f32_16x16x32_bf16 v[44:47], a[108:111], v[32:35], v[44:47]\n"
      "buffer_load_dwordx2 v[108:109], v118, s[36:39], s87 offen offset:1024 sc1\n"
      "v_mfma_f32_16x16x32_bf16 v[48:51], a[100:103], v[28:31], v[48:51]\n"
      "s_waitcnt vmcnt(15)\n"
      "v_add_f32 v179, v179, v123\n"
      "v_lshlrev_b32 v120, 16, v92\n"
      "v_mfma_f32_16x16x32_bf16 v[48:51], a[112:115], v[32:35], v[48:51]\n"
      "ds_read_b128 v[24:27], v4 offset:13312\n"
      "v_add_f32 v168, v168, v120\n"
      "v_mfma_f32_16x16x32_bf16 v[52:55], a[104:107], v[28:31], v[52:55]\n"
      "v_and_b32 v121, 0xffff0000, v92\n"
      "v_lshlrev_b32 v122, 16, v93\n"
      "v_mfma_f32_16x16x32_bf16 v[52:55], a[116:119], v[32:35], v[52:55]\n"
      "s_waitcnt lgkmcnt(3)\n"
      "v_add_f32 v169, v169, v121\n"
      "v_add_f32 v170, v170, v122\n"
      "v_mfma_f32_16x16x32_bf16 v[44:47], a[120:123], v[36:39], v[44:47]\n"
      "s_waitcnt lgkmcnt(2)\n"
      "ds_read_b128 v[28:31], v4 offset:14336\n"
      "v_and_b32 v123, 0xffff0000, v93\n"
      "v_mfma_f32_16x16x32_bf16 v[44:47], a[132:135], v[40:43], v[44:47]\n"
      "v_add_f32 v171, v171, v123\n"
      "v_lshlrev_b32 v120, 16, v94\n"
      "v_mfma_f32_16x16x32_bf16 v[48:51], a[124:127], v[36:39], v[48:51]\n"
      "v_add_f32 v172, v172, v120\n"
      "v_and_b32 v121, 0xffff0000, v94\n"
      "v_mfma_f32_16x16x32_bf16 v[48:51], a[136:139], v[40:43], v[48:51]\n"
      "ds_read_b128 v[32:35], v4 offset:15360\n"
      "v_add_f32 v173, v173, v121\n"
      "v_mfma_f32_16x16x32_bf16 v[52:55], a[128:131], v[36:39], v[52:55]\n"
      "v_lshlrev_b32 v122, 16, v95\n"
      "v_and_b32 v123, 0xffff0000, v95\n"
      "v_mfma_f32_16x16x32_bf16 v[52:55], a[140:143], v[40:43], v[52:55]\n"
      "s_waitcnt vmcnt(14) lgkmcnt(3)\n"
      "v_cmp_gt_u32 vcc, s67, v6\n"
      "v_add_f32 v174, v174, v122\n"
      "v_mfma_f32_16x16x32_bf16 v[44:47], a[144:147], v[20:23], v[44:47]\n"
      "s_waitcnt lgkmcnt(2)\n"
      "ds_read_b128 v[36:39], v4 offset:16384\n"
      "v_add_f32 v175, v175, v123\n"
      "v_mfma_f32_16x16x32_bf16 v[44:47], a[156:159], v[24:27], v[44:47]\n"
      "s_cbranch_vccnz .Lsl8\n"
      ".Lbk8:\n"
      "buffer_load_dword v6, v5, s[36:39], 0 offen sc1\n"
      "v_mfma_f32_16x16x32_bf16 v[48:51], a[148:151], v[20:23], v[48:51]\n"
      "v_mfma_f32_16x16x32_bf16 v[48:51], a[160:163], v[24:27], v[48:51]\n"
      "ds_read_b128 v[40:43], v4 offset:17408\n"
      "v_mfma_f32_16x16x32_bf16 v[52:55], a[152:155], v[20:23], v[52:55]\n"
      "buffer_load_dwordx4 v2, s[36:39], s69 offen sc1 lds\n"
      "v_mfma_f32_16x16x32_bf16 v[52:55], a[164:167], v[24:27], v[52:55]\n"
      "s_waitcnt lgkmcnt(3)\n"
      "v_mfma_f32_16x16x32_bf16 v[44:47], a[168:171], v[28:31], v[44:47]\n"
      "s_waitcnt lgkmcnt(2)\n"
      "ds_read_b128 v[20:23], v4 offset:18432\n"
      "v_mfma_f32_16x16x32_bf16 v[44:47], a[180:183], v[32:35], v[44:47]\n"
      "buffer_load_dwordx4 v2, s[36:39], s69 offen offset:1024 sc1 lds\n"
      "v_mfma_f32_16x16x32_bf16 v[48:51], a[172:175], v[28:31], v[48:51]\n"
      "v_mfma_f32_16x16x32_bf16 v[48:51], a[184:187], v[32:35], v[48:51]\n"
      "ds_read_b128 v[24:27], v4 offset:19456\n"
      "v_mfma_f32_16x16x32_bf16 v[52:55], a[176:179], v[28:31], v[52:55]\n"
      "buffer_load_dwordx4 v2, s[36:39], s69 offen offset:2048 sc1 lds\n"
      "v_mfma_f32_16x16x32_bf16 v[52:55], a[188:191], v[32:35], v[52:55]\n"
      "s_waitcnt lgkmcnt(3)\n"
      "v_mfma_f32_16x16x32_bf16 v[44:47], a[192:195], v[36:39], v[44:47]\n"
      "s_waitcnt lgkmcnt(2)\n"
      "ds_read_b128 v[28:31], v4 offset:20480\n"
      "v_mfma_f32_16x16x32_bf16 v[44:47], a[204:207], v[40:43], v[44:47]\n"
      "buffer_load_dwordx4 v2, s[36:39], s69 offen offset:3072 sc1 lds\n"
      "v_mfma_f32_16x16x32_bf16 v[48:51], a[196:199], v[36:39], v[48:51]\n"
      "s_waitcnt vmcnt(11)\n"
      "s_add_u32 m0, s73, 102400\n"
      "v_lshlrev_b32 v120, 16, v96\n"
      "v_mfma_f32_16x16x32_bf16 v[48:51], a[208:211], v[40:43], v[48:51]\n"
      "s_cmp_lt_i32 s66, 63\n"
      "ds_read_b128 v[32:35], v4 offset:21504\n"
      "v_add_f32 v176, v176, v120\n"
      "v_mfma_f32_16x16x32_bf16 v[52:55], a[200:203], v[36:39], v[52:55]\n"
      "buffer_load_dwordx4 v19, s[36:39], s69 offen sc1 lds\n"
      "v_mfma_f32_16x16x32_bf16 v[52:55], a[212:215], v[40:43], v[52:55]\n"
      "s_waitcnt lgkmcnt(3)\n"
      "v_and_b32 v121, 0xffff0000, v96\n"
      "v_lshlrev_b32 v122, 16, v97\n"
      "v_mfma_f32_16x16x32_bf16 v[44:47], a[216:219], v[20:23], v[44:47]\n"
      "s_waitcnt lgkmcnt(2)\n"
      "ds_read_b128 v[36:39], v4 offset:22528\n"
      "v_add_f32 v177, v177, v121\n"
      "v_mfma_f32_16x16x32_bf16 v[44:47], a[228:231], v[24:27], v[44:47]\n"
      "buffer_load_dwordx4 v19, s[36:39], s69 offen offset:1024 sc1 lds\n"
      "v_mfma_f32_16x16x32_bf16 v[48:51], a[220:223], v[20:23], v[48:51]\n"
      "v_add_f32 v178, v178, v122\n"
      "v_and_b32 v123, 0xffff0000, v97\n"
      ".Lmd1:\n"
      "s_cbranch_scc0 .Ldn_t2\n"
      "v_mfma_f32_16x16x32_bf16 v[48:51], a[232:235], v[24:27], v[48:51]\n"
      "s_cmp_lt_u32 s66, 64\n"
      "ds_read_b128 v[40:43], v4 offset:23552\n"
      "v_add_f32 v179, v179, v123\n"
      "v_mfma_f32_16x16x32_bf16 v[52:55], a[224:227], v[20:23], v[52:55]\n"
      "s_waitcnt vmcnt(12)\n"
      "v_lshlrev_b32 v120, 16, v98\n"
      "s_barrier\n"
      "v_mfma_f32_16x16x32_bf16 v[52:55], a[236:239], v[24:27], v[52:55]\n"
      "s_waitcnt vmcnt(10) lgkmcnt(3)\n"
      "buffer_store_dword v8, v7, s[36:39], 0 offen sc1\n"
      "v_mfma_f32_16x16x32_bf16 v[44:47], a[240:243], v[28:31], v[44:47]\n"
      "s_waitcnt lgkmcnt(2)\n"
      "ds_read_b128 v[20:23], v4 offset:24576\n"
      "v_add_f32 v168, v168, v120\n"
      "v_mfma_f32_16x16x32_bf16 v[44:47], a[252:255], v[32:35], v[44:47]\n"
      "s_cselect_b32 s71, s70, 0x40000000\n"
      "v_and_b32 v121, 0xffff0000, v98\n"
      "v_lshlrev_b32 v122, 16, v99\n"
      "v_mfma_f32_16x16x32_bf16 v[48:51], a[244:247], v[28:31], v[48:51]\n"
      "s_add_u32 s70, s70, 110592\n"
      "v_add_f32 v169, v169, v121\n"
      "v_add_f32 v170, v170, v122\n"
      "v_mfma_f32_16x16x32_bf16 v[48:51], v[216:219], v[32:35], v[48:51]\n"
      "ds_read_b128 v[24:27], v4 offset:25600\n"
      "v_and_b32 v123, 0xffff0000, v99\n"
      "v_mfma_f32_16x16x32_bf16 v[52:55], a[248:251], v[28:31], v[52:55]\n"
      "v_add_f32 v171, v171, v123\n"
      "v_lshlrev_b32 v120, 16, v100\n"
      "v_mfma_f32_16x16x32_bf16 v[52:55], v[220:223], v[32:35], v[52:55]\n"
      "s_waitcnt lgkmcnt(3)\n"
      "v_add_f32 v172, v172, v120\n"
      "v_and_b32 v121, 0xffff0000, v100\n"
      "v_mfma_f32_16x16x32_bf16 v[44:47], v[224:227], v[36:39], v[44:47]\n"
      "s_waitcnt lgkmcnt(2)\n"
      "ds_read_b128 v[28:31], v4 offset:26624\n"
      "v_add_f32 v173, v173, v121\n"
      "v_mfma_f32_16x16x32_bf16 v[44:47], v[236:239], v[40:43], v[44:47]\n"
      "v_lshlrev_b32 v122, 16, v101\n"
      "v_and_b32 v123, 0xffff0000, v101\n"
      "v_mfma_f32_16x16x32_bf16 v[48:51], v[228:231], v[36:39], v[48:51]\n"
      "v_add_f32 v174, v174, v122\n"
      "v_add_f32 v175, v175, v123\n"
      "v_mfma_f32_16x16x32_bf16 v[48:51], v[240:243], v[40:43], v[48:51]\n"
      "ds_read_b128 v[32:35], v4 offset:27648\n"
      "v_mfma_f32_16x16x32_bf16 v[52:55], v[232:235], v[36:39], v[52:55]\n"
      "v_mfma_f32_16x16x32_bf16 v[52:55], v[244:247], v[40:43], v[52:55]\n"
      "s_waitcnt lgkmcnt(3)\n"
      ".Ldn_e2:\n"
      "v_mfma_f32_16x16x32_bf16 v[68:71], a[0:3], v[20:23], 0\n"
      "s_waitcnt lgkmcnt(2)\n"
      "ds_read_b128 v[36:39], v4 offset:28672\n"
      "v_cvt_pk_bf16_f32 v112, v44, v45\n"
      "v_mfma_f32_16x16x32_bf16 v[68:71], a[12:15], v[24:27], v[68:71]\n"
      "s_add_u32 s67, s66, 4\n"
      "v_cvt_pk_bf16_f32 v113, v46, v47\n"
      "v_mfma_f32_16x16x32_bf16 v[72:75], a[4:7], v[20:23], 0\n"
      "s_min_i32 s67, s67, 64\n"
      "v_cvt_pk_bf16_f32 v114, v48, v49\n"
      "v_cvt_pk_bf16_f32 v115, v50, v51\n"
      "v_mfma_f32_16x16x32_bf16 v[72:75], a[16:19], v[24:27], v[72:75]\n"
      "s_max_i32 s67, s67, 0\n"
      "ds_read_b128 v[40:43], v4 offset:29696\n"
      "v_mfma_f32_16x16x32_bf16 v[76:79], a[8:11], v[20:23], 0\n"
      "s_add_u32 s68, s68, 147456\n"
      "buffer_store_dwordx4 v[112:115], v18, s[36:39], s71 offen sc1\n"
      "v_mfma_f32_16x16x32_bf16 v[76:79], a[20:23], v[24:27], v[76:79]\n"
      "s_waitcnt lgkmcnt(3)\n"
      "v_cvt_pk_bf16_f32 v116, v52, v53\n"
      "v_cvt_pk_bf16_f32 v117, v54, v55\n"
      "v_mfma_f32_16x16x32_bf16 v[68:71], a[24:27], v[28:31], v[68:71]\n"
      "s_waitcnt vmcnt(11) lgkmcnt(2)\n"
      "ds_read_b128 v[20:23], v4 offset:30720\n"
      "v_lshlrev_b32 v120, 16, v102\n"
      "v_mfma_f32_16x16x32_bf16 v[68:71], a[36:39], v[32:35], v[68:71]\n"
      "s_add_u32 s48, s66, 3\n"
      "v_add_f32 v176, v176, v120\n"
      "v_and_b32 v121, 0xffff0000, v102\n"
      "v_mfma_f32_16x16x32_bf16 v[72:75], a[28:31], v[28:31], v[72:75]\n"
      "s_cmp_lt_u32 s48, 64\n"
      "v_add_f32 v177, v177, v121\n"
      "v_lshlrev_b32 v122, 16, v103\n"
      "v_mfma_f32_16x16x32_bf16 v[72:75], a[40:43], v[32:35], v[72:75]\n"
      "s_cselect_b32 s69, s68, 0x40000000\n"
      "ds_read_b128 v[24:27], v4 offset:31744\n"
      "v_add_f32 v178, v178, v122\n"
      "v_mfma_f32_16x16x32_bf16 v[76:79], a[32:35], v[28:31], v[76:79]\n"
      "s_add_u32 s66, s66, 1\n"
      "buffer_store_dwordx2 v[116:117], v118, s[36:39], s71 offen offset:1024 sc1\n"
      "v_mfma_f32_16x16x32_bf16 v[76:79], a[44:47], v[32:35], v[76:79]\n"
      "s_waitcnt vmcnt(11) lgkmcnt(3)\n"
      "v_and_b32 v123, 0xffff0000, v103\n"
      "v_lshlrev_b32 v120, 16, v104\n"
      "v_mfma_f32_16x16x32_bf16 v[68:71], a[48:51], v[36:39], v[68:71]\n"
      "s_waitcnt lgkmcnt(2)\n"
      "ds_read_b128 v[28:31], v4 offset:32768\n"
      "v_add_f32 v179, v179, v123\n"
      "v_mfma_f32_16x16x32_bf16 v[68:71], a[60:63], v[40:43], v[68:71]\n"
      "s_max_i32 s72, s66, 0\n"
      "v_add_f32 v168, v168, v120\n"
      "v_and_b32 v121, 0xffff0000, v104\n"
      "v_mfma_f32_16x16x32_bf16 v[72:75], a[52:55], v[36:39], v[72:75]\n"
      "s_add_u32 m0, s73, 122880\n"
      "v_writelane_b32 v8, s72, 0\n"
      "v_add_f32 v169, v169, v121\n"
      "v_mfma_f32_16x16x32_bf16 v[72:75], a[64:67], v[40:43], v[72:75]\n"
      "ds_read_b128 v[32:35], v4 offset:33792\n"
      "v_lshlrev_b32 v122, 16, v105\n"
      "v_mfma_f32_16x16x32_bf16 v[76:79], a[56:59], v[36:39], v[76:79]\n"
      "v_add_f32 v170, v170, v122\n"
      "v_and_b32 v123, 0xffff0000, v105\n"
      "v_mfma_f32_16x16x32_bf16 v[76:79], a[68:71], v[40:43], v[76:79]\n"
      "s_waitcnt lgkmcnt(3)\n"
      "v_add_f32 v171, v171, v123\n"
      "v_lshlrev_b32 v120, 16, v106\n"
      "v_mfma_f32_16x16x32_bf16 v[68:71], a[72:75], v[20:23], v[68:71]\n"
      "s_waitcnt lgkmcnt(2)\n"
      "ds_read_b128 v[36:39], v4 offset:34816\n"
      "v_add_f32 v172, v172, v120\n"
      "v_mfma_f32_16x16x32_bf16 v[68:71], a[84:87], v[24:27], v[68:71]\n"
      "v_and_b32 v121, 0xffff0000, v106\n"
      "v_lshlrev_b32 v122, 16, v107\n"
      "v_mfma_f32_16x16x32_bf16 v[72:75], a[76:79], v[20:23], v[72:75]\n"
      "v_add_f32 v173, v173, v121\n"
      "v_add_f32 v174, v174, v122\n"
      "v_mfma_f32_16x16x32_bf16 v[72:75], a[88:91], v[24:27], v[72:75]\n"
      "ds_read_b128 v[40:43], v4 offset:35840\n"
      "v_and_b32 v123, 0xffff0000, v107\n"
      "v_mfma_f32_16x16x32_bf16 v[76:79], a[80:83], v[20:23], v[76:79]\n"
      "s_waitcnt vmcnt(10)\n"
      "v_add_f32 v175, v175, v123\n"
      "v_lshlrev_b32 v120, 16, v108\n"
      "v_mfma_f32_16x16x32_bf16 v[76:79], a[92:95], v[24:27], v[76:79]\n"
      "s_waitcnt lgkmcnt(3)\n"
      "v_add_f32 v176, v176, v120\n"
      "v_and_b32 v121, 0xffff0000, v108\n"
      "v_mfma_f32_16x16x32_bf16 v[68:71], a[96:99], v[28:31], v[68:71]\n"
      "s_waitcnt lgkmcnt(2)\n"
      "ds_read_b128 v[20:23], v4 offset:36864\n"
      "v_add_f32 v177, v177, v121\n"
      "v_mfma_f32_16x16x32_bf16 v[68:71], a[108:111], v[32:35], v[68:71]\n"
      "v_lshlrev_b32 v122, 16, v109\n"
      "v_and_b32 v123, 0xffff0000, v109\n"
      "v_mfma_f32_16x16x32_bf16 v[72:75], a[100:103], v[28:31], v[72:75]\n"
      "v_add_f32 v178, v178, v122\n"
      "v_add_f32 v179, v179, v123\n"
      "v_mfma_f32_16x16x32_bf16 v[72:75], a[112:115], v[32:35], v[72:75]\n"
      "ds_read_b128 v[24:27], v4 offset:37888\n"
      "v_cvt_pk_bf16_f32 v146, v168, v169\n"
      "v_mfma_f32_16x16x32_bf16 v[76:79], a[104:107], v[28:31], v[76:79]\n"
      "v_cvt_pk_bf16_f32 v147, v170, v171\n"
      "v_cvt_pk_bf16_f32 v148, v172, v173\n"
      "v_mfma_f32_16x16x32_bf16 v[76:79], a[116:119], v[32:35], v[76:79]\n"
      "s_waitcnt lgkmcnt(3)\n"
      "v_cvt_pk_bf16_f32 v149, v174, v175\n"
      "v_cvt_pk_bf16_f32 v150, v176, v177\n"
      "v_mfma_f32_16x16x32_bf16 v[68:71], a[120:123], v[36:39], v[68:71]\n"
      "s_waitcnt lgkmcnt(2)\n"
      "ds_read_b128 v[28:31], v4 offset:38912\n"
      "v_cvt_pk_bf16_f32 v151, v178, v179\n"
      "v_mfma_f32_16x16x32_bf16 v[68:71], a[132:135], v[40:43], v[68:71]\n"
      "v_mul_f32 v153, v168, v168\n"
      "v_add_f32 v152, v168, v169\n"
      "v_mfma_f32_16x16x32_bf16 v[72:75], a[124:127], v[36:39], v[72:75]\n"
      "v_fmac_f32 v153, v169, v169\n"
      "v_add_f32 v152, v152, v170\n"
      "v_mfma_f32_16x16x32_bf16 v[72:75], a[136:139], v[40:43], v[72:75]\n"
      "ds_read_b128 v[32:35], v4 offset:39936\n"
      "v_fmac_f32 v153, v170, v170\n"
      "v_mfma_f32_16x16x32_bf16 v[76:79], a[128:131], v[36:39], v[76:79]\n"
      "v_add_f32 v152, v152, v171\n"
      "v_fmac_f32 v153, v171, v171\n"
      "v_mfma_f32_16x16x32_bf16 v[76:79], a[140:143], v[40:43], v[76:79]\n"
      "s_waitcnt vmcnt(9) lgkmcnt(3)\n"
      "v_cmp_gt_u32 vcc, s67, v6\n"
      "v_add_f32 v152, v152, v172\n"
      "v_mfma_f32_16x16x32_bf16 v[68:71], a[144:147], v[20:23], v[68:71]\n"
      "s_waitcnt lgkmcnt(2)\n"
      "ds_read_b128 v[36:39], v4 offset:40960\n"
      "v_fmac_f32 v153, v172, v172\n"
      "v_mfma_f32_16x16x32_bf16 v[68:71], a[156:159], v[24:27], v[68:71]\n"
      "s_cbranch_vccnz .Lsl9\n"
      ".Lbk9:\n"
      "buffer_load_dword v6, v5, s[36:39], 0 offen sc1\n"
      "v_mfma_f32_16x16x32_bf16 v[72:75], a[148:151], v[20:23], v[72:75]\n"
      "v_add_f32 v152, v152, v173\n"
      "v_fmac_f32 v153, v173, v173\n"
      "v_mfma_f32_16x16x32_bf16 v[72:75], a[160:163], v[24:27], v[72:75]\n"
      "ds_read_b128 v[40:43], v4 offset:41984\n"
      "v_add_f32 v152, v152, v174\n"
      "v_mfma_f32_16x16x32_bf16 v[76:79], a[152:155], v[20:23], v[76:79]\n"
      "buffer_load_dwordx4 v2, s[36:39], s69 offen sc1 lds\n"
      "v_mfma_f32_16x16x32_bf16 v[76:79], a[164:167], v[24:27], v[76:79]\n"
      "s_waitcnt lgkmcnt(3)\n"
      "v_fmac_f32 v153, v174, v174\n"
      "v_add_f32 v152, v152, v175\n"
      "v_mfma_f32_16x16x32_bf16 v[68:71], a[168:171], v[28:31], v[68:71]\n"
      "s_waitcnt lgkmcnt(2)\n"
      "ds_read_b128 v[20:23], v4 offset:43008\n"
      "v_fmac_f32 v153, v175, v175\n"
      "v_mfma_f32_16x16x32_bf16 v[68:71], a[180:183], v[32:35], v[68:71]\n"
      "buffer_load_dwordx4 v2, s[36:39], s69 offen offset:1024 sc1 lds\n"
      "v_mfma_f32_16x16x32_bf16 v[72:75], a[172:175], v[28:31], v[72:75]\n"
      "v_add_f32 v152, v152, v176\n"
      "v_fmac_f32 v153, v176, v176\n"
      "v_mfma_f32_16x16x32_bf16 v[72:75], a[184:187], v[32:35], v[72:75]\n"
      "ds_read_b128 v[24:27], v4 offset:44032\n"
      "v_add_f32 v152, v152, v177\n"
      "v_mfma_f32_16x16x32_bf16 v[76:79], a[176:179], v[28:31], v[76:79]\n"
      "buffer_load_dwordx4 v2, s[36:39], s69 offen offset:2048 sc1 lds\n"
      "v_mfma_f32_16x16x32_bf16 v[76:79], a[188:191], v[32:35], v[76:79]\n"
      "s_waitcnt lgkmcnt(3)\n"
      "v_fmac_f32 v153, v177, v177\n"
      "v_add_f32 v152, v152, v178\n"
      "v_mfma_f32_16x16x32_bf16 v[68:71], a[192:195], v[36:39], v[68:71]\n"
      "s_waitcnt lgkmcnt(2)\n"
      "ds_read_b128 v[28:31], v4 offset:45056\n"
      "v_fmac_f32 v153, v178, v178\n"
      "v_mfma_f32_16x16x32_bf16 v[68:71], a[204:207], v[40:43], v[68:71]\n"
      "buffer_load_dwordx4 v2, s[36:39], s69 offen offset:3072 sc1 lds\n"
      "v_mfma_f32_16x16x32_bf16 v[72:75], a[196:199], v[36:39], v[72:75]\n"
      "s_add_u32 m0, s73, 126976\n"
      "v_add_f32 v152, v152, v179\n"
      "v_fmac_f32 v153, v179, v179\n"
      "v_mfma_f32_16x16x32_bf16 v[72:75], a[208:211], v[40:43], v[72:75]\n"
      "s_cmp_lt_i32 s66, 63\n"
      "ds_read_b128 v[32:35], v4 offset:46080\n"
      "v_permlane32_swap_b32 v152, v153\n"
      "v_mfma_f32_16x16x32_bf16 v[76:79], a[200:203], v[36:39], v[76:79]\n"
      "buffer_load_dwordx4 v19, s[36:39], s69 offen sc1 lds\n"
      "v_mfma_f32_16x16x32_bf16 v[76:79], a[212:215], v[40:43], v[76:79]\n"
      "s_waitcnt lgkmcnt(3)\n"
      "v_add_f32 v152, v152, v153\n"
      "v_mfma_f32_16x16x32_bf16 v[68:71], a[216:219], v[20:23], v[68:71]\n"
      "s_waitcnt lgkmcnt(2)\n"
      "ds_read_b128 v[36:39], v4 offset:47104\n"
      "v_mov_b32 v154, v152\n"
      "v_mfma_f32_16x16x32_bf16 v[68:71], a[228:231], v[24:27], v[68:71]\n"
      "buffer_load_dwordx4 v19, s[36:39], s69 offen offset:1024 sc1 lds\n"
      "v_mfma_f32_16x16x32_bf16 v[72:75], a[220:223], v[20:23], v[72:75]\n"
      "v_permlane16_swap_b32 v152, v154\n"
      ".Lmd2:\n"
      "s_cbranch_scc0 .Ldn_t3\n"
      "v_mfma_f32_16x16x32_bf16 v[72:75], a[232:235], v[24:27], v[72:75]\n"
      "s_cmp_lt_u32 s66, 64\n"
      "ds_read_b128 v[40:43], v4 offset:48128\n"
      "v_add_f32 v152, v152, v154\n"
      "v_mfma_f32_16x16x32_bf16 v[76:79], a[224:227], v[20:23], v[76:79]\n"
      "s_waitcnt vmcnt(10)\n"
      "s_barrier\n"
      "v_mfma_f32_16x16x32_bf16 v[76:79], a[236:239], v[24:27], v[76:79]\n"
      "s_waitcnt vmcnt(7) lgkmcnt(3)\n"
      "buffer_store_dword v8, v7, s[36:39], 0 offen sc1\n"
      "v_mfma_f32_16x16x32_bf16 v[68:71], a[240:243], v[28:31], v[68:71]\n"
      "s_waitcnt lgkmcnt(2)\n"
      "ds_read_b128 v[20:23], v119 offset:0\n"
      "v_mfma_f32_16x16x32_bf16 v[68:71], a[252:255], v[32:35], v[68:71]\n"
      "s_cselect_b32 s71, s70, 0x40000000\n"
      "buffer_store_dwordx4 v[146:149], v12, s[36:39], s77 offen sc1\n"
      "v_mfma_f32_16x16x32_bf16 v[72:75], a[244:247], v[28:31], v[72:75]\n"
      "s_add_u32 s70, s70, 110592\n"
      "v_mfma_f32_16x16x32_bf16 v[72:75], v[216:219], v[32:35], v[72:75]\n"
      "s_mov_b32 s75, s71\n"
      "ds_read_b128 v[24:27], v119 offset:1024\n"
      "v_mfma_f32_16x16x32_bf16 v[76:79], a[248:251], v[28:31], v[76:79]\n"
      "s_mov_b32 s88, s66\n"
      "v_mfma_f32_16x16x32_bf16 v[76:79], v[220:223], v[32:35], v[76:79]\n"
      "s_waitcnt lgkmcnt(3)\n"
      "v_mfma_f32_16x16x32_bf16 v[68:71], v[224:227], v[36:39], v[68:71]\n"
      "s_waitcnt lgkmcnt(2)\n"
      "ds_read_b128 v[28:31], v119 offset:2048\n"
      "v_mfma_f32_16x16x32_bf16 v[68:71], v[236:239], v[40:43], v[68:71]\n"
      "buffer_store_dwordx2 v[150:151], v11, s[36:39], s77 offen sc1\n"
      "v_mfma_f32_16x16x32_bf16 v[72:75], v[228:231], v[36:39], v[72:75]\n"
      "v_mfma_f32_16x16x32_bf16 v[72:75], v[240:243], v[40:43], v[72:75]\n"
      "ds_read_b128 v[32:35], v119 offset:3072\n"
      "v_mfma_f32_16x16x32_bf16 v[76:79], v[232:235], v[36:39], v[76:79]\n"
      "buffer_store_dword v152, v15, s[36:39], s77 offen sc1\n"
      "v_mfma_f32_16x16x32_bf16 v[76:79], v[244:247], v[40:43], v[76:79]\n"
      "s_waitcnt lgkmcnt(3)\n"
      ".Ldn_e3:\n"
      "v_mfma_f32_16x16x32_bf16 v[44:47], a[0:3], v[20:23], 0\n"
      "s_waitcnt lgkmcnt(2)\n"
      "ds_read_b128 v[36:39], v119 offset:4096\n"
      "v_mfma_f32_16x16x32_bf16 v[44:47], a[12:15], v[24:27], v[44:47]\n"
      "s_add_u32 s67, s66, 4\n"
      "buffer_store_dwordx4 v[146:149], v13, s[40:43], s78 offen\n"
      "v_mfma_f32_16x16x32_bf16 v[48:51], a[4:7], v[20:23], 0\n"
      "s_min_i32 s67, s67, 64\n"
      "v_mfma_f32_16x16x32_bf16 v[48:51], a[16:19], v[24:27], v[48:51]\n"
      "s_max_i32 s67, s67, 0\n"
      "ds_read_b128 v[40:43], v119 offset:5120\n"
      "v_mfma_f32_16x16x32_bf16 v[52:55], a[8:11], v[20:23], 0\n"
      "s_add_u32 s68, s68, 147456\n"
      "v_mfma_f32_16x16x32_bf16 v[52:55], a[20:23], v[24:27], v[52:55]\n"
      "s_waitcnt lgkmcnt(3)\n"
      "v_mfma_f32_16x16x32_bf16 v[44:47], a[24:27], v[28:31], v[44:47]\n"
      "s_waitcnt lgkmcnt(2)\n"
      "ds_read_b128 v[20:23], v119 offset:6144\n"
      "v_mfma_f32_16x16x32_bf16 v[44:47], a[36:39], v[32:35], v[44:47]\n"
      "s_add_u32 s48, s66, 3\n"
      "buffer_store_dwordx2 v[150:151], v14, s[40:43], s78 offen\n"
      "v_mfma_f32_16x16x32_bf16 v[48:51], a[28:31], v[28:31], v[48:51]\n"
      "s_cmp_lt_u32 s48, 64\n"
      "v_mfma_f32_16x16x32_bf16 v[48:51], a[40:43], v[32:35], v[48:51]\n"
      "s_cselect_b32 s69, s68, 0x40000000\n"
      "ds_read_b128 v[24:27], v119 offset:7168\n"
      "v_mfma_f32_16x16x32_bf16 v[52:55], a[32:35], v[28:31], v[52:55]\n"
      "s_add_u32 s66, s66, 1\n"
      "v_mfma_f32_16x16x32_bf16 v[52:55], a[44:47], v[32:35], v[52:55]\n"
      "s_waitcnt lgkmcnt(3)\n"
      "v_mfma_f32_16x16x32_bf16 v[44:47], a[48:51], v[36:39], v[44:47]\n"
      "s_waitcnt lgkmcnt(2)\n"
      "ds_read_b128 v[28:31], v119 offset:8192\n"
      "v_mfma_f32_16x16x32_bf16 v[44:47], a[60:63], v[40:43], v[44:47]\n"
      "s_max_i32 s72, s66, 0\n"
      "v_mfma_f32_16x16x32_bf16 v[48:51], a[52:55], v[36:39], v[48:51]\n"
      "s_add_u32 m0, s73, 0\n"
      "v_writelane_b32 v8, s72, 0\n"
      "v_mfma_f32_16x16x32_bf16 v[48:51], a[64:67], v[40:43], v[48:51]\n"
      "ds_read_b128 v[32:35], v119 offset:9216\n"
      "v_mfma_f32_16x16x32_bf16 v[52:55], a[56:59], v[36:39], v[52:55]\n"
      "v_mfma_f32_16x16x32_bf16 v[52:55], a[68:71], v[40:43], v[52:55]\n"
      "s_waitcnt lgkmcnt(3)\n"
      "v_mfma_f32_16x16x32_bf16 v[44:47], a[72:75], v[20:23], v[44:47]\n"
      "s_waitcnt lgkmcnt(2)\n"
      "ds_read_b128 v[36:39], v119 offset:10240\n"
      "v_mfma_f32_16x16x32_bf16 v[44:47], a[84:87], v[24:27], v[44:47]\n"
      "v_mfma_f32_16x16x32_bf16 v[48:51], a[76:79], v[20:23], v[48:51]\n"
      "v_mfma_f32_16x16x32_bf16 v[48:51], a[88:91], v[24:27], v[48:51]\n"
      "ds_read_b128 v[40:43], v119 offset:11264\n"
      "v_mfma_f32_16x16x32_bf16 v[52:55], a[80:83], v[20:23], v[52:55]\n"
      "v_mfma_f32_16x16x32_bf16 v[52:55], a[92:95], v[24:27], v[52:55]\n"
      "s_waitcnt lgkmcnt(3)\n"
      "v_mfma_f32_16x16x32_bf16 v[44:47], a[96:99], v[28:31], v[44:47]\n"
      "s_waitcnt lgkmcnt(2)\n"
      "ds_read_b128 v[20:23], v119 offset:12288\n"
      "v_mfma_f32_16x16x32_bf16 v[44:47], a[108:111], v[32:35], v[44:47]\n"
      "v_mfma_f32_16x16x32_bf16 v[48:51], a[100:103], v[28:31], v[48:51]\n"
      "v_mfma_f32_16x16x32_bf16 v[48:51], a[112:115], v[32:35], v[48:51]\n"
      "ds_read_b128 v[24:27], v119 offset:13312\n"
      "v_mfma_f32_16x16x32_bf16 v[52:55], a[104:107], v[28:31], v[52:55]\n"
      "v_mfma_f32_16x16x32_bf16 v[52:55], a[116:119], v[32:35], v[52:55]\n"
      "s_waitcnt lgkmcnt(3)\n"
      "v_mfma_f32_16x16x32_bf16 v[44:47], a[120:123], v[36:39], v[44:47]\n"
      "s_waitcnt lgkmcnt(2)\n"
      "ds_read_b128 v[28:31], v119 offset:14336\n"
      "v_mfma_f32_16x16x32_bf16 v[44:47], a[132:135], v[40:43], v[44:47]\n"
      "v_mfma_f32_16x16x32_bf16 v[48:51], a[124:127], v[36:39], v[48:51]\n"
      "v_mfma_f32_16x16x32_bf16 v[48:51], a[136:139], v[40:43], v[48:51]\n"
      "ds_read_b128 v[32:35], v119 offset:15360\n"
      "v_mfma_f32_16x16x32_bf16 v[52:55], a[128:131], v[36:39], v[52:55]\n"
      "v_mfma_f32_16x16x32_bf16 v[52:55], a[140:143], v[40:43], v[52:55]\n"
      "s_waitcnt vmcnt(12) lgkmcnt(3)\n"
      "v_cmp_gt_u32 vcc, s67, v6\n"
      "v_mfma_f32_16x16x32_bf16 v[44:47], a[144:147], v[20:23], v[44:47]\n"
      "s_waitcnt lgkmcnt(2)\n"
      "ds_read_b128 v[36:39], v119 offset:16384\n"
      "v_mfma_f32_16x16x32_bf16 v[44:47], a[156:159], v[24:27], v[44:47]\n"
      "s_cbranch_vccnz .Lsl10\n"
      ".Lbk10:\n"
      "buffer_load_dword v6, v5, s[36:39], 0 offen sc1\n"
      "v_mfma_f32_16x16x32_bf16 v[48:51], a[148:151], v[20:23], v[48:51]\n"
      "s_add_u32 s48, s88, 1\n"
      "v_mfma_f32_16x16x32_bf16 v[48:51], a[160:163], v[24:27], v[48:51]\n"
      "s_cmp_lt_u32 s88, 64\n"
      "ds_read_b128 v[40:43], v119 offset:17408\n"
      "v_mfma_f32_16x16x32_bf16 v[52:55], a[152:155], v[20:23], v[52:55]\n"
      "s_cselect_b32 s74, s48, 0\n"
      "buffer_load_dwordx4 v2, s[36:39], s69 offen sc1 lds\n"
      "v_mfma_f32_16x16x32_bf16 v[52:55], a[164:167], v[24:27], v[52:55]\n"
      "s_waitcnt lgkmcnt(3)\n"
      "v_mfma_f32_16x16x32_bf16 v[44:47], a[168:171], v[28:31], v[44:47]\n"
      "s_waitcnt lgkmcnt(2)\n"
      "ds_read_b128 v[20:23], v119 offset:18432\n"
      "v_mfma_f32_16x16x32_bf16 v[44:47], a[180:183], v[32:35], v[44:47]\n"
      "s_mul_i32 s48, s29, 65\n"
      "buffer_load_dwordx4 v2, s[36:39], s69 offen offset:1024 sc1 lds\n"
      "v_mfma_f32_16x16x32_bf16 v[48:51], a[172:175], v[28:31], v[48:51]\n"
      "s_add_u32 s48, s48, s88\n"
      "v_mfma_f32_16x16x32_bf16 v[48:51], a[184:187], v[32:35], v[48:51]\n"
      "s_mul_i32 s48, s48, 20480\n"
      "ds_read_b128 v[24:27], v119 offset:19456\n"
      "v_mfma_f32_16x16x32_bf16 v[52:55], a[176:179], v[28:31], v[52:55]\n"
      "s_add_u32 s48, s48, 10526720\n"
      "buffer_load_dwordx4 v2, s[36:39], s69 offen offset:2048 sc1 lds\n"
      "v_mfma_f32_16x16x32_bf16 v[52:55], a[188:191], v[32:35], v[52:55]\n"
      "s_waitcnt lgkmcnt(3)\n"
      "v_mfma_f32_16x16x32_bf16 v[44:47], a[192:195], v[36:39], v[44:47]\n"
      "s_waitcnt lgkmcnt(2)\n"
      "ds_read_b128 v[28:31], v119 offset:20480\n"
      "v_mfma_f32_16x16x32_bf16 v[44:47], a[204:207], v[40:43], v[44:47]\n"
      "s_cmp_lt_u32 s88, 64\n"
      "buffer_load_dwordx4 v2, s[36:39], s69 offen offset:3072 sc1 lds\n"
      "v_mfma_f32_16x16x32_bf16 v[48:51], a[196:199], v[36:39], v[48:51]\n"
      "s_cselect_b32 s76, s48, 0x40000000\n"
      "v_mfma_f32_16x16x32_bf16 v[48:51], a[208:211], v[40:43], v[48:51]\n"
      "s_add_u32 m0, s73, 4096\n"
      "ds_read_b128 v[32:35], v119 offset:21504\n"
      "v_mfma_f32_16x16x32_bf16 v[52:55], a[200:203], v[36:39], v[52:55]\n"
      "s_cmp_lt_i32 s66, 63\n"
      "buffer_load_dwordx4 v19, s[36:39], s69 offen sc1 lds\n"
      "v_mfma_f32_16x16x32_bf16 v[52:55], a[212:215], v[40:43], v[52:55]\n"
      "s_waitcnt vmcnt(8) lgkmcnt(3)\n"
      "buffer_store_dword v8, v16, s[36:39], s79 offen sc1\n"
      "v_mfma_f32_16x16x32_bf16 v[44:47], a[216:219], v[20:23], v[44:47]\n"
      "s_waitcnt lgkmcnt(2)\n"
      "ds_read_b128 v[36:39], v119 offset:22528\n"
      "v_mfma_f32_16x16x32_bf16 v[44:47], a[228:231], v[24:27], v[44:47]\n"
      "v_mfma_f32_16x16x32_bf16 v[48:51], a[220:223], v[20:23], v[48:51]\n"
      "buffer_load_dwordx4 v19, s[36:39], s69 offen offset:1024 sc1 lds\n"
      ".Lmd3:\n"
      "s_cbranch_scc0 .Ldn_t4\n"
      "v_mfma_f32_16x16x32_bf16 v[48:51], a[232:235], v[24:27], v[48:51]\n"
      "s_cmp_lt_u32 s66, 64\n"
      "ds_read_b128 v[40:43], v119 offset:23552\n"
      "v_mfma_f32_16x16x32_bf16 v[52:55], a[224:227], v[20:23], v[52:55]\n"
      "s_barrier\n"
      "v_mfma_f32_16x16x32_bf16 v[52:55], a[236:239], v[24:27], v[52:55]\n"
      "s_waitcnt lgkmcnt(3)\n"
      "buffer_store_dword v8, v7, s[36:39], 0 offen sc1\n"
      "v_mfma_f32_16x16x32_bf16 v[44:47], a[240:243], v[28:31], v[44:47]\n"
      "s_waitcnt lgkmcnt(2)\n"
      "ds_read_b128 v[20:23], v119 offset:24576\n"
      "v_mfma_f32_16x16x32_bf16 v[44:47], a[252:255], v[32:35], v[44:47]\n"
      "s_cselect_b32 s71, s70, 0x40000000\n"
      "v_mfma_f32_16x16x32_bf16 v[48:51], a[244:247], v[28:31], v[48:51]\n"
      "s_add_u32 s70, s70, 110592\n"
      "v_mfma_f32_16x16x32_bf16 v[48:51], v[216:219], v[32:35], v[48:51]\n"
      "ds_read_b128 v[24:27], v119 offset:25600\n"
      "v_mfma_f32_16x16x32_bf16 v[52:55], a[248:251], v[28:31], v[52:55]\n"
      "v_mfma_f32_16x16x32_bf16 v[52:55], v[220:223], v[32:35], v[52:55]\n"
      "s_waitcnt lgkmcnt(3)\n"
      "v_mfma_f32_16x16x32_bf16 v[44:47], v[224:227], v[36:39], v[44:47]\n"
      "s_waitcnt lgkmcnt(2)\n"
      "ds_read_b128 v[28:31], v119 offset:26624\n"
      "v_mfma_f32_16x16x32_bf16 v[44:47], v[236:239], v[40:43], v[44:47]\n"
      "v_mfma_f32_16x16x32_bf16 v[48:51], v[228:231], v[36:39], v[48:51]\n"
      "v_mfma_f32_16x16x32_bf16 v[48:51], v[240:243], v[40:43], v[48:51]\n"
      "ds_read_b128 v[32:35], v119 offset:27648\n"
      "v_mfma_f32_16x16x32_bf16 v[52:55], v[232:235], v[36:39], v[52:55]\n"
      "v_mfma_f32_16x16x32_bf16 v[52:55], v[244:247], v[40:43], v[52:55]\n"
      "s_waitcnt lgkmcnt(3)\n"
      ".Ldn_e4:\n"
      "v_mfma_f32_16x16x32_bf16 v[56:59], a[0:3], v[20:23], 0\n"
      "s_waitcnt lgkmcnt(2)\n"
      "ds_read_b128 v[36:39], v119 offset:28672\n"
      "v_cvt_pk_bf16_f32 v112, v44, v45\n"
      "v_mfma_f32_16x16x32_bf16 v[56:59], a[12:15], v[24:27], v[56:59]\n"
      "s_add_u32 s67, s66, 4\n"
      "v_cvt_pk_bf16_f32 v113, v46, v47\n"
      "v_mfma_f32_16x16x32_bf16 v[60:63], a[4:7], v[20:23], 0\n"
      "s_min_i32 s67, s67, 64\n"
      "v_cvt_pk_bf16_f32 v114, v48, v49\n"
      "v_cvt_pk_bf16_f32 v115, v50, v51\n"
      "v_mfma_f32_16x16x32_bf16 v[60:63], a[16:19], v[24:27], v[60:63]\n"
      "s_max_i32 s67, s67, 0\n"
      "ds_read_b128 v[40:43], v119 offset:29696\n"
      "v_mfma_f32_16x16x32_bf16 v[64:67], a[8:11], v[20:23], 0\n"
      "s_add_u32 s68, s68, 147456\n"
      "buffer_store_dwordx4 v[112:115], v18, s[36:39], s71 offen sc1\n"
      "v_mfma_f32_16x16x32_bf16 v[64:67], a[20:23], v[24:27], v[64:67]\n"
      "s_waitcnt lgkmcnt(3)\n"
      "v_cvt_pk_bf16_f32 v116, v52, v53\n"
      "v_cvt_pk_bf16_f32 v117, v54, v55\n"
      "v_mfma_f32_16x16x32_bf16 v[56:59], a[24:27], v[28:31], v[56:59]\n"
      "s_waitcnt lgkmcnt(2)\n"
      "ds_read_b128 v[20:23], v119 offset:30720\n"
      "v_mfma_f32_16x16x32_bf16 v[56:59], a[36:39], v[32:35], v[56:59]\n"
      "s_add_u32 s48, s66, 3\n"
      "v_mfma_f32_16x16x32_bf16 v[60:63], a[28:31], v[28:31], v[60:63]\n"
      "s_cmp_lt_u32 s48, 64\n"
      "v_mfma_f32_16x16x32_bf16 v[60:63], a[40:43], v[32:35], v[60:63]\n"
      "s_cselect_b32 s69, s68, 0x40000000\n"
      "ds_read_b128 v[24:27], v119 offset:31744\n"
      "v_mfma_f32_16x16x32_bf16 v[64:67], a[32:35], v[28:31], v[64:67]\n"
      "s_add_u32 s66, s66, 1\n"
      "buffer_store_dwordx2 v[116:117], v118, s[36:39], s71 offen offset:1024 sc1\n"
      "v_mfma_f32_16x16x32_bf16 v[64:67], a[44:47], v[32:35], v[64:67]\n"
      "s_waitcnt lgkmcnt(3)\n"
      "v_mfma_f32_16x16x32_bf16 v[56:59], a[48:51], v[36:39], v[56:59]\n"
      "s_waitcnt lgkmcnt(2)\n"
      "ds_read_b128 v[28:31], v119 offset:32768\n"
      "v_mfma_f32_16x16x32_bf16 v[56:59], a[60:63], v[40:43], v[56:59]\n"
      "s_max_i32 s72, s66, 0\n"
      "v_mfma_f32_16x16x32_bf16 v[60:63], a[52:55], v[36:39], v[60:63]\n"
      "s_add_u32 s48, s81, s88\n"
      "v_writelane_b32 v8, s72, 0\n"
      "v_mfma_f32_16x16x32_bf16 v[60:63], a[64:67], v[40:43], v[60:63]\n"
      "s_sub_u32 s48, 159, s48\n"
      "ds_read_b128 v[32:35], v119 offset:33792\n"
      "v_mfma_f32_16x16x32_bf16 v[64:67], a[56:59], v[36:39], v[64:67]\n"
      "s_mul_i32 s48, s48, 18432\n"
      "buffer_load_dwordx4 v[140:143], v12, s[36:39], s76 offen sc1\n"
      "v_mfma_f32_16x16x32_bf16 v[64:67], a[68:71], v[40:43], v[64:67]\n"
      "s_waitcnt lgkmcnt(3)\n"
      "v_mfma_f32_16x16x32_bf16 v[56:59], a[72:75], v[20:23], v[56:59]\n"
      "s_waitcnt lgkmcnt(2)\n"
      "ds_read_b128 v[36:39], v119 offset:34816\n"
      "v_mfma_f32_16x16x32_bf16 v[56:59], a[84:87], v[24:27], v[56:59]\n"
      "s_sub_u32 s49, s88, 32\n"
      "v_mfma_f32_16x16x32_bf16 v[60:63], a[76:79], v[20:23], v[60:63]\n"
      "s_cmp_lt_u32 s49, 32\n"
      "buffer_load_dwordx2 v[144:145], v11, s[36:39], s76 offen sc1\n"
      "v_mfma_f32_16x16x32_bf16 v[60:63], a[88:91], v[24:27], v[60:63]\n"
      "s_cselect_b32 s78, s48, 0x40000000\n"
      "ds_read_b128 v[40:43], v119 offset:35840\n"
      "v_mfma_f32_16x16x32_bf16 v[64:67], a[80:83], v[20:23], v[64:67]\n"
      "s_add_u32 m0, s73, 24576\n"
      "v_mfma_f32_16x16x32_bf16 v[64:67], a[92:95], v[24:27], v[64:67]\n"
      "s_waitcnt lgkmcnt(3)\n"
      "v_mfma_f32_16x16x32_bf16 v[56:59], a[96:99], v[28:31], v[56:59]\n"
      "s_waitcnt lgkmcnt(2)\n"
      "ds_read_b128 v[20:23], v119 offset:36864\n"
      "v_mfma_f32_16x16x32_bf16 v[56:59], a[108:111], v[32:35], v[56:59]\n"
      "v_mfma_f32_16x16x32_bf16 v[60:63], a[100:103], v[28:31], v[60:63]\n"
      "v_mfma_f32_16x16x32_bf16 v[60:63], a[112:115], v[32:35], v[60:63]\n"
      "ds_read_b128 v[24:27], v119 offset:37888\n"
      "v_mfma_f32_16x16x32_bf16 v[64:67], a[104:107], v[28:31], v[64:67]\n"
      "v_mfma_f32_16x16x32_bf16 v[64:67], a[116:119], v[32:35], v[64:67]\n"
      "s_waitcnt lgkmcnt(3)\n"
      "v_mfma_f32_16x16x32_bf16 v[56:59], a[120:123], v[36:39], v[56:59]\n"
      "s_waitcnt lgkmcnt(2)\n"
      "ds_read_b128 v[28:31], v119 offset:38912\n"
      "v_mfma_f32_16x16x32_bf16 v[56:59], a[132:135], v[40:43], v[56:59]\n"
      "v_mfma_f32_16x16x32_bf16 v[60:63], a[124:127], v[36:39], v[60:63]\n"
      "v_mfma_f32_16x16x32_bf16 v[60:63], a[136:139], v[40:43], v[60:63]\n"
      "ds_read_b128 v[32:35], v119 offset:39936\n"
      "v_mfma_f32_16x16x32_bf16 v[64:67], a[128:131], v[36:39], v[64:67]\n"
      "v_mfma_f32_16x16x32_bf16 v[64:67], a[140:143], v[40:43], v[64:67]\n"
      "s_waitcnt vmcnt(12) lgkmcnt(3)\n"
      "v_cmp_gt_u32 vcc, s67, v6\n"
      "v_mfma_f32_16x16x32_bf16 v[56:59], a[144:147], v[20:23], v[56:59]\n"
      "s_waitcnt lgkmcnt(2)\n"
      "ds_read_b128 v[36:39], v119 offset:40960\n"
      "v_mfma_f32_16x16x32_bf16 v[56:59], a[156:159], v[24:27], v[56:59]\n"
      "s_cbranch_vccnz .Lsl11\n"
      ".Lbk11:\n"
      "buffer_load_dword v6, v5, s[36:39], 0 offen sc1\n"
      "v_mfma_f32_16x16x32_bf16 v[60:63], a[148:151], v[20:23], v[60:63]\n"
      "v_mfma_f32_16x16x32_bf16 v[60:63], a[160:163], v[24:27], v[60:63]\n"
      "ds_read_b128 v[40:43], v119 offset:41984\n"
      "v_mfma_f32_16x16x32_bf16 v[64:67], a[152:155], v[20:23], v[64:67]\n"
      "buffer_load_dwordx4 v2, s[36:39], s69 offen sc1 lds\n"
      "v_mfma_f32_16x16x32_bf16 v[64:67], a[164:167], v[24:27], v[64:67]\n"
      "s_waitcnt lgkmcnt(3)\n"
      "v_mfma_f32_16x16x32_bf16 v[56:59], a[168:171], v[28:31], v[56:59]\n"
      "s_waitcnt lgkmcnt(2)\n"
      "ds_read_b128 v[20:23], v119 offset:43008\n"
      "v_mfma_f32_16x16x32_bf16 v[56:59], a[180:183], v[32:35], v[56:59]\n"
      "buffer_load_dwordx4 v2, s[36:39], s69 offen offset:1024 sc1 lds\n"
      "v_mfma_f32_16x16x32_bf16 v[60:63], a[172:175], v[28:31], v[60:63]\n"
      "v_mfma_f32_16x16x32_bf16 v[60:63], a[184:187], v[32:35], v[60:63]\n"
      "ds_read_b128 v[24:27], v119 offset:44032\n"
      "v_mfma_f32_16x16x32_bf16 v[64:67], a[176:179], v[28:31], v[64:67]\n"
      "buffer_load_dwordx4 v2, s[36:39], s69 offen offset:2048 sc1 lds\n"
      "v_mfma_f32_16x16x32_bf16 v[64:67], a[188:191], v[32:35], v[64:67]\n"
      "s_waitcnt lgkmcnt(3)\n"
      "v_mfma_f32_16x16x32_bf16 v[56:59], a[192:195], v[36:39], v[56:59]\n"
      "s_waitcnt lgkmcnt(2)\n"
      "ds_read_b128 v[28:31], v119 offset:45056\n"
      "v_mfma_f32_16x16x32_bf16 v[56:59], a[204:207], v[40:43], v[56:59]\n"
      "buffer_load_dwordx4 v2, s[36:39], s69 offen offset:3072 sc1 lds\n"
      "v_mfma_f32_16x16x32_bf16 v[60:63], a[196:199], v[36:39], v[60:63]\n"
      "s_add_u32 m0, s73, 28672\n"
      "v_mfma_f32_16x16x32_bf16 v[60:63], a[208:211], v[40:43], v[60:63]\n"
      "s_cmp_lt_i32 s66, 63\n"
      "ds_read_b128 v[32:35], v119 offset:46080\n"
      "v_mfma_f32_16x16x32_bf16 v[64:67], a[200:203], v[36:39], v[64:67]\n"
      "buffer_load_dwordx4 v19, s[36:39], s69 offen sc1 lds\n"
      "v_mfma_f32_16x16x32_bf16 v[64:67], a[212:215], v[40:43], v[64:67]\n"
      "s_waitcnt lgkmcnt(3)\n"
      "v_mfma_f32_16x16x32_bf16 v[56:59], a[216:219], v[20:23], v[56:59]\n"
      "s_waitcnt lgkmcnt(2)\n"
      "ds_read_b128 v[36:39], v119 offset:47104\n"
      "v_mfma_f32_16x16x32_bf16 v[56:59], a[228:231], v[24:27], v[56:59]\n"
      "buffer_load_dwordx4 v19, s[36:39], s69 offen offset:1024 sc1 lds\n"
      "v_mfma_f32_16x16x32_bf16 v[60:63], a[220:223], v[20:23], v[60:63]\n"
      ".Lmd4:\n"
      "s_cbranch_scc0 .Ldn_t5\n"
      "v_mfma_f32_16x16x32_bf16 v[60:63], a[232:235], v[24:27], v[60:63]\n"
      "s_cmp_lt_u32 s66, 64\n"
      "ds_read_b128 v[40:43], v119 offset:48128\n"
      "v_mfma_f32_16x16x32_bf16 v[64:67], a[224:227], v[20:23], v[64:67]\n"
      "s_waitcnt vmcnt(12)\n"
      "s_barrier\n"
      "v_mfma_f32_16x16x32_bf16 v[64:67], a[236:239], v[24:27], v[64:67]\n"
      "s_waitcnt vmcnt(9) lgkmcnt(3)\n"
      "buffer_store_dword v8, v7, s[36:39], 0 offen sc1\n"
      "v_mfma_f32_16x16x32_bf16 v[56:59], a[240:243], v[28:31], v[56:59]\n"
      "s_waitcnt lgkmcnt(2)\n"
      "ds_read_b128 v[20:23], v3 offset:0\n"
      "v_mfma_f32_16x16x32_bf16 v[56:59], a[252:255], v[32:35], v[56:59]\n"
      "s_cselect_b32 s71, s70, 0x40000000\n"
      "v_mfma_f32_16x16x32_bf16 v[60:63], a[244:247], v[28:31], v[60:63]\n"
      "s_add_u32 s70, s70, 110592\n"
      "v_mfma_f32_16x16x32_bf16 v[60:63], v[216:219], v[32:35], v[60:63]\n"
      "ds_read_b128 v[24:27], v3 offset:1024\n"
      "v_mfma_f32_16x16x32_bf16 v[64:67], a[248:251], v[28:31], v[64:67]\n"
      "v_mfma_f32_16x16x32_bf16 v[64:67], v[220:223], v[32:35], v[64:67]\n"
      "s_waitcnt lgkmcnt(3)\n"
      "v_mfma_f32_16x16x32_bf16 v[56:59], v[224:227], v[36:39], v[56:59]\n"
      "s_waitcnt lgkmcnt(2)\n"
      "ds_read_b128 v[28:31], v3 offset:2048\n"
      "v_mfma_f32_16x16x32_bf16 v[56:59], v[236:239], v[40:43], v[56:59]\n"
      "v_mfma_f32_16x16x32_bf16 v[60:63], v[228:231], v[36:39], v[60:63]\n"
      "v_mfma_f32_16x16x32_bf16 v[60:63], v[240:243], v[40:43], v[60:63]\n"
      "ds_read_b128 v[32:35], v3 offset:3072\n"
      "v_mfma_f32_16x16x32_bf16 v[64:67], v[232:235], v[36:39], v[64:67]\n"
      "buffer_load_dword v10, v9, s[36:39], 0 offen sc1\n"
      "v_mfma_f32_16x16x32_bf16 v[64:67], v[244:247], v[40:43], v[64:67]\n"
      "s_waitcnt lgkmcnt(3)\n"
      ".Ldn_e5:\n"
      "v_mfma_f32_16x16x32_bf16 v[44:47], a[0:3], v[20:23], 0\n"
      "s_waitcnt lgkmcnt(2)\n"
      "ds_read_b128 v[36:39], v3 offset:4096\n"
      "v_cvt_pk_bf16_f32 v112, v56, v57\n"
      "v_mfma_f32_16x16x32_bf16 v[44:47], a[12:15], v[24:27], v[44:47]\n"
      "s_add_u32 s67, s66, 4\n"
      "v_cvt_pk_bf16_f32 v113, v58, v59\n"
      "v_mfma_f32_16x16x32_bf16 v[48:51], a[4:7], v[20:23], 0\n"
      "s_min_i32 s67, s67, 64\n"
      "v_cvt_pk_bf16_f32 v114, v60, v61\n"
      "v_cvt_pk_bf16_f32 v115, v62, v63\n"
      "v_mfma_f32_16x16x32_bf16 v[48:51], a[16:19], v[24:27], v[48:51]\n"
      "s_max_i32 s67, s67, 0\n"
      "ds_read_b128 v[40:43], v3 offset:5120\n"
      "v_mfma_f32_16x16x32_bf16 v[52:55], a[8:11], v[20:23], 0\n"
      "s_add_u32 s68, s68, 147456\n"
      "buffer_store_dwordx4 v[112:115], v18, s[36:39], s71 offen sc1\n"
      "v_mfma_f32_16x16x32_bf16 v[52:55], a[20:23], v[24:27], v[52:55]\n"
      "s_waitcnt lgkmcnt(3)\n"
      "v_cvt_pk_bf16_f32 v116, v64, v65\n"
      "v_cvt_pk_bf16_f32 v117, v66, v67\n"
      "v_mfma_f32_16x16x32_bf16 v[44:47], a[24:27], v[28:31], v[44:47]\n"
      "s_waitcnt vmcnt(10) lgkmcnt(2)\n"
      "ds_read_b128 v[20:23], v3 offset:6144\n"
      "v_lshlrev_b32 v168, 16, v140\n"
      "v_mfma_f32_16x16x32_bf16 v[44:47], a[36:39], v[32:35], v[44:47]\n"
      "s_add_u32 s48, s66, 3\n"
      "v_add_f32 v168, v68, v168\n"
      "v_and_b32 v169, 0xffff0000, v140\n"
      "v_mfma_f32_16x16x32_bf16 v[48:51], a[28:31], v[28:31], v[48:51]\n"
      "s_cmp_lt_u32 s48, 64\n"
      "v_add_f32 v169, v69, v169\n"
      "v_lshlrev_b32 v170, 16, v141\n"
      "v_mfma_f32_16x16x32_bf16 v[48:51], a[40:43], v[32:35], v[48:51]\n"
      "s_cselect_b32 s69, s68, 0x40000000\n"
      "ds_read_b128 v[24:27], v3 offset:7168\n"
      "v_add_f32 v170, v70, v170\n"
      "v_mfma_f32_16x16x32_bf16 v[52:55], a[32:35], v[28:31], v[52:55]\n"
      "s_add_u32 s66, s66, 1\n"
      "buffer_store_dwordx2 v[116:117], v118, s[36:39], s71 offen offset:1024 sc1\n"
      "v_mfma_f32_16x16x32_bf16 v[52:55], a[44:47], v[32:35], v[52:55]\n"
      "s_waitcnt lgkmcnt(3)\n"
      "v_and_b32 v171, 0xffff0000, v141\n"
      "v_lshlrev_b32 v172, 16, v142\n"
      "v_mfma_f32_16x16x32_bf16 v[44:47], a[48:51], v[36:39], v[44:47]\n"
      "s_waitcnt lgkmcnt(2)\n"
      "ds_read_b128 v[28:31], v3 offset:8192\n"
      "v_add_f32 v171, v71, v171\n"
      "v_mfma_f32_16x16x32_bf16 v[44:47], a[60:63], v[40:43], v[44:47]\n"
      "s_max_i32 s72, s66, 0\n"
      "v_add_f32 v172, v72, v172\n"
      "v_and_b32 v173, 0xffff0000, v142\n"
      "v_mfma_f32_16x16x32_bf16 v[48:51], a[52:55], v[36:39], v[48:51]\n"
      "s_add_u32 s48, s81, s88\n"
      "v_writelane_b32 v8, s72, 0\n"
      "v_add_f32 v173, v73, v173\n"
      "v_mfma_f32_16x16x32_bf16 v[48:51], a[64:67], v[40:43], v[48:51]\n"
      "s_mul_i32 s49, s48, 20480\n"
      "ds_read_b128 v[32:35], v3 offset:9216\n"
      "v_lshlrev_b32 v174, 16, v143\n"
      "v_mfma_f32_16x16x32_bf16 v[52:55], a[56:59], v[36:39], v[52:55]\n"
      "s_add_u32 s49, s49, 2641920\n"
      "v_add_f32 v174, v74, v174\n"
      "v_and_b32 v175, 0xffff0000, v143\n"
      "v_mfma_f32_16x16x32_bf16 v[52:55], a[68:71], v[40:43], v[52:55]\n"
      "s_waitcnt lgkmcnt(3)\n"
      "v_add_f32 v175, v75, v175\n"
      "v_lshlrev_b32 v176, 16, v144\n"
      "v_mfma_f32_16x16x32_bf16 v[44:47], a[72:75], v[20:23], v[44:47]\n"
      "s_waitcnt lgkmcnt(2)\n"
      "ds_read_b128 v[36:39], v3 offset:10240\n"
      "v_add_f32 v176, v76, v176\n"
      "v_mfma_f32_16x16x32_bf16 v[44:47], a[84:87], v[24:27], v[44:47]\n"
      "s_mul_i32 s50, s48, 1536\n"
      "v_and_b32 v177, 0xffff0000, v144\n"
      "v_lshlrev_b32 v178, 16, v145\n"
      "v_mfma_f32_16x16x32_bf16 v[48:51], a[76:79], v[20:23], v[48:51]\n"
      "s_add_u32 s50, s50, s65\n"
      "v_add_f32 v177, v77, v177\n"
      "v_add_f32 v178, v78, v178\n"
      "v_mfma_f32_16x16x32_bf16 v[48:51], a[88:91], v[24:27], v[48:51]\n"
      "s_cmp_lt_u32 s88, 32\n"
      "ds_read_b128 v[40:43], v3 offset:11264\n"
      "v_and_b32 v179, 0xffff0000, v145\n"
      "v_mfma_f32_16x16x32_bf16 v[52:55], a[80:83], v[20:23], v[52:55]\n"
      "s_cselect_b32 s77, s49, 0x40000000\n"
      "v_add_f32 v179, v79, v179\n"
      "v_mfma_f32_16x16x32_bf16 v[52:55], a[92:95], v[24:27], v[52:55]\n"
      "s_waitcnt lgkmcnt(3)\n"
      "v_mfma_f32_16x16x32_bf16 v[44:47], a[96:99], v[28:31], v[44:47]\n"
      "s_waitcnt lgkmcnt(2)\n"
      "ds_read_b128 v[20:23], v3 offset:12288\n"
      "v_mfma_f32_16x16x32_bf16 v[44:47], a[108:111], v[32:35], v[44:47]\n"
      "s_cselect_b32 s79, s50, 0x40000000\n"
      "v_mfma_f32_16x16x32_bf16 v[48:51], a[100:103], v[28:31], v[48:51]\n"
      "s_add_u32 m0, s73, 49152\n"
      "v_mfma_f32_16x16x32_bf16 v[48:51], a[112:115], v[32:35], v[48:51]\n"
      "ds_read_b128 v[24:27], v3 offset:13312\n"
      "v_mfma_f32_16x16x32_bf16 v[52:55], a[104:107], v[28:31], v[52:55]\n"
      "v_mfma_f32_16x16x32_bf16 v[52:55], a[116:119], v[32:35], v[52:55]\n"
      "s_waitcnt lgkmcnt(3)\n"
      "v_mfma_f32_16x16x32_bf16 v[44:47], a[120:123], v[36:39], v[44:47]\n"
      "s_waitcnt lgkmcnt(2)\n"
      "ds_read_b128 v[28:31], v3 offset:14336\n"
      "v_mfma_f32_16x16x32_bf16 v[44:47], a[132:135], v[40:43], v[44:47]\n"
      "v_mfma_f32_16x16x32_bf16 v[48:51], a[124:127], v[36:39], v[48:51]\n"
      "v_mfma_f32_16x16x32_bf16 v[48:51], a[136:139], v[40:43], v[48:51]\n"
      "ds_read_b128 v[32:35], v3 offset:15360\n"
      "v_mfma_f32_16x16x32_bf16 v[52:55], a[128:131], v[36:39], v[52:55]\n"
      "v_mfma_f32_16x16x32_bf16 v[52:55], a[140:143], v[40:43], v[52:55]\n"
      "s_waitcnt vmcnt(10) lgkmcnt(3)\n"
      "v_cmp_gt_u32 vcc, s67, v6\n"
      "v_mfma_f32_16x16x32_bf16 v[44:47], a[144:147], v[20:23], v[44:47]\n"
      "s_waitcnt lgkmcnt(2)\n"
      "ds_read_b128 v[36:39], v3 offset:16384\n"
      "v_mfma_f32_16x16x32_bf16 v[44:47], a[156:159], v[24:27], v[44:47]\n"
      "s_cbranch_vccnz .Lsl12\n"
      ".Lbk12:\n"
      "buffer_load_dword v6, v5, s[36:39], 0 offen sc1\n"
      "v_mfma_f32_16x16x32_bf16 v[48:51], a[148:151], v[20:23], v[48:51]\n"
      "v_mfma_f32_16x16x32_bf16 v[48:51], a[160:163], v[24:27], v[48:51]\n"
      "ds_read_b128 v[40:43], v3 offset:17408\n"
      "v_mfma_f32_16x16x32_bf16 v[52:55], a[152:155], v[20:23], v[52:55]\n"
      "buffer_load_dwordx4 v2, s[36:39], s69 offen sc1 lds\n"
      "v_mfma_f32_16x16x32_bf16 v[52:55], a[164:167], v[24:27], v[52:55]\n"
      "s_waitcnt lgkmcnt(3)\n"
      "v_mfma_f32_16x16x32_bf16 v[44:47], a[168:171], v[28:31], v[44:47]\n"
      "s_waitcnt lgkmcnt(2)\n"
      "ds_read_b128 v[20:23], v3 offset:18432\n"
      "v_mfma_f32_16x16x32_bf16 v[44:47], a[180:183], v[32:35], v[44:47]\n"
      "buffer_load_dwordx4 v2, s[36:39], s69 offen offset:1024 sc1 lds\n"
      "v_mfma_f32_16x16x32_bf16 v[48:51], a[172:175], v[28:31], v[48:51]\n"
      "v_mfma_f32_16x16x32_bf16 v[48:51], a[184:187], v[32:35], v[48:51]\n"
      "ds_read_b128 v[24:27], v3 offset:19456\n"
      "v_mfma_f32_16x16x32_bf16 v[52:55], a[176:179], v[28:31], v[52:55]\n"
      "buffer_load_dwordx4 v2, s[36:39], s69 offen offset:2048 sc1 lds\n"
      "v_mfma_f32_16x16x32_bf16 v[52:55], a[188:191], v[32:35], v[52:55]\n"
      "s_waitcnt lgkmcnt(3)\n"
      "v_mfma_f32_16x16x32_bf16 v[44:47], a[192:195], v[36:39], v[44:47]\n"
      "s_waitcnt lgkmcnt(2)\n"
      "ds_read_b128 v[28:31], v3 offset:20480\n"
      "v_mfma_f32_16x16x32_bf16 v[44:47], a[204:207], v[40:43], v[44:47]\n"
      "buffer_load_dwordx4 v2, s[36:39], s69 offen offset:3072 sc1 lds\n"
      "v_mfma_f32_16x16x32_bf16 v[48:51], a[196:199], v[36:39], v[48:51]\n"
      "s_add_u32 m0, s73, 53248\n"
      "v_mfma_f32_16x16x32_bf16 v[48:51], a[208:211], v[40:43], v[48:51]\n"
      "ds_read_b128 v[32:35], v3 offset:21504\n"
      "v_mfma_f32_16x16x32_bf16 v[52:55], a[200:203], v[36:39], v[52:55]\n"
      "s_waitcnt vmcnt(7)\n"
      "v_cmp_gt_u32 vcc, s74, v10\n"
      "v_mfma_f32_16x16x32_bf16 v[52:55], a[212:215], v[40:43], v[52:55]\n"
      "s_waitcnt lgkmcnt(3)\n"
      "s_cbranch_vccnz .Lsl13\n"
      ".Lbk13:\n"
      "buffer_load_dwordx4 v19, s[36:39], s69 offen sc1 lds\n"
      "v_mfma_f32_16x16x32_bf16 v[44:47], a[216:219], v[20:23], v[44:47]\n"
      "s_waitcnt lgkmcnt(2)\n"
      "s_cmp_lt_i32 s66, 63\n"
      "ds_read_b128 v[36:39], v3 offset:22528\n"
      "v_mfma_f32_16x16x32_bf16 v[44:47], a[228:231], v[24:27], v[44:47]\n"
      "v_mfma_f32_16x16x32_bf16 v[48:51], a[220:223], v[20:23], v[48:51]\n"
      "buffer_load_dwordx4 v19, s[36:39], s69 offen offset:1024 sc1 lds\n"
      ".Lmd5:\n"
      "s_cbranch_scc1 .Ldn_loop\n"
      ".Ldn_t0:\n"
      "s_waitcnt lgkmcnt(0)\n"
      "v_mfma_f32_16x16x32_bf16 v[48:51], a[232:235], v[24:27], v[48:51]\n"
      "ds_read_b128 v[40:43], v3 offset:23552\n"
      "v_mfma_f32_16x16x32_bf16 v[52:55], a[224:227], v[20:23], v[52:55]\n"
      "s_add_u32 s48, s88, 1\n"
      "v_mfma_f32_16x16x32_bf16 v[52:55], a[236:239], v[24:27], v[52:55]\n"
      "s_waitcnt vmcnt(0)\n"
      "buffer_store_dword v8, v7, s[36:39], 0 offen sc1\n"
      "v_mfma_f32_16x16x32_bf16 v[44:47], a[240:243], v[28:31], v[44:47]\n"
      "v_mfma_f32_16x16x32_bf16 v[44:47], a[252:255], v[32:35], v[44:47]\n"
      "s_cmp_lt_u32 s88, 64\n"
      "v_writelane_b32 v8, 64, 0\n"
      "v_mfma_f32_16x16x32_bf16 v[48:51], a[244:247], v[28:31], v[48:51]\n"
      "s_cselect_b32 s74, s48, 0\n"
      "v_mfma_f32_16x16x32_bf16 v[48:51], v[216:219], v[32:35], v[48:51]\n"
      "s_mul_i32 s48, s29, 65\n"
      "v_mfma_f32_16x16x32_bf16 v[52:55], a[248:251], v[28:31], v[52:55]\n"
      "s_add_u32 s48, s48, s88\n"
      "v_mfma_f32_16x16x32_bf16 v[52:55], v[220:223], v[32:35], v[52:55]\n"
      "v_mfma_f32_16x16x32_bf16 v[44:47], v[224:227], v[36:39], v[44:47]\n"
      "s_waitcnt lgkmcnt(0)\n"
      "v_mfma_f32_16x16x32_bf16 v[44:47], v[236:239], v[40:43], v[44:47]\n"
      "s_mul_i32 s48, s48, 20480\n"
      "v_mfma_f32_16x16x32_bf16 v[48:51], v[228:231], v[36:39], v[48:51]\n"
      "s_add_u32 s48, s48, 10526720\n"
      "v_mfma_f32_16x16x32_bf16 v[48:51], v[240:243], v[40:43], v[48:51]\n"
      "s_cmp_lt_u32 s88, 64\n"
      "v_mfma_f32_16x16x32_bf16 v[52:55], v[232:235], v[36:39], v[52:55]\n"
      "s_cselect_b32 s76, s48, 0x40000000\n"
      "v_mfma_f32_16x16x32_bf16 v[52:55], v[244:247], v[40:43], v[52:55]\n"
      "s_add_u32 s48, s81, s88\n"
      "buffer_load_dwordx4 v[140:143], v12, s[36:39], s76 offen sc1\n"
      "s_sub_u32 s48, 159, s48\n"
      "v_cvt_pk_bf16_f32 v112, v44, v45\n"
      "v_cvt_pk_bf16_f32 v113, v46, v47\n"
      "s_mul_i32 s48, s48, 18432\n"
      "buffer_load_dwordx2 v[144:145], v11, s[36:39], s76 offen sc1\n"
      "s_sub_u32 s49, s88, 32\n"
      "v_cvt_pk_bf16_f32 v114, v48, v49\n"
      "v_cvt_pk_bf16_f32 v115, v50, v51\n"
      "s_cmp_lt_u32 s49, 32\n"
      "buffer_store_dwordx4 v[112:115], v18, s[36:39], s70 offen sc1\n"
      "s_cselect_b32 s78, s48, 0x40000000\n"
      "v_cvt_pk_bf16_f32 v116, v52, v53\n"
      "v_cvt_pk_bf16_f32 v117, v54, v55\n"
      "buffer_store_dwordx2 v[116:117], v118, s[36:39], s70 offen offset:1024 sc1\n"
      "s_branch .Ldn_r\n"
      ".Ldn_t1:\n"
      "s_waitcnt lgkmcnt(0)\n"
      "v_mfma_f32_16x16x32_bf16 v[60:63], a[232:235], v[24:27], v[60:63]\n"
      "ds_read_b128 v[40:43], v3 offset:48128\n"
      "v_mfma_f32_16x16x32_bf16 v[64:67], a[224:227], v[20:23], v[64:67]\n"
      "s_add_u32 s48, s88, 1\n"
      "v_mfma_f32_16x16x32_bf16 v[64:67], a[236:239], v[24:27], v[64:67]\n"
      "s_waitcnt vmcnt(0)\n"
      "buffer_store_dword v8, v7, s[36:39], 0 offen sc1\n"
      "v_mfma_f32_16x16x32_bf16 v[56:59], a[240:243], v[28:31], v[56:59]\n"
      "v_mfma_f32_16x16x32_bf16 v[56:59], a[252:255], v[32:35], v[56:59]\n"
      "s_cmp_lt_u32 s88, 64\n"
      "v_writelane_b32 v8, 64, 0\n"
      "v_mfma_f32_16x16x32_bf16 v[60:63], a[244:247], v[28:31], v[60:63]\n"
      "s_cselect_b32 s74, s48, 0\n"
      "v_mfma_f32_16x16x32_bf16 v[60:63], v[216:219], v[32:35], v[60:63]\n"
      "s_mul_i32 s48, s29, 65\n"
      "v_mfma_f32_16x16x32_bf16 v[64:67], a[248:251], v[28:31], v[64:67]\n"
      "s_add_u32 s48, s48, s88\n"
      "v_mfma_f32_16x16x32_bf16 v[64:67], v[220:223], v[32:35], v[64:67]\n"
      "v_mfma_f32_16x16x32_bf16 v[56:59], v[224:227], v[36:39], v[56:59]\n"
      "s_waitcnt lgkmcnt(0)\n"
      "v_mfma_f32_16x16x32_bf16 v[56:59], v[236:239], v[40:43], v[56:59]\n"
      "s_mul_i32 s48, s48, 20480\n"
      "v_mfma_f32_16x16x32_bf16 v[60:63], v[228:231], v[36:39], v[60:63]\n"
      "s_add_u32 s48, s48, 10526720\n"
      "v_mfma_f32_16x16x32_bf16 v[60:63], v[240:243], v[40:43], v[60:63]\n"
      "s_cmp_lt_u32 s88, 64\n"
      "v_mfma_f32_16x16x32_bf16 v[64:67], v[232:235], v[36:39], v[64:67]\n"
      "s_cselect_b32 s76, s48, 0x40000000\n"
      "v_mfma_f32_16x16x32_bf16 v[64:67], v[244:247], v[40:43], v[64:67]\n"
      "s_add_u32 s48, s81, s88\n"
      "buffer_load_dwordx4 v[140:143], v12, s[36:39], s76 offen sc1\n"
      "s_sub_u32 s48, 159, s48\n"
      "v_cvt_pk_bf16_f32 v112, v56, v57\n"
      "v_cvt_pk_bf16_f32 v113, v58, v59\n"
      "s_mul_i32 s48, s48, 18432\n"
      "buffer_load_dwordx2 v[144:145], v11, s[36:39], s76 offen sc1\n"
      "s_sub_u32 s49, s88, 32\n"
      "v_cvt_pk_bf16_f32 v114, v60, v61\n"
      "v_cvt_pk_bf16_f32 v115, v62, v63\n"
      "s_cmp_lt_u32 s49, 32\n"
      "buffer_store_dwordx4 v[112:115], v18, s[36:39], s70 offen sc1\n"
      "s_cselect_b32 s78, s48, 0x40000000\n"
      "v_cvt_pk_bf16_f32 v116, v64, v65\n"
      "v_cvt_pk_bf16_f32 v117, v66, v67\n"
      "buffer_store_dwordx2 v[116:117], v118, s[36:39], s70 offen offset:1024 sc1\n"
      "s_branch .Ldn_r\n"
      ".Ldn_t2:\n"
      "s_waitcnt lgkmcnt(0)\n"
      "v_mfma_f32_16x16x32_bf16 v[48:51], a[232:235], v[24:27], v[48:51]\n"
      "ds_read_b128 v[40:43], v4 offset:23552\n"
      "v_mfma_f32_16x16x32_bf16 v[52:55], a[224:227], v[20:23], v[52:55]\n"
      "s_add_u32 s48, s88, 1\n"
      "v_mfma_f32_16x16x32_bf16 v[52:55], a[236:239], v[24:27], v[52:55]\n"
      "s_waitcnt vmcnt(0)\n"
      "buffer_store_dword v8, v7, s[36:39], 0 offen sc1\n"
      "v_mfma_f32_16x16x32_bf16 v[44:47], a[240:243], v[28:31], v[44:47]\n"
      "v_mfma_f32_16x16x32_bf16 v[44:47], a[252:255], v[32:35], v[44:47]\n"
      "s_cmp_lt_u32 s88, 64\n"
      "v_writelane_b32 v8, 64, 0\n"
      "v_mfma_f32_16x16x32_bf16 v[48:51], a[244:247], v[28:31], v[48:51]\n"
      "s_cselect_b32 s74, s48, 0\n"
      "v_mfma_f32_16x16x32_bf16 v[48:51], v[216:219], v[32:35], v[48:51]\n"
      "s_mul_i32 s48, s29, 65\n"
      "v_mfma_f32_16x16x32_bf16 v[52:55], a[248:251], v[28:31], v[52:55]\n"
      "s_add_u32 s48, s48, s88\n"
      "v_mfma_f32_16x16x32_bf16 v[52:55], v[220:223], v[32:35], v[52:55]\n"
      "v_mfma_f32_16x16x32_bf16 v[44:47], v[224:227], v[36:39], v[44:47]\n"
      "s_waitcnt lgkmcnt(0)\n"
      "v_mfma_f32_16x16x32_bf16 v[44:47], v[236:239], v[40:43], v[44:47]\n"
      "s_mul_i32 s48, s48, 20480\n"
      "v_mfma_f32_16x16x32_bf16 v[48:51], v[228:231], v[36:39], v[48:51]\n"
      "s_add_u32 s48, s48, 10526720\n"
      "v_mfma_f32_16x16x32_bf16 v[48:51], v[240:243], v[40:43], v[48:51]\n"
      "s_cmp_lt_u32 s88, 64\n"
      "v_mfma_f32_16x16x32_bf16 v[52:55], v[232:235], v[36:39], v[52:55]\n"
      "s_cselect_b32 s76, s48, 0x40000000\n"
      "v_mfma_f32_16x16x32_bf16 v[52:55], v[244:247], v[40:43], v[52:55]\n"
      "s_add_u32 s48, s81, s88\n"
      "buffer_load_dwordx4 v[140:143], v12, s[36:39], s76 offen sc1\n"
      "s_sub_u32 s48, 159, s48\n"
      "v_cvt_pk_bf16_f32 v112, v44, v45\n"
      "v_cvt_pk_bf16_f32 v113, v46, v47\n"
      "s_mul_i32 s48, s48, 18432\n"
      "buffer_load_dwordx2 v[144:145], v11, s[36:39], s76 offen sc1\n"
      "s_sub_u32 s49, s88, 32\n"
      "v_cvt_pk_bf16_f32 v114, v48, v49\n"
      "v_cvt_pk_bf16_f32 v115, v50, v51\n"
      "s_cmp_lt_u32 s49, 32\n"
      "buffer_store_dwordx4 v[112:115], v18, s[36:39], s70 offen sc1\n"
      "s_cselect_b32 s78, s48, 0x40000000\n"
      "v_cvt_pk_bf16_f32 v116, v52, v53\n"
      "v_cvt_pk_bf16_f32 v117, v54, v55\n"
      "buffer_store_dwordx2 v[116:117], v118, s[36:39], s70 offen offset:1024 sc1\n"
      "s_branch .Ldn_r\n"
      ".Ldn_t3:\n"
      "s_waitcnt lgkmcnt(0)\n"
      "v_mfma_f32_16x16x32_bf16 v[72:75], a[232:235], v[24:27], v[72:75]\n"
      "buffer_store_dwordx4 v[146:149], v13, s[40:43], s78 offen\n"
      "ds_read_b128 v[40:43], v4 offset:48128\n"
      "v_mfma_f32_16x16x32_bf16 v[76:79], a[224:227], v[20:23], v[76:79]\n"
      "s_mov_b32 s75, s70\n"
      "v_mfma_f32_16x16x32_bf16 v[76:79], a[236:239], v[24:27], v[76:79]\n"
      "s_waitcnt vmcnt(0)\n"
      "buffer_store_dword v8, v7, s[36:39], 0 offen sc1\n"
      "v_mfma_f32_16x16x32_bf16 v[68:71], a[240:243], v[28:31], v[68:71]\n"
      "v_mfma_f32_16x16x32_bf16 v[68:71], a[252:255], v[32:35], v[68:71]\n"
      "s_mov_b32 s88, 63\n"
      "buffer_store_dwordx2 v[150:151], v14, s[40:43], s78 offen\n"
      "v_writelane_b32 v8, 64, 0\n"
      "v_mfma_f32_16x16x32_bf16 v[72:75], a[244:247], v[28:31], v[72:75]\n"
      "s_add_u32 s48, s88, 1\n"
      "v_mfma_f32_16x16x32_bf16 v[72:75], v[216:219], v[32:35], v[72:75]\n"
      "s_cmp_lt_u32 s88, 64\n"
      "v_mfma_f32_16x16x32_bf16 v[76:79], a[248:251], v[28:31], v[76:79]\n"
      "s_cselect_b32 s74, s48, 0\n"
      "v_mfma_f32_16x16x32_bf16 v[76:79], v[220:223], v[32:35], v[76:79]\n"
      "v_mfma_f32_16x16x32_bf16 v[68:71], v[224:227], v[36:39], v[68:71]\n"
      "s_waitcnt lgkmcnt(0)\n"
      "v_mfma_f32_16x16x32_bf16 v[68:71], v[236:239], v[40:43], v[68:71]\n"
      "s_mul_i32 s48, s29, 65\n"
      "v_mfma_f32_16x16x32_bf16 v[72:75], v[228:231], v[36:39], v[72:75]\n"
      "s_add_u32 s48, s48, s88\n"
      "v_mfma_f32_16x16x32_bf16 v[72:75], v[240:243], v[40:43], v[72:75]\n"
      "s_mul_i32 s48, s48, 20480\n"
      "v_mfma_f32_16x16x32_bf16 v[76:79], v[232:235], v[36:39], v[76:79]\n"
      "s_add_u32 s48, s48, 10526720\n"
      "v_mfma_f32_16x16x32_bf16 v[76:79], v[244:247], v[40:43], v[76:79]\n"
      "s_cmp_lt_u32 s88, 64\n"
      "s_cselect_b32 s76, s48, 0x40000000\n"
      "s_add_u32 s48, s81, s88\n"
      "buffer_load_dwordx4 v[140:143], v12, s[36:39], s76 offen sc1\n"
      "s_sub_u32 s48, 159, s48\n"
      "s_mul_i32 s48, s48, 18432\n"
      "buffer_load_dwordx2 v[144:145], v11, s[36:39], s76 offen sc1\n"
      "s_sub_u32 s49, s88, 32\n"
      "s_cmp_lt_u32 s49, 32\n"
      "s_cselect_b32 s78, s48, 0x40000000\n"
      "s_branch .Ldn_r\n"
      ".Ldn_t4:\n"
      "s_waitcnt lgkmcnt(0)\n"
      "v_mfma_f32_16x16x32_bf16 v[48:51], a[232:235], v[24:27], v[48:51]\n"
      "ds_read_b128 v[40:43], v119 offset:23552\n"
      "v_mfma_f32_16x16x32_bf16 v[52:55], a[224:227], v[20:23], v[52:55]\n"
      "s_add_u32 s48, s88, 1\n"
      "v_mfma_f32_16x16x32_bf16 v[52:55], a[236:239], v[24:27], v[52:55]\n"
      "s_waitcnt vmcnt(0)\n"
      "buffer_store_dword v8, v7, s[36:39], 0 offen sc1\n"
      "v_mfma_f32_16x16x32_bf16 v[44:47], a[240:243], v[28:31], v[44:47]\n"
      "v_mfma_f32_16x16x32_bf16 v[44:47], a[252:255], v[32:35], v[44:47]\n"
      "s_cmp_lt_u32 s88, 64\n"
      "v_writelane_b32 v8, 64, 0\n"
      "v_mfma_f32_16x16x32_bf16 v[48:51], a[244:247], v[28:31], v[48:51]\n"
      "s_cselect_b32 s74, s48, 0\n"
      "v_mfma_f32_16x16x32_bf16 v[48:51], v[216:219], v[32:35], v[48:51]\n"
      "s_mul_i32 s48, s29, 65\n"
      "v_mfma_f32_16x16x32_bf16 v[52:55], a[248:251], v[28:31], v[52:55]\n"
      "s_add_u32 s48, s48, s88\n"
      "v_mfma_f32_16x16x32_bf16 v[52:55], v[220:223], v[32:35], v[52:55]\n"
      "v_mfma_f32_16x16x32_bf16 v[44:47], v[224:227], v[36:39], v[44:47]\n"
      "s_waitcnt lgkmcnt(0)\n"
      "v_mfma_f32_16x16x32_bf16 v[44:47], v[236:239], v[40:43], v[44:47]\n"
      "s_mul_i32 s48, s48, 20480\n"
      "v_mfma_f32_16x16x32_bf16 v[48:51], v[228:231], v[36:39], v[48:51]\n"
      "s_add_u32 s48, s48, 10526720\n"
      "v_mfma_f32_16x16x32_bf16 v[48:51], v[240:243], v[40:43], v[48:51]\n"
      "s_cmp_lt_u32 s88, 64\n"
      "v_mfma_f32_16x16x32_bf16 v[52:55], v[232:235], v[36:39], v[52:55]\n"
      "s_cselect_b32 s76, s48, 0x40000000\n"
      "v_mfma_f32_16x16x32_bf16 v[52:55], v[244:247], v[40:43], v[52:55]\n"
      "s_add_u32 s48, s81, s88\n"
      "buffer_load_dwordx4 v[140:143], v12, s[36:39], s76 offen sc1\n"
      "s_sub_u32 s48, 159, s48\n"
      "v_cvt_pk_bf16_f32 v112, v44, v45\n"
      "v_cvt_pk_bf16_f32 v113, v46, v47\n"
      "s_mul_i32 s48, s48, 18432\n"
      "buffer_load_dwordx2 v[144:145], v11, s[36:39], s76 offen sc1\n"
      "s_sub_u32 s49, s88, 32\n"
      "v_cvt_pk_bf16_f32 v114, v48, v49\n"
      "v_cvt_pk_bf16_f32 v115, v50, v51\n"
      "s_cmp_lt_u32 s49, 32\n"
      "buffer_store_dwordx4 v[112:115], v18, s[36:39], s70 offen sc1\n"
      "s_cselect_b32 s78, s48, 0x40000000\n"
      "v_cvt_pk_bf16_f32 v116, v52, v53\n"
      "v_cvt_pk_bf16_f32 v117, v54, v55\n"
      "buffer_store_dwordx2 v[116:117], v118, s[36:39], s70 offen offset:1024 sc1\n"
      "s_branch .Ldn_r\n"
      ".Ldn_t5:\n"
      "s_waitcnt lgkmcnt(0)\n"
      "v_mfma_f32_16x16x32_bf16 v[60:63], a[232:235], v[24:27], v[60:63]\n"
      "ds_read_b128 v[40:43], v119 offset:48128\n"
      "v_mfma_f32_16x16x32_bf16 v[64:67], a[224:227], v[20:23], v[64:67]\n"
      "s_add_u32 s48, s88, 1\n"
      "v_mfma_f32_16x16x32_bf16 v[64:67], a[236:239], v[24:27], v[64:67]\n"
      "s_waitcnt vmcnt(0)\n"
      "buffer_store_dword v8, v7, s[36:39], 0 offen sc1\n"
      "v_mfma_f32_16x16x32_bf16 v[56:59], a[240:243], v[28:31], v[56:59]\n"
      "v_mfma_f32_16x16x32_bf16 v[56:59], a[252:255], v[32:35], v[56:59]\n"
      "s_cmp_lt_u32 s88, 64\n"
      "v_writelane_b32 v8, 64, 0\n"
      "v_mfma_f32_16x16x32_bf16 v[60:63], a[244:247], v[28:31], v[60:63]\n"
      "s_cselect_b32 s74, s48, 0\n"
      "v_mfma_f32_16x16x32_bf16 v[60:63], v[216:219], v[32:35], v[60:63]\n"
      "s_mul_i32 s48, s29, 65\n"
      "v_mfma_f32_16x16x32_bf16 v[64:67], a[248:251], v[28:31], v[64:67]\n"
      "s_add_u32 s48, s48, s88\n"
      "v_mfma_f32_16x16x32_bf16 v[64:67], v[220:223], v[32:35], v[64:67]\n"
      "v_mfma_f32_16x16x32_bf16 v[56:59], v[224:227], v[36:39], v[56:59]\n"
      "s_waitcnt lgkmcnt(0)\n"
      "v_mfma_f32_16x16x32_bf16 v[56:59], v[236:239], v[40:43], v[56:59]\n"
      "s_mul_i32 s48, s48, 20480\n"
      "v_mfma_f32_16x16x32_bf16 v[60:63], v[228:231], v[36:39], v[60:63]\n"
      "s_add_u32 s48, s48, 10526720\n"
      "v_mfma_f32_16x16x32_bf16 v[60:63], v[240:243], v[40:43], v[60:63]\n"
      "s_cmp_lt_u32 s88, 64\n"
      "v_mfma_f32_16x16x32_bf16 v[64:67], v[232:235], v[36:39], v[64:67]\n"
      "s_cselect_b32 s76, s48, 0x40000000\n"
      "v_mfma_f32_16x16x32_bf16 v[64:67], v[244:247], v[40:43], v[64:67]\n"
      "s_add_u32 s48, s81, s88\n"
      "buffer_load_dwordx4 v[140:143], v12, s[36:39], s76 offen sc1\n"
      "s_sub_u32 s48, 159, s48\n"
      "v_cvt_pk_bf16_f32 v112, v56, v57\n"
      "v_cvt_pk_bf16_f32 v113, v58, v59\n"
      "s_mul_i32 s48, s48, 18432\n"
      "buffer_load_dwordx2 v[144:145], v11, s[36:39], s76 offen sc1\n"
      "s_sub_u32 s49, s88, 32\n"
      "v_cvt_pk_bf16_f32 v114, v60, v61\n"
      "v_cvt_pk_bf16_f32 v115, v62, v63\n"
      "s_cmp_lt_u32 s49, 32\n"
      "buffer_store_dwordx4 v[112:115], v18, s[36:39], s70 offen sc1\n"
      "s_cselect_b32 s78, s48, 0x40000000\n"
      "v_cvt_pk_bf16_f32 v116, v64, v65\n"
      "v_cvt_pk_bf16_f32 v117, v66, v67\n"
      "buffer_store_dwordx2 v[116:117], v118, s[36:39], s70 offen offset:1024 sc1\n"
      ".Ldn_r:\n"
      "s_waitcnt vmcnt(0) lgkmcnt(0)\n"
      "buffer_store_dword v8, v7, s[36:39], 0 offen sc1\n"
      "v_lshlrev_b32 v168, 16, v140\n"
      "v_and_b32 v169, 0xffff0000, v140\n"
      "v_lshlrev_b32 v170, 16, v141\n"
      "v_and_b32 v171, 0xffff0000, v141\n"
      "v_add_f32 v168, v68, v168\n"
      "v_add_f32 v169, v69, v169\n"
      "v_add_f32 v170, v70, v170\n"
      "v_add_f32 v171, v71, v171\n"
      "v_lshlrev_b32 v172, 16, v142\n"
      "v_and_b32 v173, 0xffff0000, v142\n"
      "v_lshlrev_b32 v174, 16, v143\n"
      "v_and_b32 v175, 0xffff0000, v143\n"
      "v_add_f32 v172, v72, v172\n"
      "v_add_f32 v173, v73, v173\n"
      "v_add_f32 v174, v74, v174\n"
      "v_add_f32 v175, v75, v175\n"
      "v_lshlrev_b32 v176, 16, v144\n"
      "v_and_b32 v177, 0xffff0000, v144\n"
      "v_lshlrev_b32 v178, 16, v145\n"
      "v_and_b32 v179, 0xffff0000, v145\n"
      "v_add_f32 v176, v76, v176\n"
      "v_add_f32 v177, v77, v177\n"
      "v_add_f32 v178, v78, v178\n"
      "v_add_f32 v179, v79, v179\n"
      "v_cmp_gt_u32 vcc, s74, v10\n"
      "s_cbranch_vccnz .Lsl14\n"
      ".Lbk14:\n"
      "s_add_u32 s87, s75, s82\n"
      "buffer_load_dwordx4 v[80:83], v18, s[36:39], s87 offen sc1\n"
      "buffer_load_dwordx2 v[84:85], v118, s[36:39], s87 offen offset:1024 sc1\n"
      "s_add_u32 s87, s75, s83\n"
      "buffer_load_dwordx4 v[86:89], v18, s[36:39], s87 offen sc1\n"
      "buffer_load_dwordx2 v[90:91], v118, s[36:39], s87 offen offset:1024 sc1\n"
      "s_add_u32 s87, s75, s84\n"
      "buffer_load_dwordx4 v[92:95], v18, s[36:39], s87 offen sc1\n"
      "buffer_load_dwordx2 v[96:97], v118, s[36:39], s87 offen offset:1024 sc1\n"
      "s_add_u32 s87, s75, s85\n"
      "buffer_load_dwordx4 v[98:101], v18, s[36:39], s87 offen sc1\n"
      "buffer_load_dwordx2 v[102:103], v118, s[36:39], s87 offen offset:1024 sc1\n"
      "s_add_u32 s87, s75, s86\n"
      "buffer_load_dwordx4 v[104:107], v18, s[36:39], s87 offen sc1\n"
      "buffer_load_dwordx2 v[108:109], v118, s[36:39], s87 offen offset:1024 sc1\n"
      "s_waitcnt vmcnt(9)\n"
      "v_lshlrev_b32 v120, 16, v80\n"
      "v_and_b32 v121, 0xffff0000, v80\n"
      "v_lshlrev_b32 v122, 16, v81\n"
      "v_add_f32 v168, v168, v120\n"
      "v_add_f32 v169, v169, v121\n"
      "v_add_f32 v170, v170, v122\n"
      "v_and_b32 v123, 0xffff0000, v81\n"
      "v_add_f32 v171, v171, v123\n"
      "v_lshlrev_b32 v120, 16, v82\n"
      "v_and_b32 v121, 0xffff0000, v82\n"
      "v_lshlrev_b32 v122, 16, v83\n"
      "v_add_f32 v172, v172, v120\n"
      "v_add_f32 v173, v173, v121\n"
      "v_add_f32 v174, v174, v122\n"
      "v_and_b32 v123, 0xffff0000, v83\n"
      "s_waitcnt vmcnt(8)\n"
      "v_add_f32 v175, v175, v123\n"
      "v_lshlrev_b32 v120, 16, v84\n"
      "v_and_b32 v121, 0xffff0000, v84\n"
      "v_add_f32 v176, v176, v120\n"
      "v_add_f32 v177, v177, v121\n"
      "v_lshlrev_b32 v122, 16, v85\n"
      "v_and_b32 v123, 0xffff0000, v85\n"
      "s_waitcnt vmcnt(7)\n"
      "v_add_f32 v178, v178, v122\n"
      "v_add_f32 v179, v179, v123\n"
      "v_lshlrev_b32 v120, 16, v86\n"
      "v_add_f32 v168, v168, v120\n"
      "v_and_b32 v121, 0xffff0000, v86\n"
      "v_lshlrev_b32 v122, 16, v87\n"
      "v_and_b32 v123, 0xffff0000, v87\n"
      "v_add_f32 v169, v169, v121\n"
      "v_add_f32 v170, v170, v122\n"
      "v_add_f32 v171, v171, v123\n"
      "v_lshlrev_b32 v120, 16, v88\n"
      "v_add_f32 v172, v172, v120\n"
      "v_and_b32 v121, 0xffff0000, v88\n"
      "v_lshlrev_b32 v122, 16, v89\n"
      "v_and_b32 v123, 0xffff0000, v89\n"
      "v_add_f32 v173, v173, v121\n"
      "v_add_f32 v174, v174, v122\n"
      "v_add_f32 v175, v175, v123\n"
      "s_waitcnt vmcnt(6)\n"
      "v_lshlrev_b32 v120, 16, v90\n"
      "v_and_b32 v121, 0xffff0000, v90\n"
      "v_lshlrev_b32 v122, 16, v91\n"
      "v_add_f32 v176, v176, v120\n"
      "v_add_f32 v177, v177, v121\n"
      "v_add_f32 v178, v178, v122\n"
      "v_and_b32 v123, 0xffff0000, v91\n"
      "s_waitcnt vmcnt(5)\n"
      "v_add_f32 v179, v179, v123\n"
      "v_lshlrev_b32 v120, 16, v92\n"
      "v_and_b32 v121, 0xffff0000, v92\n"
      "v_add_f32 v168, v168, v120\n"
      "v_add_f32 v169, v169, v121\n"
      "v_lshlrev_b32 v122, 16, v93\n"
      "v_and_b32 v123, 0xffff0000, v93\n"
      "v_add_f32 v170, v170, v122\n"
      "v_add_f32 v171, v171, v123\n"
      "v_lshlrev_b32 v120, 16, v94\n"
      "v_and_b32 v121, 0xffff0000, v94\n"
      "v_add_f32 v172, v172, v120\n"
      "v_add_f32 v173, v173, v121\n"
      "v_lshlrev_b32 v122, 16, v95\n"
      "v_and_b32 v123, 0xffff0000, v95\n"
      "s_waitcnt vmcnt(4)\n"
      "v_add_f32 v174, v174, v122\n"
      "v_add_f32 v175, v175, v123\n"
      "v_lshlrev_b32 v120, 16, v96\n"
      "v_add_f32 v176, v176, v120\n"
      "v_and_b32 v121, 0xffff0000, v96\n"
      "v_lshlrev_b32 v122, 16, v97\n"
      "v_and_b32 v123, 0xffff0000, v97\n"
      "v_add_f32 v177, v177, v121\n"
      "v_add_f32 v178, v178, v122\n"
      "v_add_f32 v179, v179, v123\n"
      "s_waitcnt vmcnt(3)\n"
      "v_lshlrev_b32 v120, 16, v98\n"
      "v_and_b32 v121, 0xffff0000, v98\n"
      "v_lshlrev_b32 v122, 16, v99\n"
      "v_add_f32 v168, v168, v120\n"
      "v_add_f32 v169, v169, v121\n"
      "v_add_f32 v170, v170, v122\n"
      "v_and_b32 v123, 0xffff0000, v99\n"
      "v_add_f32 v171, v171, v123\n"
      "v_lshlrev_b32 v120, 16, v100\n"
      "v_and_b32 v121, 0xffff0000, v100\n"
      "v_lshlrev_b32 v122, 16, v101\n"
      "v_add_f32 v172, v172, v120\n"
      "v_add_f32 v173, v173, v121\n"
      "v_add_f32 v174, v174, v122\n"
      "v_and_b32 v123, 0xffff0000, v101\n"
      "s_waitcnt vmcnt(2)\n"
      "v_add_f32 v175, v175, v123\n"
      "v_lshlrev_b32 v120, 16, v102\n"
      "v_and_b32 v121, 0xffff0000, v102\n"
      "v_add_f32 v176, v176, v120\n"
      "v_add_f32 v177, v177, v121\n"
      "v_lshlrev_b32 v122, 16, v103\n"
      "v_and_b32 v123, 0xffff0000, v103\n"
      "s_waitcnt vmcnt(1)\n"
      "v_add_f32 v178, v178, v122\n"
      "v_add_f32 v179, v179, v123\n"
      "v_lshlrev_b32 v120, 16, v104\n"
      "v_add_f32 v168, v168, v120\n"
      "v_and_b32 v121, 0xffff0000, v104\n"
      "v_lshlrev_b32 v122, 16, v105\n"
      "v_and_b32 v123, 0xffff0000, v105\n"
      "v_add_f32 v169, v169, v121\n"
      "v_add_f32 v170, v170, v122\n"
      "v_add_f32 v171, v171, v123\n"
      "v_lshlrev_b32 v120, 16, v106\n"
      "v_add_f32 v172, v172, v120\n"
      "v_and_b32 v121, 0xffff0000, v106\n"
      "v_lshlrev_b32 v122, 16, v107\n"
      "v_and_b32 v123, 0xffff0000, v107\n"
      "v_add_f32 v173, v173, v121\n"
      "v_add_f32 v174, v174, v122\n"
      "v_add_f32 v175, v175, v123\n"
      "v_cvt_pk_bf16_f32 v146, v168, v169\n"
      "s_waitcnt vmcnt(0)\n"
      "v_lshlrev_b32 v120, 16, v108\n"
      "v_and_b32 v121, 0xffff0000, v108\n"
      "v_lshlrev_b32 v122, 16, v109\n"
      "v_add_f32 v176, v176, v120\n"
      "v_add_f32 v177, v177, v121\n"
      "v_add_f32 v178, v178, v122\n"
      "v_and_b32 v123, 0xffff0000, v109\n"
      "v_add_f32 v179, v179, v123\n"
      "v_cvt_pk_bf16_f32 v147, v170, v171\n"
      "v_cvt_pk_bf16_f32 v148, v172, v173\n"
      "v_cvt_pk_bf16_f32 v149, v174, v175\n"
      "buffer_store_dwordx4 v[146:149], v13, s[40:43], s78 offen\n"
      "v_cvt_pk_bf16_f32 v150, v176, v177\n"
      "v_cvt_pk_bf16_f32 v151, v178, v179\n"
      "buffer_store_dwordx2 v[150:151], v14, s[40:43], s78 offen\n"
      "s_branch .Lexit\n"
      ".Lgu:\n"
      "s_sub_u32 s49, s49, 19\n"
      "s_mul_i32 s50, s49, 43\n"
      "s_lshr_b32 s50, s50, 8\n"
      "s_mul_i32 s51, s50, 6\n"
      "s_sub_u32 s49, s49, s51\n"
      "s_bfe_u32 s51, s48, 0x10001\n"
      "s_lshl_b32 s51, s51, 1\n"
      "s_lshr_b32 s52, s48, 2\n"
      "s_or_b32 s51, s51, s52\n"
      "s_lshr_b32 s52, s48, 1\n"
      "s_add_u32 s52, s52, 3\n"
      "s_and_b32 s52, s52, 3\n"
      "s_add_u32 s29, s50, s51\n"
      "s_add_u32 s29, s29, 2\n"
      "s_and_b32 s29, s29, 3\n"
      "s_mov_b32 s30, s52\n"
      "s_bitcmp1_b32 s48, 0\n"
      "s_cbranch_scc1 .Lgu_m\n"
      "s_cmp_eq_u32 s50, 2\n"
      "s_cbranch_scc0 .Lgu_m0\n"
      "s_mov_b32 s49, 5\n"
      "s_add_u32 s29, s51, 1\n"
      "s_and_b32 s29, s29, 3\n"
      "s_branch .Lgu_m\n"
      ".Lgu_m0:\n"
      "s_and_b32 s53, s48, 2\n"
      "s_sub_u32 s29, 2, s53\n"
      "s_add_u32 s29, s29, s50\n"
      "s_lshr_b32 s53, s48, 1\n"
      "s_xor_b32 s53, s53, s48\n"
      "s_bfe_u32 s53, s53, 0x10001\n"
      "s_add_u32 s30, s53, 4\n"
      ".Lgu_m:\n"
      "s_mul_i32 s30, s30, 6\n"
      "s_add_u32 s30, s30, s49\n"
      "v_lshrrev_b32 v20, 4, v1\n"
      "v_and_b32 v21, 15, v1\n"
      "v_lshlrev_b32 v22, 2, v20\n"
      "v_xor_b32 v21, v21, v22\n"
      "v_mul_u32_u24 v22, 9216, v20\n"
      "v_lshl_add_u32 v10, v21, 4, v22\n"
      "v_bfe_u32 v22, v1, 2, 2\n"
      "v_lshl_add_u32 v20, v20, 3, v22\n"
      "v_lshlrev_b32 v20, 8, v20\n"
      "v_xor_b32 v22, s28, v22\n"
      "v_lshl_add_u32 v20, v22, 6, v20\n"
      "v_and_b32 v21, 3, v1\n"
      "v_lshl_add_u32 v11, v21, 4, v20\n"
      "v_add_u32 v66, 0x10000, v11\n"
      "v_add_u32 v67, 0x20000, v11\n"
      "s_lshl_b32 s73, s28, 11\n"
      "s_lshl_b32 s78, s30, 8\n"
      "s_mul_i32 s50, s28, 73728\n"
      "s_add_u32 s78, s78, s50\n"
      "v_mov_b32 v12, 0x3f803f80\n"
      "v_mov_b32 v13, 0x3f803f80\n"
      "v_mov_b32 v14, 0x3f803f80\n"
      "v_mov_b32 v15, 0x3f803f80\n"
      "s_mul_i32 s51, s29, 36\n"
      "s_add_u32 s51, s51, s30\n"
      "s_add_u32 s51, s51, 40\n"
      "s_lshl_b32 s51, s51, 2\n"
      "s_add_u32 s51, s51, s28\n"
      "s_lshl_b32 s51, s51, 7\n"
      "s_add_u32 s51, s51, 81911808\n"
      "v_lshlrev_b32 v7, 2, v1\n"
      "v_lshrrev_b32 v20, 5, v1\n"
      "v_lshl_or_b32 v7, v20, 30, v7\n"
      "v_add_u32 v7, s51, v7\n"
      "s_waitcnt lgkmcnt(0)\n"
      "s_mov_b32 s36, s20\n"
      "s_and_b32 s37, s21, 0xffff\n"
      "s_mov_b32 s38, 0x4eaf200\n"
      "s_mov_b32 s39, 0x20000\n"
      "s_mov_b32 s98, 82439168\n"
      "buffer_load_dwordx2 v[252:253], off, s[36:39], s98 sc1\n"
      "s_waitcnt vmcnt(0)\n"
      "v_readfirstlane_b32 s98, v252\n"
      "v_readfirstlane_b32 s99, v253\n"
      "v_mov_b32 v8, 0\n"
      "s_add_u32 s98, s98, 1\n"
      "s_addc_u32 s99, s99, 0\n"
      "v_writelane_b32 v8, s98, 2\n"
      "v_writelane_b32 v8, s99, 3\n"
      "buffer_store_dword v8, v7, s[36:39], 0 offen sc1\n"
      "s_mov_b32 s40, s14\n"
      "s_and_b32 s41, s15, 0xffff\n"
      "s_mov_b32 s42, 0x510000\n"
      "s_mov_b32 s43, 0x20000\n"
      "s_mov_b32 s44, s16\n"
      "s_and_b32 s45, s17, 0xffff\n"
      "s_mov_b32 s46, 0x510000\n"
      "s_mov_b32 s47, 0x20000\n"
      "s_add_u32 m0, s73, 0\n"
      "s_add_u32 s50, s78, 0\n"
      "buffer_load_dwordx4 v10, s[40:43], s50 offen lds\n"
      "s_add_u32 m0, s73, 1024\n"
      "s_add_u32 s50, s78, 36864\n"
      "buffer_load_dwordx4 v10, s[40:43], s50 offen lds\n"
      "s_add_u32 m0, s73, 8192\n"
      "s_add_u32 s50, s78, 294912\n"
      "buffer_load_dwordx4 v10, s[40:43], s50 offen lds\n"
      "s_add_u32 m0, s73, 9216\n"
      "s_add_u32 s50, s78, 331776\n"
      "buffer_load_dwordx4 v10, s[40:43], s50 offen lds\n"
      "s_add_u32 m0, s73, 16384\n"
      "s_add_u32 s50, s78, 589824\n"
      "buffer_load_dwordx4 v10, s[40:43], s50 offen lds\n"
      "s_add_u32 m0, s73, 17408\n"
      "s_add_u32 s50, s78, 626688\n"
      "buffer_load_dwordx4 v10, s[40:43], s50 offen lds\n"
      "s_add_u32 m0, s73, 24576\n"
      "s_add_u32 s50, s78, 884736\n"
      "buffer_load_dwordx4 v10, s[40:43], s50 offen lds\n"
      "s_add_u32 m0, s73, 25600\n"
      "s_add_u32 s50, s78, 921600\n"
      "buffer_load_dwordx4 v10, s[40:43], s50 offen lds\n"
      "s_add_u32 m0, s73, 32768\n"
      "s_add_u32 s50, s78, 1179648\n"
      "buffer_load_dwordx4 v10, s[40:43], s50 offen lds\n"
      "s_add_u32 m0, s73, 33792\n"
      "s_add_u32 s50, s78, 1216512\n"
      "buffer_load_dwordx4 v10, s[40:43], s50 offen lds\n"
      "s_add_u32 m0, s73, 40960\n"
      "s_add_u32 s50, s78, 1474560\n"
      "buffer_load_dwordx4 v10, s[40:43], s50 offen lds\n"
      "s_add_u32 m0, s73, 41984\n"
      "s_add_u32 s50, s78, 1511424\n"
      "buffer_load_dwordx4 v10, s[40:43], s50 offen lds\n"
      "s_add_u32 m0, s73, 49152\n"
      "s_add_u32 s50, s78, 1769472\n"
      "buffer_load_dwordx4 v10, s[40:43], s50 offen lds\n"
      "s_add_u32 m0, s73, 50176\n"
      "s_add_u32 s50, s78, 1806336\n"
      "buffer_load_dwordx4 v10, s[40:43], s50 offen lds\n"
      "s_add_u32 m0, s73, 57344\n"
      "s_add_u32 s50, s78, 2064384\n"
      "buffer_load_dwordx4 v10, s[40:43], s50 offen lds\n"
      "s_add_u32 m0, s73, 58368\n"
      "s_add_u32 s50, s78, 2101248\n"
      "buffer_load_dwordx4 v10, s[40:43], s50 offen lds\n"
      "s_add_u32 m0, s73, 65536\n"
      "s_add_u32 s50, s78, 2359296\n"
      "buffer_load_dwordx4 v10, s[40:43], s50 offen lds\n"
      "s_add_u32 m0, s73, 66560\n"
      "s_add_u32 s50, s78, 2396160\n"
      "buffer_load_dwordx4 v10, s[40:43], s50 offen lds\n"
      "v_mov_b32 v96, 0\n"
      "v_mov_b32 v97, 0\n"
      "v_mov_b32 v98, 0\n"
      "v_mov_b32 v99, 0\n"
      "v_mov_b32 v100, 0\n"
      "v_mov_b32 v101, 0\n"
      "v_mov_b32 v20, 0x3f80\n"
      "v_mov_b32 v21, 0x3f800000\n"
      "s_mov_b32 vcc_lo, 0x100001\n"
      "s_mov_b32 vcc_hi, 0x10000100\n"
      "v_cndmask_b32 v98, v98, v20, vcc\n"
      "s_mov_b32 vcc_lo, 0x200002\n"
      "s_mov_b32 vcc_hi, 0x20000200\n"
      "v_cndmask_b32 v98, v98, v21, vcc\n"
      "s_mov_b32 vcc_lo, 0x400004\n"
      "s_mov_b32 vcc_hi, 0x40000400\n"
      "v_cndmask_b32 v99, v99, v20, vcc\n"
      "s_mov_b32 vcc_lo, 0x800008\n"
      "s_mov_b32 vcc_hi, 0x80000800\n"
      "v_cndmask_b32 v99, v99, v21, vcc\n"
      "s_mov_b32 s66, -1\n"
      "s_mov_b32 s75, 0\n"
      "s_mul_i32 s68, s29, 1331200\n"
      "s_lshl_b32 s50, s28, 12\n"
      "s_add_u32 s68, s68, s50\n"
      "s_add_u32 s69, s68, 10526720\n"
      "s_add_u32 s68, s69, 40960\n"
      "s_mov_b32 s79, s50\n"
      "s_lshl_b32 s74, s28, 10\n"
      "s_add_u32 s74, s74, 16384\n"
      "s_sub_u32 s51, s74, s50\n"
      "v_add_u32 v4, s51, v2\n"
      "s_cmp_lt_u32 s28, 3\n"
      "s_cselect_b32 s51, 64, 0\n"
      "v_mov_b32 v40, 0x40000000\n"
      "v_cmp_gt_u32 vcc, s51, v1\n"
      "s_nop 1\n"
      "v_cndmask_b32 v4, v40, v4, vcc\n"
      "s_mul_i32 s70, s29, 9437184\n"
      "s_lshl_b32 s51, s30, 2\n"
      "s_add_u32 s51, s51, s28\n"
      "s_lshl_b32 s51, s51, 10\n"
      "s_add_u32 s70, s70, s51\n"
      "s_add_u32 s70, s70, 15704064\n"
      "v_and_b32 v41, 4, v1\n"
      "s_lshl_b32 s51, s29, 10\n"
      "s_add_u32 s51, s51, 81928192\n"
      "v_lshlrev_b32 v41, 7, v41\n"
      "v_add_u32 v5, s51, v41\n"
      "v_mov_b32 v6, 0\n"
      "v_mov_b32 v83, 0\n"
      "v_lshlrev_b32 v80, 3, v1\n"
      "v_mov_b32 v9, v80\n"
      "v_mov_b32 v3, v2\n"
      "v_mov_b32 v81, 0x3ae38e39\n"
      "s_mov_b32 s71, 0x40000000\n"
      "s_waitcnt vmcnt(12)\n"
      "s_barrier\n"
      "ds_read_b64_tr_b16 a[0:1], v11 offset:0\n"
      "ds_read_b64_tr_b16 a[2:3], v11 offset:1024\n"
      "ds_read_b64_tr_b16 a[4:5], v11 offset:8\n"
      "ds_read_b64_tr_b16 a[6:7], v11 offset:1032\n"
      "ds_read_b64_tr_b16 a[8:9], v11 offset:8192\n"
      "ds_read_b64_tr_b16 a[10:11], v11 offset:9216\n"
      "ds_read_b64_tr_b16 a[12:13], v11 offset:8200\n"
      "ds_read_b64_tr_b16 a[14:15], v11 offset:9224\n"
      "ds_read_b64_tr_b16 a[16:17], v11 offset:16384\n"
      "ds_read_b64_tr_b16 a[18:19], v11 offset:17408\n"
      "ds_read_b64_tr_b16 a[20:21], v11 offset:16392\n"
      "ds_read_b64_tr_b16 a[22:23], v11 offset:17416\n"
      "s_add_u32 m0, s73, 73728\n"
      "s_add_u32 s50, s78, 2654208\n"
      "buffer_load_dwordx4 v10, s[40:43], s50 offen lds\n"
      "s_add_u32 m0, s73, 74752\n"
      "s_add_u32 s50, s78, 2691072\n"
      "buffer_load_dwordx4 v10, s[40:43], s50 offen lds\n"
      "s_add_u32 m0, s73, 81920\n"
      "s_add_u32 s50, s78, 2949120\n"
      "buffer_load_dwordx4 v10, s[40:43], s50 offen lds\n"
      "s_add_u32 m0, s73, 82944\n"
      "s_add_u32 s50, s78, 2985984\n"
      "buffer_load_dwordx4 v10, s[40:43], s50 offen lds\n"
      "s_add_u32 m0, s73, 90112\n"
      "s_add_u32 s50, s78, 3244032\n"
      "buffer_load_dwordx4 v10, s[40:43], s50 offen lds\n"
      "s_add_u32 m0, s73, 91136\n"
      "s_add_u32 s50, s78, 3280896\n"
      "buffer_load_dwordx4 v10, s[40:43], s50 offen lds\n"
      "s_waitcnt vmcnt(12) lgkmcnt(0)\n"
      "s_barrier\n"
      "ds_read_b64_tr_b16 a[24:25], v11 offset:24576\n"
      "ds_read_b64_tr_b16 a[26:27], v11 offset:25600\n"
      "ds_read_b64_tr_b16 a[28:29], v11 offset:24584\n"
      "ds_read_b64_tr_b16 a[30:31], v11 offset:25608\n"
      "ds_read_b64_tr_b16 a[32:33], v11 offset:32768\n"
      "ds_read_b64_tr_b16 a[34:35], v11 offset:33792\n"
      "ds_read_b64_tr_b16 a[36:37], v11 offset:32776\n"
      "ds_read_b64_tr_b16 a[38:39], v11 offset:33800\n"
      "ds_read_b64_tr_b16 a[40:41], v11 offset:40960\n"
      "ds_read_b64_tr_b16 a[42:43], v11 offset:41984\n"
      "ds_read_b64_tr_b16 a[44:45], v11 offset:40968\n"
      "ds_read_b64_tr_b16 a[46:47], v11 offset:41992\n"
      "v_mfma_f32_16x16x32_bf16 v[16:19], v[12:15], a[0:3], 0\n"
      "v_mfma_f32_16x16x32_bf16 v[20:23], v[12:15], a[4:7], 0\n"
      "v_mfma_f32_16x16x32_bf16 v[16:19], v[12:15], a[8:11], v[16:19]\n"
      "v_mfma_f32_16x16x32_bf16 v[20:23], v[12:15], a[12:15], v[20:23]\n"
      "v_mfma_f32_16x16x32_bf16 v[16:19], v[12:15], a[16:19], v[16:19]\n"
      "v_mfma_f32_16x16x32_bf16 v[20:23], v[12:15], a[20:23], v[20:23]\n"
      "s_add_u32 m0, s73, 98304\n"
      "s_add_u32 s50, s78, 3538944\n"
      "buffer_load_dwordx4 v10, s[40:43], s50 offen lds\n"
      "s_add_u32 m0, s73, 99328\n"
      "s_add_u32 s50, s78, 3575808\n"
      "buffer_load_dwordx4 v10, s[40:43], s50 offen lds\n"
      "s_add_u32 m0, s73, 106496\n"
      "s_add_u32 s50, s78, 3833856\n"
      "buffer_load_dwordx4 v10, s[40:43], s50 offen lds\n"
      "s_add_u32 m0, s73, 107520\n"
      "s_add_u32 s50, s78, 3870720\n"
      "buffer_load_dwordx4 v10, s[40:43], s50 offen lds\n"
      "s_add_u32 m0, s73, 114688\n"
      "s_add_u32 s50, s78, 4128768\n"
      "buffer_load_dwordx4 v10, s[40:43], s50 offen lds\n"
      "s_add_u32 m0, s73, 115712\n"
      "s_add_u32 s50, s78, 4165632\n"
      "buffer_load_dwordx4 v10, s[40:43], s50 offen lds\n"
      "s_waitcnt vmcnt(12) lgkmcnt(0)\n"
      "s_barrier\n"
      "ds_read_b64_tr_b16 a[48:49], v11 offset:49152\n"
      "ds_read_b64_tr_b16 a[50:51], v11 offset:50176\n"
      "ds_read_b64_tr_b16 a[52:53], v11 offset:49160\n"
      "ds_read_b64_tr_b16 a[54:55], v11 offset:50184\n"
      "ds_read_b64_tr_b16 a[56:57], v11 offset:57344\n"
      "ds_read_b64_tr_b16 a[58:59], v11 offset:58368\n"
      "ds_read_b64_tr_b16 a[60:61], v11 offset:57352\n"
      "ds_read_b64_tr_b16 a[62:63], v11 offset:58376\n"
      "ds_read_b64_tr_b16 a[64:65], v66 offset:0\n"
      "ds_read_b64_tr_b16 a[66:67], v66 offset:1024\n"
      "ds_read_b64_tr_b16 a[68:69], v66 offset:8\n"
      "ds_read_b64_tr_b16 a[70:71], v66 offset:1032\n"
      "v_mfma_f32_16x16x32_bf16 v[16:19], v[12:15], a[24:27], v[16:19]\n"
      "v_mfma_f32_16x16x32_bf16 v[20:23], v[12:15], a[28:31], v[20:23]\n"
      "v_mfma_f32_16x16x32_bf16 v[16:19], v[12:15], a[32:35], v[16:19]\n"
      "v_mfma_f32_16x16x32_bf16 v[20:23], v[12:15], a[36:39], v[20:23]\n"
      "v_mfma_f32_16x16x32_bf16 v[16:19], v[12:15], a[40:43], v[16:19]\n"
      "v_mfma_f32_16x16x32_bf16 v[20:23], v[12:15], a[44:47], v[20:23]\n"
      "s_add_u32 m0, s73, 122880\n"
      "s_add_u32 s50, s78, 4423680\n"
      "buffer_load_dwordx4 v10, s[40:43], s50 offen lds\n"
      "s_add_u32 m0, s73, 123904\n"
      "s_add_u32 s50, s78, 4460544\n"
      "buffer_load_dwordx4 v10, s[40:43], s50 offen lds\n"
      "s_add_u32 m0, s73, 131072\n"
      "s_add_u32 s50, s78, 4718592\n"
      "buffer_load_dwordx4 v10, s[40:43], s50 offen lds\n"
      "s_add_u32 m0, s73, 132096\n"
      "s_add_u32 s50, s78, 4755456\n"
      "buffer_load_dwordx4 v10, s[40:43], s50 offen lds\n"
      "s_add_u32 m0, s73, 139264\n"
      "s_add_u32 s50, s78, 5013504\n"
      "buffer_load_dwordx4 v10, s[40:43], s50 offen lds\n"
      "s_add_u32 m0, s73, 140288\n"
      "s_add_u32 s50, s78, 5050368\n"
      "buffer_load_dwordx4 v10, s[40:43], s50 offen lds\n"
      "s_waitcnt vmcnt(12) lgkmcnt(0)\n"
      "s_barrier\n"
      "ds_read_b64_tr_b16 a[72:73], v66 offset:8192\n"
      "ds_read_b64_tr_b16 a[74:75], v66 offset:9216\n"
      "ds_read_b64_tr_b16 a[76:77], v66 offset:8200\n"
      "ds_read_b64_tr_b16 a[78:79], v66 offset:9224\n"
      "ds_read_b64_tr_b16 a[80:81], v66 offset:16384\n"
      "ds_read_b64_tr_b16 a[82:83], v66 offset:17408\n"
      "ds_read_b64_tr_b16 a[84:85], v66 offset:16392\n"
      "ds_read_b64_tr_b16 a[86:87], v66 offset:17416\n"
      "ds_read_b64_tr_b16 a[88:89], v66 offset:24576\n"
      "ds_read_b64_tr_b16 a[90:91], v66 offset:25600\n"
      "ds_read_b64_tr_b16 a[92:93], v66 offset:24584\n"
      "ds_read_b64_tr_b16 a[94:95], v66 offset:25608\n"
      "v_mfma_f32_16x16x32_bf16 v[16:19], v[12:15], a[48:51], v[16:19]\n"
      "v_mfma_f32_16x16x32_bf16 v[20:23], v[12:15], a[52:55], v[20:23]\n"
      "v_mfma_f32_16x16x32_bf16 v[16:19], v[12:15], a[56:59], v[16:19]\n"
      "v_mfma_f32_16x16x32_bf16 v[20:23], v[12:15], a[60:63], v[20:23]\n"
      "v_mfma_f32_16x16x32_bf16 v[16:19], v[12:15], a[64:67], v[16:19]\n"
      "v_mfma_f32_16x16x32_bf16 v[20:23], v[12:15], a[68:71], v[20:23]\n"
      "s_add_u32 m0, s73, 0\n"
      "s_add_u32 s50, s78, 0\n"
      "buffer_load_dwordx4 v10, s[44:47], s50 offen lds\n"
      "s_add_u32 m0, s73, 1024\n"
      "s_add_u32 s50, s78, 36864\n"
      "buffer_load_dwordx4 v10, s[44:47], s50 offen lds\n"
      "s_add_u32 m0, s73, 8192\n"
      "s_add_u32 s50, s78, 294912\n"
      "buffer_load_dwordx4 v10, s[44:47], s50 offen lds\n"
      "s_add_u32 m0, s73, 9216\n"
      "s_add_u32 s50, s78, 331776\n"
      "buffer_load_dwordx4 v10, s[44:47], s50 offen lds\n"
      "s_add_u32 m0, s73, 16384\n"
      "s_add_u32 s50, s78, 589824\n"
      "buffer_load_dwordx4 v10, s[44:47], s50 offen lds\n"
      "s_add_u32 m0, s73, 17408\n"
      "s_add_u32 s50, s78, 626688\n"
      "buffer_load_dwordx4 v10, s[44:47], s50 offen lds\n"
      "s_waitcnt vmcnt(12) lgkmcnt(0)\n"
      "s_barrier\n"
      "ds_read_b64_tr_b16 a[96:97], v66 offset:32768\n"
      "ds_read_b64_tr_b16 a[98:99], v66 offset:33792\n"
      "ds_read_b64_tr_b16 a[100:101], v66 offset:32776\n"
      "ds_read_b64_tr_b16 a[102:103], v66 offset:33800\n"
      "ds_read_b64_tr_b16 a[104:105], v66 offset:40960\n"
      "ds_read_b64_tr_b16 a[106:107], v66 offset:41984\n"
      "ds_read_b64_tr_b16 a[108:109], v66 offset:40968\n"
      "ds_read_b64_tr_b16 a[110:111], v66 offset:41992\n"
      "ds_read_b64_tr_b16 a[112:113], v66 offset:49152\n"
      "ds_read_b64_tr_b16 a[114:115], v66 offset:50176\n"
      "ds_read_b64_tr_b16 a[116:117], v66 offset:49160\n"
      "ds_read_b64_tr_b16 a[118:119], v66 offset:50184\n"
      "v_mfma_f32_16x16x32_bf16 v[16:19], v[12:15], a[72:75], v[16:19]\n"
      "v_mfma_f32_16x16x32_bf16 v[20:23], v[12:15], a[76:79], v[20:23]\n"
      "v_mfma_f32_16x16x32_bf16 v[16:19], v[12:15], a[80:83], v[16:19]\n"
      "v_mfma_f32_16x16x32_bf16 v[20:23], v[12:15], a[84:87], v[20:23]\n"
      "v_mfma_f32_16x16x32_bf16 v[16:19], v[12:15], a[88:91], v[16:19]\n"
      "v_mfma_f32_16x16x32_bf16 v[20:23], v[12:15], a[92:95], v[20:23]\n"
      "s_add_u32 m0, s73, 24576\n"
      "s_add_u32 s50, s78, 884736\n"
      "buffer_load_dwordx4 v10, s[44:47], s50 offen lds\n"
      "s_add_u32 m0, s73, 25600\n"
      "s_add_u32 s50, s78, 921600\n"
      "buffer_load_dwordx4 v10, s[44:47], s50 offen lds\n"
      "s_add_u32 m0, s73, 32768\n"
      "s_add_u32 s50, s78, 1179648\n"
      "buffer_load_dwordx4 v10, s[44:47], s50 offen lds\n"
      "s_add_u32 m0, s73, 33792\n"
      "s_add_u32 s50, s78, 1216512\n"
      "buffer_load_dwordx4 v10, s[44:47], s50 offen lds\n"
      "s_add_u32 m0, s73, 40960\n"
      "s_add_u32 s50, s78, 1474560\n"
      "buffer_load_dwordx4 v10, s[44:47], s50 offen lds\n"
      "s_add_u32 m0, s73, 41984\n"
      "s_add_u32 s50, s78, 1511424\n"
      "buffer_load_dwordx4 v10, s[44:47], s50 offen lds\n"
      "s_waitcnt vmcnt(12) lgkmcnt(0)\n"
      "s_barrier\n"
      "ds_read_b64_tr_b16 a[120:121], v66 offset:57344\n"
      "ds_read_b64_tr_b16 a[122:123], v66 offset:58368\n"
      "ds_read_b64_tr_b16 a[124:125], v66 offset:57352\n"
      "ds_read_b64_tr_b16 a[126:127], v66 offset:58376\n"
      "ds_read_b64_tr_b16 a[128:129], v67 offset:0\n"
      "ds_read_b64_tr_b16 a[130:131], v67 offset:1024\n"
      "ds_read_b64_tr_b16 a[132:133], v67 offset:8\n"
      "ds_read_b64_tr_b16 a[134:135], v67 offset:1032\n"
      "ds_read_b64_tr_b16 a[136:137], v67 offset:8192\n"
      "ds_read_b64_tr_b16 a[138:139], v67 offset:9216\n"
      "ds_read_b64_tr_b16 a[140:141], v67 offset:8200\n"
      "ds_read_b64_tr_b16 a[142:143], v67 offset:9224\n"
      "v_mfma_f32_16x16x32_bf16 v[16:19], v[12:15], a[96:99], v[16:19]\n"
      "v_mfma_f32_16x16x32_bf16 v[20:23], v[12:15], a[100:103], v[20:23]\n"
      "v_mfma_f32_16x16x32_bf16 v[16:19], v[12:15], a[104:107], v[16:19]\n"
      "v_mfma_f32_16x16x32_bf16 v[20:23], v[12:15], a[108:111], v[20:23]\n"
      "v_mfma_f32_16x16x32_bf16 v[16:19], v[12:15], a[112:115], v[16:19]\n"
      "v_mfma_f32_16x16x32_bf16 v[20:23], v[12:15], a[116:119], v[20:23]\n"
      "s_add_u32 m0, s73, 49152\n"
      "s_add_u32 s50, s78, 1769472\n"
      "buffer_load_dwordx4 v10, s[44:47], s50 offen lds\n"
      "s_add_u32 m0, s73, 50176\n"
      "s_add_u32 s50, s78, 1806336\n"
      "buffer_load_dwordx4 v10, s[44:47], s50 offen lds\n"
      "s_add_u32 m0, s73, 57344\n"
      "s_add_u32 s50, s78, 2064384\n"
      "buffer_load_dwordx4 v10, s[44:47], s50 offen lds\n"
      "s_add_u32 m0, s73, 58368\n"
      "s_add_u32 s50, s78, 2101248\n"
      "buffer_load_dwordx4 v10, s[44:47], s50 offen lds\n"
      "s_add_u32 m0, s73, 65536\n"
      "s_add_u32 s50, s78, 2359296\n"
      "buffer_load_dwordx4 v10, s[44:47], s50 offen lds\n"
      "s_add_u32 m0, s73, 66560\n"
      "s_add_u32 s50, s78, 2396160\n"
      "buffer_load_dwordx4 v10, s[44:47], s50 offen lds\n"
      "s_waitcnt vmcnt(12) lgkmcnt(0)\n"
      "s_barrier\n"
      "ds_read_b64_tr_b16 v[104:105], v11 offset:0\n"
      "ds_read_b64_tr_b16 v[106:107], v11 offset:1024\n"
      "ds_read_b64_tr_b16 v[108:109], v11 offset:8\n"
      "ds_read_b64_tr_b16 v[110:111], v11 offset:1032\n"
      "ds_read_b64_tr_b16 v[112:113], v11 offset:8192\n"
      "ds_read_b64_tr_b16 v[114:115], v11 offset:9216\n"
      "ds_read_b64_tr_b16 v[116:117], v11 offset:8200\n"
      "ds_read_b64_tr_b16 v[118:119], v11 offset:9224\n"
      "ds_read_b64_tr_b16 v[120:121], v11 offset:16384\n"
      "ds_read_b64_tr_b16 v[122:123], v11 offset:17408\n"
      "ds_read_b64_tr_b16 v[124:125], v11 offset:16392\n"
      "ds_read_b64_tr_b16 v[126:127], v11 offset:17416\n"
      "v_mfma_f32_16x16x32_bf16 v[16:19], v[12:15], a[120:123], v[16:19]\n"
      "v_mfma_f32_16x16x32_bf16 v[20:23], v[12:15], a[124:127], v[20:23]\n"
      "v_mfma_f32_16x16x32_bf16 v[16:19], v[12:15], a[128:131], v[16:19]\n"
      "v_mfma_f32_16x16x32_bf16 v[20:23], v[12:15], a[132:135], v[20:23]\n"
      "v_mfma_f32_16x16x32_bf16 v[16:19], v[12:15], a[136:139], v[16:19]\n"
      "v_mfma_f32_16x16x32_bf16 v[20:23], v[12:15], a[140:143], v[20:23]\n"
      "s_add_u32 m0, s73, 73728\n"
      "s_add_u32 s50, s78, 2654208\n"
      "buffer_load_dwordx4 v10, s[44:47], s50 offen lds\n"
      "s_add_u32 m0, s73, 74752\n"
      "s_add_u32 s50, s78, 2691072\n"
      "buffer_load_dwordx4 v10, s[44:47], s50 offen lds\n"
      "s_add_u32 m0, s73, 81920\n"
      "s_add_u32 s50, s78, 2949120\n"
      "buffer_load_dwordx4 v10, s[44:47], s50 offen lds\n"
      "s_add_u32 m0, s73, 82944\n"
      "s_add_u32 s50, s78, 2985984\n"
      "buffer_load_dwordx4 v10, s[44:47], s50 offen lds\n"
      "s_add_u32 m0, s73, 90112\n"
      "s_add_u32 s50, s78, 3244032\n"
      "buffer_load_dwordx4 v10, s[44:47], s50 offen lds\n"
      "s_add_u32 m0, s73, 91136\n"
      "s_add_u32 s50, s78, 3280896\n"
      "buffer_load_dwordx4 v10, s[44:47], s50 offen lds\n"
      "v_mul_f32 v16, 0xbae38e39, v16\n"
      "v_mul_f32 v17, 0xbae38e39, v17\n"
      "v_mul_f32 v18, 0xbae38e39, v18\n"
      "v_mul_f32 v19, 0xbae38e39, v19\n"
      "v_mul_f32 v20, 0xbae38e39, v20\n"
      "v_mul_f32 v21, 0xbae38e39, v21\n"
      "v_mul_f32 v22, 0xbae38e39, v22\n"
      "v_mul_f32 v23, 0xbae38e39, v23\n"
      "v_mfma_f32_16x16x32_bf16 v[32:35], v[12:15], a[136:139], 0\n"
      "v_mfma_f32_16x16x32_bf16 v[36:39], v[12:15], a[140:143], 0\n"
      "v_mfma_f32_16x16x32_bf16 v[48:51], v[98:101], a[0:3], v[16:19]\n"
      "v_mfma_f32_16x16x32_bf16 v[52:55], v[96:99], a[0:3], v[16:19]\n"
      "v_mfma_f32_16x16x32_bf16 v[68:71], v[98:101], a[4:7], v[20:23]\n"
      "v_mfma_f32_16x16x32_bf16 v[72:75], v[96:99], a[4:7], v[20:23]\n"
      "v_mfma_f32_16x16x32_bf16 v[84:87], v[98:101], a[8:11], v[16:19]\n"
      "s_nop 3\n"
      "v_cvt_pk_bf16_f32 v48, v48, v49\n"
      "v_cvt_pk_bf16_f32 v50, v50, v51\n"
      "v_cvt_pk_bf16_f32 v52, v52, v53\n"
      "v_mfma_f32_16x16x32_bf16 v[88:91], v[96:99], a[8:11], v[16:19]\n"
      "v_cvt_pk_bf16_f32 v54, v54, v55\n"
      "v_accvgpr_write_b32 a0, v48\n"
      "v_accvgpr_write_b32 a1, v50\n"
      "v_accvgpr_write_b32 a2, v52\n"
      "v_accvgpr_write_b32 a3, v54\n"
      "v_mfma_f32_16x16x32_bf16 v[48:51], v[98:101], a[12:15], v[20:23]\n"
      "v_cvt_pk_bf16_f32 v68, v68, v69\n"
      "v_cvt_pk_bf16_f32 v70, v70, v71\n"
      "v_cvt_pk_bf16_f32 v72, v72, v73\n"
      "v_mfma_f32_16x16x32_bf16 v[52:55], v[96:99], a[12:15], v[20:23]\n"
      "v_cvt_pk_bf16_f32 v74, v74, v75\n"
      "v_accvgpr_write_b32 a4, v68\n"
      "v_accvgpr_write_b32 a5, v70\n"
      "v_mfma_f32_16x16x32_bf16 v[32:35], v[12:15], a[0:3], v[32:35]\n"
      "v_accvgpr_write_b32 a6, v72\n"
      "v_accvgpr_write_b32 a7, v74\n"
      "v_mfma_f32_16x16x32_bf16 v[68:71], v[98:101], a[16:19], v[16:19]\n"
      "v_cvt_pk_bf16_f32 v84, v84, v85\n"
      "v_cvt_pk_bf16_f32 v86, v86, v87\n"
      "v_cvt_pk_bf16_f32 v88, v88, v89\n"
      "v_mfma_f32_16x16x32_bf16 v[72:75], v[96:99], a[16:19], v[16:19]\n"
      "v_cvt_pk_bf16_f32 v90, v90, v91\n"
      "v_accvgpr_write_b32 a8, v84\n"
      "v_accvgpr_write_b32 a9, v86\n"
      "v_mfma_f32_16x16x32_bf16 v[36:39], v[12:15], a[4:7], v[36:39]\n"
      "v_accvgpr_write_b32 a10, v88\n"
      "v_accvgpr_write_b32 a11, v90\n"
      "v_mfma_f32_16x16x32_bf16 v[84:87], v[98:101], a[20:23], v[20:23]\n"
      "v_cvt_pk_bf16_f32 v48, v48, v49\n"
      "v_cvt_pk_bf16_f32 v50, v50, v51\n"
      "v_cvt_pk_bf16_f32 v52, v52, v53\n"
      "v_mfma_f32_16x16x32_bf16 v[88:91], v[96:99], a[20:23], v[20:23]\n"
      "v_cvt_pk_bf16_f32 v54, v54, v55\n"
      "v_accvgpr_write_b32 a12, v48\n"
      "v_accvgpr_write_b32 a13, v50\n"
      "v_mfma_f32_16x16x32_bf16 v[32:35], v[12:15], a[8:11], v[32:35]\n"
      "v_accvgpr_write_b32 a14, v52\n"
      "v_accvgpr_write_b32 a15, v54\n"
      "v_mfma_f32_16x16x32_bf16 v[48:51], v[98:101], a[24:27], v[16:19]\n"
      "v_cvt_pk_bf16_f32 v68, v68, v69\n"
      "v_cvt_pk_bf16_f32 v70, v70, v71\n"
      "v_cvt_pk_bf16_f32 v72, v72, v73\n"
      "v_mfma_f32_16x16x32_bf16 v[52:55], v[96:99], a[24:27], v[16:19]\n"
      "v_cvt_pk_bf16_f32 v74, v74, v75\n"
      "v_accvgpr_write_b32 a16, v68\n"
      "v_accvgpr_write_b32 a17, v70\n"
      "v_mfma_f32_16x16x32_bf16 v[36:39], v[12:15], a[12:15], v[36:39]\n"
      "v_accvgpr_write_b32 a18, v72\n"
      "v_accvgpr_write_b32 a19, v74\n"
      "s_waitcnt vmcnt(12) lgkmcnt(0)\n"
      "s_barrier\n"
      "ds_read_b64_tr_b16 v[128:129], v11 offset:24576\n"
      "ds_read_b64_tr_b16 v[130:131], v11 offset:25600\n"
      "ds_read_b64_tr_b16 v[132:133], v11 offset:24584\n"
      "ds_read_b64_tr_b16 v[134:135], v11 offset:25608\n"
      "ds_read_b64_tr_b16 v[136:137], v11 offset:32768\n"
      "ds_read_b64_tr_b16 v[138:139], v11 offset:33792\n"
      "ds_read_b64_tr_b16 v[140:141], v11 offset:32776\n"
      "ds_read_b64_tr_b16 v[142:143], v11 offset:33800\n"
      "ds_read_b64_tr_b16 v[144:145], v11 offset:40960\n"
      "ds_read_b64_tr_b16 v[146:147], v11 offset:41984\n"
      "ds_read_b64_tr_b16 v[148:149], v11 offset:40968\n"
      "ds_read_b64_tr_b16 v[150:151], v11 offset:41992\n"
      "v_mfma_f32_16x16x32_bf16 v[24:27], v[12:15], v[104:107], 0\n"
      "v_mfma_f32_16x16x32_bf16 v[28:31], v[12:15], v[108:111], 0\n"
      "v_mfma_f32_16x16x32_bf16 v[24:27], v[12:15], v[112:115], v[24:27]\n"
      "v_mfma_f32_16x16x32_bf16 v[28:31], v[12:15], v[116:119], v[28:31]\n"
      "v_mfma_f32_16x16x32_bf16 v[24:27], v[12:15], v[120:123], v[24:27]\n"
      "v_mfma_f32_16x16x32_bf16 v[28:31], v[12:15], v[124:127], v[28:31]\n"
      "s_add_u32 m0, s73, 98304\n"
      "s_add_u32 s50, s78, 3538944\n"
      "buffer_load_dwordx4 v10, s[44:47], s50 offen lds\n"
      "s_add_u32 m0, s73, 99328\n"
      "s_add_u32 s50, s78, 3575808\n"
      "buffer_load_dwordx4 v10, s[44:47], s50 offen lds\n"
      "s_add_u32 m0, s73, 106496\n"
      "s_add_u32 s50, s78, 3833856\n"
      "buffer_load_dwordx4 v10, s[44:47], s50 offen lds\n"
      "s_add_u32 m0, s73, 107520\n"
      "s_add_u32 s50, s78, 3870720\n"
      "buffer_load_dwordx4 v10, s[44:47], s50 offen lds\n"
      "s_add_u32 m0, s73, 114688\n"
      "s_add_u32 s50, s78, 4128768\n"
      "buffer_load_dwordx4 v10, s[44:47], s50 offen lds\n"
      "s_add_u32 m0, s73, 115712\n"
      "s_add_u32 s50, s78, 4165632\n"
      "buffer_load_dwordx4 v10, s[44:47], s50 offen lds\n"
      "v_mfma_f32_16x16x32_bf16 v[68:71], v[98:101], a[28:31], v[20:23]\n"
      "v_cvt_pk_bf16_f32 v84, v84, v85\n"
      "v_cvt_pk_bf16_f32 v86, v86, v87\n"
      "v_cvt_pk_bf16_f32 v88, v88, v89\n"
      "v_mfma_f32_16x16x32_bf16 v[72:75], v[96:99], a[28:31], v[20:23]\n"
      "v_cvt_pk_bf16_f32 v90, v90, v91\n"
      "v_accvgpr_write_b32 a20, v84\n"
      "v_accvgpr_write_b32 a21, v86\n"
      "v_mfma_f32_16x16x32_bf16 v[32:35], v[12:15], a[16:19], v[32:35]\n"
      "v_accvgpr_write_b32 a22, v88\n"
      "v_accvgpr_write_b32 a23, v90\n"
      "v_mfma_f32_16x16x32_bf16 v[84:87], v[98:101], a[32:35], v[16:19]\n"
      "v_cvt_pk_bf16_f32 v48, v48, v49\n"
      "v_cvt_pk_bf16_f32 v50, v50, v51\n"
      "v_cvt_pk_bf16_f32 v52, v52, v53\n"
      "v_mfma_f32_16x16x32_bf16 v[88:91], v[96:99], a[32:35], v[16:19]\n"
      "v_cvt_pk_bf16_f32 v54, v54, v55\n"
      "v_accvgpr_write_b32 a24, v48\n"
      "v_accvgpr_write_b32 a25, v50\n"
      "v_mfma_f32_16x16x32_bf16 v[36:39], v[12:15], a[20:23], v[36:39]\n"
      "v_accvgpr_write_b32 a26, v52\n"
      "v_accvgpr_write_b32 a27, v54\n"
      "v_mfma_f32_16x16x32_bf16 v[48:51], v[98:101], a[36:39], v[20:23]\n"
      "v_cvt_pk_bf16_f32 v68, v68, v69\n"
      "v_cvt_pk_bf16_f32 v70, v70, v71\n"
      "v_cvt_pk_bf16_f32 v72, v72, v73\n"
      "v_mfma_f32_16x16x32_bf16 v[52:55], v[96:99], a[36:39], v[20:23]\n"
      "v_cvt_pk_bf16_f32 v74, v74, v75\n"
      "v_accvgpr_write_b32 a28, v68\n"
      "v_accvgpr_write_b32 a29, v70\n"
      "v_mfma_f32_16x16x32_bf16 v[32:35], v[12:15], a[24:27], v[32:35]\n"
      "v_accvgpr_write_b32 a30, v72\n"
      "v_accvgpr_write_b32 a31, v74\n"
      "v_mfma_f32_16x16x32_bf16 v[68:71], v[98:101], a[40:43], v[16:19]\n"
      "v_cvt_pk_bf16_f32 v84, v84, v85\n"
      "v_cvt_pk_bf16_f32 v86, v86, v87\n"
      "v_cvt_pk_bf16_f32 v88, v88, v89\n"
      "v_mfma_f32_16x16x32_bf16 v[72:75], v[96:99], a[40:43], v[16:19]\n"
      "v_cvt_pk_bf16_f32 v90, v90, v91\n"
      "v_accvgpr_write_b32 a32, v84\n"
      "v_accvgpr_write_b32 a33, v86\n"
      "v_mfma_f32_16x16x32_bf16 v[36:39], v[12:15], a[28:31], v[36:39]\n"
      "v_accvgpr_write_b32 a34, v88\n"
      "v_accvgpr_write_b32 a35, v90\n"
      "v_mfma_f32_16x16x32_bf16 v[84:87], v[98:101], a[44:47], v[20:23]\n"
      "v_cvt_pk_bf16_f32 v48, v48, v49\n"
      "v_cvt_pk_bf16_f32 v50, v50, v51\n"
      "v_cvt_pk_bf16_f32 v52, v52, v53\n"
      "v_mfma_f32_16x16x32_bf16 v[88:91], v[96:99], a[44:47], v[20:23]\n"
      "v_cvt_pk_bf16_f32 v54, v54, v55\n"
      "v_accvgpr_write_b32 a36, v48\n"
      "v_accvgpr_write_b32 a37, v50\n"
      "v_mfma_f32_16x16x32_bf16 v[32:35], v[12:15], a[32:35], v[32:35]\n"
      "v_accvgpr_write_b32 a38, v52\n"
      "v_accvgpr_write_b32 a39, v54\n"
      "v_mfma_f32_16x16x32_bf16 v[48:51], v[98:101], a[48:51], v[16:19]\n"
      "v_cvt_pk_bf16_f32 v68, v68, v69\n"
      "v_cvt_pk_bf16_f32 v70, v70, v71\n"
      "v_cvt_pk_bf16_f32 v72, v72, v73\n"
      "v_mfma_f32_16x16x32_bf16 v[52:55], v[96:99], a[48:51], v[16:19]\n"
      "v_cvt_pk_bf16_f32 v74, v74, v75\n"
      "v_accvgpr_write_b32 a40, v68\n"
      "v_accvgpr_write_b32 a41, v70\n"
      "v_mfma_f32_16x16x32_bf16 v[36:39], v[12:15], a[36:39], v[36:39]\n"
      "v_accvgpr_write_b32 a42, v72\n"
      "v_accvgpr_write_b32 a43, v74\n"
      "v_mfma_f32_16x16x32_bf16 v[68:71], v[98:101], a[52:55], v[20:23]\n"
      "v_cvt_pk_bf16_f32 v84, v84, v85\n"
      "v_cvt_pk_bf16_f32 v86, v86, v87\n"
      "v_cvt_pk_bf16_f32 v88, v88, v89\n"
      "v_mfma_f32_16x16x32_bf16 v[72:75], v[96:99], a[52:55], v[20:23]\n"
      "v_cvt_pk_bf16_f32 v90, v90, v91\n"
      "v_accvgpr_write_b32 a44, v84\n"
      "v_accvgpr_write_b32 a45, v86\n"
      "v_mfma_f32_16x16x32_bf16 v[32:35], v[12:15], a[40:43], v[32:35]\n"
      "v_accvgpr_write_b32 a46, v88\n"
      "v_accvgpr_write_b32 a47, v90\n"
      "v_mfma_f32_16x16x32_bf16 v[84:87], v[98:101], a[56:59], v[16:19]\n"
      "v_cvt_pk_bf16_f32 v48, v48, v49\n"
      "v_cvt_pk_bf16_f32 v50, v50, v51\n"
      "v_cvt_pk_bf16_f32 v52, v52, v53\n"
      "v_mfma_f32_16x16x32_bf16 v[88:91], v[96:99], a[56:59], v[16:19]\n"
      "v_cvt_pk_bf16_f32 v54, v54, v55\n"
      "v_accvgpr_write_b32 a48, v48\n"
      "v_accvgpr_write_b32 a49, v50\n"
      "v_mfma_f32_16x16x32_bf16 v[36:39], v[12:15], a[44:47], v[36:39]\n"
      "v_accvgpr_write_b32 a50, v52\n"
      "v_accvgpr_write_b32 a51, v54\n"
      "s_waitcnt vmcnt(12) lgkmcnt(0)\n"
      "s_barrier\n"
      "ds_read_b64_tr_b16 v[152:153], v11 offset:49152\n"
      "ds_read_b64_tr_b16 v[154:155], v11 offset:50176\n"
      "ds_read_b64_tr_b16 v[156:157], v11 offset:49160\n"
      "ds_read_b64_tr_b16 v[158:159], v11 offset:50184\n"
      "ds_read_b64_tr_b16 v[160:161], v11 offset:57344\n"
      "ds_read_b64_tr_b16 v[162:163], v11 offset:58368\n"
      "ds_read_b64_tr_b16 v[164:165], v11 offset:57352\n"
      "ds_read_b64_tr_b16 v[166:167], v11 offset:58376\n"
      "ds_read_b64_tr_b16 v[168:169], v66 offset:0\n"
      "ds_read_b64_tr_b16 v[170:171], v66 offset:1024\n"
      "ds_read_b64_tr_b16 v[172:173], v66 offset:8\n"
      "ds_read_b64_tr_b16 v[174:175], v66 offset:1032\n"
      "v_mfma_f32_16x16x32_bf16 v[24:27], v[12:15], v[128:131], v[24:27]\n"
      "v_mfma_f32_16x16x32_bf16 v[28:31], v[12:15], v[132:135], v[28:31]\n"
      "v_mfma_f32_16x16x32_bf16 v[24:27], v[12:15], v[136:139], v[24:27]\n"
      "v_mfma_f32_16x16x32_bf16 v[28:31], v[12:15], v[140:143], v[28:31]\n"
      "v_mfma_f32_16x16x32_bf16 v[24:27], v[12:15], v[144:147], v[24:27]\n"
      "v_mfma_f32_16x16x32_bf16 v[28:31], v[12:15], v[148:151], v[28:31]\n"
      "s_add_u32 m0, s73, 122880\n"
      "s_add_u32 s50, s78, 4423680\n"
      "buffer_load_dwordx4 v10, s[44:47], s50 offen lds\n"
      "s_add_u32 m0, s73, 123904\n"
      "s_add_u32 s50, s78, 4460544\n"
      "buffer_load_dwordx4 v10, s[44:47], s50 offen lds\n"
      "s_add_u32 m0, s73, 131072\n"
      "s_add_u32 s50, s78, 4718592\n"
      "buffer_load_dwordx4 v10, s[44:47], s50 offen lds\n"
      "s_add_u32 m0, s73, 132096\n"
      "s_add_u32 s50, s78, 4755456\n"
      "buffer_load_dwordx4 v10, s[44:47], s50 offen lds\n"
      "s_add_u32 m0, s73, 139264\n"
      "s_add_u32 s50, s78, 5013504\n"
      "buffer_load_dwordx4 v10, s[44:47], s50 offen lds\n"
      "s_add_u32 m0, s73, 140288\n"
      "s_add_u32 s50, s78, 5050368\n"
      "buffer_load_dwordx4 v10, s[44:47], s50 offen lds\n"
      "v_mfma_f32_16x16x32_bf16 v[48:51], v[98:101], a[60:63], v[20:23]\n"
      "v_cvt_pk_bf16_f32 v68, v68, v69\n"
      "v_cvt_pk_bf16_f32 v70, v70, v71\n"
      "v_cvt_pk_bf16_f32 v72, v72, v73\n"
      "v_mfma_f32_16x16x32_bf16 v[52:55], v[96:99], a[60:63], v[20:23]\n"
      "v_cvt_pk_bf16_f32 v74, v74, v75\n"
      "v_accvgpr_write_b32 a52, v68\n"
      "v_accvgpr_write_b32 a53, v70\n"
      "v_mfma_f32_16x16x32_bf16 v[32:35], v[12:15], a[48:51], v[32:35]\n"
      "v_accvgpr_write_b32 a54, v72\n"
      "v_accvgpr_write_b32 a55, v74\n"
      "v_mfma_f32_16x16x32_bf16 v[68:71], v[98:101], a[64:67], v[16:19]\n"
      "v_cvt_pk_bf16_f32 v84, v84, v85\n"
      "v_cvt_pk_bf16_f32 v86, v86, v87\n"
      "v_cvt_pk_bf16_f32 v88, v88, v89\n"
      "v_mfma_f32_16x16x32_bf16 v[72:75], v[96:99], a[64:67], v[16:19]\n"
      "v_cvt_pk_bf16_f32 v90, v90, v91\n"
      "v_accvgpr_write_b32 a56, v84\n"
      "v_accvgpr_write_b32 a57, v86\n"
      "v_mfma_f32_16x16x32_bf16 v[36:39], v[12:15], a[52:55], v[36:39]\n"
      "v_accvgpr_write_b32 a58, v88\n"
      "v_accvgpr_write_b32 a59, v90\n"
      "v_mfma_f32_16x16x32_bf16 v[84:87], v[98:101], a[68:71], v[20:23]\n"
      "v_cvt_pk_bf16_f32 v48, v48, v49\n"
      "v_cvt_pk_bf16_f32 v50, v50, v51\n"
      "v_cvt_pk_bf16_f32 v52, v52, v53\n"
      "v_mfma_f32_16x16x32_bf16 v[88:91], v[96:99], a[68:71], v[20:23]\n"
      "v_cvt_pk_bf16_f32 v54, v54, v55\n"
      "v_accvgpr_write_b32 a60, v48\n"
      "v_accvgpr_write_b32 a61, v50\n"
      "v_mfma_f32_16x16x32_bf16 v[32:35], v[12:15], a[56:59], v[32:35]\n"
      "v_accvgpr_write_b32 a62, v52\n"
      "v_accvgpr_write_b32 a63, v54\n"
      "v_mfma_f32_16x16x32_bf16 v[48:51], v[98:101], a[72:75], v[16:19]\n"
      "v_cvt_pk_bf16_f32 v68, v68, v69\n"
      "v_cvt_pk_bf16_f32 v70, v70, v71\n"
      "v_cvt_pk_bf16_f32 v72, v72, v73\n"
      "v_mfma_f32_16x16x32_bf16 v[52:55], v[96:99], a[72:75], v[16:19]\n"
      "v_cvt_pk_bf16_f32 v74, v74, v75\n"
      "v_accvgpr_write_b32 a64, v68\n"
      "v_accvgpr_write_b32 a65, v70\n"
      "v_mfma_f32_16x16x32_bf16 v[36:39], v[12:15], a[60:63], v[36:39]\n"
      "v_accvgpr_write_b32 a66, v72\n"
      "v_accvgpr_write_b32 a67, v74\n"
      "v_mfma_f32_16x16x32_bf16 v[68:71], v[98:101], a[76:79], v[20:23]\n"
      "v_cvt_pk_bf16_f32 v84, v84, v85\n"
      "v_cvt_pk_bf16_f32 v86, v86, v87\n"
      "v_cvt_pk_bf16_f32 v88, v88, v89\n"
      "v_mfma_f32_16x16x32_bf16 v[72:75], v[96:99], a[76:79], v[20:23]\n"
      "v_cvt_pk_bf16_f32 v90, v90, v91\n"
      "v_accvgpr_write_b32 a68, v84\n"
      "v_accvgpr_write_b32 a69, v86\n"
      "v_mfma_f32_16x16x32_bf16 v[32:35], v[12:15], a[64:67], v[32:35]\n"
      "v_accvgpr_write_b32 a70, v88\n"
      "v_accvgpr_write_b32 a71, v90\n"
      "v_mfma_f32_16x16x32_bf16 v[84:87], v[98:101], a[80:83], v[16:19]\n"
      "v_cvt_pk_bf16_f32 v48, v48, v49\n"
      "v_cvt_pk_bf16_f32 v50, v50, v51\n"
      "v_cvt_pk_bf16_f32 v52, v52, v53\n"
      "v_mfma_f32_16x16x32_bf16 v[88:91], v[96:99], a[80:83], v[16:19]\n"
      "v_cvt_pk_bf16_f32 v54, v54, v55\n"
      "v_accvgpr_write_b32 a72, v48\n"
      "v_accvgpr_write_b32 a73, v50\n"
      "v_mfma_f32_16x16x32_bf16 v[36:39], v[12:15], a[68:71], v[36:39]\n"
      "v_accvgpr_write_b32 a74, v52\n"
      "v_accvgpr_write_b32 a75, v54\n"
      "v_mfma_f32_16x16x32_bf16 v[48:51], v[98:101], a[84:87], v[20:23]\n"
      "v_cvt_pk_bf16_f32 v68, v68, v69\n"
      "v_cvt_pk_bf16_f32 v70, v70, v71\n"
      "v_cvt_pk_bf16_f32 v72, v72, v73\n"
      "v_mfma_f32_16x16x32_bf16 v[52:55], v[96:99], a[84:87], v[20:23]\n"
      "v_cvt_pk_bf16_f32 v74, v74, v75\n"
      "v_accvgpr_write_b32 a76, v68\n"
      "v_accvgpr_write_b32 a77, v70\n"
      "v_mfma_f32_16x16x32_bf16 v[32:35], v[12:15], a[72:75], v[32:35]\n"
      "v_accvgpr_write_b32 a78, v72\n"
      "v_accvgpr_write_b32 a79, v74\n"
      "v_mfma_f32_16x16x32_bf16 v[68:71], v[98:101], a[88:91], v[16:19]\n"
      "v_cvt_pk_bf16_f32 v84, v84, v85\n"
      "v_cvt_pk_bf16_f32 v86, v86, v87\n"
      "v_cvt_pk_bf16_f32 v88, v88, v89\n"
      "v_mfma_f32_16x16x32_bf16 v[72:75], v[96:99], a[88:91], v[16:19]\n"
      "v_cvt_pk_bf16_f32 v90, v90, v91\n"
      "v_accvgpr_write_b32 a80, v84\n"
      "v_accvgpr_write_b32 a81, v86\n"
      "v_mfma_f32_16x16x32_bf16 v[36:39], v[12:15], a[76:79], v[36:39]\n"
      "v_accvgpr_write_b32 a82, v88\n"
      "v_accvgpr_write_b32 a83, v90\n"
      "s_waitcnt vmcnt(12) lgkmcnt(0)\n"
      "s_barrier\n"
      "ds_read_b64_tr_b16 v[176:177], v66 offset:8192\n"
      "ds_read_b64_tr_b16 v[178:179], v66 offset:9216\n"
      "ds_read_b64_tr_b16 v[180:181], v66 offset:8200\n"
      "ds_read_b64_tr_b16 v[182:183], v66 offset:9224\n"
      "ds_read_b64_tr_b16 v[184:185], v66 offset:16384\n"
      "ds_read_b64_tr_b16 v[186:187], v66 offset:17408\n"
      "ds_read_b64_tr_b16 v[188:189], v66 offset:16392\n"
      "ds_read_b64_tr_b16 v[190:191], v66 offset:17416\n"
      "ds_read_b64_tr_b16 v[192:193], v66 offset:24576\n"
      "ds_read_b64_tr_b16 v[194:195], v66 offset:25600\n"
      "ds_read_b64_tr_b16 v[196:197], v66 offset:24584\n"
      "ds_read_b64_tr_b16 v[198:199], v66 offset:25608\n"
      "v_mfma_f32_16x16x32_bf16 v[24:27], v[12:15], v[152:155], v[24:27]\n"
      "v_mfma_f32_16x16x32_bf16 v[28:31], v[12:15], v[156:159], v[28:31]\n"
      "v_mfma_f32_16x16x32_bf16 v[24:27], v[12:15], v[160:163], v[24:27]\n"
      "v_mfma_f32_16x16x32_bf16 v[28:31], v[12:15], v[164:167], v[28:31]\n"
      "v_mfma_f32_16x16x32_bf16 v[24:27], v[12:15], v[168:171], v[24:27]\n"
      "v_mfma_f32_16x16x32_bf16 v[28:31], v[12:15], v[172:175], v[28:31]\n"
      "v_mfma_f32_16x16x32_bf16 v[84:87], v[98:101], a[92:95], v[20:23]\n"
      "v_cvt_pk_bf16_f32 v48, v48, v49\n"
      "v_cvt_pk_bf16_f32 v50, v50, v51\n"
      "v_cvt_pk_bf16_f32 v52, v52, v53\n"
      "v_mfma_f32_16x16x32_bf16 v[88:91], v[96:99], a[92:95], v[20:23]\n"
      "v_cvt_pk_bf16_f32 v54, v54, v55\n"
      "v_accvgpr_write_b32 a84, v48\n"
      "v_accvgpr_write_b32 a85, v50\n"
      "v_mfma_f32_16x16x32_bf16 v[32:35], v[12:15], a[80:83], v[32:35]\n"
      "v_accvgpr_write_b32 a86, v52\n"
      "v_accvgpr_write_b32 a87, v54\n"
      "v_mfma_f32_16x16x32_bf16 v[48:51], v[98:101], a[96:99], v[16:19]\n"
      "v_cvt_pk_bf16_f32 v68, v68, v69\n"
      "v_cvt_pk_bf16_f32 v70, v70, v71\n"
      "v_cvt_pk_bf16_f32 v72, v72, v73\n"
      "v_mfma_f32_16x16x32_bf16 v[52:55], v[96:99], a[96:99], v[16:19]\n"
      "v_cvt_pk_bf16_f32 v74, v74, v75\n"
      "v_accvgpr_write_b32 a88, v68\n"
      "v_accvgpr_write_b32 a89, v70\n"
      "v_mfma_f32_16x16x32_bf16 v[36:39], v[12:15], a[84:87], v[36:39]\n"
      "v_accvgpr_write_b32 a90, v72\n"
      "v_accvgpr_write_b32 a91, v74\n"
      "v_mfma_f32_16x16x32_bf16 v[68:71], v[98:101], a[100:103], v[20:23]\n"
      "v_cvt_pk_bf16_f32 v84, v84, v85\n"
      "v_cvt_pk_bf16_f32 v86, v86, v87\n"
      "v_cvt_pk_bf16_f32 v88, v88, v89\n"
      "v_mfma_f32_16x16x32_bf16 v[72:75], v[96:99], a[100:103], v[20:23]\n"
      "v_cvt_pk_bf16_f32 v90, v90, v91\n"
      "v_accvgpr_write_b32 a92, v84\n"
      "v_accvgpr_write_b32 a93, v86\n"
      "v_mfma_f32_16x16x32_bf16 v[32:35], v[12:15], a[88:91], v[32:35]\n"
      "v_accvgpr_write_b32 a94, v88\n"
      "v_accvgpr_write_b32 a95, v90\n"
      "v_mfma_f32_16x16x32_bf16 v[84:87], v[98:101], a[104:107], v[16:19]\n"
      "v_cvt_pk_bf16_f32 v48, v48, v49\n"
      "v_cvt_pk_bf16_f32 v50, v50, v51\n"
      "v_cvt_pk_bf16_f32 v52, v52, v53\n"
      "v_mfma_f32_16x16x32_bf16 v[88:91], v[96:99], a[104:107], v[16:19]\n"
      "v_cvt_pk_bf16_f32 v54, v54, v55\n"
      "v_accvgpr_write_b32 a96, v48\n"
      "v_accvgpr_write_b32 a97, v50\n"
      "v_mfma_f32_16x16x32_bf16 v[36:39], v[12:15], a[92:95], v[36:39]\n"
      "v_accvgpr_write_b32 a98, v52\n"
      "v_accvgpr_write_b32 a99, v54\n"
      "v_mfma_f32_16x16x32_bf16 v[48:51], v[98:101], a[108:111], v[20:23]\n"
      "v_cvt_pk_bf16_f32 v68, v68, v69\n"
      "v_cvt_pk_bf16_f32 v70, v70, v71\n"
      "v_cvt_pk_bf16_f32 v72, v72, v73\n"
      "v_mfma_f32_16x16x32_bf16 v[52:55], v[96:99], a[108:111], v[20:23]\n"
      "v_cvt_pk_bf16_f32 v74, v74, v75\n"
      "v_accvgpr_write_b32 a100, v68\n"
      "v_accvgpr_write_b32 a101, v70\n"
      "v_mfma_f32_16x16x32_bf16 v[32:35], v[12:15], a[96:99], v[32:35]\n"
      "v_accvgpr_write_b32 a102, v72\n"
      "v_accvgpr_write_b32 a103, v74\n"
      "s_nop 1\n"
      "v_mfma_f32_16x16x32_bf16 v[36:39], v[12:15], a[100:103], v[36:39]\n"
      "v_cvt_pk_bf16_f32 v84, v84, v85\n"
      "v_cvt_pk_bf16_f32 v86, v86, v87\n"
      "v_cvt_pk_bf16_f32 v88, v88, v89\n"
      "v_cvt_pk_bf16_f32 v90, v90, v91\n"
      "v_accvgpr_write_b32 a104, v84\n"
      "v_accvgpr_write_b32 a105, v86\n"
      "v_accvgpr_write_b32 a106, v88\n"
      "v_accvgpr_write_b32 a107, v90\n"
      "s_nop 1\n"
      "v_mfma_f32_16x16x32_bf16 v[32:35], v[12:15], a[104:107], v[32:35]\n"
      "v_cvt_pk_bf16_f32 v48, v48, v49\n"
      "v_cvt_pk_bf16_f32 v50, v50, v51\n"
      "v_cvt_pk_bf16_f32 v52, v52, v53\n"
      "v_cvt_pk_bf16_f32 v54, v54, v55\n"
      "v_accvgpr_write_b32 a108, v48\n"
      "v_accvgpr_write_b32 a109, v50\n"
      "v_accvgpr_write_b32 a110, v52\n"
      "v_accvgpr_write_b32 a111, v54\n"
      "s_nop 1\n"
      "v_mfma_f32_16x16x32_bf16 v[36:39], v[12:15], a[108:111], v[36:39]\n"
      "s_waitcnt vmcnt(6) lgkmcnt(0)\n"
      "s_barrier\n"
      "ds_read_b64_tr_b16 v[200:201], v66 offset:32768\n"
      "ds_read_b64_tr_b16 v[202:203], v66 offset:33792\n"
      "ds_read_b64_tr_b16 v[204:205], v66 offset:32776\n"
      "ds_read_b64_tr_b16 v[206:207], v66 offset:33800\n"
      "ds_read_b64_tr_b16 v[208:209], v66 offset:40960\n"
      "ds_read_b64_tr_b16 v[210:211], v66 offset:41984\n"
      "ds_read_b64_tr_b16 v[212:213], v66 offset:40968\n"
      "ds_read_b64_tr_b16 v[214:215], v66 offset:41992\n"
      "ds_read_b64_tr_b16 v[216:217], v66 offset:49152\n"
      "ds_read_b64_tr_b16 v[218:219], v66 offset:50176\n"
      "ds_read_b64_tr_b16 v[220:221], v66 offset:49160\n"
      "ds_read_b64_tr_b16 v[222:223], v66 offset:50184\n"
      "v_mfma_f32_16x16x32_bf16 v[24:27], v[12:15], v[176:179], v[24:27]\n"
      "v_mfma_f32_16x16x32_bf16 v[28:31], v[12:15], v[180:183], v[28:31]\n"
      "v_mfma_f32_16x16x32_bf16 v[24:27], v[12:15], v[184:187], v[24:27]\n"
      "v_mfma_f32_16x16x32_bf16 v[28:31], v[12:15], v[188:191], v[28:31]\n"
      "v_mfma_f32_16x16x32_bf16 v[24:27], v[12:15], v[192:195], v[24:27]\n"
      "v_mfma_f32_16x16x32_bf16 v[28:31], v[12:15], v[196:199], v[28:31]\n"
      "v_mfma_f32_16x16x32_bf16 v[16:19], v[12:15], a[112:115], v[32:35]\n"
      "v_mfma_f32_16x16x32_bf16 v[20:23], v[12:15], a[116:119], v[36:39]\n"
      "v_mfma_f32_16x16x32_bf16 v[16:19], v[12:15], a[120:123], v[16:19]\n"
      "v_mfma_f32_16x16x32_bf16 v[20:23], v[12:15], a[124:127], v[20:23]\n"
      "v_mfma_f32_16x16x32_bf16 v[16:19], v[12:15], a[128:131], v[16:19]\n"
      "v_mfma_f32_16x16x32_bf16 v[20:23], v[12:15], a[132:135], v[20:23]\n"
      "s_nop 6\n"
      "v_mul_f32 v16, 0xbc000000, v16\n"
      "v_mul_f32 v17, 0xbc000000, v17\n"
      "v_mul_f32 v18, 0xbc000000, v18\n"
      "v_mul_f32 v19, 0xbc000000, v19\n"
      "v_mul_f32 v20, 0xbc000000, v20\n"
      "v_mul_f32 v21, 0xbc000000, v21\n"
      "v_mul_f32 v22, 0xbc000000, v22\n"
      "v_mul_f32 v23, 0xbc000000, v23\n"
      "v_mfma_f32_16x16x32_bf16 v[68:71], v[98:101], a[112:115], v[16:19]\n"
      "v_mfma_f32_16x16x32_bf16 v[72:75], v[96:99], a[112:115], v[16:19]\n"
      "v_mfma_f32_16x16x32_bf16 v[84:87], v[98:101], a[116:119], v[20:23]\n"
      "v_mfma_f32_16x16x32_bf16 v[88:91], v[96:99], a[116:119], v[20:23]\n"
      "v_mfma_f32_16x16x32_bf16 v[48:51], v[98:101], a[120:123], v[16:19]\n"
      "s_nop 3\n"
      "v_cvt_pk_bf16_f32 v68, v68, v69\n"
      "v_cvt_pk_bf16_f32 v70, v70, v71\n"
      "v_cvt_pk_bf16_f32 v72, v72, v73\n"
      "v_mfma_f32_16x16x32_bf16 v[52:55], v[96:99], a[120:123], v[16:19]\n"
      "v_cvt_pk_bf16_f32 v74, v74, v75\n"
      "v_accvgpr_write_b32 a112, v68\n"
      "v_accvgpr_write_b32 a113, v70\n"
      "v_accvgpr_write_b32 a114, v72\n"
      "v_accvgpr_write_b32 a115, v74\n"
      "v_mfma_f32_16x16x32_bf16 v[68:71], v[98:101], a[124:127], v[20:23]\n"
      "v_cvt_pk_bf16_f32 v84, v84, v85\n"
      "v_cvt_pk_bf16_f32 v86, v86, v87\n"
      "v_cvt_pk_bf16_f32 v88, v88, v89\n"
      "v_mfma_f32_16x16x32_bf16 v[72:75], v[96:99], a[124:127], v[20:23]\n"
      "v_cvt_pk_bf16_f32 v90, v90, v91\n"
      "v_accvgpr_write_b32 a116, v84\n"
      "v_accvgpr_write_b32 a117, v86\n"
      "v_mfma_f32_16x16x32_bf16 v[32:35], v[12:15], a[112:115], v[32:35]\n"
      "v_accvgpr_write_b32 a118, v88\n"
      "v_accvgpr_write_b32 a119, v90\n"
      "v_mfma_f32_16x16x32_bf16 v[84:87], v[98:101], a[128:131], v[16:19]\n"
      "v_cvt_pk_bf16_f32 v48, v48, v49\n"
      "v_cvt_pk_bf16_f32 v50, v50, v51\n"
      "v_cvt_pk_bf16_f32 v52, v52, v53\n"
      "v_mfma_f32_16x16x32_bf16 v[88:91], v[96:99], a[128:131], v[16:19]\n"
      "v_cvt_pk_bf16_f32 v54, v54, v55\n"
      "v_accvgpr_write_b32 a120, v48\n"
      "v_accvgpr_write_b32 a121, v50\n"
      "v_mfma_f32_16x16x32_bf16 v[36:39], v[12:15], a[116:119], v[36:39]\n"
      "v_accvgpr_write_b32 a122, v52\n"
      "v_accvgpr_write_b32 a123, v54\n"
      "v_mfma_f32_16x16x32_bf16 v[48:51], v[98:101], a[132:135], v[20:23]\n"
      "v_cvt_pk_bf16_f32 v68, v68, v69\n"
      "v_cvt_pk_bf16_f32 v70, v70, v71\n"
      "v_cvt_pk_bf16_f32 v72, v72, v73\n"
      "v_mfma_f32_16x16x32_bf16 v[52:55], v[96:99], a[132:135], v[20:23]\n"
      "v_cvt_pk_bf16_f32 v74, v74, v75\n"
      "v_accvgpr_write_b32 a124, v68\n"
      "v_accvgpr_write_b32 a125, v70\n"
      "v_mfma_f32_16x16x32_bf16 v[32:35], v[12:15], a[120:123], v[32:35]\n"
      "v_accvgpr_write_b32 a126, v72\n"
      "v_accvgpr_write_b32 a127, v74\n"
      "s_nop 1\n"
      "v_mfma_f32_16x16x32_bf16 v[36:39], v[12:15], a[124:127], v[36:39]\n"
      "v_cvt_pk_bf16_f32 v84, v84, v85\n"
      "v_cvt_pk_bf16_f32 v86, v86, v87\n"
      "v_cvt_pk_bf16_f32 v88, v88, v89\n"
      "v_cvt_pk_bf16_f32 v90, v90, v91\n"
      "v_accvgpr_write_b32 a128, v84\n"
      "v_accvgpr_write_b32 a129, v86\n"
      "v_accvgpr_write_b32 a130, v88\n"
      "v_accvgpr_write_b32 a131, v90\n"
      "s_waitcnt vmcnt(0) lgkmcnt(0)\n"
      "s_barrier\n"
      "ds_read_b64_tr_b16 v[224:225], v66 offset:57344\n"
      "ds_read_b64_tr_b16 v[226:227], v66 offset:58368\n"
      "ds_read_b64_tr_b16 v[228:229], v66 offset:57352\n"
      "ds_read_b64_tr_b16 v[230:231], v66 offset:58376\n"
      "ds_read_b64_tr_b16 v[232:233], v67 offset:0\n"
      "ds_read_b64_tr_b16 v[234:235], v67 offset:1024\n"
      "ds_read_b64_tr_b16 v[236:237], v67 offset:8\n"
      "ds_read_b64_tr_b16 v[238:239], v67 offset:1032\n"
      "ds_read_b64_tr_b16 v[240:241], v67 offset:8192\n"
      "ds_read_b64_tr_b16 v[242:243], v67 offset:9216\n"
      "ds_read_b64_tr_b16 v[244:245], v67 offset:8200\n"
      "ds_read_b64_tr_b16 v[246:247], v67 offset:9224\n"
      "v_mfma_f32_16x16x32_bf16 v[24:27], v[12:15], v[200:203], v[24:27]\n"
      "v_mfma_f32_16x16x32_bf16 v[28:31], v[12:15], v[204:207], v[28:31]\n"
      "v_mfma_f32_16x16x32_bf16 v[24:27], v[12:15], v[208:211], v[24:27]\n"
      "v_mfma_f32_16x16x32_bf16 v[28:31], v[12:15], v[212:215], v[28:31]\n"
      "v_mfma_f32_16x16x32_bf16 v[24:27], v[12:15], v[216:219], v[24:27]\n"
      "v_mfma_f32_16x16x32_bf16 v[28:31], v[12:15], v[220:223], v[28:31]\n"
      "buffer_load_dwordx2 v[44:45], v5, s[36:39], 0 offen offset:8 sc1\n"
      "v_mfma_f32_16x16x32_bf16 v[32:35], v[12:15], a[128:131], v[32:35]\n"
      "v_cvt_pk_bf16_f32 v48, v48, v49\n"
      "v_cvt_pk_bf16_f32 v50, v50, v51\n"
      "v_cvt_pk_bf16_f32 v52, v52, v53\n"
      "v_cvt_pk_bf16_f32 v54, v54, v55\n"
      "v_accvgpr_write_b32 a132, v48\n"
      "v_accvgpr_write_b32 a133, v50\n"
      "v_accvgpr_write_b32 a134, v52\n"
      "v_accvgpr_write_b32 a135, v54\n"
      "s_nop 1\n"
      "v_mfma_f32_16x16x32_bf16 v[36:39], v[12:15], a[132:135], v[36:39]\n"
      "v_mul_f32 v16, 0xbd000000, v32\n"
      "v_mul_f32 v17, 0xbd000000, v33\n"
      "v_mul_f32 v18, 0xbd000000, v34\n"
      "v_mul_f32 v19, 0xbd000000, v35\n"
      "s_nop 3\n"
      "v_mul_f32 v20, 0xbd000000, v36\n"
      "v_mul_f32 v21, 0xbd000000, v37\n"
      "v_mul_f32 v22, 0xbd000000, v38\n"
      "v_mul_f32 v23, 0xbd000000, v39\n"
      "v_mfma_f32_16x16x32_bf16 v[68:71], v[98:101], a[136:139], v[16:19]\n"
      "v_mfma_f32_16x16x32_bf16 v[72:75], v[96:99], a[136:139], v[16:19]\n"
      "v_mfma_f32_16x16x32_bf16 v[84:87], v[98:101], a[140:143], v[20:23]\n"
      "v_mfma_f32_16x16x32_bf16 v[88:91], v[96:99], a[140:143], v[20:23]\n"
      "s_nop 4\n"
      "v_cvt_pk_bf16_f32 v68, v68, v69\n"
      "v_cvt_pk_bf16_f32 v70, v70, v71\n"
      "v_cvt_pk_bf16_f32 v72, v72, v73\n"
      "v_cvt_pk_bf16_f32 v74, v74, v75\n"
      "v_accvgpr_write_b32 a136, v68\n"
      "v_accvgpr_write_b32 a137, v70\n"
      "v_accvgpr_write_b32 a138, v72\n"
      "v_accvgpr_write_b32 a139, v74\n"
      "v_cvt_pk_bf16_f32 v84, v84, v85\n"
      "v_cvt_pk_bf16_f32 v86, v86, v87\n"
      "v_cvt_pk_bf16_f32 v88, v88, v89\n"
      "v_cvt_pk_bf16_f32 v90, v90, v91\n"
      "v_accvgpr_write_b32 a140, v84\n"
      "v_accvgpr_write_b32 a141, v86\n"
      "v_accvgpr_write_b32 a142, v88\n"
      "v_accvgpr_write_b32 a143, v90\n"
      "s_waitcnt lgkmcnt(0)\n"
      "s_barrier\n"
      "v_mfma_f32_16x16x32_bf16 v[24:27], v[12:15], v[224:227], v[24:27]\n"
      "v_mfma_f32_16x16x32_bf16 v[28:31], v[12:15], v[228:231], v[28:31]\n"
      "v_mfma_f32_16x16x32_bf16 v[24:27], v[12:15], v[232:235], v[24:27]\n"
      "v_mfma_f32_16x16x32_bf16 v[28:31], v[12:15], v[236:239], v[28:31]\n"
      "v_mfma_f32_16x16x32_bf16 v[24:27], v[12:15], v[240:243], v[24:27]\n"
      "v_mfma_f32_16x16x32_bf16 v[28:31], v[12:15], v[244:247], v[28:31]\n"
      "s_nop 6\n"
      "v_mul_f32 v24, 0xbae38e39, v24\n"
      "v_mul_f32 v25, 0xbae38e39, v25\n"
      "v_mul_f32 v26, 0xbae38e39, v26\n"
      "v_mul_f32 v27, 0xbae38e39, v27\n"
      "v_mul_f32 v28, 0xbae38e39, v28\n"
      "v_mul_f32 v29, 0xbae38e39, v29\n"
      "v_mul_f32 v30, 0xbae38e39, v30\n"
      "v_mul_f32 v31, 0xbae38e39, v31\n"
      "v_mfma_f32_16x16x32_bf16 v[32:35], v[12:15], v[240:243], 0\n"
      "v_mfma_f32_16x16x32_bf16 v[36:39], v[12:15], v[244:247], 0\n"
      "v_mfma_f32_16x16x32_bf16 v[48:51], v[98:101], v[104:107], v[24:27]\n"
      "v_mfma_f32_16x16x32_bf16 v[52:55], v[96:99], v[104:107], v[24:27]\n"
      "v_mfma_f32_16x16x32_bf16 v[68:71], v[98:101], v[108:111], v[28:31]\n"
      "v_mfma_f32_16x16x32_bf16 v[72:75], v[96:99], v[108:111], v[28:31]\n"
      "v_mfma_f32_16x16x32_bf16 v[84:87], v[98:101], v[112:115], v[24:27]\n"
      "s_nop 3\n"
      "v_cvt_pk_bf16_f32 v104, v48, v49\n"
      "v_cvt_pk_bf16_f32 v105, v50, v51\n"
      "v_mfma_f32_16x16x32_bf16 v[88:91], v[96:99], v[112:115], v[24:27]\n"
      "v_cvt_pk_bf16_f32 v106, v52, v53\n"
      "v_cvt_pk_bf16_f32 v107, v54, v55\n"
      "v_mfma_f32_16x16x32_bf16 v[48:51], v[98:101], v[116:119], v[28:31]\n"
      "v_cvt_pk_bf16_f32 v108, v68, v69\n"
      "v_cvt_pk_bf16_f32 v109, v70, v71\n"
      "v_mfma_f32_16x16x32_bf16 v[52:55], v[96:99], v[116:119], v[28:31]\n"
      "v_cvt_pk_bf16_f32 v110, v72, v73\n"
      "v_cvt_pk_bf16_f32 v111, v74, v75\n"
      "v_mfma_f32_16x16x32_bf16 v[32:35], v[12:15], v[104:107], v[32:35]\n"
      "v_mfma_f32_16x16x32_bf16 v[68:71], v[98:101], v[120:123], v[24:27]\n"
      "v_cvt_pk_bf16_f32 v112, v84, v85\n"
      "v_cvt_pk_bf16_f32 v113, v86, v87\n"
      "v_mfma_f32_16x16x32_bf16 v[72:75], v[96:99], v[120:123], v[24:27]\n"
      "v_cvt_pk_bf16_f32 v114, v88, v89\n"
      "v_cvt_pk_bf16_f32 v115, v90, v91\n"
      "v_mfma_f32_16x16x32_bf16 v[36:39], v[12:15], v[108:111], v[36:39]\n"
      "v_mfma_f32_16x16x32_bf16 v[84:87], v[98:101], v[124:127], v[28:31]\n"
      "v_cvt_pk_bf16_f32 v116, v48, v49\n"
      "v_cvt_pk_bf16_f32 v117, v50, v51\n"
      "v_mfma_f32_16x16x32_bf16 v[88:91], v[96:99], v[124:127], v[28:31]\n"
      "v_cvt_pk_bf16_f32 v118, v52, v53\n"
      "v_cvt_pk_bf16_f32 v119, v54, v55\n"
      "v_mfma_f32_16x16x32_bf16 v[32:35], v[12:15], v[112:115], v[32:35]\n"
      "v_mfma_f32_16x16x32_bf16 v[48:51], v[98:101], v[128:131], v[24:27]\n"
      "v_cvt_pk_bf16_f32 v120, v68, v69\n"
      "v_cvt_pk_bf16_f32 v121, v70, v71\n"
      "v_mfma_f32_16x16x32_bf16 v[52:55], v[96:99], v[128:131], v[24:27]\n"
      "v_cvt_pk_bf16_f32 v122, v72, v73\n"
      "v_cvt_pk_bf16_f32 v123, v74, v75\n"
      "v_mfma_f32_16x16x32_bf16 v[36:39], v[12:15], v[116:119], v[36:39]\n"
      "v_mfma_f32_16x16x32_bf16 v[68:71], v[98:101], v[132:135], v[28:31]\n"
      "v_cvt_pk_bf16_f32 v124, v84, v85\n"
      "v_cvt_pk_bf16_f32 v125, v86, v87\n"
      "v_mfma_f32_16x16x32_bf16 v[72:75], v[96:99], v[132:135], v[28:31]\n"
      "v_cvt_pk_bf16_f32 v126, v88, v89\n"
      "v_cvt_pk_bf16_f32 v127, v90, v91\n"
      "v_mfma_f32_16x16x32_bf16 v[32:35], v[12:15], v[120:123], v[32:35]\n"
      "v_mfma_f32_16x16x32_bf16 v[84:87], v[98:101], v[136:139], v[24:27]\n"
      "v_cvt_pk_bf16_f32 v128, v48, v49\n"
      "v_cvt_pk_bf16_f32 v129, v50, v51\n"
      "v_mfma_f32_16x16x32_bf16 v[88:91], v[96:99], v[136:139], v[24:27]\n"
      "v_cvt_pk_bf16_f32 v130, v52, v53\n"
      "v_cvt_pk_bf16_f32 v131, v54, v55\n"
      "v_mfma_f32_16x16x32_bf16 v[36:39], v[12:15], v[124:127], v[36:39]\n"
      "v_mfma_f32_16x16x32_bf16 v[48:51], v[98:101], v[140:143], v[28:31]\n"
      "v_cvt_pk_bf16_f32 v132, v68, v69\n"
      "v_cvt_pk_bf16_f32 v133, v70, v71\n"
      "v_mfma_f32_16x16x32_bf16 v[52:55], v[96:99], v[140:143], v[28:31]\n"
      "v_cvt_pk_bf16_f32 v134, v72, v73\n"
      "v_cvt_pk_bf16_f32 v135, v74, v75\n"
      "v_mfma_f32_16x16x32_bf16 v[32:35], v[12:15], v[128:131], v[32:35]\n"
      "v_mfma_f32_16x16x32_bf16 v[68:71], v[98:101], v[144:147], v[24:27]\n"
      "v_cvt_pk_bf16_f32 v136, v84, v85\n"
      "v_cvt_pk_bf16_f32 v137, v86, v87\n"
      "v_mfma_f32_16x16x32_bf16 v[72:75], v[96:99], v[144:147], v[24:27]\n"
      "v_cvt_pk_bf16_f32 v138, v88, v89\n"
      "v_cvt_pk_bf16_f32 v139, v90, v91\n"
      "v_mfma_f32_16x16x32_bf16 v[36:39], v[12:15], v[132:135], v[36:39]\n"
      "v_mfma_f32_16x16x32_bf16 v[84:87], v[98:101], v[148:151], v[28:31]\n"
      "v_cvt_pk_bf16_f32 v140, v48, v49\n"
      "v_cvt_pk_bf16_f32 v141, v50, v51\n"
      "v_mfma_f32_16x16x32_bf16 v[88:91], v[96:99], v[148:151], v[28:31]\n"
      "v_cvt_pk_bf16_f32 v142, v52, v53\n"
      "v_cvt_pk_bf16_f32 v143, v54, v55\n"
      "v_mfma_f32_16x16x32_bf16 v[32:35], v[12:15], v[136:139], v[32:35]\n"
      "v_mfma_f32_16x16x32_bf16 v[48:51], v[98:101], v[152:155], v[24:27]\n"
      "v_cvt_pk_bf16_f32 v144, v68, v69\n"
      "v_cvt_pk_bf16_f32 v145, v70, v71\n"
      "v_mfma_f32_16x16x32_bf16 v[52:55], v[96:99], v[152:155], v[24:27]\n"
      "v_cvt_pk_bf16_f32 v146, v72, v73\n"
      "v_cvt_pk_bf16_f32 v147, v74, v75\n"
      "v_mfma_f32_16x16x32_bf16 v[36:39], v[12:15], v[140:143], v[36:39]\n"
      "v_mfma_f32_16x16x32_bf16 v[68:71], v[98:101], v[156:159], v[28:31]\n"
      "v_cvt_pk_bf16_f32 v148, v84, v85\n"
      "v_cvt_pk_bf16_f32 v149, v86, v87\n"
      "v_mfma_f32_16x16x32_bf16 v[72:75], v[96:99], v[156:159], v[28:31]\n"
      "v_cvt_pk_bf16_f32 v150, v88, v89\n"
      "v_cvt_pk_bf16_f32 v151, v90, v91\n"
      "v_mfma_f32_16x16x32_bf16 v[32:35], v[12:15], v[144:147], v[32:35]\n"
      "v_mfma_f32_16x16x32_bf16 v[84:87], v[98:101], v[160:163], v[24:27]\n"
      "v_cvt_pk_bf16_f32 v152, v48, v49\n"
      "v_cvt_pk_bf16_f32 v153, v50, v51\n"
      "v_mfma_f32_16x16x32_bf16 v[88:91], v[96:99], v[160:163], v[24:27]\n"
      "v_cvt_pk_bf16_f32 v154, v52, v53\n"
      "v_cvt_pk_bf16_f32 v155, v54, v55\n"
      "v_mfma_f32_16x16x32_bf16 v[36:39], v[12:15], v[148:151], v[36:39]\n"
      "v_mfma_f32_16x16x32_bf16 v[48:51], v[98:101], v[164:167], v[28:31]\n"
      "v_cvt_pk_bf16_f32 v156, v68, v69\n"
      "v_cvt_pk_bf16_f32 v157, v70, v71\n"
      "v_mfma_f32_16x16x32_bf16 v[52:55], v[96:99], v[164:167], v[28:31]\n"
      "v_cvt_pk_bf16_f32 v158, v72, v73\n"
      "v_cvt_pk_bf16_f32 v159, v74, v75\n"
      "v_mfma_f32_16x16x32_bf16 v[32:35], v[12:15], v[152:155], v[32:35]\n"
      "v_mfma_f32_16x16x32_bf16 v[68:71], v[98:101], v[168:171], v[24:27]\n"
      "v_cvt_pk_bf16_f32 v160, v84, v85\n"
      "v_cvt_pk_bf16_f32 v161, v86, v87\n"
      "v_mfma_f32_16x16x32_bf16 v[72:75], v[96:99], v[168:171], v[24:27]\n"
      "v_cvt_pk_bf16_f32 v162, v88, v89\n"
      "v_cvt_pk_bf16_f32 v163, v90, v91\n"
      "v_mfma_f32_16x16x32_bf16 v[36:39], v[12:15], v[156:159], v[36:39]\n"
      "v_mfma_f32_16x16x32_bf16 v[84:87], v[98:101], v[172:175], v[28:31]\n"
      "v_cvt_pk_bf16_f32 v164, v48, v49\n"
      "v_cvt_pk_bf16_f32 v165, v50, v51\n"
      "v_mfma_f32_16x16x32_bf16 v[88:91], v[96:99], v[172:175], v[28:31]\n"
      "v_cvt_pk_bf16_f32 v166, v52, v53\n"
      "v_cvt_pk_bf16_f32 v167, v54, v55\n"
      "v_mfma_f32_16x16x32_bf16 v[32:35], v[12:15], v[160:163], v[32:35]\n"
      "v_mfma_f32_16x16x32_bf16 v[48:51], v[98:101], v[176:179], v[24:27]\n"
      "v_cvt_pk_bf16_f32 v168, v68, v69\n"
      "v_cvt_pk_bf16_f32 v169, v70, v71\n"
      "v_mfma_f32_16x16x32_bf16 v[52:55], v[96:99], v[176:179], v[24:27]\n"
      "v_cvt_pk_bf16_f32 v170, v72, v73\n"
      "v_cvt_pk_bf16_f32 v171, v74, v75\n"
      "v_mfma_f32_16x16x32_bf16 v[36:39], v[12:15], v[164:167], v[36:39]\n"
      "v_mfma_f32_16x16x32_bf16 v[68:71], v[98:101], v[180:183], v[28:31]\n"
      "v_cvt_pk_bf16_f32 v172, v84, v85\n"
      "v_cvt_pk_bf16_f32 v173, v86, v87\n"
      "v_mfma_f32_16x16x32_bf16 v[72:75], v[96:99], v[180:183], v[28:31]\n"
      "v_cvt_pk_bf16_f32 v174, v88, v89\n"
      "v_cvt_pk_bf16_f32 v175, v90, v91\n"
      "v_mfma_f32_16x16x32_bf16 v[32:35], v[12:15], v[168:171], v[32:35]\n"
      "v_mfma_f32_16x16x32_bf16 v[84:87], v[98:101], v[184:187], v[24:27]\n"
      "v_cvt_pk_bf16_f32 v176, v48, v49\n"
      "v_cvt_pk_bf16_f32 v177, v50, v51\n"
      "v_mfma_f32_16x16x32_bf16 v[88:91], v[96:99], v[184:187], v[24:27]\n"
      "v_cvt_pk_bf16_f32 v178, v52, v53\n"
      "v_cvt_pk_bf16_f32 v179, v54, v55\n"
      "v_mfma_f32_16x16x32_bf16 v[36:39], v[12:15], v[172:175], v[36:39]\n"
      "v_mfma_f32_16x16x32_bf16 v[48:51], v[98:101], v[188:191], v[28:31]\n"
      "v_cvt_pk_bf16_f32 v180, v68, v69\n"
      "v_cvt_pk_bf16_f32 v181, v70, v71\n"
      "v_mfma_f32_16x16x32_bf16 v[52:55], v[96:99], v[188:191], v[28:31]\n"
      "v_cvt_pk_bf16_f32 v182, v72, v73\n"
      "v_cvt_pk_bf16_f32 v183, v74, v75\n"
      "v_mfma_f32_16x16x32_bf16 v[32:35], v[12:15], v[176:179], v[32:35]\n"
      "v_mfma_f32_16x16x32_bf16 v[68:71], v[98:101], v[192:195], v[24:27]\n"
      "v_cvt_pk_bf16_f32 v184, v84, v85\n"
      "v_cvt_pk_bf16_f32 v185, v86, v87\n"
      "v_mfma_f32_16x16x32_bf16 v[72:75], v[96:99], v[192:195], v[24:27]\n"
      "v_cvt_pk_bf16_f32 v186, v88, v89\n"
      "v_cvt_pk_bf16_f32 v187, v90, v91\n"
      "v_mfma_f32_16x16x32_bf16 v[36:39], v[12:15], v[180:183], v[36:39]\n"
      "v_mfma_f32_16x16x32_bf16 v[84:87], v[98:101], v[196:199], v[28:31]\n"
      "v_cvt_pk_bf16_f32 v188, v48, v49\n"
      "v_cvt_pk_bf16_f32 v189, v50, v51\n"
      "v_mfma_f32_16x16x32_bf16 v[88:91], v[96:99], v[196:199], v[28:31]\n"
      "v_cvt_pk_bf16_f32 v190, v52, v53\n"
      "v_cvt_pk_bf16_f32 v191, v54, v55\n"
      "v_mfma_f32_16x16x32_bf16 v[32:35], v[12:15], v[184:187], v[32:35]\n"
      "v_mfma_f32_16x16x32_bf16 v[48:51], v[98:101], v[200:203], v[24:27]\n"
      "v_cvt_pk_bf16_f32 v192, v68, v69\n"
      "v_cvt_pk_bf16_f32 v193, v70, v71\n"
      "v_mfma_f32_16x16x32_bf16 v[52:55], v[96:99], v[200:203], v[24:27]\n"
      "v_cvt_pk_bf16_f32 v194, v72, v73\n"
      "v_cvt_pk_bf16_f32 v195, v74, v75\n"
      "v_mfma_f32_16x16x32_bf16 v[36:39], v[12:15], v[188:191], v[36:39]\n"
      "v_mfma_f32_16x16x32_bf16 v[68:71], v[98:101], v[204:207], v[28:31]\n"
      "v_cvt_pk_bf16_f32 v196, v84, v85\n"
      "v_cvt_pk_bf16_f32 v197, v86, v87\n"
      "v_mfma_f32_16x16x32_bf16 v[72:75], v[96:99], v[204:207], v[28:31]\n"
      "v_cvt_pk_bf16_f32 v198, v88, v89\n"
      "v_cvt_pk_bf16_f32 v199, v90, v91\n"
      "v_mfma_f32_16x16x32_bf16 v[32:35], v[12:15], v[192:195], v[32:35]\n"
      "v_mfma_f32_16x16x32_bf16 v[84:87], v[98:101], v[208:211], v[24:27]\n"
      "v_cvt_pk_bf16_f32 v200, v48, v49\n"
      "v_cvt_pk_bf16_f32 v201, v50, v51\n"
      "v_mfma_f32_16x16x32_bf16 v[88:91], v[96:99], v[208:211], v[24:27]\n"
      "v_cvt_pk_bf16_f32 v202, v52, v53\n"
      "v_cvt_pk_bf16_f32 v203, v54, v55\n"
      "v_mfma_f32_16x16x32_bf16 v[36:39], v[12:15], v[196:199], v[36:39]\n"
      "v_mfma_f32_16x16x32_bf16 v[48:51], v[98:101], v[212:215], v[28:31]\n"
      "v_cvt_pk_bf16_f32 v204, v68, v69\n"
      "v_cvt_pk_bf16_f32 v205, v70, v71\n"
      "v_mfma_f32_16x16x32_bf16 v[52:55], v[96:99], v[212:215], v[28:31]\n"
      "v_cvt_pk_bf16_f32 v206, v72, v73\n"
      "v_cvt_pk_bf16_f32 v207, v74, v75\n"
      "v_mfma_f32_16x16x32_bf16 v[32:35], v[12:15], v[200:203], v[32:35]\n"
      "s_nop 0\n"
      "v_mfma_f32_16x16x32_bf16 v[36:39], v[12:15], v[204:207], v[36:39]\n"
      "v_cvt_pk_bf16_f32 v208, v84, v85\n"
      "v_cvt_pk_bf16_f32 v209, v86, v87\n"
      "v_cvt_pk_bf16_f32 v210, v88, v89\n"
      "v_cvt_pk_bf16_f32 v211, v90, v91\n"
      "s_nop 1\n"
      "v_mfma_f32_16x16x32_bf16 v[32:35], v[12:15], v[208:211], v[32:35]\n"
      "v_cvt_pk_bf16_f32 v212, v48, v49\n"
      "v_cvt_pk_bf16_f32 v213, v50, v51\n"
      "v_cvt_pk_bf16_f32 v214, v52, v53\n"
      "v_cvt_pk_bf16_f32 v215, v54, v55\n"
      "s_nop 1\n"
      "v_mfma_f32_16x16x32_bf16 v[36:39], v[12:15], v[212:215], v[36:39]\n"
      "v_mfma_f32_16x16x32_bf16 v[24:27], v[12:15], v[216:219], v[32:35]\n"
      "v_mfma_f32_16x16x32_bf16 v[28:31], v[12:15], v[220:223], v[36:39]\n"
      "v_mfma_f32_16x16x32_bf16 v[24:27], v[12:15], v[224:227], v[24:27]\n"
      "v_mfma_f32_16x16x32_bf16 v[28:31], v[12:15], v[228:231], v[28:31]\n"
      "v_mfma_f32_16x16x32_bf16 v[24:27], v[12:15], v[232:235], v[24:27]\n"
      "v_mfma_f32_16x16x32_bf16 v[28:31], v[12:15], v[236:239], v[28:31]\n"
      "s_nop 6\n"
      "v_mul_f32 v24, 0xbc000000, v24\n"
      "v_mul_f32 v25, 0xbc000000, v25\n"
      "v_mul_f32 v26, 0xbc000000, v26\n"
      "v_mul_f32 v27, 0xbc000000, v27\n"
      "v_mul_f32 v28, 0xbc000000, v28\n"
      "v_mul_f32 v29, 0xbc000000, v29\n"
      "v_mul_f32 v30, 0xbc000000, v30\n"
      "v_mul_f32 v31, 0xbc000000, v31\n"
      "v_mfma_f32_16x16x32_bf16 v[68:71], v[98:101], v[216:219], v[24:27]\n"
      "v_mfma_f32_16x16x32_bf16 v[72:75], v[96:99], v[216:219], v[24:27]\n"
      "v_mfma_f32_16x16x32_bf16 v[84:87], v[98:101], v[220:223], v[28:31]\n"
      "v_mfma_f32_16x16x32_bf16 v[88:91], v[96:99], v[220:223], v[28:31]\n"
      "v_mfma_f32_16x16x32_bf16 v[48:51], v[98:101], v[224:227], v[24:27]\n"
      "s_nop 3\n"
      "v_cvt_pk_bf16_f32 v216, v68, v69\n"
      "v_cvt_pk_bf16_f32 v217, v70, v71\n"
      "v_mfma_f32_16x16x32_bf16 v[52:55], v[96:99], v[224:227], v[24:27]\n"
      "v_cvt_pk_bf16_f32 v218, v72, v73\n"
      "v_cvt_pk_bf16_f32 v219, v74, v75\n"
      "v_mfma_f32_16x16x32_bf16 v[68:71], v[98:101], v[228:231], v[28:31]\n"
      "v_cvt_pk_bf16_f32 v220, v84, v85\n"
      "v_cvt_pk_bf16_f32 v221, v86, v87\n"
      "v_mfma_f32_16x16x32_bf16 v[72:75], v[96:99], v[228:231], v[28:31]\n"
      "v_cvt_pk_bf16_f32 v222, v88, v89\n"
      "v_cvt_pk_bf16_f32 v223, v90, v91\n"
      "v_mfma_f32_16x16x32_bf16 v[32:35], v[12:15], v[216:219], v[32:35]\n"
      "v_mfma_f32_16x16x32_bf16 v[84:87], v[98:101], v[232:235], v[24:27]\n"
      "v_cvt_pk_bf16_f32 v224, v48, v49\n"
      "v_cvt_pk_bf16_f32 v225, v50, v51\n"
      "v_mfma_f32_16x16x32_bf16 v[88:91], v[96:99], v[232:235], v[24:27]\n"
      "v_cvt_pk_bf16_f32 v226, v52, v53\n"
      "v_cvt_pk_bf16_f32 v227, v54, v55\n"
      "v_mfma_f32_16x16x32_bf16 v[36:39], v[12:15], v[220:223], v[36:39]\n"
      "v_mfma_f32_16x16x32_bf16 v[48:51], v[98:101], v[236:239], v[28:31]\n"
      "v_cvt_pk_bf16_f32 v228, v68, v69\n"
      "v_cvt_pk_bf16_f32 v229, v70, v71\n"
      "v_mfma_f32_16x16x32_bf16 v[52:55], v[96:99], v[236:239], v[28:31]\n"
      "v_cvt_pk_bf16_f32 v230, v72, v73\n"
      "v_cvt_pk_bf16_f32 v231, v74, v75\n"
      "v_mfma_f32_16x16x32_bf16 v[32:35], v[12:15], v[224:227], v[32:35]\n"
      "s_nop 0\n"
      "v_mfma_f32_16x16x32_bf16 v[36:39], v[12:15], v[228:231], v[36:39]\n"
      "v_cvt_pk_bf16_f32 v232, v84, v85\n"
      "v_cvt_pk_bf16_f32 v233, v86, v87\n"
      "v_cvt_pk_bf16_f32 v234, v88, v89\n"
      "v_cvt_pk_bf16_f32 v235, v90, v91\n"
      "s_nop 1\n"
      "v_mfma_f32_16x16x32_bf16 v[32:35], v[12:15], v[232:235], v[32:35]\n"
      "v_cvt_pk_bf16_f32 v236, v48, v49\n"
      "v_cvt_pk_bf16_f32 v237, v50, v51\n"
      "v_cvt_pk_bf16_f32 v238, v52, v53\n"
      "v_cvt_pk_bf16_f32 v239, v54, v55\n"
      "s_nop 1\n"
      "v_mfma_f32_16x16x32_bf16 v[36:39], v[12:15], v[236:239], v[36:39]\n"
      "s_nop 0\n"
      "v_mul_f32 v24, 0xbd000000, v32\n"
      "v_mul_f32 v25, 0xbd000000, v33\n"
      "v_mul_f32 v26, 0xbd000000, v34\n"
      "v_mul_f32 v27, 0xbd000000, v35\n"
      "s_nop 2\n"
      "v_mul_f32 v28, 0xbd000000, v36\n"
      "v_mul_f32 v29, 0xbd000000, v37\n"
      "v_mul_f32 v30, 0xbd000000, v38\n"
      "v_mul_f32 v31, 0xbd000000, v39\n"
      "v_mfma_f32_16x16x32_bf16 v[68:71], v[98:101], v[240:243], v[24:27]\n"
      "v_mfma_f32_16x16x32_bf16 v[72:75], v[96:99], v[240:243], v[24:27]\n"
      "v_mfma_f32_16x16x32_bf16 v[84:87], v[98:101], v[244:247], v[28:31]\n"
      "v_mfma_f32_16x16x32_bf16 v[88:91], v[96:99], v[244:247], v[28:31]\n"
      "s_nop 4\n"
      "v_cvt_pk_bf16_f32 v240, v68, v69\n"
      "v_cvt_pk_bf16_f32 v241, v70, v71\n"
      "v_cvt_pk_bf16_f32 v242, v72, v73\n"
      "v_cvt_pk_bf16_f32 v243, v74, v75\n"
      "v_cvt_pk_bf16_f32 v244, v84, v85\n"
      "v_cvt_pk_bf16_f32 v245, v86, v87\n"
      "v_cvt_pk_bf16_f32 v246, v88, v89\n"
      "v_cvt_pk_bf16_f32 v247, v90, v91\n"
      "s_waitcnt vmcnt(0)\n"
      "v_cmp_ne_u64 vcc, s[98:99], v[44:45]\n"
      "s_cbranch_vccz .Lbk15\n"
      "s_mov_b32 s56, 0x20000\n"
      ".Lsp15:\n"
      "buffer_load_dwordx2 v[44:45], v5, s[36:39], 0 offen offset:8 sc1\n"
      "s_waitcnt vmcnt(0)\n"
      "v_cmp_ne_u64 vcc, s[98:99], v[44:45]\n"
      "s_cbranch_vccz .Lbk15\n"
      "s_sleep 4\n"
      "s_sub_u32 s56, s56, 1\n"
      "s_cbranch_scc0 .Lsp15\n"
      ".Lbk15:\n"
      "s_cmp_eq_u32 s28, 0\n"
      "s_cselect_b32 s50, 0, 0x40000000\n"
      "v_or_b32 v7, s50, v7\n"
      "s_mov_b32 s73, s79\n"
      "s_mov_b32 s56, 0x20000\n"
      ".Lsp16:\n"
      "v_cmp_gt_u32 vcc, 1, v6\n"
      "s_cbranch_vccz .Lbk16\n"
      "s_sleep 4\n"
      "buffer_load_dword v6, v5, s[36:39], 0 offen sc1\n"
      "s_waitcnt vmcnt(0)\n"
      "s_sub_u32 s56, s56, 1\n"
      "s_cbranch_scc0 .Lsp16\n"
      ".Lbk16:\n"
      "s_add_u32 m0, s73, 0\n"
      "s_nop 0\n"
      "buffer_load_dwordx4 v2, s[36:39], s69 offen sc1 lds\n"
      "buffer_load_dwordx4 v2, s[36:39], s69 offen offset:1024 sc1 lds\n"
      "buffer_load_dwordx4 v2, s[36:39], s69 offen offset:2048 sc1 lds\n"
      "buffer_load_dwordx4 v2, s[36:39], s69 offen offset:3072 sc1 lds\n"
      "s_add_u32 m0, s74, 0\n"
      "s_nop 0\n"
      "buffer_load_dwordx4 v4, s[36:39], s69 offen sc1 lds\n"
      "s_add_u32 s69, s69, 20480\n"
      "s_mov_b32 s56, 0x20000\n"
      ".Lsp17:\n"
      "v_cmp_gt_u32 vcc, 2, v6\n"
      "s_cbranch_vccz .Lbk17\n"
      "s_sleep 4\n"
      "buffer_load_dword v6, v5, s[36:39], 0 offen sc1\n"
      "s_waitcnt vmcnt(0)\n"
      "s_sub_u32 s56, s56, 1\n"
      "s_cbranch_scc0 .Lsp17\n"
      ".Lbk17:\n"
      "s_add_u32 m0, s73, 20480\n"
      "s_nop 0\n"
      "buffer_load_dwordx4 v2, s[36:39], s69 offen sc1 lds\n"
      "buffer_load_dwordx4 v2, s[36:39], s69 offen offset:1024 sc1 lds\n"
      "buffer_load_dwordx4 v2, s[36:39], s69 offen offset:2048 sc1 lds\n"
      "buffer_load_dwordx4 v2, s[36:39], s69 offen offset:3072 sc1 lds\n"
      "s_add_u32 m0, s74, 20480\n"
      "s_nop 0\n"
      "buffer_load_dwordx4 v4, s[36:39], s69 offen sc1 lds\n"
      "s_add_u32 s69, s69, 20480\n"
      "s_waitcnt vmcnt(5)\n"
      "s_barrier\n"
      "ds_read_b128 v[20:23], v3 offset:0\n"
      "ds_read_b128 v[24:27], v3 offset:1024\n"
      "ds_read_b128 v[28:31], v3 offset:2048\n"
      "buffer_load_dword v6, v5, s[36:39], 0 offen sc1\n"
      "s_mov_b32 s77, s76\n"
      "s_waitcnt lgkmcnt(0)\n"
      "s_branch .Lgu_in0\n"
      ".Lgu_loop0:\n"
      "v_mfma_f32_16x16x32_bf16 v[52:55], a[112:115], v[28:31], v[52:55]\n"
      "s_waitcnt lgkmcnt(1)\n"
      "ds_read_b128 v[92:95], v3 offset:37888\n"
      "v_add_u32 v9, s75, v80\n"
      "v_mfma_f32_16x16x32_bf16 v[52:55], a[120:123], v[32:35], v[52:55]\n"
      "v_add_u32 v3, s75, v2\n"
      "v_mul_f32 v84, v84, v72\n"
      "v_mfma_f32_16x16x32_bf16 v[56:59], v[216:219], v[28:31], v[56:59]\n"
      "v_rcp_f32 v74, v74\n"
      "v_mfma_f32_16x16x32_bf16 v[56:59], v[224:227], v[32:35], v[56:59]\n"
      "s_waitcnt vmcnt(6)\n"
      "v_mul_f32 v85, v85, v73\n"
      "s_barrier\n"
      "v_mfma_f32_16x16x32_bf16 v[60:63], a[116:119], v[28:31], v[60:63]\n"
      "ds_read_b128 v[20:23], v3 offset:0\n"
      "v_mul_f32 v86, v86, v74\n"
      "v_mfma_f32_16x16x32_bf16 v[60:63], a[124:127], v[32:35], v[60:63]\n"
      "buffer_store_dword v8, v7, s[36:39], 0 offen sc1\n"
      "v_mfma_f32_16x16x32_bf16 v[64:67], v[220:223], v[28:31], v[64:67]\n"
      "v_rcp_f32 v75, v75\n"
      "v_mfma_f32_16x16x32_bf16 v[64:67], v[228:231], v[32:35], v[64:67]\n"
      "s_waitcnt lgkmcnt(2)\n"
      "v_mul_f32 v87, v87, v75\n"
      "v_cvt_pk_bf16_f32 v78, v84, v85\n"
      "v_mfma_f32_16x16x32_bf16 v[52:55], a[128:131], v[88:91], v[52:55]\n"
      "s_waitcnt lgkmcnt(1)\n"
      "ds_read_b128 v[24:27], v3 offset:1024\n"
      "v_cvt_pk_bf16_f32 v79, v86, v87\n"
      "v_mfma_f32_16x16x32_bf16 v[52:55], a[136:139], v[92:95], v[52:55]\n"
      "buffer_store_dwordx4 v[76:79], v2, s[36:39], s71 offen sc1\n"
      "v_mfma_f32_16x16x32_bf16 v[56:59], v[232:235], v[88:91], v[56:59]\n"
      "ds_read_b64 v[10:11], v9 offset:18432\n"
      "ds_read_b64 v[12:13], v9 offset:18944\n"
      "v_mfma_f32_16x16x32_bf16 v[56:59], v[240:243], v[92:95], v[56:59]\n"
      "v_mfma_f32_16x16x32_bf16 v[60:63], a[132:135], v[88:91], v[60:63]\n"
      "ds_read_b128 v[28:31], v3 offset:2048\n"
      "v_mfma_f32_16x16x32_bf16 v[60:63], a[140:143], v[92:95], v[60:63]\n"
      "v_mfma_f32_16x16x32_bf16 v[64:67], v[236:239], v[88:91], v[64:67]\n"
      "v_mul_f32 v68, v52, v52\n"
      "v_mul_f32 v69, v53, v53\n"
      "v_mfma_f32_16x16x32_bf16 v[64:67], v[244:247], v[92:95], v[64:67]\n"
      "s_waitcnt lgkmcnt(1)\n"
      "v_add_f32 v10, v10, v12\n"
      "v_add_f32 v11, v11, v13\n"
      ".Lgu_in0:\n"
      "v_mfma_f32_16x16x32_bf16 v[36:39], a[0:3], v[20:23], 0\n"
      "ds_read_b128 v[32:35], v3 offset:3072\n"
      "v_permlane32_swap_b32 v10, v11\n"
      "v_mfma_f32_16x16x32_bf16 v[36:39], a[8:11], v[24:27], v[36:39]\n"
      "s_cmp_lt_u32 s66, 64\n"
      "v_add_f32 v10, v10, v11\n"
      "v_mov_b32 v12, v10\n"
      "v_mfma_f32_16x16x32_bf16 v[40:43], v[104:107], v[20:23], 0\n"
      "s_cselect_b32 s71, s70, 0x40000000\n"
      "v_permlane16_swap_b32 v10, v12\n"
      "v_mul_f32 v70, v54, v54\n"
      "v_mfma_f32_16x16x32_bf16 v[40:43], v[112:115], v[24:27], v[40:43]\n"
      "s_add_u32 s70, s70, 147456\n"
      "v_add_f32 v10, v10, v12\n"
      "v_mov_b32 v11, v10\n"
      "v_mfma_f32_16x16x32_bf16 v[44:47], a[4:7], v[20:23], 0\n"
      "s_max_i32 s72, s66, 0\n"
      "ds_read_b128 v[88:91], v3 offset:4096\n"
      "v_permlane32_swap_b32 v10, v11\n"
      "v_mfma_f32_16x16x32_bf16 v[44:47], a[12:15], v[24:27], v[44:47]\n"
      "s_add_u32 s67, s66, 4\n"
      "v_mul_f32 v10, 0x3ae38e39, v10\n"
      "v_fmaak_f32 v11, v81, v11, 0x3727c5ac\n"
      "v_mfma_f32_16x16x32_bf16 v[48:51], v[108:111], v[20:23], 0\n"
      "s_min_i32 s67, s67, 65\n"
      "v_fma_f32 v11, -v10, v10, v11\n"
      "v_max_f32 v14, 0x3727c5ac, v11\n"
      "v_mfma_f32_16x16x32_bf16 v[48:51], v[116:119], v[24:27], v[48:51]\n"
      "s_waitcnt lgkmcnt(2)\n"
      "v_rsq_f32 v12, v14\n"
      "v_mfma_f32_16x16x32_bf16 v[36:39], a[16:19], v[28:31], v[36:39]\n"
      "s_waitcnt lgkmcnt(1)\n"
      "ds_read_b128 v[92:95], v3 offset:5120\n"
      "v_mul_f32 v15, 0xc0135761, v12\n"
      "v_mfma_f32_16x16x32_bf16 v[36:39], a[24:27], v[32:35], v[36:39]\n"
      "s_cmp_le_i32 s66, 61\n"
      "v_mul_f32 v13, v12, v12\n"
      "v_mul_f32 v71, v55, v55\n"
      "v_mfma_f32_16x16x32_bf16 v[40:43], v[120:123], v[28:31], v[40:43]\n"
      "s_cselect_b32 s69, s68, 0x40000000\n"
      "v_mul_f32 v13, 0x3d372713, v13\n"
      "v_mul_f32 v16, v13, v15\n"
      "v_mfma_f32_16x16x32_bf16 v[40:43], v[128:131], v[32:35], v[40:43]\n"
      "s_add_u32 s68, s68, 20480\n"
      "v_fma_f32 v68, v68, v16, v15\n"
      "v_fma_f32 v69, v69, v16, v15\n"
      "v_mfma_f32_16x16x32_bf16 v[44:47], a[20:23], v[28:31], v[44:47]\n"
      "s_add_u32 s66, s66, 1\n"
      "ds_read_b128 v[20:23], v3 offset:6144\n"
      "v_fma_f32 v70, v70, v16, v15\n"
      "v_mfma_f32_16x16x32_bf16 v[44:47], a[28:31], v[32:35], v[44:47]\n"
      "s_xor_b32 s75, s75, 40960\n"
      "v_writelane_b32 v8, s72, 0\n"
      "v_fma_f32 v71, v71, v16, v15\n"
      "v_mfma_f32_16x16x32_bf16 v[48:51], v[124:127], v[28:31], v[48:51]\n"
      "s_xor_b32 s48, s75, 40960\n"
      "v_mul_f32 v68, v52, v68\n"
      "v_mul_f32 v69, v53, v69\n"
      "v_mfma_f32_16x16x32_bf16 v[48:51], v[132:135], v[32:35], v[48:51]\n"
      "s_waitcnt lgkmcnt(2)\n"
      "v_mul_f32 v70, v54, v70\n"
      "v_mul_f32 v71, v55, v71\n"
      "v_mfma_f32_16x16x32_bf16 v[36:39], a[32:35], v[88:91], v[36:39]\n"
      "s_waitcnt lgkmcnt(1)\n"
      "ds_read_b128 v[24:27], v3 offset:7168\n"
      "v_mul_f32 v56, v52, v56\n"
      "v_mfma_f32_16x16x32_bf16 v[36:39], a[40:43], v[92:95], v[36:39]\n"
      "s_sub_u32 s48, s75, s48\n"
      "v_exp_f32 v68, v68\n"
      "v_mfma_f32_16x16x32_bf16 v[40:43], v[136:139], v[88:91], v[40:43]\n"
      "s_add_u32 s73, s73, s48\n"
      "v_exp_f32 v69, v69\n"
      "v_mfma_f32_16x16x32_bf16 v[40:43], v[144:147], v[92:95], v[40:43]\n"
      "s_add_u32 s74, s74, s48\n"
      "v_mul_f32 v57, v53, v57\n"
      "v_mul_f32 v58, v54, v58\n"
      "v_mfma_f32_16x16x32_bf16 v[44:47], a[36:39], v[88:91], v[44:47]\n"
      "s_add_u32 m0, s73, 0\n"
      "ds_read_b128 v[28:31], v3 offset:8192\n"
      "v_mul_f32 v59, v55, v59\n"
      "v_mfma_f32_16x16x32_bf16 v[44:47], a[44:47], v[92:95], v[44:47]\n"
      "v_exp_f32 v70, v70\n"
      "v_mfma_f32_16x16x32_bf16 v[48:51], v[140:143], v[88:91], v[48:51]\n"
      "v_exp_f32 v71, v71\n"
      "v_mfma_f32_16x16x32_bf16 v[48:51], v[148:151], v[92:95], v[48:51]\n"
      "s_waitcnt lgkmcnt(2)\n"
      "v_fma_f32 v68, v68, v14, v14\n"
      "v_fma_f32 v69, v69, v14, v14\n"
      "v_mfma_f32_16x16x32_bf16 v[36:39], a[48:51], v[20:23], v[36:39]\n"
      "s_waitcnt lgkmcnt(1)\n"
      "ds_read_b128 v[32:35], v3 offset:9216\n"
      "v_fma_f32 v70, v70, v14, v14\n"
      "v_mfma_f32_16x16x32_bf16 v[36:39], a[56:59], v[24:27], v[36:39]\n"
      "v_fma_f32 v71, v71, v14, v14\n"
      "v_mul_f32 v72, v60, v60\n"
      "v_mfma_f32_16x16x32_bf16 v[40:43], v[152:155], v[20:23], v[40:43]\n"
      "v_rcp_f32 v68, v68\n"
      "v_mfma_f32_16x16x32_bf16 v[40:43], v[160:163], v[24:27], v[40:43]\n"
      "v_rcp_f32 v69, v69\n"
      "v_mfma_f32_16x16x32_bf16 v[44:47], a[52:55], v[20:23], v[44:47]\n"
      "ds_read_b128 v[88:91], v3 offset:10240\n"
      "v_mul_f32 v56, v56, v68\n"
      "v_mfma_f32_16x16x32_bf16 v[44:47], a[60:63], v[24:27], v[44:47]\n"
      "v_rcp_f32 v70, v70\n"
      "v_mfma_f32_16x16x32_bf16 v[48:51], v[156:159], v[20:23], v[48:51]\n"
      "v_rcp_f32 v71, v71\n"
      "v_mfma_f32_16x16x32_bf16 v[48:51], v[164:167], v[24:27], v[48:51]\n"
      "s_waitcnt lgkmcnt(2)\n"
      "v_mul_f32 v57, v57, v69\n"
      "v_mul_f32 v58, v58, v70\n"
      "v_mfma_f32_16x16x32_bf16 v[36:39], a[64:67], v[28:31], v[36:39]\n"
      "s_waitcnt lgkmcnt(1)\n"
      "ds_read_b128 v[92:95], v3 offset:11264\n"
      "v_mul_f32 v59, v59, v71\n"
      "v_mfma_f32_16x16x32_bf16 v[36:39], a[72:75], v[32:35], v[36:39]\n"
      "v_cvt_pk_bf16_f32 v76, v56, v57\n"
      "v_cvt_pk_bf16_f32 v77, v58, v59\n"
      "v_mfma_f32_16x16x32_bf16 v[40:43], v[168:171], v[28:31], v[40:43]\n"
      "v_mul_f32 v73, v61, v61\n"
      "v_mul_f32 v74, v62, v62\n"
      "v_mfma_f32_16x16x32_bf16 v[40:43], v[176:179], v[32:35], v[40:43]\n"
      "v_mul_f32 v75, v63, v63\n"
      "v_fma_f32 v72, v72, v16, v15\n"
      "v_mfma_f32_16x16x32_bf16 v[44:47], a[68:71], v[28:31], v[44:47]\n"
      "ds_read_b128 v[20:23], v3 offset:12288\n"
      "v_fma_f32 v73, v73, v16, v15\n"
      "v_mfma_f32_16x16x32_bf16 v[44:47], a[76:79], v[32:35], v[44:47]\n"
      "v_fma_f32 v74, v74, v16, v15\n"
      "v_fma_f32 v75, v75, v16, v15\n"
      "v_mfma_f32_16x16x32_bf16 v[48:51], v[172:175], v[28:31], v[48:51]\n"
      "v_mul_f32 v72, v60, v72\n"
      "v_mul_f32 v73, v61, v73\n"
      "v_mfma_f32_16x16x32_bf16 v[48:51], v[180:183], v[32:35], v[48:51]\n"
      "s_waitcnt vmcnt(7) lgkmcnt(2)\n"
      "v_cmp_gt_u32 vcc, s67, v6\n"
      "v_mul_f32 v74, v62, v74\n"
      "v_mfma_f32_16x16x32_bf16 v[36:39], a[80:83], v[88:91], v[36:39]\n"
      "s_waitcnt lgkmcnt(1)\n"
      "s_cbranch_vccnz .Lsl18\n"
      ".Lbk18:\n"
      "ds_read_b128 v[24:27], v3 offset:13312\n"
      "v_mfma_f32_16x16x32_bf16 v[36:39], a[88:91], v[92:95], v[36:39]\n"
      "buffer_load_dword v6, v5, s[36:39], 0 offen sc1\n"
      "v_mul_f32 v75, v63, v75\n"
      "v_mfma_f32_16x16x32_bf16 v[40:43], v[184:187], v[88:91], v[40:43]\n"
      "v_exp_f32 v72, v72\n"
      "v_mfma_f32_16x16x32_bf16 v[40:43], v[192:195], v[92:95], v[40:43]\n"
      "buffer_load_dwordx4 v2, s[36:39], s69 offen sc1 lds\n"
      "v_mul_f32 v84, v60, v64\n"
      "v_mfma_f32_16x16x32_bf16 v[44:47], a[84:87], v[88:91], v[44:47]\n"
      "ds_read_b128 v[28:31], v3 offset:14336\n"
      "v_mul_f32 v85, v61, v65\n"
      "v_mfma_f32_16x16x32_bf16 v[44:47], a[92:95], v[92:95], v[44:47]\n"
      "v_exp_f32 v73, v73\n"
      "v_mfma_f32_16x16x32_bf16 v[48:51], v[188:191], v[88:91], v[48:51]\n"
      "buffer_load_dwordx4 v2, s[36:39], s69 offen offset:1024 sc1 lds\n"
      "v_mul_f32 v86, v62, v66\n"
      "v_mfma_f32_16x16x32_bf16 v[48:51], v[196:199], v[92:95], v[48:51]\n"
      "s_waitcnt lgkmcnt(2)\n"
      "v_exp_f32 v74, v74\n"
      "v_mfma_f32_16x16x32_bf16 v[36:39], a[96:99], v[20:23], v[36:39]\n"
      "s_waitcnt lgkmcnt(1)\n"
      "ds_read_b128 v[32:35], v3 offset:15360\n"
      "v_mul_f32 v87, v63, v67\n"
      "v_mfma_f32_16x16x32_bf16 v[36:39], a[104:107], v[24:27], v[36:39]\n"
      "buffer_load_dwordx4 v2, s[36:39], s69 offen offset:2048 sc1 lds\n"
      "v_fma_f32 v72, v72, v14, v14\n"
      "v_mfma_f32_16x16x32_bf16 v[40:43], v[200:203], v[20:23], v[40:43]\n"
      "v_exp_f32 v75, v75\n"
      "v_mfma_f32_16x16x32_bf16 v[40:43], v[208:211], v[24:27], v[40:43]\n"
      "v_fma_f32 v73, v73, v14, v14\n"
      "v_fma_f32 v74, v74, v14, v14\n"
      "v_mfma_f32_16x16x32_bf16 v[44:47], a[100:103], v[20:23], v[44:47]\n"
      "buffer_load_dwordx4 v2, s[36:39], s69 offen offset:3072 sc1 lds\n"
      "ds_read_b128 v[88:91], v3 offset:16384\n"
      "v_mfma_f32_16x16x32_bf16 v[44:47], a[108:111], v[24:27], v[44:47]\n"
      "s_add_u32 m0, s74, 0\n"
      "v_fma_f32 v75, v75, v14, v14\n"
      "v_mfma_f32_16x16x32_bf16 v[48:51], v[204:207], v[20:23], v[48:51]\n"
      "v_rcp_f32 v72, v72\n"
      "v_mfma_f32_16x16x32_bf16 v[48:51], v[212:215], v[24:27], v[48:51]\n"
      "s_waitcnt lgkmcnt(2)\n"
      "buffer_load_dwordx4 v4, s[36:39], s69 offen sc1 lds\n"
      "v_rcp_f32 v73, v73\n"
      ".Lmg00:\n"
      "v_mfma_f32_16x16x32_bf16 v[36:39], a[112:115], v[28:31], v[36:39]\n"
      "s_waitcnt lgkmcnt(1)\n"
      "ds_read_b128 v[92:95], v3 offset:17408\n"
      "v_mul_f32 v84, v84, v72\n"
      "v_mfma_f32_16x16x32_bf16 v[36:39], a[120:123], v[32:35], v[36:39]\n"
      "v_rcp_f32 v74, v74\n"
      "v_mfma_f32_16x16x32_bf16 v[40:43], v[216:219], v[28:31], v[40:43]\n"
      "v_rcp_f32 v75, v75\n"
      "v_mfma_f32_16x16x32_bf16 v[40:43], v[224:227], v[32:35], v[40:43]\n"
      "s_waitcnt vmcnt(6)\n"
      "v_mul_f32 v85, v85, v73\n"
      "s_barrier\n"
      "v_mfma_f32_16x16x32_bf16 v[44:47], a[116:119], v[28:31], v[44:47]\n"
      "ds_read_b128 v[20:23], v3 offset:20480\n"
      "v_mul_f32 v86, v86, v74\n"
      "v_mfma_f32_16x16x32_bf16 v[44:47], a[124:127], v[32:35], v[44:47]\n"
      "buffer_store_dword v8, v7, s[36:39], 0 offen sc1\n"
      "v_mfma_f32_16x16x32_bf16 v[48:51], v[220:223], v[28:31], v[48:51]\n"
      "v_mul_f32 v87, v87, v75\n"
      "v_cvt_pk_bf16_f32 v78, v84, v85\n"
      "v_mfma_f32_16x16x32_bf16 v[48:51], v[228:231], v[32:35], v[48:51]\n"
      "s_waitcnt lgkmcnt(2)\n"
      "v_cvt_pk_bf16_f32 v79, v86, v87\n"
      "ds_read_b64 v[10:11], v9 offset:38912\n"
      "v_mfma_f32_16x16x32_bf16 v[36:39], a[128:131], v[88:91], v[36:39]\n"
      "s_waitcnt lgkmcnt(2)\n"
      "ds_read_b128 v[24:27], v3 offset:21504\n"
      "ds_read_b64 v[12:13], v9 offset:39424\n"
      "v_mfma_f32_16x16x32_bf16 v[36:39], a[136:139], v[92:95], v[36:39]\n"
      "buffer_store_dwordx4 v[76:79], v2, s[36:39], s71 offen sc1\n"
      "v_mfma_f32_16x16x32_bf16 v[40:43], v[232:235], v[88:91], v[40:43]\n"
      "v_mfma_f32_16x16x32_bf16 v[40:43], v[240:243], v[92:95], v[40:43]\n"
      "v_mfma_f32_16x16x32_bf16 v[44:47], a[132:135], v[88:91], v[44:47]\n"
      "ds_read_b128 v[28:31], v3 offset:22528\n"
      "v_mfma_f32_16x16x32_bf16 v[44:47], a[140:143], v[92:95], v[44:47]\n"
      "s_waitcnt lgkmcnt(1)\n"
      "v_add_f32 v10, v10, v12\n"
      "v_add_f32 v11, v11, v13\n"
      "v_mfma_f32_16x16x32_bf16 v[48:51], v[236:239], v[88:91], v[48:51]\n"
      "s_nop 0\n"
      "v_permlane32_swap_b32 v10, v11\n"
      "v_mul_f32 v68, v36, v36\n"
      "v_mfma_f32_16x16x32_bf16 v[48:51], v[244:247], v[92:95], v[48:51]\n"
      "v_add_f32 v10, v10, v11\n"
      "v_mov_b32 v12, v10\n"
      "v_mfma_f32_16x16x32_bf16 v[52:55], a[0:3], v[20:23], 0\n"
      "ds_read_b128 v[32:35], v3 offset:23552\n"
      "v_permlane16_swap_b32 v10, v12\n"
      "v_mfma_f32_16x16x32_bf16 v[52:55], a[8:11], v[24:27], v[52:55]\n"
      "s_cmp_lt_u32 s66, 64\n"
      "v_add_f32 v10, v10, v12\n"
      "v_mov_b32 v11, v10\n"
      "v_mfma_f32_16x16x32_bf16 v[56:59], v[104:107], v[20:23], 0\n"
      "s_cselect_b32 s71, s70, 0x40000000\n"
      "v_permlane32_swap_b32 v10, v11\n"
      "v_mul_f32 v69, v37, v37\n"
      "v_mfma_f32_16x16x32_bf16 v[56:59], v[112:115], v[24:27], v[56:59]\n"
      "s_add_u32 s70, s70, 147456\n"
      "v_mul_f32 v10, 0x3ae38e39, v10\n"
      "v_fmaak_f32 v11, v81, v11, 0x3727c5ac\n"
      "v_mfma_f32_16x16x32_bf16 v[60:63], a[4:7], v[20:23], 0\n"
      "s_max_i32 s72, s66, 0\n"
      "ds_read_b128 v[88:91], v3 offset:24576\n"
      "v_fma_f32 v11, -v10, v10, v11\n"
      "v_mfma_f32_16x16x32_bf16 v[60:63], a[12:15], v[24:27], v[60:63]\n"
      "s_add_u32 s67, s66, 4\n"
      "v_max_f32 v17, 0x3727c5ac, v11\n"
      "v_mul_f32 v70, v38, v38\n"
      "v_mfma_f32_16x16x32_bf16 v[64:67], v[108:111], v[20:23], 0\n"
      "s_min_i32 s67, s67, 65\n"
      "v_rsq_f32 v12, v17\n"
      "v_mfma_f32_16x16x32_bf16 v[64:67], v[116:119], v[24:27], v[64:67]\n"
      "s_waitcnt lgkmcnt(2)\n"
      "v_mul_f32 v18, 0xc0135761, v12\n"
      "v_mul_f32 v13, v12, v12\n"
      "v_mfma_f32_16x16x32_bf16 v[52:55], a[16:19], v[28:31], v[52:55]\n"
      "s_waitcnt lgkmcnt(1)\n"
      "ds_read_b128 v[92:95], v3 offset:25600\n"
      "v_mul_f32 v13, 0x3d372713, v13\n"
      "v_mfma_f32_16x16x32_bf16 v[52:55], a[24:27], v[32:35], v[52:55]\n"
      "s_cmp_le_i32 s66, 61\n"
      "v_mul_f32 v19, v13, v18\n"
      "v_mul_f32 v71, v39, v39\n"
      "v_mfma_f32_16x16x32_bf16 v[56:59], v[120:123], v[28:31], v[56:59]\n"
      "s_cselect_b32 s69, s68, 0x40000000\n"
      "v_fma_f32 v68, v68, v19, v18\n"
      "v_fma_f32 v69, v69, v19, v18\n"
      "v_mfma_f32_16x16x32_bf16 v[56:59], v[128:131], v[32:35], v[56:59]\n"
      "s_add_u32 s68, s68, 20480\n"
      "v_fma_f32 v70, v70, v19, v18\n"
      "v_fma_f32 v71, v71, v19, v18\n"
      "v_mfma_f32_16x16x32_bf16 v[60:63], a[20:23], v[28:31], v[60:63]\n"
      "s_add_u32 s66, s66, 1\n"
      "ds_read_b128 v[20:23], v3 offset:26624\n"
      "v_mul_f32 v68, v36, v68\n"
      "v_mfma_f32_16x16x32_bf16 v[60:63], a[28:31], v[32:35], v[60:63]\n"
      "s_add_u32 m0, s73, 20480\n"
      "v_writelane_b32 v8, s72, 0\n"
      "v_mul_f32 v69, v37, v69\n"
      "v_mfma_f32_16x16x32_bf16 v[64:67], v[124:127], v[28:31], v[64:67]\n"
      "v_mul_f32 v70, v38, v70\n"
      "v_mul_f32 v71, v39, v71\n"
      "v_mfma_f32_16x16x32_bf16 v[64:67], v[132:135], v[32:35], v[64:67]\n"
      "s_waitcnt lgkmcnt(2)\n"
      "v_exp_f32 v68, v68\n"
      "v_mfma_f32_16x16x32_bf16 v[52:55], a[32:35], v[88:91], v[52:55]\n"
      "s_waitcnt lgkmcnt(1)\n"
      "ds_read_b128 v[24:27], v3 offset:27648\n"
      "v_mul_f32 v40, v36, v40\n"
      "v_mfma_f32_16x16x32_bf16 v[52:55], a[40:43], v[92:95], v[52:55]\n"
      "v_exp_f32 v69, v69\n"
      "v_mfma_f32_16x16x32_bf16 v[56:59], v[136:139], v[88:91], v[56:59]\n"
      "v_mul_f32 v41, v37, v41\n"
      "v_mul_f32 v42, v38, v42\n"
      "v_mfma_f32_16x16x32_bf16 v[56:59], v[144:147], v[92:95], v[56:59]\n"
      "v_exp_f32 v70, v70\n"
      "v_mfma_f32_16x16x32_bf16 v[60:63], a[36:39], v[88:91], v[60:63]\n"
      "ds_read_b128 v[28:31], v3 offset:28672\n"
      "v_mul_f32 v43, v39, v43\n"
      "v_mfma_f32_16x16x32_bf16 v[60:63], a[44:47], v[92:95], v[60:63]\n"
      "v_exp_f32 v71, v71\n"
      "v_mfma_f32_16x16x32_bf16 v[64:67], v[140:143], v[88:91], v[64:67]\n"
      "v_fma_f32 v68, v68, v17, v17\n"
      "v_fma_f32 v69, v69, v17, v17\n"
      "v_mfma_f32_16x16x32_bf16 v[64:67], v[148:151], v[92:95], v[64:67]\n"
      "s_waitcnt lgkmcnt(2)\n"
      "v_fma_f32 v70, v70, v17, v17\n"
      "v_fma_f32 v71, v71, v17, v17\n"
      "v_mfma_f32_16x16x32_bf16 v[52:55], a[48:51], v[20:23], v[52:55]\n"
      "s_waitcnt lgkmcnt(1)\n"
      "ds_read_b128 v[32:35], v3 offset:29696\n"
      "v_mul_f32 v72, v44, v44\n"
      "v_mfma_f32_16x16x32_bf16 v[52:55], a[56:59], v[24:27], v[52:55]\n"
      "v_rcp_f32 v68, v68\n"
      "v_mfma_f32_16x16x32_bf16 v[56:59], v[152:155], v[20:23], v[56:59]\n"
      "v_rcp_f32 v69, v69\n"
      "v_mfma_f32_16x16x32_bf16 v[56:59], v[160:163], v[24:27], v[56:59]\n"
      "v_rcp_f32 v70, v70\n"
      "v_mfma_f32_16x16x32_bf16 v[60:63], a[52:55], v[20:23], v[60:63]\n"
      "ds_read_b128 v[88:91], v3 offset:30720\n"
      "v_mul_f32 v40, v40, v68\n"
      "v_mfma_f32_16x16x32_bf16 v[60:63], a[60:63], v[24:27], v[60:63]\n"
      "v_rcp_f32 v71, v71\n"
      "v_mfma_f32_16x16x32_bf16 v[64:67], v[156:159], v[20:23], v[64:67]\n"
      "v_mul_f32 v41, v41, v69\n"
      "v_mul_f32 v42, v42, v70\n"
      "v_mfma_f32_16x16x32_bf16 v[64:67], v[164:167], v[24:27], v[64:67]\n"
      "s_waitcnt lgkmcnt(2)\n"
      "v_mul_f32 v43, v43, v71\n"
      "v_cvt_pk_bf16_f32 v76, v40, v41\n"
      "v_mfma_f32_16x16x32_bf16 v[52:55], a[64:67], v[28:31], v[52:55]\n"
      "s_waitcnt lgkmcnt(1)\n"
      "ds_read_b128 v[92:95], v3 offset:31744\n"
      "v_cvt_pk_bf16_f32 v77, v42, v43\n"
      "v_mfma_f32_16x16x32_bf16 v[52:55], a[72:75], v[32:35], v[52:55]\n"
      "v_mul_f32 v73, v45, v45\n"
      "v_mul_f32 v74, v46, v46\n"
      "v_mfma_f32_16x16x32_bf16 v[56:59], v[168:171], v[28:31], v[56:59]\n"
      "v_mul_f32 v75, v47, v47\n"
      "v_fma_f32 v72, v72, v19, v18\n"
      "v_mfma_f32_16x16x32_bf16 v[56:59], v[176:179], v[32:35], v[56:59]\n"
      "v_fma_f32 v73, v73, v19, v18\n"
      "v_fma_f32 v74, v74, v19, v18\n"
      "v_mfma_f32_16x16x32_bf16 v[60:63], a[68:71], v[28:31], v[60:63]\n"
      "ds_read_b128 v[20:23], v3 offset:32768\n"
      "v_fma_f32 v75, v75, v19, v18\n"
      "v_mfma_f32_16x16x32_bf16 v[60:63], a[76:79], v[32:35], v[60:63]\n"
      "v_mul_f32 v72, v44, v72\n"
      "v_mul_f32 v73, v45, v73\n"
      "v_mfma_f32_16x16x32_bf16 v[64:67], v[172:175], v[28:31], v[64:67]\n"
      "v_mul_f32 v74, v46, v74\n"
      "v_mul_f32 v75, v47, v75\n"
      "v_mfma_f32_16x16x32_bf16 v[64:67], v[180:183], v[32:35], v[64:67]\n"
      "s_waitcnt vmcnt(7) lgkmcnt(2)\n"
      "v_cmp_gt_u32 vcc, s67, v6\n"
      "v_mul_f32 v84, v44, v48\n"
      "v_mfma_f32_16x16x32_bf16 v[52:55], a[80:83], v[88:91], v[52:55]\n"
      "s_waitcnt lgkmcnt(1)\n"
      "s_cbranch_vccnz .Lsl19\n"
      ".Lbk19:\n"
      "ds_read_b128 v[24:27], v3 offset:33792\n"
      "v_mfma_f32_16x16x32_bf16 v[52:55], a[88:91], v[92:95], v[52:55]\n"
      "buffer_load_dword v6, v5, s[36:39], 0 offen sc1\n"
      "v_mul_f32 v85, v45, v49\n"
      "v_mfma_f32_16x16x32_bf16 v[56:59], v[184:187], v[88:91], v[56:59]\n"
      "v_exp_f32 v72, v72\n"
      "v_mfma_f32_16x16x32_bf16 v[56:59], v[192:195], v[92:95], v[56:59]\n"
      "buffer_load_dwordx4 v2, s[36:39], s69 offen sc1 lds\n"
      "v_mul_f32 v86, v46, v50\n"
      "v_mfma_f32_16x16x32_bf16 v[60:63], a[84:87], v[88:91], v[60:63]\n"
      "ds_read_b128 v[28:31], v3 offset:34816\n"
      "v_mul_f32 v87, v47, v51\n"
      "v_mfma_f32_16x16x32_bf16 v[60:63], a[92:95], v[92:95], v[60:63]\n"
      "v_exp_f32 v73, v73\n"
      "v_mfma_f32_16x16x32_bf16 v[64:67], v[188:191], v[88:91], v[64:67]\n"
      "buffer_load_dwordx4 v2, s[36:39], s69 offen offset:1024 sc1 lds\n"
      "v_fma_f32 v72, v72, v17, v17\n"
      "v_mfma_f32_16x16x32_bf16 v[64:67], v[196:199], v[92:95], v[64:67]\n"
      "s_waitcnt lgkmcnt(2)\n"
      "v_exp_f32 v74, v74\n"
      "v_mfma_f32_16x16x32_bf16 v[52:55], a[96:99], v[20:23], v[52:55]\n"
      "s_waitcnt lgkmcnt(1)\n"
      "ds_read_b128 v[32:35], v3 offset:35840\n"
      "v_fma_f32 v73, v73, v17, v17\n"
      "v_mfma_f32_16x16x32_bf16 v[52:55], a[104:107], v[24:27], v[52:55]\n"
      "buffer_load_dwordx4 v2, s[36:39], s69 offen offset:2048 sc1 lds\n"
      "v_fma_f32 v74, v74, v17, v17\n"
      "v_mfma_f32_16x16x32_bf16 v[56:59], v[200:203], v[20:23], v[56:59]\n"
      "v_exp_f32 v75, v75\n"
      "v_mfma_f32_16x16x32_bf16 v[56:59], v[208:211], v[24:27], v[56:59]\n"
      "v_fma_f32 v75, v75, v17, v17\n"
      "v_mfma_f32_16x16x32_bf16 v[60:63], a[100:103], v[20:23], v[60:63]\n"
      "buffer_load_dwordx4 v2, s[36:39], s69 offen offset:3072 sc1 lds\n"
      "ds_read_b128 v[88:91], v3 offset:36864\n"
      "v_mfma_f32_16x16x32_bf16 v[60:63], a[108:111], v[24:27], v[60:63]\n"
      "s_add_u32 m0, s74, 20480\n"
      "v_rcp_f32 v72, v72\n"
      "v_mfma_f32_16x16x32_bf16 v[64:67], v[204:207], v[20:23], v[64:67]\n"
      "s_cmp_lt_i32 s66, 63\n"
      "v_rcp_f32 v73, v73\n"
      "v_mfma_f32_16x16x32_bf16 v[64:67], v[212:215], v[24:27], v[64:67]\n"
      "s_waitcnt lgkmcnt(2)\n"
      "buffer_load_dwordx4 v4, s[36:39], s69 offen sc1 lds\n"
      ".Lmg10:\n"
      "s_cbranch_scc1 .Lgu_loop0\n"
      ".Lgu_t:\n"
      "s_waitcnt lgkmcnt(0)\n"
      "v_mfma_f32_16x16x32_bf16 v[52:55], a[112:115], v[28:31], v[52:55]\n"
      "ds_read_b128 v[92:95], v3 offset:37888\n"
      "v_add_u32 v9, s75, v80\n"
      "v_mfma_f32_16x16x32_bf16 v[52:55], a[120:123], v[32:35], v[52:55]\n"
      "s_waitcnt vmcnt(6)\n"
      "s_barrier\n"
      "buffer_store_dword v8, v7, s[36:39], 0 offen sc1\n"
      "v_add_u32 v3, s75, v2\n"
      "v_mul_f32 v84, v84, v72\n"
      "v_mfma_f32_16x16x32_bf16 v[56:59], v[216:219], v[28:31], v[56:59]\n"
      "v_rcp_f32 v74, v74\n"
      "v_mfma_f32_16x16x32_bf16 v[56:59], v[224:227], v[32:35], v[56:59]\n"
      "v_rcp_f32 v75, v75\n"
      "v_mfma_f32_16x16x32_bf16 v[60:63], a[116:119], v[28:31], v[60:63]\n"
      "v_mul_f32 v85, v85, v73\n"
      "v_mul_f32 v86, v86, v74\n"
      "v_mfma_f32_16x16x32_bf16 v[60:63], a[124:127], v[32:35], v[60:63]\n"
      "v_mul_f32 v87, v87, v75\n"
      "v_cvt_pk_bf16_f32 v78, v84, v85\n"
      "v_mfma_f32_16x16x32_bf16 v[64:67], v[220:223], v[28:31], v[64:67]\n"
      "v_cvt_pk_bf16_f32 v79, v86, v87\n"
      "ds_read_b64 v[10:11], v9 offset:18432\n"
      "v_mfma_f32_16x16x32_bf16 v[64:67], v[228:231], v[32:35], v[64:67]\n"
      "buffer_store_dwordx4 v[76:79], v2, s[36:39], s71 offen sc1\n"
      "ds_read_b64 v[12:13], v9 offset:18944\n"
      "v_mfma_f32_16x16x32_bf16 v[52:55], a[128:131], v[88:91], v[52:55]\n"
      "s_waitcnt lgkmcnt(2)\n"
      "v_writelane_b32 v8, 63, 0\n"
      "v_mfma_f32_16x16x32_bf16 v[52:55], a[136:139], v[92:95], v[52:55]\n"
      "v_mfma_f32_16x16x32_bf16 v[56:59], v[232:235], v[88:91], v[56:59]\n"
      "v_mfma_f32_16x16x32_bf16 v[56:59], v[240:243], v[92:95], v[56:59]\n"
      "v_mfma_f32_16x16x32_bf16 v[60:63], a[132:135], v[88:91], v[60:63]\n"
      "s_waitcnt lgkmcnt(0)\n"
      "v_add_f32 v10, v10, v12\n"
      "v_add_f32 v11, v11, v13\n"
      "v_mfma_f32_16x16x32_bf16 v[60:63], a[140:143], v[92:95], v[60:63]\n"
      "s_nop 0\n"
      "v_permlane32_swap_b32 v10, v11\n"
      "v_mfma_f32_16x16x32_bf16 v[64:67], v[236:239], v[88:91], v[64:67]\n"
      "v_add_f32 v10, v10, v11\n"
      "v_mov_b32 v12, v10\n"
      "v_mfma_f32_16x16x32_bf16 v[64:67], v[244:247], v[92:95], v[64:67]\n"
      "s_nop 0\n"
      "v_permlane16_swap_b32 v10, v12\n"
      "v_mul_f32 v68, v52, v52\n"
      "v_add_f32 v10, v10, v12\n"
      "v_mov_b32 v11, v10\n"
      "v_mul_f32 v69, v53, v53\n"
      "v_mul_f32 v70, v54, v54\n"
      "v_permlane32_swap_b32 v10, v11\n"
      "v_mul_f32 v71, v55, v55\n"
      "v_mul_f32 v56, v52, v56\n"
      "v_mul_f32 v57, v53, v57\n"
      "v_mul_f32 v10, 0x3ae38e39, v10\n"
      "v_fmaak_f32 v11, v81, v11, 0x3727c5ac\n"
      "v_mul_f32 v58, v54, v58\n"
      "v_mul_f32 v59, v55, v59\n"
      "v_fma_f32 v11, -v10, v10, v11\n"
      "v_max_f32 v14, 0x3727c5ac, v11\n"
      "v_mul_f32 v72, v60, v60\n"
      "v_mul_f32 v73, v61, v61\n"
      "v_rsq_f32 v12, v14\n"
      "v_mul_f32 v74, v62, v62\n"
      "v_mul_f32 v75, v63, v63\n"
      "v_mul_f32 v15, 0xc0135761, v12\n"
      "v_mul_f32 v13, v12, v12\n"
      "v_mul_f32 v84, v60, v64\n"
      "v_mul_f32 v85, v61, v65\n"
      "v_mul_f32 v13, 0x3d372713, v13\n"
      "v_mul_f32 v16, v13, v15\n"
      "v_mul_f32 v86, v62, v66\n"
      "v_mul_f32 v87, v63, v67\n"
      "v_fma_f32 v68, v68, v16, v15\n"
      "v_fma_f32 v69, v69, v16, v15\n"
      "v_fma_f32 v70, v70, v16, v15\n"
      "v_fma_f32 v71, v71, v16, v15\n"
      "v_mul_f32 v68, v52, v68\n"
      "v_mul_f32 v69, v53, v69\n"
      "v_mul_f32 v70, v54, v70\n"
      "v_mul_f32 v71, v55, v71\n"
      "v_exp_f32 v68, v68\n"
      "v_exp_f32 v69, v69\n"
      "v_exp_f32 v70, v70\n"
      "v_exp_f32 v71, v71\n"
      "v_fma_f32 v68, v68, v14, v14\n"
      "v_fma_f32 v69, v69, v14, v14\n"
      "v_fma_f32 v70, v70, v14, v14\n"
      "v_fma_f32 v71, v71, v14, v14\n"
      "v_rcp_f32 v68, v68\n"
      "v_rcp_f32 v69, v69\n"
      "v_rcp_f32 v70, v70\n"
      "v_rcp_f32 v71, v71\n"
      "v_mul_f32 v56, v56, v68\n"
      "v_mul_f32 v57, v57, v69\n"
      "v_mul_f32 v58, v58, v70\n"
      "v_mul_f32 v59, v59, v71\n"
      "v_cvt_pk_bf16_f32 v76, v56, v57\n"
      "v_cvt_pk_bf16_f32 v77, v58, v59\n"
      "v_fma_f32 v72, v72, v16, v15\n"
      "v_fma_f32 v73, v73, v16, v15\n"
      "v_fma_f32 v74, v74, v16, v15\n"
      "v_fma_f32 v75, v75, v16, v15\n"
      "v_mul_f32 v72, v60, v72\n"
      "v_mul_f32 v73, v61, v73\n"
      "v_mul_f32 v74, v62, v74\n"
      "v_mul_f32 v75, v63, v75\n"
      "v_exp_f32 v72, v72\n"
      "v_exp_f32 v73, v73\n"
      "v_exp_f32 v74, v74\n"
      "v_exp_f32 v75, v75\n"
      "v_fma_f32 v72, v72, v14, v14\n"
      "v_fma_f32 v73, v73, v14, v14\n"
      "v_fma_f32 v74, v74, v14, v14\n"
      "v_fma_f32 v75, v75, v14, v14\n"
      "v_rcp_f32 v72, v72\n"
      "v_rcp_f32 v73, v73\n"
      "v_rcp_f32 v74, v74\n"
      "v_rcp_f32 v75, v75\n"
      "v_mul_f32 v84, v84, v72\n"
      "v_mul_f32 v85, v85, v73\n"
      "v_mul_f32 v86, v86, v74\n"
      "v_mul_f32 v87, v87, v75\n"
      "v_cvt_pk_bf16_f32 v78, v84, v85\n"
      "v_cvt_pk_bf16_f32 v79, v86, v87\n"
      "buffer_store_dwordx4 v[76:79], v2, s[36:39], s70 offen sc1\n"
      "s_waitcnt vmcnt(1)\n"
      "s_barrier\n"
      "buffer_store_dword v8, v7, s[36:39], 0 offen sc1\n"
      "v_mov_b32 v9, v8\n"
      "v_writelane_b32 v9, 64, 0\n"
      "s_waitcnt vmcnt(1)\n"
      "s_barrier\n"
      "buffer_store_dword v9, v7, s[36:39], 0 offen sc1\n"
      "s_branch .Lexit\n"
      ".Lwo:\n"
      "s_bfe_u32 s29, s48, 0x10001\n"
      "s_lshl_b32 s29, s29, 1\n"
      "s_lshr_b32 s50, s48, 2\n"
      "s_or_b32 s29, s29, s50\n"
      "s_sub_u32 s30, s49, 8\n"
      "s_and_b32 s31, s28, 1\n"
      "v_lshrrev_b32 v130, 4, v1\n"
      "v_and_b32 v131, 15, v1\n"
      "v_lshrrev_b32 v132, 3, v1\n"
      "v_and_b32 v133, 7, v1\n"
      "v_lshrrev_b32 v134, 2, v1\n"
      "v_and_b32 v134, 4, v134\n"
      "v_xor_b32 v133, v133, v134\n"
      "v_lshlrev_b32 v133, 4, v133\n"
      "v_mul_u32_u24 v140, 1152, v132\n"
      "v_add_u32 v140, v140, v133\n"
      "v_and_b32 v134, 3, v132\n"
      "v_lshrrev_b32 v135, 2, v132\n"
      "v_mul_u32_u24 v135, 36, v135\n"
      "v_add3_u32 v134, v134, v135, 16\n"
      "v_mul_u32_u24 v141, 1152, v134\n"
      "v_add_u32 v141, v141, v133\n"
      "v_bfe_u32 v132, v1, 1, 3\n"
      "v_and_b32 v133, 1, v1\n"
      "v_lshlrev_b32 v133, 4, v133\n"
      "v_lshrrev_b32 v134, 1, v130\n"
      "v_mul_u32_u24 v134, 20, v134\n"
      "v_and_b32 v135, 1, v130\n"
      "v_lshl_add_u32 v134, v135, 3, v134\n"
      "v_add_u32 v134, v134, v132\n"
      "v_mul_u32_u24 v142, 1152, v134\n"
      "v_add_u32 v142, v142, v133\n"
      "v_and_b32 v134, 3, v132\n"
      "v_lshrrev_b32 v135, 2, v132\n"
      "v_mul_u32_u24 v135, 36, v135\n"
      "v_add3_u32 v134, v134, v135, 16\n"
      "v_mul_u32_u24 v135, 72, v130\n"
      "v_add_u32 v134, v134, v135\n"
      "v_mul_u32_u24 v143, 1152, v134\n"
      "v_add_u32 v143, v143, v133\n"
      "v_bfe_u32 v132, v1, 2, 2\n"
      "v_lshl_add_u32 v133, v130, 3, v132\n"
      "v_and_b32 v134, 3, v1\n"
      "s_mul_i32 s74, s28, 40960\n"
      "v_lshlrev_b32 v149, 5, v133\n"
      "v_lshl_add_u32 v149, v134, 3, v149\n"
      "v_add_u32 v149, s74, v149\n"
      "v_lshlrev_b32 v133, 7, v133\n"
      "v_lshl_add_u32 v133, v134, 4, v133\n"
      "v_add_u32 v133, s74, v133\n"
      "v_lshrrev_b32 v132, 1, v132\n"
      "v_lshl_add_u32 v147, v132, 6, v133\n"
      "v_xor_b32 v148, 64, v147\n"
      "s_mul_i32 s79, s30, 576\n"
      "s_lshr_b32 s50, s28, 1\n"
      "s_lshl_b32 s50, s50, 5\n"
      "s_add_u32 s80, s79, s50\n"
      "s_add_u32 s80, s80, 512\n"
      "s_lshl_b32 s50, s28, 7\n"
      "s_add_u32 s79, s79, s50\n"
      "s_lshl_b32 s51, s29, 1\n"
      "s_add_u32 s51, s51, s30\n"
      "s_add_u32 s51, s51, 32\n"
      "s_lshl_b32 s51, s51, 2\n"
      "s_add_u32 s51, s51, s28\n"
      "s_lshl_b32 s51, s51, 7\n"
      "s_add_u32 s51, s51, 81911808\n"
      "v_lshlrev_b32 v6, 2, v1\n"
      "v_lshrrev_b32 v132, 5, v1\n"
      "v_lshl_or_b32 v6, v132, 30, v6\n"
      "v_add_u32 v6, s51, v6\n"
      "s_waitcnt lgkmcnt(0)\n"
      "s_mov_b32 s36, s20\n"
      "s_and_b32 s37, s21, 0xffff\n"
      "s_mov_b32 s38, 0x4eaf200\n"
      "s_mov_b32 s39, 0x20000\n"
      "s_mov_b32 s98, 82439168\n"
      "buffer_load_dwordx2 v[252:253], off, s[36:39], s98 sc1\n"
      "s_mov_b32 s40, s12\n"
      "s_and_b32 s41, s13, 0xffff\n"
      "s_mov_b32 s42, 0xa2000\n"
      "s_mov_b32 s43, 0x20000\n"
      "s_add_u32 m0, s74, 0\n"
      "s_add_u32 s50, s79, 0\n"
      "buffer_load_dwordx4 v140, s[40:43], s50 offen lds\n"
      "s_add_u32 m0, s74, 1024\n"
      "s_add_u32 s50, s79, 9216\n"
      "buffer_load_dwordx4 v140, s[40:43], s50 offen lds\n"
      "s_add_u32 m0, s74, 2048\n"
      "s_add_u32 s50, s79, 23040\n"
      "buffer_load_dwordx4 v140, s[40:43], s50 offen lds\n"
      "s_add_u32 m0, s74, 3072\n"
      "s_add_u32 s50, s79, 32256\n"
      "buffer_load_dwordx4 v140, s[40:43], s50 offen lds\n"
      "s_add_u32 m0, s74, 4096\n"
      "s_add_u32 s50, s80, 0\n"
      "buffer_load_dwordx4 v142, s[40:43], s50 offen lds\n"
      "s_add_u32 m0, s74, 5120\n"
      "s_add_u32 s50, s79, 41472\n"
      "buffer_load_dwordx4 v140, s[40:43], s50 offen lds\n"
      "s_add_u32 m0, s74, 6144\n"
      "s_add_u32 s50, s79, 50688\n"
      "buffer_load_dwordx4 v140, s[40:43], s50 offen lds\n"
      "s_add_u32 m0, s74, 7168\n"
      "s_add_u32 s50, s79, 64512\n"
      "buffer_load_dwordx4 v140, s[40:43], s50 offen lds\n"
      "s_add_u32 m0, s74, 8192\n"
      "s_add_u32 s50, s79, 73728\n"
      "buffer_load_dwordx4 v140, s[40:43], s50 offen lds\n"
      "s_add_u32 m0, s74, 9216\n"
      "s_add_u32 s50, s80, 41472\n"
      "buffer_load_dwordx4 v142, s[40:43], s50 offen lds\n"
      "s_add_u32 m0, s74, 10240\n"
      "s_add_u32 s50, s79, 82944\n"
      "buffer_load_dwordx4 v140, s[40:43], s50 offen lds\n"
      "s_add_u32 m0, s74, 11264\n"
      "s_add_u32 s50, s79, 92160\n"
      "buffer_load_dwordx4 v140, s[40:43], s50 offen lds\n"
      "s_add_u32 m0, s74, 12288\n"
      "s_add_u32 s50, s79, 105984\n"
      "buffer_load_dwordx4 v140, s[40:43], s50 offen lds\n"
      "s_add_u32 m0, s74, 13312\n"
      "s_add_u32 s50, s79, 115200\n"
      "buffer_load_dwordx4 v140, s[40:43], s50 offen lds\n"
      "s_add_u32 m0, s74, 14336\n"
      "s_add_u32 s50, s80, 82944\n"
      "buffer_load_dwordx4 v142, s[40:43], s50 offen lds\n"
      "s_add_u32 m0, s74, 15360\n"
      "s_add_u32 s50, s79, 124416\n"
      "buffer_load_dwordx4 v140, s[40:43], s50 offen lds\n"
      "s_add_u32 m0, s74, 16384\n"
      "s_add_u32 s50, s79, 133632\n"
      "buffer_load_dwordx4 v140, s[40:43], s50 offen lds\n"
      "s_add_u32 m0, s74, 17408\n"
      "s_add_u32 s50, s79, 147456\n"
      "buffer_load_dwordx4 v140, s[40:43], s50 offen lds\n"
      "s_add_u32 m0, s74, 18432\n"
      "s_add_u32 s50, s79, 156672\n"
      "buffer_load_dwordx4 v140, s[40:43], s50 offen lds\n"
      "s_add_u32 m0, s74, 19456\n"
      "s_add_u32 s50, s80, 124416\n"
      "buffer_load_dwordx4 v142, s[40:43], s50 offen lds\n"
      "s_add_u32 m0, s74, 20480\n"
      "s_add_u32 s50, s79, 165888\n"
      "buffer_load_dwordx4 v140, s[40:43], s50 offen lds\n"
      "s_add_u32 m0, s74, 21504\n"
      "s_add_u32 s50, s79, 175104\n"
      "buffer_load_dwordx4 v140, s[40:43], s50 offen lds\n"
      "s_add_u32 m0, s74, 22528\n"
      "s_add_u32 s50, s79, 188928\n"
      "buffer_load_dwordx4 v140, s[40:43], s50 offen lds\n"
      "s_add_u32 m0, s74, 23552\n"
      "s_add_u32 s50, s79, 198144\n"
      "buffer_load_dwordx4 v140, s[40:43], s50 offen lds\n"
      "s_add_u32 m0, s74, 24576\n"
      "s_add_u32 s50, s80, 165888\n"
      "buffer_load_dwordx4 v142, s[40:43], s50 offen lds\n"
      "s_add_u32 m0, s74, 25600\n"
      "s_add_u32 s50, s79, 207360\n"
      "buffer_load_dwordx4 v140, s[40:43], s50 offen lds\n"
      "s_add_u32 m0, s74, 26624\n"
      "s_add_u32 s50, s79, 216576\n"
      "buffer_load_dwordx4 v140, s[40:43], s50 offen lds\n"
      "s_add_u32 m0, s74, 27648\n"
      "s_add_u32 s50, s79, 230400\n"
      "buffer_load_dwordx4 v140, s[40:43], s50 offen lds\n"
      "s_add_u32 m0, s74, 28672\n"
      "s_add_u32 s50, s79, 239616\n"
      "buffer_load_dwordx4 v140, s[40:43], s50 offen lds\n"
      "s_add_u32 m0, s74, 29696\n"
      "s_add_u32 s50, s80, 207360\n"
      "buffer_load_dwordx4 v142, s[40:43], s50 offen lds\n"
      "s_add_u32 m0, s74, 30720\n"
      "s_add_u32 s50, s79, 248832\n"
      "buffer_load_dwordx4 v140, s[40:43], s50 offen lds\n"
      "s_add_u32 m0, s74, 31744\n"
      "s_add_u32 s50, s79, 258048\n"
      "buffer_load_dwordx4 v140, s[40:43], s50 offen lds\n"
      "s_add_u32 m0, s74, 32768\n"
      "s_add_u32 s50, s79, 271872\n"
      "buffer_load_dwordx4 v140, s[40:43], s50 offen lds\n"
      "s_add_u32 m0, s74, 33792\n"
      "s_add_u32 s50, s79, 281088\n"
      "buffer_load_dwordx4 v140, s[40:43], s50 offen lds\n"
      "s_add_u32 m0, s74, 34816\n"
      "s_add_u32 s50, s80, 248832\n"
      "buffer_load_dwordx4 v142, s[40:43], s50 offen lds\n"
      "s_add_u32 m0, s74, 35840\n"
      "s_add_u32 s50, s79, 290304\n"
      "buffer_load_dwordx4 v140, s[40:43], s50 offen lds\n"
      "s_add_u32 m0, s74, 36864\n"
      "s_add_u32 s50, s79, 299520\n"
      "buffer_load_dwordx4 v140, s[40:43], s50 offen lds\n"
      "s_add_u32 m0, s74, 37888\n"
      "s_add_u32 s50, s79, 313344\n"
      "buffer_load_dwordx4 v140, s[40:43], s50 offen lds\n"
      "s_add_u32 m0, s74, 38912\n"
      "s_add_u32 s50, s79, 322560\n"
      "buffer_load_dwordx4 v140, s[40:43], s50 offen lds\n"
      "s_add_u32 m0, s74, 39936\n"
      "s_add_u32 s50, s80, 290304\n"
      "buffer_load_dwordx4 v142, s[40:43], s50 offen lds\n"
      "s_waitcnt vmcnt(40)\n"
      "v_readfirstlane_b32 s98, v252\n"
      "v_readfirstlane_b32 s99, v253\n"
      "v_mov_b32 v7, 0\n"
      "s_add_u32 s98, s98, 1\n"
      "s_addc_u32 s99, s99, 0\n"
      "v_writelane_b32 v7, s98, 2\n"
      "v_writelane_b32 v7, s99, 3\n"
      "buffer_store_dword v7, v6, s[36:39], 0 offen sc1\n"
      "v_and_b32 v132, 4, v1\n"
      "v_lshlrev_b32 v132, 7, v132\n"
      "s_lshl_b32 s51, s29, 2\n"
      "s_add_u32 s51, s51, s28\n"
      "s_lshl_b32 s51, s51, 10\n"
      "s_add_u32 s51, s51, 81911808\n"
      "v_add_u32 v4, s51, v132\n"
      "s_lshl_b32 s51, s29, 5\n"
      "s_sub_u32 s51, 96, s51\n"
      "s_mul_i32 s53, s51, 1536\n"
      "s_add_u32 s53, s53, 82042880\n"
      "v_min_u32 v28, 11, v1\n"
      "v_lshlrev_b32 v28, 7, v28\n"
      "s_waitcnt vmcnt(36)\n"
      "ds_read_b64_tr_b16 a[0:1], v147 offset:0\n"
      "ds_read_b64_tr_b16 a[2:3], v147 offset:512\n"
      "ds_read_b64_tr_b16 a[4:5], v147 offset:8\n"
      "ds_read_b64_tr_b16 a[6:7], v147 offset:520\n"
      "ds_read_b64_tr_b16 a[8:9], v148 offset:0\n"
      "ds_read_b64_tr_b16 a[10:11], v148 offset:512\n"
      "ds_read_b64_tr_b16 a[12:13], v148 offset:8\n"
      "ds_read_b64_tr_b16 a[14:15], v148 offset:520\n"
      "ds_read_b64_tr_b16 a[16:17], v149 offset:4096\n"
      "ds_read_b64_tr_b16 a[18:19], v149 offset:4224\n"
      "s_waitcnt vmcnt(31)\n"
      "ds_read_b64_tr_b16 a[20:21], v147 offset:5120\n"
      "ds_read_b64_tr_b16 a[22:23], v147 offset:5632\n"
      "ds_read_b64_tr_b16 a[24:25], v147 offset:5128\n"
      "ds_read_b64_tr_b16 a[26:27], v147 offset:5640\n"
      "ds_read_b64_tr_b16 a[28:29], v148 offset:5120\n"
      "ds_read_b64_tr_b16 a[30:31], v148 offset:5632\n"
      "ds_read_b64_tr_b16 a[32:33], v148 offset:5128\n"
      "ds_read_b64_tr_b16 a[34:35], v148 offset:5640\n"
      "ds_read_b64_tr_b16 a[36:37], v149 offset:9216\n"
      "ds_read_b64_tr_b16 a[38:39], v149 offset:9344\n"
      "s_waitcnt lgkmcnt(10)\n"
      "s_add_u32 m0, s74, 0\n"
      "s_add_u32 s50, s79, 331776\n"
      "buffer_load_dwordx4 v140, s[40:43], s50 offen lds\n"
      "s_add_u32 m0, s74, 1024\n"
      "s_add_u32 s50, s79, 340992\n"
      "buffer_load_dwordx4 v140, s[40:43], s50 offen lds\n"
      "s_add_u32 m0, s74, 2048\n"
      "s_add_u32 s50, s79, 354816\n"
      "buffer_load_dwordx4 v140, s[40:43], s50 offen lds\n"
      "s_add_u32 m0, s74, 3072\n"
      "s_add_u32 s50, s79, 364032\n"
      "buffer_load_dwordx4 v140, s[40:43], s50 offen lds\n"
      "s_add_u32 m0, s74, 4096\n"
      "s_add_u32 s50, s80, 331776\n"
      "buffer_load_dwordx4 v142, s[40:43], s50 offen lds\n"
      "s_waitcnt vmcnt(31)\n"
      "ds_read_b64_tr_b16 a[40:41], v147 offset:10240\n"
      "ds_read_b64_tr_b16 a[42:43], v147 offset:10752\n"
      "ds_read_b64_tr_b16 a[44:45], v147 offset:10248\n"
      "ds_read_b64_tr_b16 a[46:47], v147 offset:10760\n"
      "ds_read_b64_tr_b16 a[48:49], v148 offset:10240\n"
      "ds_read_b64_tr_b16 a[50:51], v148 offset:10752\n"
      "ds_read_b64_tr_b16 a[52:53], v148 offset:10248\n"
      "ds_read_b64_tr_b16 a[54:55], v148 offset:10760\n"
      "ds_read_b64_tr_b16 a[56:57], v149 offset:14336\n"
      "ds_read_b64_tr_b16 a[58:59], v149 offset:14464\n"
      "s_waitcnt lgkmcnt(10)\n"
      "s_add_u32 m0, s74, 5120\n"
      "s_add_u32 s50, s79, 373248\n"
      "buffer_load_dwordx4 v140, s[40:43], s50 offen lds\n"
      "s_add_u32 m0, s74, 6144\n"
      "s_add_u32 s50, s79, 382464\n"
      "buffer_load_dwordx4 v140, s[40:43], s50 offen lds\n"
      "s_add_u32 m0, s74, 7168\n"
      "s_add_u32 s50, s79, 396288\n"
      "buffer_load_dwordx4 v140, s[40:43], s50 offen lds\n"
      "s_add_u32 m0, s74, 8192\n"
      "s_add_u32 s50, s79, 405504\n"
      "buffer_load_dwordx4 v140, s[40:43], s50 offen lds\n"
      "s_add_u32 m0, s74, 9216\n"
      "s_add_u32 s50, s80, 373248\n"
      "buffer_load_dwordx4 v142, s[40:43], s50 offen lds\n"
      "s_waitcnt vmcnt(31)\n"
      "ds_read_b64_tr_b16 a[60:61], v147 offset:15360\n"
      "ds_read_b64_tr_b16 a[62:63], v147 offset:15872\n"
      "ds_read_b64_tr_b16 a[64:65], v147 offset:15368\n"
      "ds_read_b64_tr_b16 a[66:67], v147 offset:15880\n"
      "ds_read_b64_tr_b16 a[68:69], v148 offset:15360\n"
      "ds_read_b64_tr_b16 a[70:71], v148 offset:15872\n"
      "ds_read_b64_tr_b16 a[72:73], v148 offset:15368\n"
      "ds_read_b64_tr_b16 a[74:75], v148 offset:15880\n"
      "ds_read_b64_tr_b16 a[76:77], v149 offset:19456\n"
      "ds_read_b64_tr_b16 a[78:79], v149 offset:19584\n"
      "s_waitcnt lgkmcnt(10)\n"
      "s_add_u32 m0, s74, 10240\n"
      "s_add_u32 s50, s79, 414720\n"
      "buffer_load_dwordx4 v140, s[40:43], s50 offen lds\n"
      "s_add_u32 m0, s74, 11264\n"
      "s_add_u32 s50, s79, 423936\n"
      "buffer_load_dwordx4 v140, s[40:43], s50 offen lds\n"
      "s_add_u32 m0, s74, 12288\n"
      "s_add_u32 s50, s79, 437760\n"
      "buffer_load_dwordx4 v140, s[40:43], s50 offen lds\n"
      "s_add_u32 m0, s74, 13312\n"
      "s_add_u32 s50, s79, 446976\n"
      "buffer_load_dwordx4 v140, s[40:43], s50 offen lds\n"
      "s_add_u32 m0, s74, 14336\n"
      "s_add_u32 s50, s80, 414720\n"
      "buffer_load_dwordx4 v142, s[40:43], s50 offen lds\n"
      "s_waitcnt vmcnt(31)\n"
      "ds_read_b64_tr_b16 a[80:81], v147 offset:20480\n"
      "ds_read_b64_tr_b16 a[82:83], v147 offset:20992\n"
      "ds_read_b64_tr_b16 a[84:85], v147 offset:20488\n"
      "ds_read_b64_tr_b16 a[86:87], v147 offset:21000\n"
      "ds_read_b64_tr_b16 a[88:89], v148 offset:20480\n"
      "ds_read_b64_tr_b16 a[90:91], v148 offset:20992\n"
      "ds_read_b64_tr_b16 a[92:93], v148 offset:20488\n"
      "ds_read_b64_tr_b16 a[94:95], v148 offset:21000\n"
      "ds_read_b64_tr_b16 a[96:97], v149 offset:24576\n"
      "ds_read_b64_tr_b16 a[98:99], v149 offset:24704\n"
      "s_waitcnt lgkmcnt(10)\n"
      "s_add_u32 m0, s74, 15360\n"
      "s_add_u32 s50, s79, 456192\n"
      "buffer_load_dwordx4 v140, s[40:43], s50 offen lds\n"
      "s_add_u32 m0, s74, 16384\n"
      "s_add_u32 s50, s79, 465408\n"
      "buffer_load_dwordx4 v140, s[40:43], s50 offen lds\n"
      "s_add_u32 m0, s74, 17408\n"
      "s_add_u32 s50, s79, 479232\n"
      "buffer_load_dwordx4 v140, s[40:43], s50 offen lds\n"
      "s_add_u32 m0, s74, 18432\n"
      "s_add_u32 s50, s79, 488448\n"
      "buffer_load_dwordx4 v140, s[40:43], s50 offen lds\n"
      "s_add_u32 m0, s74, 19456\n"
      "s_add_u32 s50, s80, 456192\n"
      "buffer_load_dwordx4 v142, s[40:43], s50 offen lds\n"
      "s_waitcnt vmcnt(31)\n"
      "ds_read_b64_tr_b16 a[100:101], v147 offset:25600\n"
      "ds_read_b64_tr_b16 a[102:103], v147 offset:26112\n"
      "ds_read_b64_tr_b16 a[104:105], v147 offset:25608\n"
      "ds_read_b64_tr_b16 a[106:107], v147 offset:26120\n"
      "ds_read_b64_tr_b16 a[108:109], v148 offset:25600\n"
      "ds_read_b64_tr_b16 a[110:111], v148 offset:26112\n"
      "ds_read_b64_tr_b16 a[112:113], v148 offset:25608\n"
      "ds_read_b64_tr_b16 a[114:115], v148 offset:26120\n"
      "ds_read_b64_tr_b16 a[116:117], v149 offset:29696\n"
      "ds_read_b64_tr_b16 a[118:119], v149 offset:29824\n"
      "s_waitcnt lgkmcnt(10)\n"
      "s_add_u32 m0, s74, 20480\n"
      "s_add_u32 s50, s79, 497664\n"
      "buffer_load_dwordx4 v140, s[40:43], s50 offen lds\n"
      "s_add_u32 m0, s74, 21504\n"
      "s_add_u32 s50, s79, 506880\n"
      "buffer_load_dwordx4 v140, s[40:43], s50 offen lds\n"
      "s_add_u32 m0, s74, 22528\n"
      "s_add_u32 s50, s79, 520704\n"
      "buffer_load_dwordx4 v140, s[40:43], s50 offen lds\n"
      "s_add_u32 m0, s74, 23552\n"
      "s_add_u32 s50, s79, 529920\n"
      "buffer_load_dwordx4 v140, s[40:43], s50 offen lds\n"
      "s_add_u32 m0, s74, 24576\n"
      "s_add_u32 s50, s80, 497664\n"
      "buffer_load_dwordx4 v142, s[40:43], s50 offen lds\n"
      "s_waitcnt vmcnt(31)\n"
      "ds_read_b64_tr_b16 a[120:121], v147 offset:30720\n"
      "ds_read_b64_tr_b16 a[122:123], v147 offset:31232\n"
      "ds_read_b64_tr_b16 a[124:125], v147 offset:30728\n"
      "ds_read_b64_tr_b16 a[126:127], v147 offset:31240\n"
      "ds_read_b64_tr_b16 a[128:129], v148 offset:30720\n"
      "ds_read_b64_tr_b16 a[130:131], v148 offset:31232\n"
      "ds_read_b64_tr_b16 a[132:133], v148 offset:30728\n"
      "ds_read_b64_tr_b16 a[134:135], v148 offset:31240\n"
      "ds_read_b64_tr_b16 a[136:137], v149 offset:34816\n"
      "ds_read_b64_tr_b16 a[138:139], v149 offset:34944\n"
      "s_waitcnt lgkmcnt(10)\n"
      "s_add_u32 m0, s74, 25600\n"
      "s_add_u32 s50, s79, 539136\n"
      "buffer_load_dwordx4 v140, s[40:43], s50 offen lds\n"
      "s_add_u32 m0, s74, 26624\n"
      "s_add_u32 s50, s79, 548352\n"
      "buffer_load_dwordx4 v140, s[40:43], s50 offen lds\n"
      "s_add_u32 m0, s74, 27648\n"
      "s_add_u32 s50, s79, 562176\n"
      "buffer_load_dwordx4 v140, s[40:43], s50 offen lds\n"
      "s_add_u32 m0, s74, 28672\n"
      "s_add_u32 s50, s79, 571392\n"
      "buffer_load_dwordx4 v140, s[40:43], s50 offen lds\n"
      "s_add_u32 m0, s74, 29696\n"
      "s_add_u32 s50, s80, 539136\n"
      "buffer_load_dwordx4 v142, s[40:43], s50 offen lds\n"
      "s_waitcnt vmcnt(31)\n"
      "ds_read_b64_tr_b16 a[140:141], v147 offset:35840\n"
      "ds_read_b64_tr_b16 a[142:143], v147 offset:36352\n"
      "ds_read_b64_tr_b16 a[144:145], v147 offset:35848\n"
      "ds_read_b64_tr_b16 a[146:147], v147 offset:36360\n"
      "ds_read_b64_tr_b16 a[148:149], v148 offset:35840\n"
      "ds_read_b64_tr_b16 a[150:151], v148 offset:36352\n"
      "ds_read_b64_tr_b16 a[152:153], v148 offset:35848\n"
      "ds_read_b64_tr_b16 a[154:155], v148 offset:36360\n"
      "ds_read_b64_tr_b16 a[156:157], v149 offset:39936\n"
      "ds_read_b64_tr_b16 a[158:159], v149 offset:40064\n"
      "s_waitcnt lgkmcnt(10)\n"
      "s_add_u32 m0, s74, 30720\n"
      "s_add_u32 s50, s79, 580608\n"
      "buffer_load_dwordx4 v140, s[40:43], s50 offen lds\n"
      "s_add_u32 m0, s74, 31744\n"
      "s_add_u32 s50, s79, 589824\n"
      "buffer_load_dwordx4 v140, s[40:43], s50 offen lds\n"
      "s_add_u32 m0, s74, 32768\n"
      "s_add_u32 s50, s79, 603648\n"
      "buffer_load_dwordx4 v140, s[40:43], s50 offen lds\n"
      "s_add_u32 m0, s74, 33792\n"
      "s_add_u32 s50, s79, 612864\n"
      "buffer_load_dwordx4 v140, s[40:43], s50 offen lds\n"
      "s_add_u32 m0, s74, 34816\n"
      "s_add_u32 s50, s80, 580608\n"
      "buffer_load_dwordx4 v142, s[40:43], s50 offen lds\n"
      "s_waitcnt vmcnt(30)\n"
      "ds_read_b64_tr_b16 a[160:161], v147 offset:0\n"
      "ds_read_b64_tr_b16 a[162:163], v147 offset:512\n"
      "ds_read_b64_tr_b16 a[164:165], v147 offset:8\n"
      "ds_read_b64_tr_b16 a[166:167], v147 offset:520\n"
      "ds_read_b64_tr_b16 a[168:169], v148 offset:0\n"
      "ds_read_b64_tr_b16 a[170:171], v148 offset:512\n"
      "ds_read_b64_tr_b16 a[172:173], v148 offset:8\n"
      "ds_read_b64_tr_b16 a[174:175], v148 offset:520\n"
      "ds_read_b64_tr_b16 a[176:177], v149 offset:4096\n"
      "ds_read_b64_tr_b16 a[178:179], v149 offset:4224\n"
      "s_waitcnt lgkmcnt(10)\n"
      "s_add_u32 m0, s74, 35840\n"
      "s_add_u32 s50, s79, 622080\n"
      "buffer_load_dwordx4 v140, s[40:43], s50 offen lds\n"
      "s_add_u32 m0, s74, 36864\n"
      "s_add_u32 s50, s79, 631296\n"
      "buffer_load_dwordx4 v140, s[40:43], s50 offen lds\n"
      "s_add_u32 m0, s74, 37888\n"
      "s_add_u32 s50, s79, 645120\n"
      "buffer_load_dwordx4 v140, s[40:43], s50 offen lds\n"
      "s_add_u32 m0, s74, 38912\n"
      "s_add_u32 s50, s79, 654336\n"
      "buffer_load_dwordx4 v140, s[40:43], s50 offen lds\n"
      "s_add_u32 m0, s74, 39936\n"
      "s_add_u32 s50, s80, 622080\n"
      "buffer_load_dwordx4 v142, s[40:43], s50 offen lds\n"
      "s_waitcnt vmcnt(30)\n"
      "ds_read_b64_tr_b16 a[180:181], v147 offset:5120\n"
      "ds_read_b64_tr_b16 a[182:183], v147 offset:5632\n"
      "ds_read_b64_tr_b16 a[184:185], v147 offset:5128\n"
      "ds_read_b64_tr_b16 a[186:187], v147 offset:5640\n"
      "ds_read_b64_tr_b16 a[188:189], v148 offset:5120\n"
      "ds_read_b64_tr_b16 a[190:191], v148 offset:5632\n"
      "ds_read_b64_tr_b16 a[192:193], v148 offset:5128\n"
      "ds_read_b64_tr_b16 a[194:195], v148 offset:5640\n"
      "ds_read_b64_tr_b16 a[196:197], v149 offset:9216\n"
      "ds_read_b64_tr_b16 a[198:199], v149 offset:9344\n"
      "s_waitcnt lgkmcnt(10)\n"
      "s_add_u32 m0, s74, 0\n"
      "s_add_u32 s50, s79, 0\n"
      "buffer_load_dwordx4 v141, s[40:43], s50 offen lds\n"
      "s_add_u32 m0, s74, 1024\n"
      "s_add_u32 s50, s79, 82944\n"
      "buffer_load_dwordx4 v141, s[40:43], s50 offen lds\n"
      "s_add_u32 m0, s74, 2048\n"
      "s_add_u32 s50, s79, 165888\n"
      "buffer_load_dwordx4 v141, s[40:43], s50 offen lds\n"
      "s_add_u32 m0, s74, 3072\n"
      "s_add_u32 s50, s79, 248832\n"
      "buffer_load_dwordx4 v141, s[40:43], s50 offen lds\n"
      "s_add_u32 m0, s74, 4096\n"
      "s_add_u32 s50, s80, 0\n"
      "buffer_load_dwordx4 v143, s[40:43], s50 offen lds\n"
      "s_waitcnt vmcnt(30)\n"
      "ds_read_b64_tr_b16 a[200:201], v147 offset:10240\n"
      "ds_read_b64_tr_b16 a[202:203], v147 offset:10752\n"
      "ds_read_b64_tr_b16 a[204:205], v147 offset:10248\n"
      "ds_read_b64_tr_b16 a[206:207], v147 offset:10760\n"
      "ds_read_b64_tr_b16 a[208:209], v148 offset:10240\n"
      "ds_read_b64_tr_b16 a[210:211], v148 offset:10752\n"
      "ds_read_b64_tr_b16 a[212:213], v148 offset:10248\n"
      "ds_read_b64_tr_b16 a[214:215], v148 offset:10760\n"
      "ds_read_b64_tr_b16 a[216:217], v149 offset:14336\n"
      "ds_read_b64_tr_b16 a[218:219], v149 offset:14464\n"
      "s_waitcnt lgkmcnt(10)\n"
      "s_add_u32 m0, s74, 5120\n"
      "s_add_u32 s50, s79, 331776\n"
      "buffer_load_dwordx4 v141, s[40:43], s50 offen lds\n"
      "s_add_u32 m0, s74, 6144\n"
      "s_add_u32 s50, s79, 414720\n"
      "buffer_load_dwordx4 v141, s[40:43], s50 offen lds\n"
      "s_add_u32 m0, s74, 7168\n"
      "s_add_u32 s50, s79, 497664\n"
      "buffer_load_dwordx4 v141, s[40:43], s50 offen lds\n"
      "s_add_u32 m0, s74, 8192\n"
      "s_add_u32 s50, s79, 580608\n"
      "buffer_load_dwordx4 v141, s[40:43], s50 offen lds\n"
      "s_add_u32 m0, s74, 9216\n"
      "s_add_u32 s50, s80, 331776\n"
      "buffer_load_dwordx4 v143, s[40:43], s50 offen lds\n"
      "s_waitcnt vmcnt(30)\n"
      "ds_read_b64_tr_b16 a[220:221], v147 offset:15360\n"
      "ds_read_b64_tr_b16 a[222:223], v147 offset:15872\n"
      "ds_read_b64_tr_b16 a[224:225], v147 offset:15368\n"
      "ds_read_b64_tr_b16 a[226:227], v147 offset:15880\n"
      "ds_read_b64_tr_b16 a[228:229], v148 offset:15360\n"
      "ds_read_b64_tr_b16 a[230:231], v148 offset:15872\n"
      "ds_read_b64_tr_b16 a[232:233], v148 offset:15368\n"
      "ds_read_b64_tr_b16 a[234:235], v148 offset:15880\n"
      "ds_read_b64_tr_b16 a[236:237], v149 offset:19456\n"
      "ds_read_b64_tr_b16 a[238:239], v149 offset:19584\n"
      "buffer_load_dwordx2 v[20:21], v28, s[36:39], s53 offen offset:8 sc1\n"
      "buffer_load_dwordx2 v[24:25], v4, s[36:39], 0 offen offset:8 sc1\n"
      "s_waitcnt vmcnt(27)\n"
      "ds_read_b64_tr_b16 a[240:241], v147 offset:20480\n"
      "ds_read_b64_tr_b16 a[242:243], v147 offset:20992\n"
      "ds_read_b64_tr_b16 a[244:245], v147 offset:20488\n"
      "ds_read_b64_tr_b16 a[246:247], v147 offset:21000\n"
      "ds_read_b64_tr_b16 a[248:249], v148 offset:20480\n"
      "ds_read_b64_tr_b16 a[250:251], v148 offset:20992\n"
      "ds_read_b64_tr_b16 a[252:253], v148 offset:20488\n"
      "ds_read_b64_tr_b16 a[254:255], v148 offset:21000\n"
      "ds_read_b64_tr_b16 v[150:151], v149 offset:24576\n"
      "ds_read_b64_tr_b16 v[152:153], v149 offset:24704\n"
      "s_waitcnt vmcnt(22)\n"
      "ds_read_b64_tr_b16 v[154:155], v147 offset:25600\n"
      "ds_read_b64_tr_b16 v[156:157], v147 offset:26112\n"
      "ds_read_b64_tr_b16 v[158:159], v147 offset:25608\n"
      "ds_read_b64_tr_b16 v[160:161], v147 offset:26120\n"
      "ds_read_b64_tr_b16 v[162:163], v148 offset:25600\n"
      "ds_read_b64_tr_b16 v[164:165], v148 offset:26112\n"
      "ds_read_b64_tr_b16 v[166:167], v148 offset:25608\n"
      "ds_read_b64_tr_b16 v[168:169], v148 offset:26120\n"
      "ds_read_b64_tr_b16 v[170:171], v149 offset:29696\n"
      "ds_read_b64_tr_b16 v[172:173], v149 offset:29824\n"
      "s_waitcnt vmcnt(17)\n"
      "ds_read_b64_tr_b16 v[174:175], v147 offset:30720\n"
      "ds_read_b64_tr_b16 v[176:177], v147 offset:31232\n"
      "ds_read_b64_tr_b16 v[178:179], v147 offset:30728\n"
      "ds_read_b64_tr_b16 v[180:181], v147 offset:31240\n"
      "ds_read_b64_tr_b16 v[182:183], v148 offset:30720\n"
      "ds_read_b64_tr_b16 v[184:185], v148 offset:31232\n"
      "ds_read_b64_tr_b16 v[186:187], v148 offset:30728\n"
      "ds_read_b64_tr_b16 v[188:189], v148 offset:31240\n"
      "ds_read_b64_tr_b16 v[190:191], v149 offset:34816\n"
      "ds_read_b64_tr_b16 v[192:193], v149 offset:34944\n"
      "s_waitcnt vmcnt(12)\n"
      "ds_read_b64_tr_b16 v[194:195], v147 offset:35840\n"
      "ds_read_b64_tr_b16 v[196:197], v147 offset:36352\n"
      "ds_read_b64_tr_b16 v[198:199], v147 offset:35848\n"
      "ds_read_b64_tr_b16 v[200:201], v147 offset:36360\n"
      "ds_read_b64_tr_b16 v[202:203], v148 offset:35840\n"
      "ds_read_b64_tr_b16 v[204:205], v148 offset:36352\n"
      "ds_read_b64_tr_b16 v[206:207], v148 offset:35848\n"
      "ds_read_b64_tr_b16 v[208:209], v148 offset:36360\n"
      "ds_read_b64_tr_b16 v[210:211], v149 offset:39936\n"
      "ds_read_b64_tr_b16 v[212:213], v149 offset:40064\n"
      "s_waitcnt vmcnt(7)\n"
      "ds_read_b64_tr_b16 v[214:215], v147 offset:0\n"
      "ds_read_b64_tr_b16 v[216:217], v147 offset:512\n"
      "ds_read_b64_tr_b16 v[218:219], v147 offset:8\n"
      "ds_read_b64_tr_b16 v[220:221], v147 offset:520\n"
      "ds_read_b64_tr_b16 v[222:223], v148 offset:0\n"
      "ds_read_b64_tr_b16 v[224:225], v148 offset:512\n"
      "ds_read_b64_tr_b16 v[226:227], v148 offset:8\n"
      "ds_read_b64_tr_b16 v[228:229], v148 offset:520\n"
      "ds_read_b64_tr_b16 v[230:231], v149 offset:4096\n"
      "ds_read_b64_tr_b16 v[232:233], v149 offset:4224\n"
      "s_waitcnt vmcnt(2)\n"
      "ds_read_b64_tr_b16 v[234:235], v147 offset:5120\n"
      "ds_read_b64_tr_b16 v[236:237], v147 offset:5632\n"
      "ds_read_b64_tr_b16 v[238:239], v147 offset:5128\n"
      "ds_read_b64_tr_b16 v[240:241], v147 offset:5640\n"
      "ds_read_b64_tr_b16 v[242:243], v148 offset:5120\n"
      "ds_read_b64_tr_b16 v[244:245], v148 offset:5632\n"
      "ds_read_b64_tr_b16 v[246:247], v148 offset:5128\n"
      "ds_read_b64_tr_b16 v[248:249], v148 offset:5640\n"
      "ds_read_b64_tr_b16 v[250:251], v149 offset:9216\n"
      "ds_read_b64_tr_b16 v[252:253], v149 offset:9344\n"
      "s_mov_b32 s66, -5\n"
      "s_mov_b32 s76, 0\n"
      "v_mov_b32 v134, 0x40000000\n"
      "s_mul_i32 s68, s29, 1310720\n"
      "s_lshl_b32 s50, s28, 12\n"
      "s_add_u32 s68, s68, s50\n"
      "s_add_u32 s68, s68, 5222400\n"
      "s_mov_b32 s67, 0\n"
      "s_mov_b32 s72, 40960\n"
      "s_mov_b32 s74, s50\n"
      "s_lshl_b32 s75, s28, 9\n"
      "s_add_u32 s75, s75, 16384\n"
      "v_and_b32 v132, 3, v1\n"
      "v_lshlrev_b32 v132, 7, v132\n"
      "v_and_b32 v133, 60, v1\n"
      "v_add_u32 v132, v132, v133\n"
      "s_mul_i32 s51, s28, 3072\n"
      "s_sub_u32 s51, 16384, s51\n"
      "v_add_u32 v8, s51, v132\n"
      "v_add_u32 v9, 256, v8\n"
      "s_lshl_b32 s51, s79, 4\n"
      "v_add_u32 v10, s51, v2\n"
      "v_lshrrev_b32 v132, 1, v130\n"
      "v_lshl_add_u32 v132, v132, 4, v131\n"
      "v_lshlrev_b32 v132, 4, v132\n"
      "v_and_b32 v133, 1, v130\n"
      "v_lshl_add_u32 v132, v133, 3, v132\n"
      "s_lshl_b32 s51, s80, 4\n"
      "v_add_u32 v11, s51, v132\n"
      "s_mul_i32 s70, s29, 1331200\n"
      "s_add_u32 s70, s70, 10424320\n"
      "s_lshl_b32 s51, s29, 5\n"
      "s_sub_u32 s51, 96, s51\n"
      "s_mul_i32 s78, s51, 20480\n"
      "v_mov_b32 v3, v2\n"
      "v_lshrrev_b32 v132, 5, v1\n"
      "v_lshlrev_b32 v132, 2, v132\n"
      "v_lshl_add_u32 v132, v131, 3, v132\n"
      "s_lshl_b32 s51, s30, 2\n"
      "s_add_u32 s51, s51, s28\n"
      "s_lshl_b32 s51, s51, 7\n"
      "s_add_u32 s51, s51, 38912\n"
      "v_add_u32 v12, s51, v132\n"
      "v_mov_b32 v5, 0\n"
      "s_mov_b32 s77, 0\n"
      "s_mov_b32 s79, 20480\n"
      "s_add_u32 s80, s78, 4294926336\n"
      "s_mov_b32 s78, 0x40000000\n"
      "s_waitcnt vmcnt(0)\n"
      "v_cmp_ne_u64 vcc, s[98:99], v[20:21]\n"
      "s_cbranch_vccz .Lbk20\n"
      "s_mov_b32 s56, 0x20000\n"
      ".Lsp20:\n"
      "buffer_load_dwordx2 v[20:21], v28, s[36:39], s53 offen offset:8 sc1\n"
      "s_waitcnt vmcnt(0)\n"
      "v_cmp_ne_u64 vcc, s[98:99], v[20:21]\n"
      "s_cbranch_vccz .Lbk20\n"
      "s_sleep 4\n"
      "s_sub_u32 s56, s56, 1\n"
      "s_cbranch_scc0 .Lsp20\n"
      ".Lbk20:\n"
      "v_cmp_ne_u64 vcc, s[98:99], v[24:25]\n"
      "s_cbranch_vccz .Lbk21\n"
      "s_mov_b32 s56, 0x20000\n"
      ".Lsp21:\n"
      "buffer_load_dwordx2 v[20:21], v4, s[36:39], 0 offen offset:8 sc1\n"
      "s_waitcnt vmcnt(0)\n"
      "v_cmp_ne_u64 vcc, s[98:99], v[20:21]\n"
      "s_cbranch_vccz .Lbk21\n"
      "s_sleep 4\n"
      "s_sub_u32 s56, s56, 1\n"
      "s_cbranch_scc0 .Lsp21\n"
      ".Lbk21:\n"
      "s_cmp_eq_u32 s28, 0\n"
      "s_cselect_b32 s50, 0, 0x40000000\n"
      "v_or_b32 v6, s50, v6\n"
      "s_waitcnt vmcnt(0) lgkmcnt(0)\n"
      "s_barrier\n"
      "s_cmp_eq_u32 s31, 1\n"
      "s_cbranch_scc1 .Lwo_e0\n"
      ".Lwo_loop0:\n"
      "v_mfma_f32_16x16x32_bf16 v[44:47], v[182:185], v[28:31], v[44:47]\n"
      "ds_read_b128 v[20:23], v3 offset:0\n"
      "v_mfma_f32_16x16x32_bf16 v[44:47], v[202:205], v[32:35], v[44:47]\n"
      "buffer_store_dword v7, v6, s[36:39], 0 offen sc1\n"
      "v_mfma_f32_16x16x32_bf16 v[48:51], v[186:189], v[28:31], v[48:51]\n"
      "s_cmp_lt_u32 s66, 64\n"
      "v_mfma_f32_16x16x32_bf16 v[48:51], v[206:209], v[32:35], v[48:51]\n"
      "s_waitcnt lgkmcnt(2)\n"
      "v_mfma_f32_16x16x32_bf16 v[36:39], v[214:217], v[14:17], v[36:39]\n"
      "s_waitcnt lgkmcnt(1)\n"
      "ds_read_b128 v[24:27], v3 offset:1024\n"
      "v_mfma_f32_16x16x32_bf16 v[36:39], v[234:237], v[72:75], v[36:39]\n"
      "s_cselect_b32 s71, s70, 0x40000000\n"
      "v_mfma_f32_16x16x32_bf16 v[40:43], v[218:221], v[14:17], v[40:43]\n"
      "s_add_u32 s70, s70, 20480\n"
      "v_mfma_f32_16x16x32_bf16 v[40:43], v[238:241], v[72:75], v[40:43]\n"
      "s_add_u32 s66, s66, 1\n"
      "v_mfma_f32_16x16x32_bf16 v[44:47], v[222:225], v[14:17], v[44:47]\n"
      "s_max_i32 s73, s66, 0\n"
      "ds_read_b128 v[28:31], v3 offset:2048\n"
      "v_mfma_f32_16x16x32_bf16 v[44:47], v[242:245], v[72:75], v[44:47]\n"
      "s_add_u32 s68, s68, 20480\n"
      "v_mfma_f32_16x16x32_bf16 v[48:51], v[226:229], v[14:17], v[48:51]\n"
      "s_add_u32 s48, s66, 2\n"
      "v_cvt_pk_bf16_f32 v106, v36, v37\n"
      "v_cvt_pk_bf16_f32 v107, v38, v39\n"
      "v_mfma_f32_16x16x32_bf16 v[48:51], v[246:249], v[72:75], v[48:51]\n"
      "s_waitcnt lgkmcnt(2)\n"
      "v_mul_f32 v122, v36, v36\n"
      "v_mul_f32 v123, v37, v37\n"
      "v_mfma_f32_16x16x32_bf16 v[52:55], a[0:3], v[20:23], v[76:79]\n"
      "s_waitcnt lgkmcnt(1)\n"
      "ds_read_b128 v[32:35], v3 offset:3072\n"
      "v_cvt_pk_bf16_f32 v108, v40, v41\n"
      "v_mfma_f32_16x16x32_bf16 v[52:55], a[20:23], v[24:27], v[52:55]\n"
      "s_cmp_lt_u32 s48, 64\n"
      "v_cvt_pk_bf16_f32 v109, v42, v43\n"
      "v_add_f32 v120, v36, v38\n"
      "v_mfma_f32_16x16x32_bf16 v[56:59], a[4:7], v[20:23], v[80:83]\n"
      "s_cselect_b32 s69, s68, 0x40000000\n"
      "v_cvt_pk_bf16_f32 v110, v44, v45\n"
      "v_cvt_pk_bf16_f32 v111, v46, v47\n"
      "v_mfma_f32_16x16x32_bf16 v[56:59], a[24:27], v[24:27], v[56:59]\n"
      "s_cselect_b32 s78, s80, 0x40000000\n"
      "v_fmac_f32 v122, v38, v38\n"
      "v_add_f32 v121, v37, v39\n"
      "v_mfma_f32_16x16x32_bf16 v[60:63], a[8:11], v[20:23], v[84:87]\n"
      "s_add_u32 m0, s74, s72\n"
      "buffer_store_dwordx4 v[106:109], v10, s[36:39], s71 offen sc1\n"
      "v_mfma_f32_16x16x32_bf16 v[60:63], a[28:31], v[24:27], v[60:63]\n"
      "ds_read_b128 v[14:17], v3 offset:4096\n"
      "v_cvt_pk_bf16_f32 v112, v48, v49\n"
      "v_mfma_f32_16x16x32_bf16 v[64:67], a[12:15], v[20:23], v[88:91]\n"
      "v_cvt_pk_bf16_f32 v113, v50, v51\n"
      "v_writelane_b32 v7, s73, 0\n"
      "v_mfma_f32_16x16x32_bf16 v[64:67], a[32:35], v[24:27], v[64:67]\n"
      "v_fmac_f32 v123, v39, v39\n"
      "v_add_f32 v120, v120, v40\n"
      "v_mfma_f32_16x16x32_bf16 v[68:71], a[16:19], v[20:23], v[92:95]\n"
      "v_fmac_f32 v122, v40, v40\n"
      "v_add_f32 v121, v121, v41\n"
      "v_mfma_f32_16x16x32_bf16 v[68:71], a[36:39], v[24:27], v[68:71]\n"
      "s_waitcnt lgkmcnt(2)\n"
      "v_fmac_f32 v123, v41, v41\n"
      "v_add_f32 v120, v120, v42\n"
      "v_mfma_f32_16x16x32_bf16 v[52:55], a[40:43], v[28:31], v[52:55]\n"
      "s_waitcnt lgkmcnt(1)\n"
      "ds_read_b128 v[72:75], v3 offset:5120\n"
      "v_fmac_f32 v122, v42, v42\n"
      "v_mfma_f32_16x16x32_bf16 v[52:55], a[60:63], v[32:35], v[52:55]\n"
      "buffer_store_dwordx4 v[110:113], v10, s[36:39], s71 offen offset:1024 sc1\n"
      "v_mfma_f32_16x16x32_bf16 v[56:59], a[44:47], v[28:31], v[56:59]\n"
      "v_add_f32 v121, v121, v43\n"
      "v_fmac_f32 v123, v43, v43\n"
      "v_mfma_f32_16x16x32_bf16 v[56:59], a[64:67], v[32:35], v[56:59]\n"
      "v_add_f32 v120, v120, v44\n"
      "v_fmac_f32 v122, v44, v44\n"
      "v_mfma_f32_16x16x32_bf16 v[60:63], a[48:51], v[28:31], v[60:63]\n"
      "v_add_f32 v121, v121, v45\n"
      "v_fmac_f32 v123, v45, v45\n"
      "v_mfma_f32_16x16x32_bf16 v[60:63], a[68:71], v[32:35], v[60:63]\n"
      "ds_read_b128 v[20:23], v3 offset:6144\n"
      "v_add_f32 v120, v120, v46\n"
      "v_mfma_f32_16x16x32_bf16 v[64:67], a[52:55], v[28:31], v[64:67]\n"
      "v_fmac_f32 v122, v46, v46\n"
      "v_add_f32 v121, v121, v47\n"
      "v_mfma_f32_16x16x32_bf16 v[64:67], a[72:75], v[32:35], v[64:67]\n"
      "buffer_load_dwordx4 v[96:99], v10, s[36:39], s78 offen sc1\n"
      "v_mfma_f32_16x16x32_bf16 v[68:71], a[56:59], v[28:31], v[68:71]\n"
      "v_fmac_f32 v123, v47, v47\n"
      "v_add_f32 v120, v120, v48\n"
      "v_mfma_f32_16x16x32_bf16 v[68:71], a[76:79], v[32:35], v[68:71]\n"
      "s_waitcnt lgkmcnt(2)\n"
      "v_fmac_f32 v122, v48, v48\n"
      "v_add_f32 v121, v121, v49\n"
      "v_mfma_f32_16x16x32_bf16 v[52:55], a[80:83], v[14:17], v[52:55]\n"
      "s_waitcnt lgkmcnt(1)\n"
      "ds_read_b128 v[24:27], v3 offset:7168\n"
      "v_fmac_f32 v123, v49, v49\n"
      "v_mfma_f32_16x16x32_bf16 v[52:55], a[100:103], v[72:75], v[52:55]\n"
      "v_add_f32 v120, v120, v50\n"
      "v_fmac_f32 v122, v50, v50\n"
      "v_mfma_f32_16x16x32_bf16 v[56:59], a[84:87], v[14:17], v[56:59]\n"
      "buffer_load_dwordx4 v[100:103], v10, s[36:39], s78 offen offset:1024 sc1\n"
      "v_mfma_f32_16x16x32_bf16 v[56:59], a[104:107], v[72:75], v[56:59]\n"
      "v_add_f32 v121, v121, v51\n"
      "v_fmac_f32 v123, v51, v51\n"
      "v_mfma_f32_16x16x32_bf16 v[60:63], a[88:91], v[14:17], v[60:63]\n"
      "v_add_f32 v120, v120, v121\n"
      "v_add_f32 v122, v122, v123\n"
      "v_mfma_f32_16x16x32_bf16 v[60:63], a[108:111], v[72:75], v[60:63]\n"
      "ds_read_b128 v[28:31], v3 offset:8192\n"
      "v_permlane32_swap_b32 v120, v122\n"
      "v_mfma_f32_16x16x32_bf16 v[64:67], a[92:95], v[14:17], v[64:67]\n"
      "v_add_f32 v120, v120, v122\n"
      "v_mfma_f32_16x16x32_bf16 v[64:67], a[112:115], v[72:75], v[64:67]\n"
      "buffer_load_dwordx2 v[104:105], v11, s[36:39], s78 offen sc1\n"
      "v_mfma_f32_16x16x32_bf16 v[68:71], a[96:99], v[14:17], v[68:71]\n"
      "v_mov_b32 v121, v120\n"
      "v_mfma_f32_16x16x32_bf16 v[68:71], a[116:119], v[72:75], v[68:71]\n"
      "s_waitcnt vmcnt(13) lgkmcnt(2)\n"
      "v_cmp_gt_u32 vcc, s67, v5\n"
      "v_permlane16_swap_b32 v120, v121\n"
      "v_mfma_f32_16x16x32_bf16 v[52:55], a[120:123], v[20:23], v[52:55]\n"
      "s_waitcnt lgkmcnt(1)\n"
      "ds_read_b128 v[32:35], v3 offset:9216\n"
      "v_add_f32 v120, v120, v121\n"
      "v_mfma_f32_16x16x32_bf16 v[52:55], a[140:143], v[24:27], v[52:55]\n"
      "buffer_store_dword v120, v12, s[36:39], s71 offen sc1\n"
      "v_mfma_f32_16x16x32_bf16 v[56:59], a[124:127], v[20:23], v[56:59]\n"
      "s_cbranch_vccnz .Lsl22\n"
      ".Lbk22:\n"
      "v_mfma_f32_16x16x32_bf16 v[56:59], a[144:147], v[24:27], v[56:59]\n"
      "s_cmp_eq_u32 s66, 29\n"
      "buffer_load_dword v5, v4, s[36:39], 0 offen sc1\n"
      "v_mfma_f32_16x16x32_bf16 v[60:63], a[128:131], v[20:23], v[60:63]\n"
      "s_cselect_b32 s48, 2007040, s79\n"
      "v_mfma_f32_16x16x32_bf16 v[60:63], a[148:151], v[24:27], v[60:63]\n"
      "s_add_u32 s80, s80, s48\n"
      "ds_read_b128 v[14:17], v3 offset:10240\n"
      "v_mfma_f32_16x16x32_bf16 v[64:67], a[132:135], v[20:23], v[64:67]\n"
      "s_add_u32 s67, s66, 4\n"
      "buffer_load_dwordx4 v2, s[36:39], s69 offen sc1 lds\n"
      "v_mfma_f32_16x16x32_bf16 v[64:67], a[152:155], v[24:27], v[64:67]\n"
      "s_min_i32 s67, s67, 64\n"
      "v_mfma_f32_16x16x32_bf16 v[68:71], a[136:139], v[20:23], v[68:71]\n"
      "v_lshlrev_b32 v128, 16, v116\n"
      "v_and_b32 v129, 0xffff0000, v116\n"
      "v_mfma_f32_16x16x32_bf16 v[68:71], a[156:159], v[24:27], v[68:71]\n"
      "s_waitcnt lgkmcnt(2)\n"
      "v_lshlrev_b32 v130, 16, v117\n"
      "v_and_b32 v131, 0xffff0000, v117\n"
      "v_mfma_f32_16x16x32_bf16 v[52:55], a[160:163], v[28:31], v[52:55]\n"
      "s_waitcnt lgkmcnt(1)\n"
      "ds_read_b128 v[72:75], v3 offset:11264\n"
      "v_lshlrev_b32 v132, 16, v118\n"
      "v_mfma_f32_16x16x32_bf16 v[52:55], a[180:183], v[32:35], v[52:55]\n"
      "s_max_i32 s67, s67, 0\n"
      "buffer_load_dwordx4 v2, s[36:39], s69 offen offset:1024 sc1 lds\n"
      "v_mfma_f32_16x16x32_bf16 v[56:59], a[164:167], v[28:31], v[56:59]\n"
      "s_mov_b32 s76, s77\n"
      "v_and_b32 v133, 0xffff0000, v118\n"
      "v_lshlrev_b32 v134, 16, v119\n"
      "v_mfma_f32_16x16x32_bf16 v[56:59], a[184:187], v[32:35], v[56:59]\n"
      "s_add_u32 s49, s75, s72\n"
      "v_and_b32 v135, 0xffff0000, v119\n"
      "v_lshlrev_b32 v136, 16, v124\n"
      "v_mfma_f32_16x16x32_bf16 v[60:63], a[168:171], v[28:31], v[60:63]\n"
      "s_mov_b32 s77, s72\n"
      "v_and_b32 v137, 0xffff0000, v124\n"
      "v_lshlrev_b32 v138, 16, v125\n"
      "v_mfma_f32_16x16x32_bf16 v[60:63], a[188:191], v[32:35], v[60:63]\n"
      "s_add_u32 s72, s72, 20480\n"
      "ds_read_b128 v[20:23], v3 offset:12288\n"
      "v_and_b32 v139, 0xffff0000, v125\n"
      "v_mfma_f32_16x16x32_bf16 v[64:67], a[172:175], v[28:31], v[64:67]\n"
      "s_cmp_lt_u32 s72, 81920\n"
      "buffer_load_dwordx4 v2, s[36:39], s69 offen offset:2048 sc1 lds\n"
      "v_mfma_f32_16x16x32_bf16 v[64:67], a[192:195], v[32:35], v[64:67]\n"
      "s_cselect_b32 s72, s72, 0\n"
      "v_lshlrev_b32 v140, 16, v126\n"
      "v_and_b32 v141, 0xffff0000, v126\n"
      "v_mfma_f32_16x16x32_bf16 v[68:71], a[176:179], v[28:31], v[68:71]\n"
      "v_lshlrev_b32 v142, 16, v127\n"
      "v_and_b32 v143, 0xffff0000, v127\n"
      "v_mfma_f32_16x16x32_bf16 v[68:71], a[196:199], v[32:35], v[68:71]\n"
      "s_waitcnt lgkmcnt(2)\n"
      "v_mfma_f32_16x16x32_bf16 v[52:55], a[200:203], v[14:17], v[52:55]\n"
      "s_waitcnt lgkmcnt(1)\n"
      "ds_read_b128 v[24:27], v3 offset:13312\n"
      "v_mfma_f32_16x16x32_bf16 v[52:55], a[220:223], v[72:75], v[52:55]\n"
      "buffer_load_dwordx4 v2, s[36:39], s69 offen offset:3072 sc1 lds\n"
      "v_mfma_f32_16x16x32_bf16 v[56:59], a[204:207], v[14:17], v[56:59]\n"
      "s_mov_b32 m0, s49\n"
      "v_mfma_f32_16x16x32_bf16 v[56:59], a[224:227], v[72:75], v[56:59]\n"
      "v_mfma_f32_16x16x32_bf16 v[60:63], a[208:211], v[14:17], v[60:63]\n"
      "v_mfma_f32_16x16x32_bf16 v[60:63], a[228:231], v[72:75], v[60:63]\n"
      "ds_read_b128 v[28:31], v3 offset:14336\n"
      "v_mfma_f32_16x16x32_bf16 v[64:67], a[212:215], v[14:17], v[64:67]\n"
      "buffer_load_dword v8, s[36:39], s69 offen sc1 lds\n"
      "v_mfma_f32_16x16x32_bf16 v[64:67], a[232:235], v[72:75], v[64:67]\n"
      "v_mfma_f32_16x16x32_bf16 v[68:71], a[216:219], v[14:17], v[68:71]\n"
      "v_mfma_f32_16x16x32_bf16 v[68:71], a[236:239], v[72:75], v[68:71]\n"
      "s_waitcnt lgkmcnt(2)\n"
      "buffer_load_dword v9, s[36:39], s69 offen offset:256 sc1 lds\n"
      "v_mfma_f32_16x16x32_bf16 v[52:55], a[240:243], v[20:23], v[52:55]\n"
      "s_waitcnt lgkmcnt(1)\n"
      "ds_read_b128 v[32:35], v3 offset:15360\n"
      "v_mfma_f32_16x16x32_bf16 v[52:55], v[154:157], v[24:27], v[52:55]\n"
      "s_cmp_lt_i32 s66, 63\n"
      "v_mfma_f32_16x16x32_bf16 v[56:59], a[244:247], v[20:23], v[56:59]\n"
      "v_mfma_f32_16x16x32_bf16 v[56:59], v[158:161], v[24:27], v[56:59]\n"
      "v_mfma_f32_16x16x32_bf16 v[60:63], a[248:251], v[20:23], v[60:63]\n"
      "v_mfma_f32_16x16x32_bf16 v[60:63], v[162:165], v[24:27], v[60:63]\n"
      "ds_read_b128 v[14:17], v3 offset:16384\n"
      "v_mfma_f32_16x16x32_bf16 v[64:67], a[252:255], v[20:23], v[64:67]\n"
      "s_waitcnt vmcnt(11)\n"
      "s_barrier\n"
      "v_mfma_f32_16x16x32_bf16 v[64:67], v[166:169], v[24:27], v[64:67]\n"
      "v_mfma_f32_16x16x32_bf16 v[68:71], v[150:153], v[20:23], v[68:71]\n"
      ".Lmw00:\n"
      "s_cbranch_scc0 .Lwo_t1\n"
      ".Lwo_e0:\n"
      "v_mfma_f32_16x16x32_bf16 v[68:71], v[170:173], v[24:27], v[68:71]\n"
      "s_waitcnt lgkmcnt(2)\n"
      "buffer_store_dword v7, v6, s[36:39], 0 offen sc1\n"
      "v_mfma_f32_16x16x32_bf16 v[52:55], v[174:177], v[28:31], v[52:55]\n"
      "s_waitcnt lgkmcnt(1)\n"
      "ds_read_b128 v[72:75], v3 offset:17408\n"
      "v_mfma_f32_16x16x32_bf16 v[52:55], v[194:197], v[32:35], v[52:55]\n"
      "s_cmp_lt_u32 s66, 64\n"
      "v_add_u32 v3, s76, v2\n"
      "v_mfma_f32_16x16x32_bf16 v[56:59], v[178:181], v[28:31], v[56:59]\n"
      "s_cselect_b32 s71, s70, 0x40000000\n"
      "v_mfma_f32_16x16x32_bf16 v[56:59], v[198:201], v[32:35], v[56:59]\n"
      "s_add_u32 s70, s70, 20480\n"
      "v_mfma_f32_16x16x32_bf16 v[60:63], v[182:185], v[28:31], v[60:63]\n"
      "s_add_u32 s66, s66, 1\n"
      "v_mfma_f32_16x16x32_bf16 v[60:63], v[202:205], v[32:35], v[60:63]\n"
      "s_max_i32 s73, s66, 0\n"
      "ds_read_b128 v[20:23], v3 offset:0\n"
      "v_mfma_f32_16x16x32_bf16 v[64:67], v[186:189], v[28:31], v[64:67]\n"
      "s_add_u32 s68, s68, 20480\n"
      "v_mfma_f32_16x16x32_bf16 v[64:67], v[206:209], v[32:35], v[64:67]\n"
      "s_add_u32 s48, s66, 2\n"
      "v_mfma_f32_16x16x32_bf16 v[68:71], v[190:193], v[28:31], v[68:71]\n"
      "s_cmp_lt_u32 s48, 64\n"
      "v_mfma_f32_16x16x32_bf16 v[68:71], v[210:213], v[32:35], v[68:71]\n"
      "s_waitcnt lgkmcnt(2)\n"
      "v_mfma_f32_16x16x32_bf16 v[52:55], v[214:217], v[14:17], v[52:55]\n"
      "s_waitcnt lgkmcnt(1)\n"
      "ds_read_b128 v[24:27], v3 offset:1024\n"
      "v_mfma_f32_16x16x32_bf16 v[52:55], v[234:237], v[72:75], v[52:55]\n"
      "s_cselect_b32 s69, s68, 0x40000000\n"
      "v_mfma_f32_16x16x32_bf16 v[56:59], v[218:221], v[14:17], v[56:59]\n"
      "s_cselect_b32 s78, s80, 0x40000000\n"
      "v_mfma_f32_16x16x32_bf16 v[56:59], v[238:241], v[72:75], v[56:59]\n"
      "s_add_u32 m0, s74, s72\n"
      "v_writelane_b32 v7, s73, 0\n"
      "v_mfma_f32_16x16x32_bf16 v[60:63], v[222:225], v[14:17], v[60:63]\n"
      "v_mfma_f32_16x16x32_bf16 v[60:63], v[242:245], v[72:75], v[60:63]\n"
      "ds_read_b128 v[28:31], v3 offset:2048\n"
      "v_mfma_f32_16x16x32_bf16 v[64:67], v[226:229], v[14:17], v[64:67]\n"
      "v_cvt_pk_bf16_f32 v106, v52, v53\n"
      "v_cvt_pk_bf16_f32 v107, v54, v55\n"
      "v_mfma_f32_16x16x32_bf16 v[64:67], v[246:249], v[72:75], v[64:67]\n"
      "v_mul_f32 v122, v52, v52\n"
      "v_mul_f32 v123, v53, v53\n"
      "v_mfma_f32_16x16x32_bf16 v[68:71], v[230:233], v[14:17], v[68:71]\n"
      "v_cvt_pk_bf16_f32 v108, v56, v57\n"
      "v_cvt_pk_bf16_f32 v109, v58, v59\n"
      "v_mfma_f32_16x16x32_bf16 v[68:71], v[250:253], v[72:75], v[68:71]\n"
      "s_waitcnt lgkmcnt(2)\n"
      "buffer_store_dwordx4 v[106:109], v10, s[36:39], s71 offen sc1\n"
      "v_mfma_f32_16x16x32_bf16 v[36:39], a[0:3], v[20:23], v[128:131]\n"
      "s_waitcnt lgkmcnt(1)\n"
      "ds_read_b128 v[32:35], v3 offset:3072\n"
      "v_cvt_pk_bf16_f32 v110, v60, v61\n"
      "v_mfma_f32_16x16x32_bf16 v[36:39], a[20:23], v[24:27], v[36:39]\n"
      "v_cvt_pk_bf16_f32 v111, v62, v63\n"
      "v_add_f32 v120, v52, v54\n"
      "v_mfma_f32_16x16x32_bf16 v[40:43], a[4:7], v[20:23], v[132:135]\n"
      "v_cvt_pk_bf16_f32 v112, v64, v65\n"
      "v_cvt_pk_bf16_f32 v113, v66, v67\n"
      "v_mfma_f32_16x16x32_bf16 v[40:43], a[24:27], v[24:27], v[40:43]\n"
      "v_cvt_pk_bf16_f32 v114, v68, v69\n"
      "v_cvt_pk_bf16_f32 v115, v70, v71\n"
      "v_mfma_f32_16x16x32_bf16 v[44:47], a[8:11], v[20:23], v[136:139]\n"
      "ds_read_b128 v[14:17], v3 offset:4096\n"
      "v_fmac_f32 v122, v54, v54\n"
      "v_mfma_f32_16x16x32_bf16 v[44:47], a[28:31], v[24:27], v[44:47]\n"
      "buffer_store_dwordx4 v[110:113], v10, s[36:39], s71 offen offset:1024 sc1\n"
      "v_mfma_f32_16x16x32_bf16 v[48:51], a[12:15], v[20:23], v[140:143]\n"
      "v_add_f32 v121, v53, v55\n"
      "v_fmac_f32 v123, v55, v55\n"
      "v_mfma_f32_16x16x32_bf16 v[48:51], a[32:35], v[24:27], v[48:51]\n"
      "s_waitcnt lgkmcnt(2)\n"
      "v_add_f32 v120, v120, v56\n"
      "v_fmac_f32 v122, v56, v56\n"
      "v_mfma_f32_16x16x32_bf16 v[36:39], a[40:43], v[28:31], v[36:39]\n"
      "s_waitcnt lgkmcnt(1)\n"
      "ds_read_b128 v[72:75], v3 offset:5120\n"
      "v_add_f32 v121, v121, v57\n"
      "v_mfma_f32_16x16x32_bf16 v[36:39], a[60:63], v[32:35], v[36:39]\n"
      "v_fmac_f32 v123, v57, v57\n"
      "v_add_f32 v120, v120, v58\n"
      "v_mfma_f32_16x16x32_bf16 v[40:43], a[44:47], v[28:31], v[40:43]\n"
      "v_fmac_f32 v122, v58, v58\n"
      "v_add_f32 v121, v121, v59\n"
      "v_mfma_f32_16x16x32_bf16 v[40:43], a[64:67], v[32:35], v[40:43]\n"
      "buffer_store_dwordx2 v[114:115], v11, s[36:39], s71 offen sc1\n"
      "v_mfma_f32_16x16x32_bf16 v[44:47], a[48:51], v[28:31], v[44:47]\n"
      "ds_read_b128 v[20:23], v3 offset:6144\n"
      "v_fmac_f32 v123, v59, v59\n"
      "v_mfma_f32_16x16x32_bf16 v[44:47], a[68:71], v[32:35], v[44:47]\n"
      "v_add_f32 v120, v120, v60\n"
      "v_fmac_f32 v122, v60, v60\n"
      "v_mfma_f32_16x16x32_bf16 v[48:51], a[52:55], v[28:31], v[48:51]\n"
      "v_add_f32 v121, v121, v61\n"
      "v_fmac_f32 v123, v61, v61\n"
      "v_mfma_f32_16x16x32_bf16 v[48:51], a[72:75], v[32:35], v[48:51]\n"
      "s_waitcnt lgkmcnt(2)\n"
      "buffer_load_dwordx4 v[116:119], v10, s[36:39], s78 offen sc1\n"
      "v_mfma_f32_16x16x32_bf16 v[36:39], a[80:83], v[14:17], v[36:39]\n"
      "s_waitcnt lgkmcnt(1)\n"
      "ds_read_b128 v[24:27], v3 offset:7168\n"
      "v_add_f32 v120, v120, v62\n"
      "v_mfma_f32_16x16x32_bf16 v[36:39], a[100:103], v[72:75], v[36:39]\n"
      "v_fmac_f32 v122, v62, v62\n"
      "v_add_f32 v121, v121, v63\n"
      "v_mfma_f32_16x16x32_bf16 v[40:43], a[84:87], v[14:17], v[40:43]\n"
      "v_fmac_f32 v123, v63, v63\n"
      "v_add_f32 v120, v120, v64\n"
      "v_mfma_f32_16x16x32_bf16 v[40:43], a[104:107], v[72:75], v[40:43]\n"
      "v_fmac_f32 v122, v64, v64\n"
      "v_add_f32 v121, v121, v65\n"
      "v_mfma_f32_16x16x32_bf16 v[44:47], a[88:91], v[14:17], v[44:47]\n"
      "ds_read_b128 v[28:31], v3 offset:8192\n"
      "v_fmac_f32 v123, v65, v65\n"
      "v_mfma_f32_16x16x32_bf16 v[44:47], a[108:111], v[72:75], v[44:47]\n"
      "buffer_load_dwordx4 v[124:127], v10, s[36:39], s78 offen offset:1024 sc1\n"
      "v_mfma_f32_16x16x32_bf16 v[48:51], a[92:95], v[14:17], v[48:51]\n"
      "v_add_f32 v120, v120, v66\n"
      "v_fmac_f32 v122, v66, v66\n"
      "v_mfma_f32_16x16x32_bf16 v[48:51], a[112:115], v[72:75], v[48:51]\n"
      "s_waitcnt vmcnt(12) lgkmcnt(2)\n"
      "v_cmp_gt_u32 vcc, s67, v5\n"
      "v_add_f32 v121, v121, v67\n"
      "v_mfma_f32_16x16x32_bf16 v[36:39], a[120:123], v[20:23], v[36:39]\n"
      "s_waitcnt lgkmcnt(1)\n"
      "ds_read_b128 v[32:35], v3 offset:9216\n"
      "v_fmac_f32 v123, v67, v67\n"
      "v_mfma_f32_16x16x32_bf16 v[36:39], a[140:143], v[24:27], v[36:39]\n"
      "v_add_f32 v120, v120, v68\n"
      "v_fmac_f32 v122, v68, v68\n"
      "v_mfma_f32_16x16x32_bf16 v[40:43], a[124:127], v[20:23], v[40:43]\n"
      "s_cbranch_vccnz .Lsl23\n"
      ".Lbk23:\n"
      "v_mfma_f32_16x16x32_bf16 v[40:43], a[144:147], v[24:27], v[40:43]\n"
      "s_cmp_eq_u32 s66, 29\n"
      "buffer_load_dword v5, v4, s[36:39], 0 offen sc1\n"
      "v_mfma_f32_16x16x32_bf16 v[44:47], a[128:131], v[20:23], v[44:47]\n"
      "s_cselect_b32 s48, 2007040, s79\n"
      "ds_read_b128 v[14:17], v3 offset:10240\n"
      "v_add_f32 v121, v121, v69\n"
      "v_mfma_f32_16x16x32_bf16 v[44:47], a[148:151], v[24:27], v[44:47]\n"
      "s_add_u32 s80, s80, s48\n"
      "buffer_load_dwordx4 v2, s[36:39], s69 offen sc1 lds\n"
      "v_mfma_f32_16x16x32_bf16 v[48:51], a[132:135], v[20:23], v[48:51]\n"
      "v_lshlrev_b32 v76, 16, v96\n"
      "v_and_b32 v77, 0xffff0000, v96\n"
      "v_mfma_f32_16x16x32_bf16 v[48:51], a[152:155], v[24:27], v[48:51]\n"
      "s_waitcnt lgkmcnt(2)\n"
      "v_lshlrev_b32 v78, 16, v97\n"
      "v_and_b32 v79, 0xffff0000, v97\n"
      "v_mfma_f32_16x16x32_bf16 v[36:39], a[160:163], v[28:31], v[36:39]\n"
      "s_waitcnt lgkmcnt(1)\n"
      "ds_read_b128 v[72:75], v3 offset:11264\n"
      "v_lshlrev_b32 v80, 16, v98\n"
      "v_mfma_f32_16x16x32_bf16 v[36:39], a[180:183], v[32:35], v[36:39]\n"
      "s_add_u32 s67, s66, 4\n"
      "v_and_b32 v81, 0xffff0000, v98\n"
      "v_lshlrev_b32 v82, 16, v99\n"
      "v_mfma_f32_16x16x32_bf16 v[40:43], a[164:167], v[28:31], v[40:43]\n"
      "s_min_i32 s67, s67, 64\n"
      "buffer_load_dwordx4 v2, s[36:39], s69 offen offset:1024 sc1 lds\n"
      "v_mfma_f32_16x16x32_bf16 v[40:43], a[184:187], v[32:35], v[40:43]\n"
      "s_max_i32 s67, s67, 0\n"
      "v_and_b32 v83, 0xffff0000, v99\n"
      "v_lshlrev_b32 v84, 16, v100\n"
      "v_mfma_f32_16x16x32_bf16 v[44:47], a[168:171], v[28:31], v[44:47]\n"
      "s_mov_b32 s76, s77\n"
      "ds_read_b128 v[20:23], v3 offset:12288\n"
      "v_and_b32 v85, 0xffff0000, v100\n"
      "v_mfma_f32_16x16x32_bf16 v[44:47], a[188:191], v[32:35], v[44:47]\n"
      "s_add_u32 s49, s75, s72\n"
      "v_lshlrev_b32 v86, 16, v101\n"
      "v_and_b32 v87, 0xffff0000, v101\n"
      "v_mfma_f32_16x16x32_bf16 v[48:51], a[172:175], v[28:31], v[48:51]\n"
      "s_mov_b32 s77, s72\n"
      "v_lshlrev_b32 v88, 16, v102\n"
      "v_and_b32 v89, 0xffff0000, v102\n"
      "v_mfma_f32_16x16x32_bf16 v[48:51], a[192:195], v[32:35], v[48:51]\n"
      "s_waitcnt lgkmcnt(2)\n"
      "buffer_load_dwordx4 v2, s[36:39], s69 offen offset:2048 sc1 lds\n"
      "v_mfma_f32_16x16x32_bf16 v[36:39], a[200:203], v[14:17], v[36:39]\n"
      "s_waitcnt lgkmcnt(1)\n"
      "ds_read_b128 v[24:27], v3 offset:13312\n"
      "v_lshlrev_b32 v90, 16, v103\n"
      "v_mfma_f32_16x16x32_bf16 v[36:39], a[220:223], v[72:75], v[36:39]\n"
      "s_add_u32 s72, s72, 20480\n"
      "v_and_b32 v91, 0xffff0000, v103\n"
      "v_lshlrev_b32 v92, 16, v104\n"
      "v_mfma_f32_16x16x32_bf16 v[40:43], a[204:207], v[14:17], v[40:43]\n"
      "s_cmp_lt_u32 s72, 81920\n"
      "v_and_b32 v93, 0xffff0000, v104\n"
      "v_lshlrev_b32 v94, 16, v105\n"
      "v_mfma_f32_16x16x32_bf16 v[40:43], a[224:227], v[72:75], v[40:43]\n"
      "s_cselect_b32 s72, s72, 0\n"
      "v_and_b32 v95, 0xffff0000, v105\n"
      "v_fmac_f32 v123, v69, v69\n"
      "v_mfma_f32_16x16x32_bf16 v[44:47], a[208:211], v[14:17], v[44:47]\n"
      "ds_read_b128 v[28:31], v3 offset:14336\n"
      "v_add_f32 v120, v120, v70\n"
      "v_mfma_f32_16x16x32_bf16 v[44:47], a[228:231], v[72:75], v[44:47]\n"
      "buffer_load_dwordx4 v2, s[36:39], s69 offen offset:3072 sc1 lds\n"
      "v_mfma_f32_16x16x32_bf16 v[48:51], a[212:215], v[14:17], v[48:51]\n"
      "s_mov_b32 m0, s49\n"
      "v_fmac_f32 v122, v70, v70\n"
      "v_add_f32 v121, v121, v71\n"
      "v_mfma_f32_16x16x32_bf16 v[48:51], a[232:235], v[72:75], v[48:51]\n"
      "s_waitcnt lgkmcnt(2)\n"
      "v_fmac_f32 v123, v71, v71\n"
      "v_add_f32 v120, v120, v121\n"
      "v_mfma_f32_16x16x32_bf16 v[36:39], a[240:243], v[20:23], v[36:39]\n"
      "s_waitcnt lgkmcnt(1)\n"
      "ds_read_b128 v[32:35], v3 offset:15360\n"
      "v_add_f32 v122, v122, v123\n"
      "v_mfma_f32_16x16x32_bf16 v[36:39], v[154:157], v[24:27], v[36:39]\n"
      "s_nop 0\n"
      "v_permlane32_swap_b32 v120, v122\n"
      "v_mfma_f32_16x16x32_bf16 v[40:43], a[244:247], v[20:23], v[40:43]\n"
      "buffer_load_dword v8, s[36:39], s69 offen sc1 lds\n"
      "v_mfma_f32_16x16x32_bf16 v[40:43], v[158:161], v[24:27], v[40:43]\n"
      "v_add_f32 v120, v120, v122\n"
      "v_mfma_f32_16x16x32_bf16 v[44:47], a[248:251], v[20:23], v[44:47]\n"
      "s_cmp_lt_i32 s66, 63\n"
      "ds_read_b128 v[14:17], v3 offset:16384\n"
      "v_mov_b32 v121, v120\n"
      "v_mfma_f32_16x16x32_bf16 v[44:47], v[162:165], v[24:27], v[44:47]\n"
      "buffer_load_dword v9, s[36:39], s69 offen offset:256 sc1 lds\n"
      "v_mfma_f32_16x16x32_bf16 v[48:51], a[252:255], v[20:23], v[48:51]\n"
      "v_permlane16_swap_b32 v120, v121\n"
      "v_mfma_f32_16x16x32_bf16 v[48:51], v[166:169], v[24:27], v[48:51]\n"
      "s_waitcnt lgkmcnt(2)\n"
      "v_add_f32 v120, v120, v121\n"
      "v_mfma_f32_16x16x32_bf16 v[36:39], v[174:177], v[28:31], v[36:39]\n"
      "s_waitcnt lgkmcnt(1)\n"
      "ds_read_b128 v[72:75], v3 offset:17408\n"
      "v_mfma_f32_16x16x32_bf16 v[36:39], v[194:197], v[32:35], v[36:39]\n"
      "s_waitcnt vmcnt(9)\n"
      "v_add_u32 v3, s76, v2\n"
      "s_barrier\n"
      "v_mfma_f32_16x16x32_bf16 v[40:43], v[178:181], v[28:31], v[40:43]\n"
      "buffer_store_dword v120, v12, s[36:39], s71 offen sc1\n"
      "v_mfma_f32_16x16x32_bf16 v[40:43], v[198:201], v[32:35], v[40:43]\n"
      ".Lmw10:\n"
      "s_cbranch_scc1 .Lwo_loop0\n"
      ".Lwo_t0:\n"
      "s_waitcnt lgkmcnt(0)\n"
      "v_mfma_f32_16x16x32_bf16 v[44:47], v[182:185], v[28:31], v[44:47]\n"
      "v_mfma_f32_16x16x32_bf16 v[44:47], v[202:205], v[32:35], v[44:47]\n"
      "v_mfma_f32_16x16x32_bf16 v[48:51], v[186:189], v[28:31], v[48:51]\n"
      "v_mfma_f32_16x16x32_bf16 v[48:51], v[206:209], v[32:35], v[48:51]\n"
      "v_mfma_f32_16x16x32_bf16 v[36:39], v[214:217], v[14:17], v[36:39]\n"
      "v_mfma_f32_16x16x32_bf16 v[36:39], v[234:237], v[72:75], v[36:39]\n"
      "v_mfma_f32_16x16x32_bf16 v[40:43], v[218:221], v[14:17], v[40:43]\n"
      "s_waitcnt vmcnt(10)\n"
      "buffer_store_dword v7, v6, s[36:39], 0 offen sc1\n"
      "v_mfma_f32_16x16x32_bf16 v[40:43], v[238:241], v[72:75], v[40:43]\n"
      "v_mfma_f32_16x16x32_bf16 v[44:47], v[222:225], v[14:17], v[44:47]\n"
      "v_mfma_f32_16x16x32_bf16 v[44:47], v[242:245], v[72:75], v[44:47]\n"
      "s_movk_i32 s73, 65\n"
      "v_writelane_b32 v7, s73, 0\n"
      "v_mfma_f32_16x16x32_bf16 v[48:51], v[226:229], v[14:17], v[48:51]\n"
      "v_cvt_pk_bf16_f32 v106, v36, v37\n"
      "v_cvt_pk_bf16_f32 v107, v38, v39\n"
      "v_mfma_f32_16x16x32_bf16 v[48:51], v[246:249], v[72:75], v[48:51]\n"
      "v_mul_f32 v122, v36, v36\n"
      "v_mul_f32 v123, v37, v37\n"
      "v_cvt_pk_bf16_f32 v108, v40, v41\n"
      "v_cvt_pk_bf16_f32 v109, v42, v43\n"
      "v_add_f32 v120, v36, v38\n"
      "v_fmac_f32 v122, v38, v38\n"
      "buffer_store_dwordx4 v[106:109], v10, s[36:39], s70 offen sc1\n"
      "v_add_f32 v121, v37, v39\n"
      "v_fmac_f32 v123, v39, v39\n"
      "v_add_f32 v120, v120, v40\n"
      "v_cvt_pk_bf16_f32 v110, v44, v45\n"
      "v_cvt_pk_bf16_f32 v111, v46, v47\n"
      "v_fmac_f32 v122, v40, v40\n"
      "v_add_f32 v121, v121, v41\n"
      "v_fmac_f32 v123, v41, v41\n"
      "v_add_f32 v120, v120, v42\n"
      "v_fmac_f32 v122, v42, v42\n"
      "v_add_f32 v121, v121, v43\n"
      "v_cvt_pk_bf16_f32 v112, v48, v49\n"
      "v_cvt_pk_bf16_f32 v113, v50, v51\n"
      "v_fmac_f32 v123, v43, v43\n"
      "v_add_f32 v120, v120, v44\n"
      "v_fmac_f32 v122, v44, v44\n"
      "v_add_f32 v121, v121, v45\n"
      "v_fmac_f32 v123, v45, v45\n"
      "v_add_f32 v120, v120, v46\n"
      "v_fmac_f32 v122, v46, v46\n"
      "v_add_f32 v121, v121, v47\n"
      "v_fmac_f32 v123, v47, v47\n"
      "v_add_f32 v120, v120, v48\n"
      "buffer_store_dwordx4 v[110:113], v10, s[36:39], s70 offen offset:1024 sc1\n"
      "v_fmac_f32 v122, v48, v48\n"
      "v_add_f32 v121, v121, v49\n"
      "v_fmac_f32 v123, v49, v49\n"
      "v_add_f32 v120, v120, v50\n"
      "v_fmac_f32 v122, v50, v50\n"
      "v_add_f32 v121, v121, v51\n"
      "v_fmac_f32 v123, v51, v51\n"
      "v_add_f32 v120, v120, v121\n"
      "v_add_f32 v122, v122, v123\n"
      "s_nop 1\n"
      "v_permlane32_swap_b32 v120, v122\n"
      "v_add_f32 v120, v120, v122\n"
      "v_mov_b32 v121, v120\n"
      "s_nop 1\n"
      "v_permlane16_swap_b32 v120, v121\n"
      "v_add_f32 v120, v120, v121\n"
      "buffer_store_dword v120, v12, s[36:39], s70 offen sc1\n"
      "s_waitcnt vmcnt(0) lgkmcnt(0)\n"
      "s_barrier\n"
      "buffer_store_dword v7, v6, s[36:39], 0 offen sc1\n"
      "s_branch .Lexit\n"
      ".Lwo_t1:\n"
      "s_waitcnt lgkmcnt(0)\n"
      "v_mfma_f32_16x16x32_bf16 v[68:71], v[170:173], v[24:27], v[68:71]\n"
      "v_mfma_f32_16x16x32_bf16 v[52:55], v[174:177], v[28:31], v[52:55]\n"
      "s_waitcnt vmcnt(11)\n"
      "buffer_store_dword v7, v6, s[36:39], 0 offen sc1\n"
      "ds_read_b128 v[72:75], v3 offset:17408\n"
      "v_mfma_f32_16x16x32_bf16 v[52:55], v[194:197], v[32:35], v[52:55]\n"
      "v_mfma_f32_16x16x32_bf16 v[56:59], v[178:181], v[28:31], v[56:59]\n"
      "v_mfma_f32_16x16x32_bf16 v[56:59], v[198:201], v[32:35], v[56:59]\n"
      "v_mfma_f32_16x16x32_bf16 v[60:63], v[182:185], v[28:31], v[60:63]\n"
      "v_mfma_f32_16x16x32_bf16 v[60:63], v[202:205], v[32:35], v[60:63]\n"
      "v_mfma_f32_16x16x32_bf16 v[64:67], v[186:189], v[28:31], v[64:67]\n"
      "v_mfma_f32_16x16x32_bf16 v[64:67], v[206:209], v[32:35], v[64:67]\n"
      "v_mfma_f32_16x16x32_bf16 v[68:71], v[190:193], v[28:31], v[68:71]\n"
      "s_movk_i32 s73, 65\n"
      "v_writelane_b32 v7, s73, 0\n"
      "v_mfma_f32_16x16x32_bf16 v[68:71], v[210:213], v[32:35], v[68:71]\n"
      "v_mfma_f32_16x16x32_bf16 v[52:55], v[214:217], v[14:17], v[52:55]\n"
      "s_waitcnt lgkmcnt(0)\n"
      "v_mfma_f32_16x16x32_bf16 v[52:55], v[234:237], v[72:75], v[52:55]\n"
      "v_mfma_f32_16x16x32_bf16 v[56:59], v[218:221], v[14:17], v[56:59]\n"
      "v_mfma_f32_16x16x32_bf16 v[56:59], v[238:241], v[72:75], v[56:59]\n"
      "v_mfma_f32_16x16x32_bf16 v[60:63], v[222:225], v[14:17], v[60:63]\n"
      "v_mfma_f32_16x16x32_bf16 v[60:63], v[242:245], v[72:75], v[60:63]\n"
      "v_mfma_f32_16x16x32_bf16 v[64:67], v[226:229], v[14:17], v[64:67]\n"
      "s_nop 2\n"
      "v_cvt_pk_bf16_f32 v106, v52, v53\n"
      "v_cvt_pk_bf16_f32 v107, v54, v55\n"
      "v_mfma_f32_16x16x32_bf16 v[64:67], v[246:249], v[72:75], v[64:67]\n"
      "v_mul_f32 v122, v52, v52\n"
      "v_mul_f32 v123, v53, v53\n"
      "v_mfma_f32_16x16x32_bf16 v[68:71], v[230:233], v[14:17], v[68:71]\n"
      "v_cvt_pk_bf16_f32 v108, v56, v57\n"
      "v_cvt_pk_bf16_f32 v109, v58, v59\n"
      "v_mfma_f32_16x16x32_bf16 v[68:71], v[250:253], v[72:75], v[68:71]\n"
      "buffer_store_dwordx4 v[106:109], v10, s[36:39], s70 offen sc1\n"
      "v_add_f32 v120, v52, v54\n"
      "v_cvt_pk_bf16_f32 v110, v60, v61\n"
      "v_cvt_pk_bf16_f32 v111, v62, v63\n"
      "v_fmac_f32 v122, v54, v54\n"
      "v_add_f32 v121, v53, v55\n"
      "v_fmac_f32 v123, v55, v55\n"
      "v_add_f32 v120, v120, v56\n"
      "v_fmac_f32 v122, v56, v56\n"
      "v_add_f32 v121, v121, v57\n"
      "v_cvt_pk_bf16_f32 v112, v64, v65\n"
      "v_cvt_pk_bf16_f32 v113, v66, v67\n"
      "v_fmac_f32 v123, v57, v57\n"
      "v_add_f32 v120, v120, v58\n"
      "v_cvt_pk_bf16_f32 v114, v68, v69\n"
      "v_cvt_pk_bf16_f32 v115, v70, v71\n"
      "v_fmac_f32 v122, v58, v58\n"
      "v_add_f32 v121, v121, v59\n"
      "v_fmac_f32 v123, v59, v59\n"
      "v_add_f32 v120, v120, v60\n"
      "v_fmac_f32 v122, v60, v60\n"
      "v_add_f32 v121, v121, v61\n"
      "buffer_store_dwordx4 v[110:113], v10, s[36:39], s70 offen offset:1024 sc1\n"
      "v_fmac_f32 v123, v61, v61\n"
      "v_add_f32 v120, v120, v62\n"
      "v_fmac_f32 v122, v62, v62\n"
      "v_add_f32 v121, v121, v63\n"
      "v_fmac_f32 v123, v63, v63\n"
      "v_add_f32 v120, v120, v64\n"
      "v_fmac_f32 v122, v64, v64\n"
      "v_add_f32 v121, v121, v65\n"
      "v_fmac_f32 v123, v65, v65\n"
      "v_add_f32 v120, v120, v66\n"
      "v_fmac_f32 v122, v66, v66\n"
      "v_add_f32 v121, v121, v67\n"
      "v_fmac_f32 v123, v67, v67\n"
      "v_add_f32 v120, v120, v68\n"
      "v_fmac_f32 v122, v68, v68\n"
      "v_add_f32 v121, v121, v69\n"
      "v_fmac_f32 v123, v69, v69\n"
      "v_add_f32 v120, v120, v70\n"
      "v_fmac_f32 v122, v70, v70\n"
      "v_add_f32 v121, v121, v71\n"
      "v_fmac_f32 v123, v71, v71\n"
      "buffer_store_dwordx2 v[114:115], v11, s[36:39], s70 offen sc1\n"
      "v_add_f32 v120, v120, v121\n"
      "v_add_f32 v122, v122, v123\n"
      "s_nop 1\n"
      "v_permlane32_swap_b32 v120, v122\n"
      "v_add_f32 v120, v120, v122\n"
      "v_mov_b32 v121, v120\n"
      "s_nop 1\n"
      "v_permlane16_swap_b32 v120, v121\n"
      "v_add_f32 v120, v120, v121\n"
      "buffer_store_dword v120, v12, s[36:39], s70 offen sc1\n"
      "s_waitcnt vmcnt(0) lgkmcnt(0)\n"
      "s_barrier\n"
      "buffer_store_dword v7, v6, s[36:39], 0 offen sc1\n"
      "s_branch .Lexit\n"
      ".Lexit:\n"
      ".Lend:\n"
      "s_endpgm\n"
      ".Lsl0:\n"
      "s_mov_b32 s56, 0x20000\n"
      ".Lsp0:\n"
      "buffer_load_dwordx2 v[18:19], v102, s[36:39], s71 offen offset:8 sc1\n"
      "s_waitcnt vmcnt(0)\n"
      "v_cmp_ne_u64 vcc, s[98:99], v[18:19]\n"
      "s_cbranch_vccz .Lbk0\n"
      "s_sleep 4\n"
      "s_sub_u32 s56, s56, 1\n"
      "s_cbranch_scc0 .Lsp0\n"
      "s_branch .Lbk0\n"
      ".Lsl1:\n"
      "s_mov_b32 s56, 0x20000\n"
      ".Lsp1:\n"
      "buffer_load_dword v18, v5, s[36:39], s71 offen sc1\n"
      "s_waitcnt vmcnt(0)\n"
      "v_cmp_ne_u32 vcc, v16, v18\n"
      "s_cbranch_vccz .Lbk1\n"
      "s_sleep 4\n"
      "s_sub_u32 s56, s56, 1\n"
      "s_cbranch_scc0 .Lsp1\n"
      "s_branch .Lbk1\n"
      ".Ltg0:\n"
      "v_swap_b32 v108, v112\n"
      "v_swap_b32 v109, v113\n"
      "v_swap_b32 v110, v114\n"
      "v_swap_b32 v111, v115\n"
      "s_add_u32 s80, s80, s81\n"
      "s_xor_b32 s81, s81, 33\n"
      "s_branch .Ltb0\n"
      ".Lsl2:\n"
      "s_mov_b32 s56, 0x20000\n"
      ".Lsp2:\n"
      "buffer_load_dword v18, v5, s[36:39], s71 offen sc1\n"
      "s_waitcnt vmcnt(0)\n"
      "v_cmp_ne_u32 vcc, v16, v18\n"
      "s_cbranch_vccz .Lbk2\n"
      "s_sleep 4\n"
      "s_sub_u32 s56, s56, 1\n"
      "s_cbranch_scc0 .Lsp2\n"
      "s_branch .Lbk2\n"
      ".Ltg1:\n"
      "v_swap_b32 v108, v112\n"
      "v_swap_b32 v109, v113\n"
      "v_swap_b32 v110, v114\n"
      "v_swap_b32 v111, v115\n"
      "s_add_u32 s80, s80, s81\n"
      "s_xor_b32 s81, s81, 33\n"
      "s_branch .Ltb1\n"
      ".Lsl7:\n"
      "s_mov_b32 s56, 0x20000\n"
      ".Lsp7:\n"
      "buffer_load_dword v6, v5, s[36:39], 0 offen sc1\n"
      "s_waitcnt vmcnt(0)\n"
      "v_cmp_gt_u32 vcc, s67, v6\n"
      "s_cbranch_vccz .Lbk7\n"
      "s_sleep 4\n"
      "s_sub_u32 s56, s56, 1\n"
      "s_cbranch_scc0 .Lsp7\n"
      "s_branch .Lbk7\n"
      ".Lsl8:\n"
      "s_mov_b32 s56, 0x20000\n"
      ".Lsp8:\n"
      "buffer_load_dword v6, v5, s[36:39], 0 offen sc1\n"
      "s_waitcnt vmcnt(0)\n"
      "v_cmp_gt_u32 vcc, s67, v6\n"
      "s_cbranch_vccz .Lbk8\n"
      "s_sleep 4\n"
      "s_sub_u32 s56, s56, 1\n"
      "s_cbranch_scc0 .Lsp8\n"
      "s_branch .Lbk8\n"
      ".Lsl9:\n"
      "s_mov_b32 s56, 0x20000\n"
      ".Lsp9:\n"
      "buffer_load_dword v6, v5, s[36:39], 0 offen sc1\n"
      "s_waitcnt vmcnt(0)\n"
      "v_cmp_gt_u32 vcc, s67, v6\n"
      "s_cbranch_vccz .Lbk9\n"
      "s_sleep 4\n"
      "s_sub_u32 s56, s56, 1\n"
      "s_cbranch_scc0 .Lsp9\n"
      "s_branch .Lbk9\n"
      ".Lsl10:\n"
      "s_mov_b32 s56, 0x20000\n"
      ".Lsp10:\n"
      "buffer_load_dword v6, v5, s[36:39], 0 offen sc1\n"
      "s_waitcnt vmcnt(0)\n"
      "v_cmp_gt_u32 vcc, s67, v6\n"
      "s_cbranch_vccz .Lbk10\n"
      "s_sleep 4\n"
      "s_sub_u32 s56, s56, 1\n"
      "s_cbranch_scc0 .Lsp10\n"
      "s_branch .Lbk10\n"
      ".Lsl11:\n"
      "s_mov_b32 s56, 0x20000\n"
      ".Lsp11:\n"
      "buffer_load_dword v6, v5, s[36:39], 0 offen sc1\n"
      "s_waitcnt vmcnt(0)\n"
      "v_cmp_gt_u32 vcc, s67, v6\n"
      "s_cbranch_vccz .Lbk11\n"
      "s_sleep 4\n"
      "s_sub_u32 s56, s56, 1\n"
      "s_cbranch_scc0 .Lsp11\n"
      "s_branch .Lbk11\n"
      ".Lsl12:\n"
      "s_mov_b32 s56, 0x20000\n"
      ".Lsp12:\n"
      "buffer_load_dword v6, v5, s[36:39], 0 offen sc1\n"
      "s_waitcnt vmcnt(0)\n"
      "v_cmp_gt_u32 vcc, s67, v6\n"
      "s_cbranch_vccz .Lbk12\n"
      "s_sleep 4\n"
      "s_sub_u32 s56, s56, 1\n"
      "s_cbranch_scc0 .Lsp12\n"
      "s_branch .Lbk12\n"
      ".Lsl13:\n"
      "s_mov_b32 s56, 0x20000\n"
      ".Lsp13:\n"
      "buffer_load_dword v10, v9, s[36:39], 0 offen sc1\n"
      "s_waitcnt vmcnt(0)\n"
      "v_cmp_gt_u32 vcc, s74, v10\n"
      "s_cbranch_vccz .Lbk13\n"
      "s_sleep 4\n"
      "s_sub_u32 s56, s56, 1\n"
      "s_cbranch_scc0 .Lsp13\n"
      "s_branch .Lbk13\n"
      ".Lsl14:\n"
      "s_mov_b32 s56, 0x20000\n"
      ".Lsp14:\n"
      "buffer_load_dword v10, v9, s[36:39], 0 offen sc1\n"
      "s_waitcnt vmcnt(0)\n"
      "v_cmp_gt_u32 vcc, s74, v10\n"
      "s_cbranch_vccz .Lbk14\n"
      "s_sleep 4\n"
      "s_sub_u32 s56, s56, 1\n"
      "s_cbranch_scc0 .Lsp14\n"
      "s_branch .Lbk14\n"
      ".Lsl18:\n"
      "s_mov_b32 s56, 0x20000\n"
      ".Lsp18:\n"
      "buffer_load_dword v6, v5, s[36:39], 0 offen sc1\n"
      "s_waitcnt vmcnt(0)\n"
      "v_cmp_gt_u32 vcc, s67, v6\n"
      "s_cbranch_vccz .Lbk18\n"
      "s_sleep 4\n"
      "s_sub_u32 s56, s56, 1\n"
      "s_cbranch_scc0 .Lsp18\n"
      "s_branch .Lbk18\n"
      ".Lsl19:\n"
      "s_mov_b32 s56, 0x20000\n"
      ".Lsp19:\n"
      "buffer_load_dword v6, v5, s[36:39], 0 offen sc1\n"
      "s_waitcnt vmcnt(0)\n"
      "v_cmp_gt_u32 vcc, s67, v6\n"
      "s_cbranch_vccz .Lbk19\n"
      "s_sleep 4\n"
      "s_sub_u32 s56, s56, 1\n"
      "s_cbranch_scc0 .Lsp19\n"
      "s_branch .Lbk19\n"
      ".Lsl22:\n"
      "s_mov_b32 s56, 0x20000\n"
      ".Lsp22:\n"
      "buffer_load_dword v5, v4, s[36:39], 0 offen sc1\n"
      "s_waitcnt vmcnt(0)\n"
      "v_cmp_gt_u32 vcc, s67, v5\n"
      "s_cbranch_vccz .Lbk22\n"
      "s_sleep 4\n"
      "s_sub_u32 s56, s56, 1\n"
      "s_cbranch_scc0 .Lsp22\n"
      "s_branch .Lbk22\n"
      ".Lsl23:\n"
      "s_mov_b32 s56, 0x20000\n"
      ".Lsp23:\n"
      "buffer_load_dword v5, v4, s[36:39], 0 offen sc1\n"
      "s_waitcnt vmcnt(0)\n"
      "v_cmp_gt_u32 vcc, s67, v5\n"
      "s_cbranch_vccz .Lbk23\n"
      "s_sleep 4\n"
      "s_sub_u32 s56, s56, 1\n"
      "s_cbranch_scc0 .Lsp23\n"
      "s_branch .Lbk23\n"
      "s_endpgm\n"
      ".Lfin:\n"
      : "+v"(tid)
      : "s"(blockIdx.x), "s"(__builtin_amdgcn_kernarg_segment_ptr())
      : "v1", "v2", "v3", "v4", "v5", "v6", "v7", "v8", "v9", "v10", "v11", "v12", "v13", "v14", "v15", "v16", "v17", "v18", "v19", "v20", "v21", "v22", "v23", "v24", "v25", "v26", "v27", "v28", "v29", "v30", "v31", "v32", "v33", "v34", "v35", "v36", "v37", "v38", "v39", "v40", "v41", "v42", "v43", "v44", "v45", "v46", "v47", "v48", "v49", "v50", "v51", "v52", "v53", "v54", "v55", "v56", "v57", "v58", "v59", "v60", "v61", "v62", "v63", "v64", "v65", "v66", "v67", "v68", "v69", "v70", "v71", "v72", "v73", "v74", "v75", "v76", "v77", "v78", "v79", "v80", "v81", "v82", "v83", "v84", "v85", "v86", "v87", "v88", "v89", "v90", "v91", "v92", "v93", "v94", "v95", "v96", "v97", "v98", "v99", "v100", "v101", "v102", "v103", "v104", "v105", "v106", "v107", "v108", "v109", "v110", "v111", "v112", "v113", "v114", "v115", "v116", "v117", "v118", "v119", "v120", "v121", "v122", "v123", "v124", "v125", "v126", "v127", "v128", "v129", "v130", "v131", "v132", "v133", "v134", "v135", "v136", "v137", "v138", "v139", "v140", "v141", "v142", "v143", "v144", "v145", "v146", "v147", "v148", "v149", "v150", "v151", "v152", "v153", "v154", "v155", "v156", "v157", "v158", "v159", "v160", "v161", "v162", "v163", "v164", "v165", "v166", "v167", "v168", "v169", "v170", "v171", "v172", "v173", "v174", "v175", "v176", "v177", "v178", "v179", "v180", "v181", "v182", "v183", "v184", "v185", "v186", "v187", "v188", "v189", "v190", "v191", "v192", "v193", "v194", "v195", "v196", "v197", "v198", "v199", "v200", "v201", "v202", "v203", "v204", "v205", "v206", "v207", "v208", "v209", "v210", "v211", "v212", "v213", "v214", "v215", "v216", "v217", "v218", "v219", "v220", "v221", "v222", "v223", "v224", "v225", "v226", "v227", "v228", "v229", "v230", "v231", "v232", "v233", "v234", "v235", "v236", "v237", "v238", "v239", "v240", "v241", "v242", "v243", "v244", "v245", "v246", "v247", "v248", "v249", "v250", "v251", "v252", "v253", "v254", "v255", "a0", "a1", "a2", "a3", "a4", "a5", "a6", "a7", "a8", "a9", "a10", "a11", "a12", "a13", "a14", "a15", "a16", "a17", "a18", "a19", "a20", "a21", "a22", "a23", "a24", "a25", "a26", "a27", "a28", "a29", "a30", "a31", "a32", "a33", "a34", "a35", "a36", "a37", "a38", "a39", "a40", "a41", "a42", "a43", "a44", "a45", "a46", "a47", "a48", "a49", "a50", "a51", "a52", "a53", "a54", "a55", "a56", "a57", "a58", "a59", "a60", "a61", "a62", "a63", "a64", "a65", "a66", "a67", "a68", "a69", "a70", "a71", "a72", "a73", "a74", "a75", "a76", "a77", "a78", "a79", "a80", "a81", "a82", "a83", "a84", "a85", "a86", "a87", "a88", "a89", "a90", "a91", "a92", "a93", "a94", "a95", "a96", "a97", "a98", "a99", "a100", "a101", "a102", "a103", "a104", "a105", "a106", "a107", "a108", "a109", "a110", "a111", "a112", "a113", "a114", "a115", "a116", "a117", "a118", "a119", "a120", "a121", "a122", "a123", "a124", "a125", "a126", "a127", "a128", "a129", "a130", "a131", "a132", "a133", "a134", "a135", "a136", "a137", "a138", "a139", "a140", "a141", "a142", "a143", "a144", "a145", "a146", "a147", "a148", "a149", "a150", "a151", "a152", "a153", "a154", "a155", "a156", "a157", "a158", "a159", "a160", "a161", "a162", "a163", "a164", "a165", "a166", "a167", "a168", "a169", "a170", "a171", "a172", "a173", "a174", "a175", "a176", "a177", "a178", "a179", "a180", "a181", "a182", "a183", "a184", "a185", "a186", "a187", "a188", "a189", "a190", "a191", "a192", "a193", "a194", "a195", "a196", "a197", "a198", "a199", "a200", "a201", "a202", "a203", "a204", "a205", "a206", "a207", "a208", "a209", "a210", "a211", "a212", "a213", "a214", "a215", "a216", "a217", "a218", "a219", "a220", "a221", "a222", "a223", "a224", "a225", "a226", "a227", "a228", "a229", "a230", "a231", "a232", "a233", "a234", "a235", "a236", "a237", "a238", "a239", "a240", "a241", "a242", "a243", "a244", "a245", "a246", "a247", "a248", "a249", "a250", "a251", "a252", "a253", "a254", "a255", "s8", "s9", "s10", "s11", "s12", "s13", "s14", "s15", "s16", "s17", "s18", "s19", "s20", "s21", "s22", "s23", "s24", "s25", "s26", "s27", "s28", "s29", "s30", "s31", "s34", "s35", "s36", "s37", "s38", "s39", "s40", "s41", "s42", "s43", "s44", "s45", "s46", "s47", "s48", "s49", "s50", "s51", "s52", "s53", "s54", "s55", "s56", "s57", "s58", "s59", "s60", "s61", "s62", "s63", "s64", "s65", "s66", "s67", "s68", "s69", "s70", "s71", "s72", "s73", "s74", "s75", "s76", "s77", "s78", "s79", "s80", "s81", "s82", "s83", "s84", "s85", "s86", "s87", "s88", "s89", "s90", "s91", "s92", "s93", "s94", "s95", "s96", "s97", "s98", "s99", "vcc", "scc", "memory"
  );
}

extern "C" void kernel_launch(void* const* d_in, const int* in_sizes, int n_in, void* d_out, int out_size, void* d_ws,
                              size_t ws_size, hipStream_t stream) {
  char* ws = (char*)d_ws + ((256 - ((size_t)d_ws & 255)) & 255);
  (void)hipFuncSetAttribute((const void*)k_sp, hipFuncAttributeMaxDynamicSharedMemorySize, SP_LDS);
  k_sp<<<256, 256, SP_LDS, stream>>>((const u16*)d_in[0], (const u16*)d_in[1], (const u16*)d_in[2], (const u16*)d_in[4],
                                     (const u16*)d_in[5], (const u16*)d_in[6], ws, (u16*)d_out
  );
}
```

```cpp
#include <hip/hip_runtime.h>
#include <stdint.h>
#define SPDBG 0
constexpr size_t XS = 20480, WS_XB = 0, WS_O = 5283840, WS_X1 = 10526720, HS = 147456, WS_H = 15851520, PSS = 110592, WS_PS = 53600256, WS_FL = 81911808, WS_FX = 82042880, WS_EP = 82439168, WS_FW = 82439680, WS_END = 82506240;
typedef unsigned short u16;
constexpr int SP_LDS = 160 * 1024;

extern "C" __global__ __launch_bounds__(256, 1) void k_sp(const u16* X, const u16* Wqkv, const u16* Wo, const u16* Wg, const u16* Wu,
                                                          const u16* Wd, char* ws, u16* out
) {
  unsigned tid = threadIdx.x;
  asm volatile(
      ".Lbeg:\n"
      "s_load_dwordx16 s[8:23], %2, 0\n"
      "s_mov_b32 s34, %1\n"
      "v_lshrrev_b32 v3, 6, %0\n"
      "v_and_b32 v1, 63, %0\n"
      "s_nop 0\n"
      "v_readfirstlane_b32 s28, v3\n"
      "v_lshlrev_b32 v2, 4, v1\n"
      "s_and_b32 s48, s34, 7\n"
      "s_lshr_b32 s49, s34, 3\n"
      "s_and_b32 s50, s48, 1\n"
      "s_mul_i32 s50, s50, 10\n"
      "s_add_u32 s49, s49, s50\n"
      "s_cmp_lt_u32 s49, 8\n"
      "s_cbranch_scc1 .Lqkv\n"
      "s_cmp_lt_u32 s49, 19\n"
      "s_cbranch_scc0 .Lhelp\n"
      "s_cmp_lt_u32 s49, 10\n"
      "s_cbranch_scc0 .Ldn\n"
      "s_getpc_b64 vcc\n"
      "s_add_u32 vcc_lo, vcc_lo, .Lbeg-.\n"
      "s_addc_u32 vcc_hi, vcc_hi, -1\n"
      "s_lshl_b32 s50, s28, 13\n"
      "v_lshlrev_b32 v20, 7, v1\n"
      "v_add_u32 v20, s50, v20\n"
      "v_add_u32 v21, 0, v20\n"
      "v_min_u32 v21, .Lfin-.Lbeg-4, v21\n"
      "global_load_dword v249, v21, vcc\n"
      "v_add_u32 v21, 32768, v20\n"
      "v_min_u32 v21, .Lfin-.Lbeg-4, v21\n"
      "global_load_dword v250, v21, vcc\n"
      "v_add_u32 v21, 65536, v20\n"
      "v_min_u32 v21, .Lfin-.Lbeg-4, v21\n"
      "global_load_dword v251, v21, vcc\n"
      "s_branch .Lwo\n"
      ".Lhelp:\n"
      "s_bitcmp1_b32 s48, 0\n"
      "s_cbranch_scc1 .Lhelp1\n"
      "s_sub_u32 s53, s49, 19\n"
      "s_lshl_b32 s53, s53, 2\n"
      "s_add_u32 s53, s53, s28\n"
      "s_cmp_lt_u32 s53, 48\n"
      "s_cbranch_scc0 .Lgu\n"
      "v_min_u32 v62, 26, v1\n"
      "v_mul_u32_u24 v61, 57, v62\n"
      "v_lshrrev_b32 v61, 9, v61\n"
      "v_mul_u32_u24 v63, 9, v61\n"
      "v_sub_u32 v62, v62, v63\n"
      "v_mul_u32_u24 v63, 43, v62\n"
      "v_lshrrev_b32 v63, 7, v63\n"
      "v_mul_u32_u24 v64, 3, v63\n"
      "v_sub_u32 v64, v62, v64\n"
      "v_lshlrev_b32 v64, 7, v64\n"
      "v_add_u32 v64, 28, v64\n"
      "v_min_u32 v64, 284, v64\n"
      "v_mul_u32_u24 v63, 1152, v63\n"
      "s_mul_i32 s54, s53, 3\n"
      "v_add_u32 v61, s54, v61\n"
      "v_mul_u32_u24 v60, 3456, v61\n"
      "v_add3_u32 v60, v60, v63, v64\n"
      "s_mul_i32 s54, s48, 144\n"
      "s_mul_i32 s55, s53, 24\n"
      "s_mul_i32 s52, s53, 5462\n"
      "s_lshr_b32 s52, s52, 16\n"
      "s_mul_i32 s52, s52, 552960\n"
      "s_lshl_b32 s55, s55, 7\n"
      "s_add_u32 s52, s52, s55\n"
      "s_add_u32 s52, s52, 552960\n"
      "v_min_u32 v65, 23, v1\n"
      "v_lshlrev_b32 v65, 7, v65\n"
      "s_mul_i32 s55, s53, 6912\n"
      "v_min_u32 v59, 53, v1\n"
      "v_lshlrev_b32 v59, 7, v59\n"
      "s_mov_b32 s46, 0x240000\n"
      "s_mov_b32 s47, 0x20000\n"
      "s_mov_b32 s42, 0x1e6000\n"
      "s_mov_b32 s43, 0x20000\n"
      "s_waitcnt lgkmcnt(0)\n"
      "s_mov_b32 s44, s8\n"
      "s_and_b32 s45, s9, 0xffff\n"
      "buffer_load_dword v52, v65, s[44:47], s52 offen\n"
      "s_mov_b32 s40, s10\n"
      "s_and_b32 s41, s11, 0xffff\n"
      "s_mov_b32 s44, s12\n"
      "s_and_b32 s45, s13, 0xffff\n"
      "s_mov_b32 s46, 0xa2000\n"
      "buffer_load_dword v53, v60, s[40:43], s54 offen\n"
      "s_add_u32 s54, s54, 497664\n"
      "buffer_load_dword v54, v60, s[40:43], s54 offen\n"
      "s_add_u32 s54, s54, 497664\n"
      "buffer_load_dword v57, v59, s[44:47], s55 offen\n"
      "s_add_u32 s55, s55, 331776\n"
      "buffer_load_dword v55, v60, s[40:43], s54 offen\n"
      "s_add_u32 s54, s54, 497664\n"
      "buffer_load_dword v56, v60, s[40:43], s54 offen\n"
      "s_add_u32 s54, s54, 497664\n"
      "buffer_load_dword v58, v59, s[44:47], s55 offen\n"
      "s_sleep 8\n"
      "s_sleep 8\n"
      "s_sleep 8\n"
      "s_sleep 8\n"
      "s_branch .Lgu\n"
      ".Lhelp1:\n"
      "s_lshr_b32 s53, s48, 1\n"
      "s_mul_i32 s53, s53, 92\n"
      "s_sub_u32 s54, s49, 19\n"
      "s_lshl_b32 s54, s54, 2\n"
      "s_add_u32 s53, s53, s54\n"
      "s_add_u32 s53, s53, s28\n"
      "s_mul_i32 s54, s53, 5504\n"
      "s_mul_i32 s55, s53, 1920\n"
      "v_min_u32 v60, 42, v1\n"
      "v_lshlrev_b32 v60, 7, v60\n"
      "v_min_u32 v61, 14, v1\n"
      "v_lshlrev_b32 v61, 7, v61\n"
      "s_mov_b32 s42, 0x1e6000\n"
      "s_mov_b32 s43, 0x20000\n"
      "s_mov_b32 s46, 0xa2000\n"
      "s_mov_b32 s47, 0x20000\n"
      "s_waitcnt lgkmcnt(0)\n"
      "s_mov_b32 s40, s10\n"
      "s_and_b32 s41, s11, 0xffff\n"
      "s_mov_b32 s44, s12\n"
      "s_and_b32 s45, s13, 0xffff\n"
      "buffer_load_dword v55, v60, s[40:43], s54 offen\n"
      "buffer_load_dword v56, v61, s[44:47], s55 offen\n"
      "s_cmp_eq_u32 s49, 19\n"
      "s_cbranch_scc0 .Lgu\n"
      "s_getpc_b64 vcc\n"
      "s_add_u32 vcc_lo, vcc_lo, .Lbeg-.\n"
      "s_addc_u32 vcc_hi, vcc_hi, -1\n"
      "s_lshl_b32 s50, s28, 13\n"
      "v_lshlrev_b32 v60, 7, v1\n"
      "v_add_u32 v60, s50, v60\n"
      "v_add_u32 v61, 0, v60\n"
      "v_min_u32 v61, .Lfin-.Lbeg-4, v61\n"
      "global_load_dword v52, v61, vcc\n"
      "v_add_u32 v61, 32768, v60\n"
      "v_min_u32 v61, .Lfin-.Lbeg-4, v61\n"
      "global_load_dword v53, v61, vcc\n"
      "v_add_u32 v61, 65536, v60\n"
      "v_min_u32 v61, .Lfin-.Lbeg-4, v61\n"
      "global_load_dword v54, v61, vcc\n"
      "s_branch .Lgu\n"
      ".Lqkv:\n"
      "s_and_b32 s29, s49, 3\n"
      "s_lshr_b32 s30, s49, 2\n"
      "s_add_u32 s30, s30, s48\n"
      "s_and_b32 s31, s28, 1\n"
      "v_lshrrev_b32 v168, 4, v1\n"
      "v_and_b32 v169, 15, v1\n"
      "v_mov_b32 v172, 0x40000000\n"
      "v_cmp_le_u32 vcc, 36, v1\n"
      "v_subrev_u32 v170, 36, v1\n"
      "s_nop 0\n"
      "v_cndmask_b32 v170, v1, v170, vcc\n"
      "v_mov_b32 v171, 3456\n"
      "v_cndmask_b32 v171, 0, v171, vcc\n"
      "v_mul_u32_u24 v173, 57, v170\n"
      "v_lshrrev_b32 v173, 9, v173\n"
      "v_mul_u32_u24 v173, 1008, v173\n"
      "v_lshl_add_u32 v171, v170, 4, v171\n"
      "v_add_u32 v171, v171, v173\n"
      "v_cmp_gt_u32 vcc, 27, v170\n"
      "s_mul_i32 s82, s30, 144\n"
      "s_mul_i32 s50, s28, 27648\n"
      "v_cndmask_b32 v96, v172, v171, vcc\n"
      "s_add_u32 s82, s82, s50\n"
      "s_mul_i32 s77, s28, 4224\n"
      "s_add_u32 s77, s77, 61440\n"
      "v_lshrrev_b32 v170, 3, v1\n"
      "v_and_b32 v171, 7, v1\n"
      "v_xor_b32 v171, v171, v170\n"
      "v_mul_u32_u24 v101, 1152, v170\n"
      "v_lshl_add_u32 v101, v171, 4, v101\n"
      "s_lshl_b32 s84, s29, 5\n"
      "s_add_u32 s84, s84, 31\n"
      "s_sub_u32 s84, s84, s31\n"
      "s_mul_i32 s84, s84, 18432\n"
      "s_lshr_b32 s50, s28, 1\n"
      "s_mul_i32 s51, s50, 9216\n"
      "s_add_u32 s84, s84, s51\n"
      "s_mul_i32 s85, s31, 20480\n"
      "s_lshl_b32 s50, s50, 10\n"
      "s_add_u32 s85, s85, s50\n"
      "s_waitcnt lgkmcnt(0)\n"
      "s_mov_b32 s36, s20\n"
      "s_and_b32 s37, s21, 0xffff\n"
      "s_mov_b32 s38, 0x4eaf200\n"
      "s_mov_b32 s39, 0x20000\n"
      "s_mov_b32 s98, 82439168\n"
      "buffer_load_dwordx2 v[252:253], off, s[36:39], s98 sc1\n"
      "s_mov_b32 s40, s10\n"
      "s_and_b32 s41, s11, 0xffff\n"
      "s_mov_b32 s42, 0x1e6000\n"
      "s_mov_b32 s43, 0x20000\n"
      "s_mov_b32 s44, s8\n"
      "s_and_b32 s45, s9, 0xffff\n"
      "s_mov_b32 s46, 0x240000\n"
      "s_mov_b32 s47, 0x20000\n"
      "s_add_u32 m0, s85, 0\n"
      "s_add_u32 s50, s84, 0\n"
      "buffer_load_dwordx4 v101, s[44:47], s50 offen lds\n"
      "s_add_u32 m0, s85, 2048\n"
      "s_add_u32 s50, s84, 128\n"
      "buffer_load_dwordx4 v101, s[44:47], s50 offen lds\n"
      "s_add_u32 m0, s85, 4096\n"
      "s_add_u32 s50, s84, 256\n"
      "buffer_load_dwordx4 v101, s[44:47], s50 offen lds\n"
      "s_add_u32 m0, s85, 6144\n"
      "s_add_u32 s50, s84, 384\n"
      "buffer_load_dwordx4 v101, s[44:47], s50 offen lds\n"
      "s_add_u32 m0, s85, 8192\n"
      "s_add_u32 s50, s84, 512\n"
      "buffer_load_dwordx4 v101, s[44:47], s50 offen lds\n"
      "s_add_u32 m0, s85, 10240\n"
      "s_add_u32 s50, s84, 640\n"
      "buffer_load_dwordx4 v101, s[44:47], s50 offen lds\n"
      "s_add_u32 m0, s85, 12288\n"
      "s_add_u32 s50, s84, 768\n"
      "buffer_load_dwordx4 v101, s[44:47], s50 offen lds\n"
      "s_add_u32 m0, s85, 14336\n"
      "s_add_u32 s50, s84, 896\n"
      "buffer_load_dwordx4 v101, s[44:47], s50 offen lds\n"
      "s_add_u32 m0, s85, 16384\n"
      "s_add_u32 s50, s84, 1024\n"
      "buffer_load_dwordx4 v101, s[44:47], s50 offen lds\n"
      "s_add_u32 m0, s77, 0\n"
      "s_add_u32 s50, s82, 0\n"
      "buffer_load_dwordx4 v96, s[40:43], s50 offen lds\n"
      "s_add_u32 m0, s77, 1056\n"
      "s_add_u32 s50, s82, 6912\n"
      "buffer_load_dwordx4 v96, s[40:43], s50 offen lds\n"
      "s_add_u32 m0, s77, 2112\n"
      "s_add_u32 s50, s82, 13824\n"
      "buffer_load_dwordx4 v96, s[40:43], s50 offen lds\n"
      "s_add_u32 m0, s77, 3168\n"
      "s_add_u32 s50, s82, 20736\n"
      "buffer_load_dwordx4 v96, s[40:43], s50 offen lds\n"
      "s_add_u32 m0, s77, 16896\n"
      "s_add_u32 s50, s82, 110592\n"
      "buffer_load_dwordx4 v96, s[40:43], s50 offen lds\n"
      "s_add_u32 m0, s77, 17952\n"
      "s_add_u32 s50, s82, 117504\n"
      "buffer_load_dwordx4 v96, s[40:43], s50 offen lds\n"
      "s_add_u32 m0, s77, 19008\n"
      "s_add_u32 s50, s82, 124416\n"
      "buffer_load_dwordx4 v96, s[40:43], s50 offen lds\n"
      "s_add_u32 m0, s77, 20064\n"
      "s_add_u32 s50, s82, 131328\n"
      "buffer_load_dwordx4 v96, s[40:43], s50 offen lds\n"
      "v_and_b32 v170, 7, v1\n"
      "v_xor_b32 v170, v170, v168\n"
      "v_lshlrev_b32 v171, 7, v169\n"
      "v_lshl_add_u32 v101, v170, 4, v171\n"
      "v_xor_b32 v103, 64, v101\n"
      "v_bfe_u32 v170, v1, 3, 1\n"
      "v_lshl_add_u32 v170, v168, 2, v170\n"
      "v_mul_u32_u24 v170, 1056, v170\n"
      "v_bfe_u32 v171, v1, 2, 1\n"
      "v_mul_u32_u24 v171, 576, v171\n"
      "v_and_b32 v173, 3, v1\n"
      "v_lshl_add_u32 v171, v173, 3, v171\n"
      "s_mul_i32 s51, s28, 36\n"
      "s_lshl_b32 s52, s31, 2\n"
      "s_add_u32 s52, s52, s51\n"
      "s_add_u32 s52, s52, 61440\n"
      "v_add3_u32 v97, v170, v171, s52\n"
      "v_add_u32 v98, 50688, v97\n"
      "v_and_b32 v170, 12, v1\n"
      "v_bfe_u32 v171, v1, 1, 1\n"
      "v_add_u32 v170, v170, v171\n"
      "v_mul_u32_u24 v170, 1056, v170\n"
      "v_and_b32 v171, 1, v1\n"
      "v_mul_u32_u24 v171, 576, v171\n"
      "v_add_u32 v170, v170, v171\n"
      "s_lshl_b32 s52, s31, 5\n"
      "s_sub_u32 s52, s51, s52\n"
      "s_add_u32 s52, s52, 32\n"
      "v_mul_u32_u24 v171, 144, v168\n"
      "v_add_u32 v171, s52, v171\n"
      "v_min_u32 v171, 432, v171\n"
      "v_add_u32 v170, v170, v171\n"
      "v_add_u32 v99, 61440, v170\n"
      "v_add_u32 v100, 112128, v170\n"
      "v_mov_b32 v152, 0\n"
      "v_mov_b32 v153, 0\n"
      "v_mov_b32 v155, 0\n"
      "v_mov_b32 v156, 0\n"
      "v_mov_b32 v157, 0\n"
      "v_mov_b32 v145, 0\n"
      "v_mov_b32 v147, 0\n"
      "v_mov_b32 v149, 0\n"
      "v_mov_b32 v151, 0\n"
      "v_mov_b32 v170, 0x3f80\n"
      "v_mov_b32 v171, 0x3f800000\n"
      "s_mov_b32 vcc_lo, 0x20001\n"
      "s_mov_b32 vcc_hi, 0x4\n"
      "v_cndmask_b32 v154, v152, v170, vcc\n"
      "s_mov_b32 vcc_lo, 0x200010\n"
      "s_mov_b32 vcc_hi, 0x40\n"
      "v_cndmask_b32 v154, v154, v171, vcc\n"
      "v_mov_b32 v164, 0x3f803f80\n"
      "v_mov_b32 v165, 0x3f803f80\n"
      "v_mov_b32 v166, 0x3f803f80\n"
      "v_mov_b32 v167, 0x3f803f80\n"
      "s_mov_b32 s66, 1\n"
      "s_mov_b32 s79, 40960\n"
      "s_lshl_b32 s83, s28, 12\n"
      "s_lshl_b32 s73, s29, 5\n"
      "s_sub_u32 s73, 96, s73\n"
      "s_mul_i32 s71, s73, 1536\n"
      "s_add_u32 s71, s71, 82042880\n"
      "s_mul_i32 s69, s73, 20480\n"
      "s_add_u32 s69, s69, s83\n"
      "s_add_u32 s69, s69, 0\n"
      "s_add_u32 s67, s73, 161\n"
      "s_min_u32 s67, s67, 256\n"
      "s_lshl_b32 s78, s28, 10\n"
      "s_add_u32 s78, s78, 16384\n"
      "v_min_u32 v5, 11, v169\n"
      "v_lshlrev_b32 v5, 7, v5\n"
      "v_lshrrev_b32 v170, 5, v1\n"
      "v_lshl_add_u32 v5, v170, 2, v5\n"
      "v_add_u32 v5, 8, v5\n"
      "v_min_u32 v102, 59, v1\n"
      "v_lshlrev_b32 v102, 7, v102\n"
      "s_sub_u32 s51, s78, s83\n"
      "v_add_u32 v4, s51, v2\n"
      "s_cmp_lt_u32 s28, 3\n"
      "s_cselect_b32 s51, 64, 32\n"
      "v_cmp_gt_u32 vcc, s51, v1\n"
      "s_nop 1\n"
      "v_cndmask_b32 v4, v172, v4, vcc\n"
      "s_mul_i32 s74, s29, 1310720\n"
      "s_add_u32 s74, s74, 5283840\n"
      "s_mov_b32 s76, 0\n"
      "s_lshl_b32 s51, s30, 2\n"
      "s_add_u32 s51, s51, s28\n"
      "v_lshrrev_b32 v170, 1, v168\n"
      "v_lshl_add_u32 v170, v170, 4, v169\n"
      "v_lshlrev_b32 v170, 4, v170\n"
      "v_and_b32 v171, 1, v168\n"
      "v_lshl_add_u32 v170, v171, 3, v170\n"
      "s_lshl_b32 s52, s51, 9\n"
      "v_add_u32 v11, s52, v170\n"
      "s_lshl_b32 s52, s51, 7\n"
      "s_add_u32 s52, s52, 16384\n"
      "v_lshlrev_b32 v170, 2, v1\n"
      "v_add_u32 v6, s52, v170\n"
      "v_cmp_gt_u32 vcc, 32, v1\n"
      "s_nop 1\n"
      "v_cndmask_b32 v6, v172, v6, vcc\n"
      "s_lshl_b32 s51, s29, 3\n"
      "s_add_u32 s51, s51, s30\n"
      "s_lshl_b32 s51, s51, 2\n"
      "s_add_u32 s51, s51, s28\n"
      "s_lshl_b32 s51, s51, 7\n"
      "s_add_u32 s51, s51, 81911808\n"
      "v_lshlrev_b32 v7, 2, v1\n"
      "v_lshrrev_b32 v170, 5, v1\n"
      "v_lshl_or_b32 v7, v170, 30, v7\n"
      "v_add_u32 v7, s51, v7\n"
      "v_lshlrev_b32 v10, 3, v1\n"
      "v_add_u32 v9, 40960, v10\n"
      "v_add_u32 v3, 40960, v2\n"
      "v_mov_b32 v17, 0x3ae38e39\n"
      "s_cmp_eq_u32 s29, 0\n"
      "s_cselect_b32 s80, 32, 1000\n"
      "s_mov_b32 s81, 1\n"
      "v_mov_b32 v87, 0\n"
      "v_mov_b32 v91, 0\n"
      "v_mov_b32 v95, 0\n"
      "v_mov_b32 v163, 0\n"
      "v_mov_b32 v250, 0\n"
      "v_mov_b32 v251, 0\n"
      "v_mov_b32 v12, 0\n"
      "v_mov_b32 v13, 0\n"
      "v_mov_b32 v15, 0\n"
      "v_mov_b32 v170, 0x3f80\n"
      "v_mov_b32 v171, 0x3f800000\n"
      "s_mov_b32 vcc_lo, 0x100001\n"
      "s_mov_b32 vcc_hi, 0x10000100\n"
      "v_cndmask_b32 v248, v95, v170, vcc\n"
      "s_mov_b32 vcc_lo, 0x200002\n"
      "s_mov_b32 vcc_hi, 0x20000200\n"
      "v_cndmask_b32 v248, v248, v171, vcc\n"
      "s_mov_b32 vcc_lo, 0x400004\n"
      "s_mov_b32 vcc_hi, 0x40000400\n"
      "v_cndmask_b32 v249, v95, v170, vcc\n"
      "s_mov_b32 vcc_lo, 0x800008\n"
      "s_mov_b32 vcc_hi, 0x80000800\n"
      "v_cndmask_b32 v249, v249, v171, vcc\n"
      "s_mov_b32 vcc_lo, 0x20001\n"
      "s_mov_b32 vcc_hi, 0x80004\n"
      "v_cndmask_b32 v14, v95, v170, vcc\n"
      "v_mov_b32 v170, 0xff61b1e6\n"
      "v_mov_b32 v112, v170\n"
      "v_mov_b32 v171, 0xffff\n"
      "v_mov_b32 v116, 0\n"
      "v_mov_b32 v118, 0\n"
      "s_mov_b32 vcc_lo, 0x55505555\n"
      "s_mov_b32 vcc_hi, 0x50005500\n"
      "v_cndmask_b32 v104, v170, v95, vcc\n"
      "v_cndmask_b32 v116, v116, v171, vcc\n"
      "s_mov_b32 vcc_lo, 0x50000\n"
      "s_mov_b32 vcc_hi, 0x5550055\n"
      "v_cndmask_b32 v108, v170, v95, vcc\n"
      "v_cndmask_b32 v118, v118, v171, vcc\n"
      "v_mov_b32 v170, 0xff61b1e6\n"
      "v_mov_b32 v113, v170\n"
      "v_mov_b32 v171, 0xffff0000\n"
      "s_mov_b32 vcc_lo, 0xaaa0aaaa\n"
      "s_mov_b32 vcc_hi, 0xa000aa00\n"
      "v_cndmask_b32 v105, v170, v95, vcc\n"
      "v_cndmask_b32 v116, v116, v171, vcc\n"
      "s_mov_b32 vcc_lo, 0xa0000\n"
      "s_mov_b32 vcc_hi, 0xaaa00aa\n"
      "v_cndmask_b32 v109, v170, v95, vcc\n"
      "v_cndmask_b32 v118, v118, v171, vcc\n"
      "v_mov_b32 v170, 0xff61b1e6\n"
      "v_mov_b32 v114, v170\n"
      "v_mov_b32 v171, 0xffff\n"
      "v_mov_b32 v117, 0\n"
      "v_mov_b32 v119, 0\n"
      "s_mov_b32 vcc_lo, 0x55405554\n"
      "s_mov_b32 vcc_hi, 0x40005400\n"
      "v_cndmask_b32 v106, v170, v95, vcc\n"
      "v_cndmask_b32 v117, v117, v171, vcc\n"
      "s_mov_b32 vcc_lo, 0x150001\n"
      "s_mov_b32 vcc_hi, 0x15550155\n"
      "v_cndmask_b32 v110, v170, v95, vcc\n"
      "v_cndmask_b32 v119, v119, v171, vcc\n"
      "v_mov_b32 v170, 0xff61b1e6\n"
      "v_mov_b32 v115, v170\n"
      "v_mov_b32 v171, 0xffff0000\n"
      "s_mov_b32 vcc_lo, 0xaa80aaa8\n"
      "s_mov_b32 vcc_hi, 0x8000a800\n"
      "v_cndmask_b32 v107, v170, v95, vcc\n"
      "v_cndmask_b32 v117, v117, v171, vcc\n"
      "s_mov_b32 vcc_lo, 0x2a0002\n"
      "s_mov_b32 vcc_hi, 0x2aaa02aa\n"
      "v_cndmask_b32 v111, v170, v95, vcc\n"
      "v_cndmask_b32 v119, v119, v171, vcc\n"
      "s_add_u32 m0, s77, 33792\n"
      "s_add_u32 s50, s82, 221184\n"
      "buffer_load_dwordx4 v96, s[40:43], s50 offen lds\n"
      "s_add_u32 m0, s77, 34848\n"
      "s_add_u32 s50, s82, 228096\n"
      "buffer_load_dwordx4 v96, s[40:43], s50 offen lds\n"
      "s_add_u32 m0, s77, 35904\n"
      "s_add_u32 s50, s82, 235008\n"
      "buffer_load_dwordx4 v96, s[40:43], s50 offen lds\n"
      "s_add_u32 m0, s77, 36960\n"
      "s_add_u32 s50, s82, 241920\n"
      "buffer_load_dwordx4 v96, s[40:43], s50 offen lds\n"
      "s_waitcnt vmcnt(21)\n"
      "v_readfirstlane_b32 s98, v252\n"
      "v_readfirstlane_b32 s99, v253\n"
      "v_mov_b32 v8, 0\n"
      "s_add_u32 s98, s98, 1\n"
      "s_addc_u32 s99, s99, 0\n"
      "v_writelane_b32 v8, s98, 2\n"
      "v_writelane_b32 v8, s99, 3\n"
      "buffer_store_dword v8, v7, s[36:39], 0 offen sc1\n"
      "s_cmp_eq_u32 s28, 0\n"
      "s_cselect_b32 s50, 0, 0x40000000\n"
      "v_or_b32 v7, s50, v7\n"
      "v_mov_b32 v16, s98\n"
      "v_mov_b32 v170, s99\n"
      "s_mov_b32 vcc_lo, 0x0\n"
      "s_mov_b32 vcc_hi, 0xffffffff\n"
      "v_cndmask_b32 v16, v16, v170, vcc\n"
      "s_waitcnt vmcnt(9)\n"
      "s_barrier\n"
      "ds_read_b32 v144, v99 offset:0\n"
      "ds_read_b32 v146, v99 offset:2112\n"
      "ds_read_b64_tr_b16 a[0:1], v97 offset:0\n"
      "ds_read_b64_tr_b16 a[2:3], v97 offset:2112\n"
      "ds_read_b64_tr_b16 a[4:5], v97 offset:144\n"
      "ds_read_b64_tr_b16 a[6:7], v97 offset:2256\n"
      "ds_read_b64_tr_b16 a[8:9], v97 offset:288\n"
      "ds_read_b64_tr_b16 a[10:11], v97 offset:2400\n"
      "s_add_u32 m0, s77, 50688\n"
      "s_add_u32 s50, s82, 331776\n"
      "buffer_load_dwordx4 v96, s[40:43], s50 offen lds\n"
      "s_add_u32 m0, s77, 51744\n"
      "s_add_u32 s50, s82, 338688\n"
      "buffer_load_dwordx4 v96, s[40:43], s50 offen lds\n"
      "s_add_u32 m0, s77, 52800\n"
      "s_add_u32 s50, s82, 345600\n"
      "buffer_load_dwordx4 v96, s[40:43], s50 offen lds\n"
      "s_add_u32 m0, s77, 53856\n"
      "s_add_u32 s50, s82, 352512\n"
      "buffer_load_dwordx4 v96, s[40:43], s50 offen lds\n"
      "s_add_u32 m0, s77, 67584\n"
      "s_add_u32 s50, s82, 442368\n"
      "buffer_load_dwordx4 v96, s[40:43], s50 offen lds\n"
      "s_add_u32 m0, s77, 68640\n"
      "s_add_u32 s50, s82, 449280\n"
      "buffer_load_dwordx4 v96, s[40:43], s50 offen lds\n"
      "s_add_u32 m0, s77, 69696\n"
      "s_add_u32 s50, s82, 456192\n"
      "buffer_load_dwordx4 v96, s[40:43], s50 offen lds\n"
      "s_add_u32 m0, s77, 70752\n"
      "s_add_u32 s50, s82, 463104\n"
      "buffer_load_dwordx4 v96, s[40:43], s50 offen lds\n"
      "s_waitcnt vmcnt(13) lgkmcnt(0)\n"
      "s_barrier\n"
      "v_mfma_f32_16x16x32_bf16 v[128:131], v[144:147], v[154:157], 0\n"
      "ds_read_b32 v148, v99 offset:16896\n"
      "ds_read_b32 v150, v99 offset:19008\n"
      "ds_read_b64_tr_b16 a[12:13], v97 offset:16896\n"
      "ds_read_b64_tr_b16 a[14:15], v97 offset:19008\n"
      "ds_read_b64_tr_b16 a[16:17], v97 offset:17040\n"
      "ds_read_b64_tr_b16 a[18:19], v97 offset:19152\n"
      "ds_read_b64_tr_b16 a[20:21], v97 offset:17184\n"
      "ds_read_b64_tr_b16 a[22:23], v97 offset:19296\n"
      "ds_read_b128 v[120:123], v101 offset:0\n"
      "ds_read_b128 v[28:31], v101 offset:20480\n"
      "s_add_u32 m0, s77, 84480\n"
      "s_add_u32 s50, s82, 552960\n"
      "buffer_load_dwordx4 v96, s[40:43], s50 offen lds\n"
      "s_add_u32 m0, s77, 85536\n"
      "s_add_u32 s50, s82, 559872\n"
      "buffer_load_dwordx4 v96, s[40:43], s50 offen lds\n"
      "s_add_u32 m0, s77, 86592\n"
      "v_mfma_f32_16x16x32_bf16 v[132:135], v[144:147], v[152:155], 0\n"
      "s_add_u32 s50, s82, 566784\n"
      "buffer_load_dwordx4 v96, s[40:43], s50 offen lds\n"
      "s_add_u32 m0, s77, 87648\n"
      "s_add_u32 s50, s82, 573696\n"
      "buffer_load_dwordx4 v96, s[40:43], s50 offen lds\n"
      "s_add_u32 m0, s77, 0\n"
      "s_add_u32 s50, s82, 663552\n"
      "buffer_load_dwordx4 v96, s[40:43], s50 offen lds\n"
      "s_add_u32 m0, s77, 1056\n"
      "s_add_u32 s50, s82, 670464\n"
      "buffer_load_dwordx4 v96, s[40:43], s50 offen lds\n"
      "s_add_u32 m0, s77, 2112\n"
      "s_add_u32 s50, s82, 677376\n"
      "buffer_load_dwordx4 v96, s[40:43], s50 offen lds\n"
      "s_add_u32 m0, s77, 3168\n"
      "s_add_u32 s50, s82, 684288\n"
      "buffer_load_dwordx4 v96, s[40:43], s50 offen lds\n"
      "s_waitcnt vmcnt(17) lgkmcnt(0)\n"
      "s_barrier\n"
      "v_cvt_pk_bf16_f32 v176, v128, v129\n"
      "v_cvt_pk_bf16_f32 v177, v130, v131\n"
      "v_cvt_pk_bf16_f32 v178, v132, v133\n"
      "v_cvt_pk_bf16_f32 v179, v134, v135\n"
      "v_mfma_f32_16x16x32_bf16 v[136:139], v[148:151], v[154:157], 0\n"
      "ds_read_b32 v144, v99 offset:33792\n"
      "ds_read_b32 v146, v99 offset:35904\n"
      "v_mfma_f32_16x16x32_bf16 v[140:143], v[148:151], v[152:155], 0\n"
      "ds_read_b64_tr_b16 a[24:25], v97 offset:33792\n"
      "ds_read_b64_tr_b16 a[26:27], v97 offset:35904\n"
      "v_mfma_f32_16x16x32_bf16 v[36:39], a[4:7], v[120:123], 0\n"
      "ds_read_b64_tr_b16 a[28:29], v97 offset:33936\n"
      "ds_read_b64_tr_b16 a[30:31], v97 offset:36048\n"
      "v_mfma_f32_16x16x32_bf16 v[40:43], v[176:179], v[120:123], 0\n"
      "ds_read_b64_tr_b16 a[32:33], v97 offset:34080\n"
      "ds_read_b64_tr_b16 a[34:35], v97 offset:36192\n"
      "v_mfma_f32_16x16x32_bf16 v[44:47], a[8:11], v[120:123], 0\n"
      "ds_read_b128 v[124:127], v103 offset:0\n"
      "ds_read_b128 v[32:35], v103 offset:20480\n"
      "v_mfma_f32_16x16x32_bf16 v[48:51], a[0:3], v[120:123], 0\n"
      "s_add_u32 m0, s77, 16896\n"
      "s_add_u32 s50, s82, 774144\n"
      "v_mfma_f32_16x16x32_bf16 v[68:71], a[4:7], v[164:167], 0\n"
      "buffer_load_dwordx4 v96, s[40:43], s50 offen lds\n"
      "s_add_u32 m0, s77, 17952\n"
      "v_mfma_f32_16x16x32_bf16 v[72:75], v[176:179], v[164:167], 0\n"
      "s_add_u32 s50, s82, 781056\n"
      "buffer_load_dwordx4 v96, s[40:43], s50 offen lds\n"
      "v_mfma_f32_16x16x32_bf16 v[76:79], a[8:11], v[164:167], 0\n"
      "s_add_u32 m0, s77, 19008\n"
      "s_add_u32 s50, s82, 787968\n"
      "v_mfma_f32_16x16x32_bf16 v[80:83], a[0:3], v[164:167], 0\n"
      "buffer_load_dwordx4 v96, s[40:43], s50 offen lds\n"
      "s_add_u32 m0, s77, 20064\n"
      "v_mfma_f32_16x16x32_bf16 v[52:55], a[4:7], v[28:31], 0\n"
      "s_add_u32 s50, s82, 794880\n"
      "buffer_load_dwordx4 v96, s[40:43], s50 offen lds\n"
      "v_mfma_f32_16x16x32_bf16 v[56:59], v[176:179], v[28:31], 0\n"
      "v_mfma_f32_16x16x32_bf16 v[60:63], a[8:11], v[28:31], 0\n"
      "v_mfma_f32_16x16x32_bf16 v[64:67], a[0:3], v[28:31], 0\n"
      "s_waitcnt vmcnt(16) lgkmcnt(0)\n"
      "s_barrier\n"
      "v_cvt_pk_bf16_f32 v180, v136, v137\n"
      "v_cvt_pk_bf16_f32 v181, v138, v139\n"
      "v_cvt_pk_bf16_f32 v182, v140, v141\n"
      "v_cvt_pk_bf16_f32 v183, v142, v143\n"
      "v_mfma_f32_16x16x32_bf16 v[128:131], v[144:147], v[154:157], 0\n"
      "ds_read_b32 v148, v100 offset:0\n"
      "ds_read_b32 v150, v100 offset:2112\n"
      "v_mfma_f32_16x16x32_bf16 v[132:135], v[144:147], v[152:155], 0\n"
      "ds_read_b64_tr_b16 a[36:37], v98 offset:0\n"
      "ds_read_b64_tr_b16 a[38:39], v98 offset:2112\n"
      "v_mfma_f32_16x16x32_bf16 v[36:39], a[16:19], v[124:127], v[36:39]\n"
      "ds_read_b64_tr_b16 a[40:41], v98 offset:144\n"
      "ds_read_b64_tr_b16 a[42:43], v98 offset:2256\n"
      "v_mfma_f32_16x16x32_bf16 v[40:43], v[180:183], v[124:127], v[40:43]\n"
      "ds_read_b64_tr_b16 a[44:45], v98 offset:288\n"
      "ds_read_b64_tr_b16 a[46:47], v98 offset:2400\n"
      "v_mfma_f32_16x16x32_bf16 v[44:47], a[20:23], v[124:127], v[44:47]\n"
      "ds_read_b128 v[120:123], v101 offset:2048\n"
      "ds_read_b128 v[20:23], v101 offset:22528\n"
      "v_mfma_f32_16x16x32_bf16 v[48:51], a[12:15], v[124:127], v[48:51]\n"
      "s_add_u32 m0, s77, 33792\n"
      "s_add_u32 s50, s82, 884736\n"
      "v_mfma_f32_16x16x32_bf16 v[68:71], a[16:19], v[164:167], v[68:71]\n"
      "buffer_load_dwordx4 v96, s[40:43], s50 offen lds\n"
      "s_add_u32 m0, s77, 34848\n"
      "v_mfma_f32_16x16x32_bf16 v[72:75], v[180:183], v[164:167], v[72:75]\n"
      "s_add_u32 s50, s82, 891648\n"
      "buffer_load_dwordx4 v96, s[40:43], s50 offen lds\n"
      "v_mfma_f32_16x16x32_bf16 v[76:79], a[20:23], v[164:167], v[76:79]\n"
      "s_add_u32 m0, s77, 35904\n"
      "s_add_u32 s50, s82, 898560\n"
      "v_mfma_f32_16x16x32_bf16 v[80:83], a[12:15], v[164:167], v[80:83]\n"
      "buffer_load_dwordx4 v96, s[40:43], s50 offen lds\n"
      "s_add_u32 m0, s77, 36960\n"
      "v_mfma_f32_16x16x32_bf16 v[52:55], a[16:19], v[32:35], v[52:55]\n"
      "s_add_u32 s50, s82, 905472\n"
      "buffer_load_dwordx4 v96, s[40:43], s50 offen lds\n"
      "v_mfma_f32_16x16x32_bf16 v[56:59], v[180:183], v[32:35], v[56:59]\n"
      "v_mfma_f32_16x16x32_bf16 v[60:63], a[20:23], v[32:35], v[60:63]\n"
      "v_mfma_f32_16x16x32_bf16 v[64:67], a[12:15], v[32:35], v[64:67]\n"
      "s_waitcnt vmcnt(16) lgkmcnt(0)\n"
      "s_barrier\n"
      "v_cvt_pk_bf16_f32 v184, v128, v129\n"
      "v_cvt_pk_bf16_f32 v185, v130, v131\n"
      "v_cvt_pk_bf16_f32 v186, v132, v133\n"
      "v_cvt_pk_bf16_f32 v187, v134, v135\n"
      "v_mfma_f32_16x16x32_bf16 v[136:139], v[148:151], v[154:157], 0\n"
      "ds_read_b32 v144, v100 offset:16896\n"
      "ds_read_b32 v146, v100 offset:19008\n"
      "v_mfma_f32_16x16x32_bf16 v[140:143], v[148:151], v[152:155], 0\n"
      "ds_read_b64_tr_b16 a[48:49], v98 offset:16896\n"
      "ds_read_b64_tr_b16 a[50:51], v98 offset:19008\n"
      "v_mfma_f32_16x16x32_bf16 v[36:39], a[28:31], v[120:123], v[36:39]\n"
      "ds_read_b64_tr_b16 a[52:53], v98 offset:17040\n"
      "ds_read_b64_tr_b16 a[54:55], v98 offset:19152\n"
      "v_mfma_f32_16x16x32_bf16 v[40:43], v[184:187], v[120:123], v[40:43]\n"
      "ds_read_b64_tr_b16 a[56:57], v98 offset:17184\n"
      "ds_read_b64_tr_b16 a[58:59], v98 offset:19296\n"
      "v_mfma_f32_16x16x32_bf16 v[44:47], a[32:35], v[120:123], v[44:47]\n"
      "ds_read_b128 v[124:127], v103 offset:2048\n"
      "ds_read_b128 v[24:27], v103 offset:22528\n"
      "v_mfma_f32_16x16x32_bf16 v[48:51], a[24:27], v[120:123], v[48:51]\n"
      "s_add_u32 m0, s77, 50688\n"
      "s_add_u32 s50, s82, 995328\n"
      "v_mfma_f32_16x16x32_bf16 v[68:71], a[28:31], v[164:167], v[68:71]\n"
      "buffer_load_dwordx4 v96, s[40:43], s50 offen lds\n"
      "s_add_u32 m0, s77, 51744\n"
      "v_mfma_f32_16x16x32_bf16 v[72:75], v[184:187], v[164:167], v[72:75]\n"
      "s_add_u32 s50, s82, 1002240\n"
      "buffer_load_dwordx4 v96, s[40:43], s50 offen lds\n"
      "v_mfma_f32_16x16x32_bf16 v[76:79], a[32:35], v[164:167], v[76:79]\n"
      "s_add_u32 m0, s77, 52800\n"
      "s_add_u32 s50, s82, 1009152\n"
      "v_mfma_f32_16x16x32_bf16 v[80:83], a[24:27], v[164:167], v[80:83]\n"
      "buffer_load_dwordx4 v96, s[40:43], s50 offen lds\n"
      "s_add_u32 m0, s77, 53856\n"
      "v_mfma_f32_16x16x32_bf16 v[52:55], a[28:31], v[20:23], v[52:55]\n"
      "s_add_u32 s50, s82, 1016064\n"
      "buffer_load_dwordx4 v96, s[40:43], s50 offen lds\n"
      "v_mfma_f32_16x16x32_bf16 v[56:59], v[184:187], v[20:23], v[56:59]\n"
      "v_mfma_f32_16x16x32_bf16 v[60:63], a[32:35], v[20:23], v[60:63]\n"
      "v_mfma_f32_16x16x32_bf16 v[64:67], a[24:27], v[20:23], v[64:67]\n"
      "s_waitcnt vmcnt(16) lgkmcnt(0)\n"
      "s_barrier\n"
      "v_cvt_pk_bf16_f32 v188, v136, v137\n"
      "v_cvt_pk_bf16_f32 v189, v138, v139\n"
      "v_cvt_pk_bf16_f32 v190, v140, v141\n"
      "v_cvt_pk_bf16_f32 v191, v142, v143\n"
      "v_mfma_f32_16x16x32_bf16 v[128:131], v[144:147], v[154:157], 0\n"
      "ds_read_b32 v148, v100 offset:33792\n"
      "ds_read_b32 v150, v100 offset:35904\n"
      "v_mfma_f32_16x16x32_bf16 v[132:135], v[144:147], v[152:155], 0\n"
      "ds_read_b64_tr_b16 a[60:61], v98 offset:33792\n"
      "ds_read_b64_tr_b16 a[62:63], v98 offset:35904\n"
      "v_mfma_f32_16x16x32_bf16 v[36:39], a[40:43], v[124:127], v[36:39]\n"
      "ds_read_b64_tr_b16 a[64:65], v98 offset:33936\n"
      "ds_read_b64_tr_b16 a[66:67], v98 offset:36048\n"
      "v_mfma_f32_16x16x32_bf16 v[40:43], v[188:191], v[124:127], v[40:43]\n"
      "ds_read_b64_tr_b16 a[68:69], v98 offset:34080\n"
      "ds_read_b64_tr_b16 a[70:71], v98 offset:36192\n"
      "v_mfma_f32_16x16x32_bf16 v[44:47], a[44:47], v[124:127], v[44:47]\n"
      "ds_read_b128 v[120:123], v101 offset:4096\n"
      "ds_read_b128 v[28:31], v101 offset:24576\n"
      "v_mfma_f32_16x16x32_bf16 v[48:51], a[36:39], v[124:127], v[48:51]\n"
      "s_add_u32 m0, s77, 67584\n"
      "s_add_u32 s50, s82, 1105920\n"
      "v_mfma_f32_16x16x32_bf16 v[68:71], a[40:43], v[164:167], v[68:71]\n"
      "buffer_load_dwordx4 v96, s[40:43], s50 offen lds\n"
      "s_add_u32 m0, s77, 68640\n"
      "v_mfma_f32_16x16x32_bf16 v[72:75], v[188:191], v[164:167], v[72:75]\n"
      "s_add_u32 s50, s82, 1112832\n"
      "buffer_load_dwordx4 v96, s[40:43], s50 offen lds\n"
      "v_mfma_f32_16x16x32_bf16 v[76:79], a[44:47], v[164:167], v[76:79]\n"
      "s_add_u32 m0, s77, 69696\n"
      "s_add_u32 s50, s82, 1119744\n"
      "v_mfma_f32_16x16x32_bf16 v[80:83], a[36:39], v[164:167], v[80:83]\n"
      "buffer_load_dwordx4 v96, s[40:43], s50 offen lds\n"
      "s_add_u32 m0, s77, 70752\n"
      "v_mfma_f32_16x16x32_bf16 v[52:55], a[40:43], v[24:27], v[52:55]\n"
      "s_add_u32 s50, s82, 1126656\n"
      "buffer_load_dwordx4 v96, s[40:43], s50 offen lds\n"
      "v_mfma_f32_16x16x32_bf16 v[56:59], v[188:191], v[24:27], v[56:59]\n"
      "v_mfma_f32_16x16x32_bf16 v[60:63], a[44:47], v[24:27], v[60:63]\n"
      "v_mfma_f32_16x16x32_bf16 v[64:67], a[36:39], v[24:27], v[64:67]\n"
      "s_waitcnt vmcnt(16) lgkmcnt(0)\n"
      "s_barrier\n"
      "v_cvt_pk_bf16_f32 v192, v128, v129\n"
      "v_cvt_pk_bf16_f32 v193, v130, v131\n"
      "v_cvt_pk_bf16_f32 v194, v132, v133\n"
      "v_cvt_pk_bf16_f32 v195, v134, v135\n"
      "v_mfma_f32_16x16x32_bf16 v[136:139], v[148:151], v[154:157], 0\n"
      "ds_read_b32 v144, v99 offset:0\n"
      "ds_read_b32 v146, v99 offset:2112\n"
      "v_mfma_f32_16x16x32_bf16 v[140:143], v[148:151], v[152:155], 0\n"
      "ds_read_b64_tr_b16 a[72:73], v97 offset:0\n"
      "ds_read_b64_tr_b16 a[74:75], v97 offset:2112\n"
      "v_mfma_f32_16x16x32_bf16 v[36:39], a[52:55], v[120:123], v[36:39]\n"
      "ds_read_b64_tr_b16 a[76:77], v97 offset:144\n"
      "ds_read_b64_tr_b16 a[78:79], v97 offset:2256\n"
      "v_mfma_f32_16x16x32_bf16 v[40:43], v[192:195], v[120:123], v[40:43]\n"
      "ds_read_b64_tr_b16 a[80:81], v97 offset:288\n"
      "ds_read_b64_tr_b16 a[82:83], v97 offset:2400\n"
      "v_mfma_f32_16x16x32_bf16 v[44:47], a[56:59], v[120:123], v[44:47]\n"
      "ds_read_b128 v[124:127], v103 offset:4096\n"
      "ds_read_b128 v[32:35], v103 offset:24576\n"
      "v_mfma_f32_16x16x32_bf16 v[48:51], a[48:51], v[120:123], v[48:51]\n"
      "s_add_u32 m0, s77, 84480\n"
      "s_add_u32 s50, s82, 1216512\n"
      "v_mfma_f32_16x16x32_bf16 v[68:71], a[52:55], v[164:167], v[68:71]\n"
      "buffer_load_dwordx4 v96, s[40:43], s50 offen lds\n"
      "s_add_u32 m0, s77, 85536\n"
      "v_mfma_f32_16x16x32_bf16 v[72:75], v[192:195], v[164:167], v[72:75]\n"
      "s_add_u32 s50, s82, 1223424\n"
      "buffer_load_dwordx4 v96, s[40:43], s50 offen lds\n"
      "v_mfma_f32_16x16x32_bf16 v[76:79], a[56:59], v[164:167], v[76:79]\n"
      "s_add_u32 m0, s77, 86592\n"
      "s_add_u32 s50, s82, 1230336\n"
      "v_mfma_f32_16x16x32_bf16 v[80:83], a[48:51], v[164:167], v[80:83]\n"
      "buffer_load_dwordx4 v96, s[40:43], s50 offen lds\n"
      "s_add_u32 m0, s77, 87648\n"
      "v_mfma_f32_16x16x32_bf16 v[52:55], a[52:55], v[28:31], v[52:55]\n"
      "s_add_u32 s50, s82, 1237248\n"
      "buffer_load_dwordx4 v96, s[40:43], s50 offen lds\n"
      "v_mfma_f32_16x16x32_bf16 v[56:59], v[192:195], v[28:31], v[56:59]\n"
      "v_mfma_f32_16x16x32_bf16 v[60:63], a[56:59], v[28:31], v[60:63]\n"
      "v_mfma_f32_16x16x32_bf16 v[64:67], a[48:51], v[28:31], v[64:67]\n"
      "s_waitcnt vmcnt(16) lgkmcnt(0)\n"
      "s_barrier\n"
      "v_cvt_pk_bf16_f32 v196, v136, v137\n"
      "v_cvt_pk_bf16_f32 v197, v138, v139\n"
      "v_cvt_pk_bf16_f32 v198, v140, v141\n"
      "v_cvt_pk_bf16_f32 v199, v142, v143\n"
      "v_mfma_f32_16x16x32_bf16 v[128:131], v[144:147], v[154:157], 0\n"
      "ds_read_b32 v148, v99 offset:16896\n"
      "ds_read_b32 v150, v99 offset:19008\n"
      "v_mfma_f32_16x16x32_bf16 v[132:135], v[144:147], v[152:155], 0\n"
      "ds_read_b64_tr_b16 a[84:85], v97 offset:16896\n"
      "ds_read_b64_tr_b16 a[86:87], v97 offset:19008\n"
      "v_mfma_f32_16x16x32_bf16 v[36:39], a[64:67], v[124:127], v[36:39]\n"
      "ds_read_b64_tr_b16 a[88:89], v97 offset:17040\n"
      "ds_read_b64_tr_b16 a[90:91], v97 offset:19152\n"
      "v_mfma_f32_16x16x32_bf16 v[40:43], v[196:199], v[124:127], v[40:43]\n"
      "ds_read_b64_tr_b16 a[92:93], v97 offset:17184\n"
      "ds_read_b64_tr_b16 a[94:95], v97 offset:19296\n"
      "v_mfma_f32_16x16x32_bf16 v[44:47], a[68:71], v[124:127], v[44:47]\n"
      "ds_read_b128 v[120:123], v101 offset:6144\n"
      "ds_read_b128 v[20:23], v101 offset:26624\n"
      "v_mfma_f32_16x16x32_bf16 v[48:51], a[60:63], v[124:127], v[48:51]\n"
      "s_add_u32 m0, s77, 0\n"
      "s_add_u32 s50, s82, 1327104\n"
      "v_mfma_f32_16x16x32_bf16 v[68:71], a[64:67], v[164:167], v[68:71]\n"
      "buffer_load_dwordx4 v96, s[40:43], s50 offen lds\n"
      "s_add_u32 m0, s77, 1056\n"
      "v_mfma_f32_16x16x32_bf16 v[72:75], v[196:199], v[164:167], v[72:75]\n"
      "s_add_u32 s50, s82, 1334016\n"
      "buffer_load_dwordx4 v96, s[40:43], s50 offen lds\n"
      "v_mfma_f32_16x16x32_bf16 v[76:79], a[68:71], v[164:167], v[76:79]\n"
      "s_add_u32 m0, s77, 2112\n"
      "s_add_u32 s50, s82, 1340928\n"
      "v_mfma_f32_16x16x32_bf16 v[80:83], a[60:63], v[164:167], v[80:83]\n"
      "buffer_load_dwordx4 v96, s[40:43], s50 offen lds\n"
      "s_add_u32 m0, s77, 3168\n"
      "v_mfma_f32_16x16x32_bf16 v[52:55], a[64:67], v[32:35], v[52:55]\n"
      "s_add_u32 s50, s82, 1347840\n"
      "buffer_load_dwordx4 v96, s[40:43], s50 offen lds\n"
      "v_mfma_f32_16x16x32_bf16 v[56:59], v[196:199], v[32:35], v[56:59]\n"
      "v_mfma_f32_16x16x32_bf16 v[60:63], a[68:71], v[32:35], v[60:63]\n"
      "v_mfma_f32_16x16x32_bf16 v[64:67], a[60:63], v[32:35], v[64:67]\n"
      "s_waitcnt vmcnt(16) lgkmcnt(0)\n"
      "s_barrier\n"
      "v_cvt_pk_bf16_f32 v200, v128, v129\n"
      "v_cvt_pk_bf16_f32 v201, v130, v131\n"
      "v_cvt_pk_bf16_f32 v202, v132, v133\n"
      "v_cvt_pk_bf16_f32 v203, v134, v135\n"
      "v_mfma_f32_16x16x32_bf16 v[136:139], v[148:151], v[154:157], 0\n"
      "ds_read_b32 v144, v99 offset:33792\n"
      "ds_read_b32 v146, v99 offset:35904\n"
      "v_mfma_f32_16x16x32_bf16 v[140:143], v[148:151], v[152:155], 0\n"
      "ds_read_b64_tr_b16 a[96:97], v97 offset:33792\n"
      "ds_read_b64_tr_b16 a[98:99], v97 offset:35904\n"
      "v_mfma_f32_16x16x32_bf16 v[36:39], a[76:79], v[120:123], v[36:39]\n"
      "ds_read_b64_tr_b16 a[100:101], v97 offset:33936\n"
      "ds_read_b64_tr_b16 a[102:103], v97 offset:36048\n"
      "v_mfma_f32_16x16x32_bf16 v[40:43], v[200:203], v[120:123], v[40:43]\n"
      "ds_read_b64_tr_b16 a[104:105], v97 offset:34080\n"
      "ds_read_b64_tr_b16 a[106:107], v97 offset:36192\n"
      "v_mfma_f32_16x16x32_bf16 v[44:47], a[80:83], v[120:123], v[44:47]\n"
      "ds_read_b128 v[124:127], v103 offset:6144\n"
      "ds_read_b128 v[24:27], v103 offset:26624\n"
      "v_mfma_f32_16x16x32_bf16 v[48:51], a[72:75], v[120:123], v[48:51]\n"
      "s_add_u32 m0, s77, 16896\n"
      "s_add_u32 s50, s82, 1437696\n"
      "v_mfma_f32_16x16x32_bf16 v[68:71], a[76:79], v[164:167], v[68:71]\n"
      "buffer_load_dwordx4 v96, s[40:43], s50 offen lds\n"
      "s_add_u32 m0, s77, 17952\n"
      "v_mfma_f32_16x16x32_bf16 v[72:75], v[200:203], v[164:167], v[72:75]\n"
      "s_add_u32 s50, s82, 1444608\n"
      "buffer_load_dwordx4 v96, s[40:43], s50 offen lds\n"
      "v_mfma_f32_16x16x32_bf16 v[76:79], a[80:83], v[164:167], v[76:79]\n"
      "s_add_u32 m0, s77, 19008\n"
      "s_add_u32 s50, s82, 1451520\n"
      "v_mfma_f32_16x16x32_bf16 v[80:83], a[72:75], v[164:167], v[80:83]\n"
      "buffer_load_dwordx4 v96, s[40:43], s50 offen lds\n"
      "s_add_u32 m0, s77, 20064\n"
      "v_mfma_f32_16x16x32_bf16 v[52:55], a[76:79], v[20:23], v[52:55]\n"
      "s_add_u32 s50, s82, 1458432\n"
      "buffer_load_dwordx4 v96, s[40:43], s50 offen lds\n"
      "v_mfma_f32_16x16x32_bf16 v[56:59], v[200:203], v[20:23], v[56:59]\n"
      "v_mfma_f32_16x16x32_bf16 v[60:63], a[80:83], v[20:23], v[60:63]\n"
      "v_mfma_f32_16x16x32_bf16 v[64:67], a[72:75], v[20:23], v[64:67]\n"
      "s_waitcnt vmcnt(16) lgkmcnt(0)\n"
      "s_barrier\n"
      "v_cvt_pk_bf16_f32 v204, v136, v137\n"
      "v_cvt_pk_bf16_f32 v205, v138, v139\n"
      "v_cvt_pk_bf16_f32 v206, v140, v141\n"
      "v_cvt_pk_bf16_f32 v207, v142, v143\n"
      "v_mfma_f32_16x16x32_bf16 v[128:131], v[144:147], v[154:157], 0\n"
      "ds_read_b32 v148, v100 offset:0\n"
      "ds_read_b32 v150, v100 offset:2112\n"
      "v_mfma_f32_16x16x32_bf16 v[132:135], v[144:147], v[152:155], 0\n"
      "ds_read_b64_tr_b16 a[108:109], v98 offset:0\n"
      "ds_read_b64_tr_b16 a[110:111], v98 offset:2112\n"
      "v_mfma_f32_16x16x32_bf16 v[36:39], a[88:91], v[124:127], v[36:39]\n"
      "ds_read_b64_tr_b16 a[112:113], v98 offset:144\n"
      "ds_read_b64_tr_b16 a[114:115], v98 offset:2256\n"
      "v_mfma_f32_16x16x32_bf16 v[40:43], v[204:207], v[124:127], v[40:43]\n"
      "ds_read_b64_tr_b16 a[116:117], v98 offset:288\n"
      "ds_read_b64_tr_b16 a[118:119], v98 offset:2400\n"
      "v_mfma_f32_16x16x32_bf16 v[44:47], a[92:95], v[124:127], v[44:47]\n"
      "ds_read_b128 v[120:123], v101 offset:8192\n"
      "ds_read_b128 v[28:31], v101 offset:28672\n"
      "v_mfma_f32_16x16x32_bf16 v[48:51], a[84:87], v[124:127], v[48:51]\n"
      "s_add_u32 m0, s77, 33792\n"
      "s_add_u32 s50, s82, 1548288\n"
      "v_mfma_f32_16x16x32_bf16 v[68:71], a[88:91], v[164:167], v[68:71]\n"
      "buffer_load_dwordx4 v96, s[40:43], s50 offen lds\n"
      "s_add_u32 m0, s77, 34848\n"
      "v_mfma_f32_16x16x32_bf16 v[72:75], v[204:207], v[164:167], v[72:75]\n"
      "s_add_u32 s50, s82, 1555200\n"
      "buffer_load_dwordx4 v96, s[40:43], s50 offen lds\n"
      "v_mfma_f32_16x16x32_bf16 v[76:79], a[92:95], v[164:167], v[76:79]\n"
      "s_add_u32 m0, s77, 35904\n"
      "s_add_u32 s50, s82, 1562112\n"
      "v_mfma_f32_16x16x32_bf16 v[80:83], a[84:87], v[164:167], v[80:83]\n"
      "buffer_load_dwordx4 v96, s[40:43], s50 offen lds\n"
      "s_add_u32 m0, s77, 36960\n"
      "v_mfma_f32_16x16x32_bf16 v[52:55], a[88:91], v[24:27], v[52:55]\n"
      "s_add_u32 s50, s82, 1569024\n"
      "buffer_load_dwordx4 v96, s[40:43], s50 offen lds\n"
      "v_mfma_f32_16x16x32_bf16 v[56:59], v[204:207], v[24:27], v[56:59]\n"
      "v_mfma_f32_16x16x32_bf16 v[60:63], a[92:95], v[24:27], v[60:63]\n"
      "v_mfma_f32_16x16x32_bf16 v[64:67], a[84:87], v[24:27], v[64:67]\n"
      "s_waitcnt vmcnt(16) lgkmcnt(0)\n"
      "s_barrier\n"
      "v_cvt_pk_bf16_f32 v208, v128, v129\n"
      "v_cvt_pk_bf16_f32 v209, v130, v131\n"
      "v_cvt_pk_bf16_f32 v210, v132, v133\n"
      "v_cvt_pk_bf16_f32 v211, v134, v135\n"
      "v_mfma_f32_16x16x32_bf16 v[136:139], v[148:151], v[154:157], 0\n"
      "ds_read_b32 v144, v100 offset:16896\n"
      "ds_read_b32 v146, v100 offset:19008\n"
      "v_mfma_f32_16x16x32_bf16 v[140:143], v[148:151], v[152:155], 0\n"
      "ds_read_b64_tr_b16 a[120:121], v98 offset:16896\n"
      "ds_read_b64_tr_b16 a[122:123], v98 offset:19008\n"
      "v_mfma_f32_16x16x32_bf16 v[36:39], a[100:103], v[120:123], v[36:39]\n"
      "ds_read_b64_tr_b16 a[124:125], v98 offset:17040\n"
      "ds_read_b64_tr_b16 a[126:127], v98 offset:19152\n"
      "v_mfma_f32_16x16x32_bf16 v[40:43], v[208:211], v[120:123], v[40:43]\n"
      "ds_read_b64_tr_b16 a[128:129], v98 offset:17184\n"
      "ds_read_b64_tr_b16 a[130:131], v98 offset:19296\n"
      "v_mfma_f32_16x16x32_bf16 v[44:47], a[104:107], v[120:123], v[44:47]\n"
      "ds_read_b128 v[124:127], v103 offset:8192\n"
      "ds_read_b128 v[32:35], v103 offset:28672\n"
      "v_mfma_f32_16x16x32_bf16 v[48:51], a[96:99], v[120:123], v[48:51]\n"
      "s_add_u32 m0, s77, 50688\n"
      "s_add_u32 s50, s82, 1658880\n"
      "v_mfma_f32_16x16x32_bf16 v[68:71], a[100:103], v[164:167], v[68:71]\n"
      "buffer_load_dwordx4 v96, s[40:43], s50 offen lds\n"
      "s_add_u32 m0, s77, 51744\n"
      "v_mfma_f32_16x16x32_bf16 v[72:75], v[208:211], v[164:167], v[72:75]\n"
      "s_add_u32 s50, s82, 1665792\n"
      "buffer_load_dwordx4 v96, s[40:43], s50 offen lds\n"
      "v_mfma_f32_16x16x32_bf16 v[76:79], a[104:107], v[164:167], v[76:79]\n"
      "s_add_u32 m0, s77, 52800\n"
      "s_add_u32 s50, s82, 1672704\n"
      "v_mfma_f32_16x16x32_bf16 v[80:83], a[96:99], v[164:167], v[80:83]\n"
      "buffer_load_dwordx4 v96, s[40:43], s50 offen lds\n"
      "s_add_u32 m0, s77, 53856\n"
      "v_mfma_f32_16x16x32_bf16 v[52:55], a[100:103], v[28:31], v[52:55]\n"
      "s_add_u32 s50, s82, 1679616\n"
      "buffer_load_dwordx4 v96, s[40:43], s50 offen lds\n"
      "v_mfma_f32_16x16x32_bf16 v[56:59], v[208:211], v[28:31], v[56:59]\n"
      "v_mfma_f32_16x16x32_bf16 v[60:63], a[104:107], v[28:31], v[60:63]\n"
      "v_mfma_f32_16x16x32_bf16 v[64:67], a[96:99], v[28:31], v[64:67]\n"
      "s_waitcnt vmcnt(16) lgkmcnt(0)\n"
      "s_barrier\n"
      "v_cvt_pk_bf16_f32 v212, v136, v137\n"
      "v_cvt_pk_bf16_f32 v213, v138, v139\n"
      "v_cvt_pk_bf16_f32 v214, v140, v141\n"
      "v_cvt_pk_bf16_f32 v215, v142, v143\n"
      "v_mfma_f32_16x16x32_bf16 v[128:131], v[144:147], v[154:157], 0\n"
      "ds_read_b32 v148, v100 offset:33792\n"
      "ds_read_b32 v150, v100 offset:35904\n"
      "v_mfma_f32_16x16x32_bf16 v[132:135], v[144:147], v[152:155], 0\n"
      "ds_read_b64_tr_b16 a[132:133], v98 offset:33792\n"
      "ds_read_b64_tr_b16 a[134:135], v98 offset:35904\n"
      "v_mfma_f32_16x16x32_bf16 v[36:39], a[112:115], v[124:127], v[36:39]\n"
      "ds_read_b64_tr_b16 a[136:137], v98 offset:33936\n"
      "ds_read_b64_tr_b16 a[138:139], v98 offset:36048\n"
      "v_mfma_f32_16x16x32_bf16 v[40:43], v[212:215], v[124:127], v[40:43]\n"
      "ds_read_b64_tr_b16 a[140:141], v98 offset:34080\n"
      "ds_read_b64_tr_b16 a[142:143], v98 offset:36192\n"
      "v_mfma_f32_16x16x32_bf16 v[44:47], a[116:119], v[124:127], v[44:47]\n"
      "ds_read_b128 v[120:123], v101 offset:10240\n"
      "ds_read_b128 v[20:23], v101 offset:30720\n"
      "v_mfma_f32_16x16x32_bf16 v[48:51], a[108:111], v[124:127], v[48:51]\n"
      "s_add_u32 m0, s77, 67584\n"
      "s_add_u32 s50, s82, 1769472\n"
      "v_mfma_f32_16x16x32_bf16 v[68:71], a[112:115], v[164:167], v[68:71]\n"
      "buffer_load_dwordx4 v96, s[40:43], s50 offen lds\n"
      "s_add_u32 m0, s77, 68640\n"
      "v_mfma_f32_16x16x32_bf16 v[72:75], v[212:215], v[164:167], v[72:75]\n"
      "s_add_u32 s50, s82, 1776384\n"
      "buffer_load_dwordx4 v96, s[40:43], s50 offen lds\n"
      "v_mfma_f32_16x16x32_bf16 v[76:79], a[116:119], v[164:167], v[76:79]\n"
      "s_add_u32 m0, s77, 69696\n"
      "s_add_u32 s50, s82, 1783296\n"
      "v_mfma_f32_16x16x32_bf16 v[80:83], a[108:111], v[164:167], v[80:83]\n"
      "buffer_load_dwordx4 v96, s[40:43], s50 offen lds\n"
      "s_add_u32 m0, s77, 70752\n"
      "v_mfma_f32_16x16x32_bf16 v[52:55], a[112:115], v[32:35], v[52:55]\n"
      "s_add_u32 s50, s82, 1790208\n"
      "buffer_load_dwordx4 v96, s[40:43], s50 offen lds\n"
      "v_mfma_f32_16x16x32_bf16 v[56:59], v[212:215], v[32:35], v[56:59]\n"
      "v_mfma_f32_16x16x32_bf16 v[60:63], a[116:119], v[32:35], v[60:63]\n"
      "v_mfma_f32_16x16x32_bf16 v[64:67], a[108:111], v[32:35], v[64:67]\n"
      "s_waitcnt vmcnt(16) lgkmcnt(0)\n"
      "s_barrier\n"
      "v_cvt_pk_bf16_f32 v216, v128, v129\n"
      "v_cvt_pk_bf16_f32 v217, v130, v131\n"
      "v_cvt_pk_bf16_f32 v218, v132, v133\n"
      "v_cvt_pk_bf16_f32 v219, v134, v135\n"
      "v_mfma_f32_16x16x32_bf16 v[136:139], v[148:151], v[154:157], 0\n"
      "ds_read_b32 v144, v99 offset:0\n"
      "ds_read_b32 v146, v99 offset:2112\n"
      "v_mfma_f32_16x16x32_bf16 v[140:143], v[148:151], v[152:155], 0\n"
      "ds_read_b64_tr_b16 a[144:145], v97 offset:0\n"
      "ds_read_b64_tr_b16 a[146:147], v97 offset:2112\n"
      "v_mfma_f32_16x16x32_bf16 v[36:39], a[124:127], v[120:123], v[36:39]\n"
      "ds_read_b64_tr_b16 a[148:149], v97 offset:144\n"
      "ds_read_b64_tr_b16 a[150:151], v97 offset:2256\n"
      "v_mfma_f32_16x16x32_bf16 v[40:43], v[216:219], v[120:123], v[40:43]\n"
      "ds_read_b64_tr_b16 a[152:153], v97 offset:288\n"
      "ds_read_b64_tr_b16 a[154:155], v97 offset:2400\n"
      "v_mfma_f32_16x16x32_bf16 v[44:47], a[128:131], v[120:123], v[44:47]\n"
      "ds_read_b128 v[124:127], v103 offset:10240\n"
      "ds_read_b128 v[24:27], v103 offset:30720\n"
      "v_mfma_f32_16x16x32_bf16 v[48:51], a[120:123], v[120:123], v[48:51]\n"
      "s_add_u32 m0, s77, 84480\n"
      "s_add_u32 s50, s82, 1880064\n"
      "v_mfma_f32_16x16x32_bf16 v[68:71], a[124:127], v[164:167], v[68:71]\n"
      "buffer_load_dwordx4 v96, s[40:43], s50 offen lds\n"
      "s_add_u32 m0, s77, 85536\n"
      "v_mfma_f32_16x16x32_bf16 v[72:75], v[216:219], v[164:167], v[72:75]\n"
      "s_add_u32 s50, s82, 1886976\n"
      "buffer_load_dwordx4 v96, s[40:43], s50 offen lds\n"
      "v_mfma_f32_16x16x32_bf16 v[76:79], a[128:131], v[164:167], v[76:79]\n"
      "s_add_u32 m0, s77, 86592\n"
      "s_add_u32 s50, s82, 1893888\n"
      "v_mfma_f32_16x16x32_bf16 v[80:83], a[120:123], v[164:167], v[80:83]\n"
      "buffer_load_dwordx4 v96, s[40:43], s50 offen lds\n"
      "s_add_u32 m0, s77, 87648\n"
      "v_mfma_f32_16x16x32_bf16 v[52:55], a[124:127], v[20:23], v[52:55]\n"
      "s_add_u32 s50, s82, 1900800\n"
      "buffer_load_dwordx4 v96, s[40:43], s50 offen lds\n"
      "v_mfma_f32_16x16x32_bf16 v[56:59], v[216:219], v[20:23], v[56:59]\n"
      "v_mfma_f32_16x16x32_bf16 v[60:63], a[128:131], v[20:23], v[60:63]\n"
      "v_mfma_f32_16x16x32_bf16 v[64:67], a[120:123], v[20:23], v[64:67]\n"
      "s_waitcnt vmcnt(16) lgkmcnt(0)\n"
      "s_barrier\n"
      "v_cvt_pk_bf16_f32 v220, v136, v137\n"
      "v_cvt_pk_bf16_f32 v221, v138, v139\n"
      "v_cvt_pk_bf16_f32 v222, v140, v141\n"
      "v_cvt_pk_bf16_f32 v223, v142, v143\n"
      "v_mfma_f32_16x16x32_bf16 v[128:131], v[144:147], v[154:157], 0\n"
      "ds_read_b32 v148, v99 offset:16896\n"
      "ds_read_b32 v150, v99 offset:19008\n"
      "v_mfma_f32_16x16x32_bf16 v[132:135], v[144:147], v[152:155], 0\n"
      "ds_read_b64_tr_b16 a[156:157], v97 offset:16896\n"
      "ds_read_b64_tr_b16 a[158:159], v97 offset:19008\n"
      "v_mfma_f32_16x16x32_bf16 v[36:39], a[136:139], v[124:127], v[36:39]\n"
      "ds_read_b64_tr_b16 a[160:161], v97 offset:17040\n"
      "ds_read_b64_tr_b16 a[162:163], v97 offset:19152\n"
      "v_mfma_f32_16x16x32_bf16 v[40:43], v[220:223], v[124:127], v[40:43]\n"
      "ds_read_b64_tr_b16 a[164:165], v97 offset:17184\n"
      "ds_read_b64_tr_b16 a[166:167], v97 offset:19296\n"
      "v_mfma_f32_16x16x32_bf16 v[44:47], a[140:143], v[124:127], v[44:47]\n"
      "ds_read_b128 v[120:123], v101 offset:12288\n"
      "ds_read_b128 v[28:31], v101 offset:32768\n"
      "v_mfma_f32_16x16x32_bf16 v[48:51], a[132:135], v[124:127], v[48:51]\n"
      "buffer_load_dwordx2 v[18:19], v102, s[36:39], s71 offen offset:8 sc1\n"
      "v_mfma_f32_16x16x32_bf16 v[68:71], a[136:139], v[164:167], v[68:71]\n"
      "v_mfma_f32_16x16x32_bf16 v[72:75], v[220:223], v[164:167], v[72:75]\n"
      "v_mfma_f32_16x16x32_bf16 v[76:79], a[140:143], v[164:167], v[76:79]\n"
      "v_mfma_f32_16x16x32_bf16 v[80:83], a[132:135], v[164:167], v[80:83]\n"
      "v_mfma_f32_16x16x32_bf16 v[52:55], a[136:139], v[24:27], v[52:55]\n"
      "v_mfma_f32_16x16x32_bf16 v[56:59], v[220:223], v[24:27], v[56:59]\n"
      "v_mfma_f32_16x16x32_bf16 v[60:63], a[140:143], v[24:27], v[60:63]\n"
      "v_mfma_f32_16x16x32_bf16 v[64:67], a[132:135], v[24:27], v[64:67]\n"
      "s_waitcnt vmcnt(13) lgkmcnt(0)\n"
      "s_barrier\n"
      "v_cvt_pk_bf16_f32 v224, v128, v129\n"
      "v_cvt_pk_bf16_f32 v225, v130, v131\n"
      "v_cvt_pk_bf16_f32 v226, v132, v133\n"
      "v_cvt_pk_bf16_f32 v227, v134, v135\n"
      "v_mfma_f32_16x16x32_bf16 v[136:139], v[148:151], v[154:157], 0\n"
      "ds_read_b32 v144, v99 offset:33792\n"
      "ds_read_b32 v146, v99 offset:35904\n"
      "v_mfma_f32_16x16x32_bf16 v[140:143], v[148:151], v[152:155], 0\n"
      "ds_read_b64_tr_b16 a[168:169], v97 offset:33792\n"
      "ds_read_b64_tr_b16 a[170:171], v97 offset:35904\n"
      "v_mfma_f32_16x16x32_bf16 v[36:39], a[148:151], v[120:123], v[36:39]\n"
      "ds_read_b64_tr_b16 a[172:173], v97 offset:33936\n"
      "ds_read_b64_tr_b16 a[174:175], v97 offset:36048\n"
      "v_mfma_f32_16x16x32_bf16 v[40:43], v[224:227], v[120:123], v[40:43]\n"
      "ds_read_b64_tr_b16 a[176:177], v97 offset:34080\n"
      "ds_read_b64_tr_b16 a[178:179], v97 offset:36192\n"
      "v_mfma_f32_16x16x32_bf16 v[44:47], a[152:155], v[120:123], v[44:47]\n"
      "ds_read_b128 v[124:127], v103 offset:12288\n"
      "ds_read_b128 v[32:35], v103 offset:32768\n"
      "v_mfma_f32_16x16x32_bf16 v[48:51], a[144:147], v[120:123], v[48:51]\n"
      "v_mfma_f32_16x16x32_bf16 v[68:71], a[148:151], v[164:167], v[68:71]\n"
      "v_mfma_f32_16x16x32_bf16 v[72:75], v[224:227], v[164:167], v[72:75]\n"
      "v_mfma_f32_16x16x32_bf16 v[76:79], a[152:155], v[164:167], v[76:79]\n"
      "v_mfma_f32_16x16x32_bf16 v[80:83], a[144:147], v[164:167], v[80:83]\n"
      "v_mfma_f32_16x16x32_bf16 v[52:55], a[148:151], v[28:31], v[52:55]\n"
      "v_mfma_f32_16x16x32_bf16 v[56:59], v[224:227], v[28:31], v[56:59]\n"
      "v_mfma_f32_16x16x32_bf16 v[60:63], a[152:155], v[28:31], v[60:63]\n"
      "v_mfma_f32_16x16x32_bf16 v[64:67], a[144:147], v[28:31], v[64:67]\n"
      "s_waitcnt vmcnt(9) lgkmcnt(0)\n"
      "s_barrier\n"
      "v_cvt_pk_bf16_f32 v228, v136, v137\n"
      "v_cvt_pk_bf16_f32 v229, v138, v139\n"
      "v_cvt_pk_bf16_f32 v230, v140, v141\n"
      "v_cvt_pk_bf16_f32 v231, v142, v143\n"
      "v_mfma_f32_16x16x32_bf16 v[128:131], v[144:147], v[154:157], 0\n"
      "ds_read_b32 v148, v100 offset:0\n"
      "ds_read_b32 v150, v100 offset:2112\n"
      "ds_read_b64_tr_b16 a[180:181], v98 offset:0\n"
      "v_mfma_f32_16x16x32_bf16 v[132:135], v[144:147], v[152:155], 0\n"
      "ds_read_b64_tr_b16 a[182:183], v98 offset:2112\n"
      "ds_read_b64_tr_b16 a[184:185], v98 offset:144\n"
      "ds_read_b64_tr_b16 a[186:187], v98 offset:2256\n"
      "v_mfma_f32_16x16x32_bf16 v[36:39], a[160:163], v[124:127], v[36:39]\n"
      "ds_read_b64_tr_b16 a[188:189], v98 offset:288\n"
      "ds_read_b64_tr_b16 a[190:191], v98 offset:2400\n"
      "ds_read_b128 v[120:123], v101 offset:14336\n"
      "v_mfma_f32_16x16x32_bf16 v[40:43], v[228:231], v[124:127], v[40:43]\n"
      "ds_read_b128 v[20:23], v101 offset:34816\n"
      "s_waitcnt vmcnt(0)\n"
      "v_cmp_ne_u64 vcc, s[98:99], v[18:19]\n"
      "s_cbranch_vccnz .Lsl0\n"
      ".Lbk0:\n"
      "v_mfma_f32_16x16x32_bf16 v[44:47], a[164:167], v[124:127], v[44:47]\n"
      "s_cmp_lt_u32 s28, 2\n"
      "s_cselect_b32 s51, 162816, s78\n"
      "s_cselect_b32 s52, 0, 20480\n"
      "v_mfma_f32_16x16x32_bf16 v[48:51], a[156:159], v[124:127], v[48:51]\n"
      "s_mov_b32 m0, s51\n"
      "s_nop 0\n"
      "buffer_load_dwordx4 v4, s[36:39], s69 offen sc1 lds\n"
      "v_mfma_f32_16x16x32_bf16 v[68:71], a[160:163], v[164:167], v[68:71]\n"
      "s_add_u32 s69, s69, 20480\n"
      "s_add_u32 m0, s51, s52\n"
      "s_nop 0\n"
      "v_mfma_f32_16x16x32_bf16 v[72:75], v[228:231], v[164:167], v[72:75]\n"
      "buffer_load_dwordx4 v4, s[36:39], s69 offen sc1 lds\n"
      "s_add_u32 s69, s69, 20480\n"
      "s_add_u32 m0, s83, 40960\n"
      "v_mfma_f32_16x16x32_bf16 v[76:79], a[164:167], v[164:167], v[76:79]\n"
      "s_nop 0\n"
      "buffer_load_dwordx4 v2, s[36:39], s69 offen sc1 lds\n"
      "buffer_load_dwordx4 v2, s[36:39], s69 offen offset:1024 sc1 lds\n"
      "v_mfma_f32_16x16x32_bf16 v[80:83], a[156:159], v[164:167], v[80:83]\n"
      "buffer_load_dwordx4 v2, s[36:39], s69 offen offset:2048 sc1 lds\n"
      "buffer_load_dwordx4 v2, s[36:39], s69 offen offset:3072 sc1 lds\n"
      "s_add_u32 m0, s78, 40960\n"
      "v_mfma_f32_16x16x32_bf16 v[52:55], a[160:163], v[32:35], v[52:55]\n"
      "s_nop 0\n"
      "buffer_load_dwordx4 v4, s[36:39], s69 offen sc1 lds\n"
      "s_add_u32 s69, s69, 20480\n"
      "v_mfma_f32_16x16x32_bf16 v[56:59], v[228:231], v[32:35], v[56:59]\n"
      "v_mfma_f32_16x16x32_bf16 v[60:63], a[164:167], v[32:35], v[60:63]\n"
      "v_mfma_f32_16x16x32_bf16 v[64:67], a[156:159], v[32:35], v[64:67]\n"
      "s_waitcnt lgkmcnt(0)\n"
      "s_barrier\n"
      "v_cvt_pk_bf16_f32 v232, v128, v129\n"
      "v_cvt_pk_bf16_f32 v233, v130, v131\n"
      "v_cvt_pk_bf16_f32 v234, v132, v133\n"
      "v_cvt_pk_bf16_f32 v235, v134, v135\n"
      "v_mfma_f32_16x16x32_bf16 v[136:139], v[148:151], v[154:157], 0\n"
      "ds_read_b32 v144, v100 offset:16896\n"
      "ds_read_b32 v146, v100 offset:19008\n"
      "ds_read_b64_tr_b16 a[192:193], v98 offset:16896\n"
      "v_mfma_f32_16x16x32_bf16 v[140:143], v[148:151], v[152:155], 0\n"
      "ds_read_b64_tr_b16 a[194:195], v98 offset:19008\n"
      "ds_read_b64_tr_b16 a[196:197], v98 offset:17040\n"
      "ds_read_b64_tr_b16 a[198:199], v98 offset:19152\n"
      "v_mfma_f32_16x16x32_bf16 v[36:39], a[172:175], v[120:123], v[36:39]\n"
      "ds_read_b64_tr_b16 a[200:201], v98 offset:17184\n"
      "ds_read_b64_tr_b16 a[202:203], v98 offset:19296\n"
      "ds_read_b128 v[124:127], v103 offset:14336\n"
      "v_mfma_f32_16x16x32_bf16 v[40:43], v[232:235], v[120:123], v[40:43]\n"
      "ds_read_b128 v[24:27], v103 offset:34816\n"
      "s_add_u32 m0, s83, 61440\n"
      "s_nop 0\n"
      "v_mfma_f32_16x16x32_bf16 v[44:47], a[176:179], v[120:123], v[44:47]\n"
      "buffer_load_dwordx4 v2, s[36:39], s69 offen sc1 lds\n"
      "buffer_load_dwordx4 v2, s[36:39], s69 offen offset:1024 sc1 lds\n"
      "buffer_load_dwordx4 v2, s[36:39], s69 offen offset:2048 sc1 lds\n"
      "v_mfma_f32_16x16x32_bf16 v[48:51], a[168:171], v[120:123], v[48:51]\n"
      "buffer_load_dwordx4 v2, s[36:39], s69 offen offset:3072 sc1 lds\n"
      "s_add_u32 m0, s78, 61440\n"
      "s_nop 0\n"
      "v_mfma_f32_16x16x32_bf16 v[68:71], a[172:175], v[164:167], v[68:71]\n"
      "buffer_load_dwordx4 v4, s[36:39], s69 offen sc1 lds\n"
      "s_add_u32 s69, s69, 20480\n"
      "s_add_u32 s71, s71, 6144\n"
      "v_mfma_f32_16x16x32_bf16 v[72:75], v[232:235], v[164:167], v[72:75]\n"
      "s_add_u32 s73, s73, 6\n"
      "s_add_u32 s72, s71, 1536\n"
      "s_add_u32 s70, s69, 20480\n"
      "v_mfma_f32_16x16x32_bf16 v[76:79], a[176:179], v[164:167], v[76:79]\n"
      "v_mov_b32 v18, v16\n"
      "v_mfma_f32_16x16x32_bf16 v[80:83], a[168:171], v[164:167], v[80:83]\n"
      "v_mfma_f32_16x16x32_bf16 v[52:55], a[172:175], v[20:23], v[52:55]\n"
      "v_mfma_f32_16x16x32_bf16 v[56:59], v[232:235], v[20:23], v[56:59]\n"
      "s_waitcnt lgkmcnt(0)\n"
      "s_barrier\n"
      "v_cvt_pk_bf16_f32 v236, v136, v137\n"
      "v_cvt_pk_bf16_f32 v237, v138, v139\n"
      "v_cvt_pk_bf16_f32 v238, v140, v141\n"
      "v_cvt_pk_bf16_f32 v239, v142, v143\n"
      "v_mfma_f32_16x16x32_bf16 v[128:131], v[144:147], v[154:157], 0\n"
      "ds_read_b32 v148, v100 offset:33792\n"
      "ds_read_b32 v150, v100 offset:35904\n"
      "v_mfma_f32_16x16x32_bf16 v[132:135], v[144:147], v[152:155], 0\n"
      "ds_read_b64_tr_b16 a[204:205], v98 offset:33792\n"
      "ds_read_b64_tr_b16 a[206:207], v98 offset:35904\n"
      "v_mfma_f32_16x16x32_bf16 v[36:39], a[184:187], v[124:127], v[36:39]\n"
      "ds_read_b64_tr_b16 a[208:209], v98 offset:33936\n"
      "ds_read_b64_tr_b16 a[210:211], v98 offset:36048\n"
      "v_mfma_f32_16x16x32_bf16 v[40:43], v[236:239], v[124:127], v[40:43]\n"
      "ds_read_b64_tr_b16 a[212:213], v98 offset:34080\n"
      "ds_read_b64_tr_b16 a[214:215], v98 offset:36192\n"
      "v_mfma_f32_16x16x32_bf16 v[44:47], a[188:191], v[124:127], v[44:47]\n"
      "ds_read_b128 v[120:123], v101 offset:16384\n"
      "ds_read_b128 v[28:31], v101 offset:36864\n"
      "v_mfma_f32_16x16x32_bf16 v[48:51], a[180:183], v[124:127], v[48:51]\n"
      "v_mfma_f32_16x16x32_bf16 v[68:71], a[184:187], v[164:167], v[68:71]\n"
      "v_mfma_f32_16x16x32_bf16 v[72:75], v[236:239], v[164:167], v[72:75]\n"
      "v_mfma_f32_16x16x32_bf16 v[76:79], a[188:191], v[164:167], v[76:79]\n"
      "v_mfma_f32_16x16x32_bf16 v[80:83], a[180:183], v[164:167], v[80:83]\n"
      "v_mfma_f32_16x16x32_bf16 v[52:55], a[184:187], v[24:27], v[52:55]\n"
      "v_mfma_f32_16x16x32_bf16 v[56:59], v[236:239], v[24:27], v[56:59]\n"
      "s_waitcnt vmcnt(5) lgkmcnt(0)\n"
      "s_barrier\n"
      "v_cvt_pk_bf16_f32 v240, v128, v129\n"
      "v_cvt_pk_bf16_f32 v241, v130, v131\n"
      "v_cvt_pk_bf16_f32 v242, v132, v133\n"
      "v_cvt_pk_bf16_f32 v243, v134, v135\n"
      "v_mfma_f32_16x16x32_bf16 v[136:139], v[148:151], v[154:157], 0\n"
      "ds_read_b128 v[124:127], v103 offset:16384\n"
      "ds_read_b128 v[32:35], v103 offset:36864\n"
      "ds_read_b64 v[158:159], v10 offset:18432\n"
      "v_mfma_f32_16x16x32_bf16 v[140:143], v[148:151], v[152:155], 0\n"
      "ds_read_b64 v[160:161], v10 offset:18944\n"
      "ds_read_b64 v[162:163], v10 offset:19456\n"
      "s_waitcnt lgkmcnt(0)\n"
      "v_add_f32 v158, v158, v160\n"
      "v_mfma_f32_16x16x32_bf16 v[36:39], a[196:199], v[120:123], v[36:39]\n"
      "v_add_f32 v159, v159, v161\n"
      "v_add_f32 v158, v158, v162\n"
      "v_add_f32 v159, v159, v163\n"
      "v_mfma_f32_16x16x32_bf16 v[40:43], v[240:243], v[120:123], v[40:43]\n"
      "s_nop 0\n"
      "v_permlane32_swap_b32 v158, v159\n"
      "v_add_f32 v158, v158, v159\n"
      "v_mov_b32 v160, v158\n"
      "v_mfma_f32_16x16x32_bf16 v[44:47], a[200:203], v[120:123], v[44:47]\n"
      "s_nop 0\n"
      "v_permlane16_swap_b32 v158, v160\n"
      "v_add_f32 v158, v158, v160\n"
      "v_mov_b32 v252, v158\n"
      "v_mfma_f32_16x16x32_bf16 v[48:51], a[192:195], v[120:123], v[48:51]\n"
      "s_nop 0\n"
      "v_permlane32_swap_b32 v252, v158\n"
      "v_mul_f32 v252, 0xbae38e39, v252\n"
      "v_fmaak_f32 v158, v17, v158, 0x3727c5ac\n"
      "v_mfma_f32_16x16x32_bf16 v[68:71], a[196:199], v[164:167], v[68:71]\n"
      "v_fma_f32 v158, -v252, v252, v158\n"
      "v_max_f32 v158, 0x3727c5ac, v158\n"
      "v_rsq_f32 v174, v158\n"
      "v_mfma_f32_16x16x32_bf16 v[72:75], v[240:243], v[164:167], v[72:75]\n"
      "v_mfma_f32_16x16x32_bf16 v[76:79], a[200:203], v[164:167], v[76:79]\n"
      "v_mfma_f32_16x16x32_bf16 v[80:83], a[192:195], v[164:167], v[80:83]\n"
      "s_barrier\n"
      "v_cvt_pk_bf16_f32 v244, v136, v137\n"
      "v_cvt_pk_bf16_f32 v245, v138, v139\n"
      "v_cvt_pk_bf16_f32 v246, v140, v141\n"
      "v_cvt_pk_bf16_f32 v247, v142, v143\n"
      "v_mfma_f32_16x16x32_bf16 v[36:39], a[208:211], v[124:127], v[36:39]\n"
      "ds_read_b64 v[158:159], v10 offset:38912\n"
      "ds_read_b64 v[160:161], v10 offset:39424\n"
      "ds_read_b64 v[162:163], v10 offset:39936\n"
      "v_mfma_f32_16x16x32_bf16 v[40:43], v[244:247], v[124:127], v[40:43]\n"
      "s_waitcnt lgkmcnt(0)\n"
      "v_add_f32 v158, v158, v160\n"
      "v_add_f32 v159, v159, v161\n"
      "v_add_f32 v158, v158, v162\n"
      "v_mfma_f32_16x16x32_bf16 v[44:47], a[212:215], v[124:127], v[44:47]\n"
      "v_add_f32 v159, v159, v163\n"
      "s_nop 1\n"
      "v_permlane32_swap_b32 v158, v159\n"
      "v_add_f32 v158, v158, v159\n"
      "v_mfma_f32_16x16x32_bf16 v[48:51], a[204:207], v[124:127], v[48:51]\n"
      "v_mov_b32 v160, v158\n"
      "s_nop 1\n"
      "v_permlane16_swap_b32 v158, v160\n"
      "v_add_f32 v158, v158, v160\n"
      "v_mfma_f32_16x16x32_bf16 v[68:71], a[208:211], v[164:167], v[68:71]\n"
      "v_mov_b32 v253, v158\n"
      "s_nop 1\n"
      "v_permlane32_swap_b32 v253, v158\n"
      "v_mul_f32 v253, 0xbae38e39, v253\n"
      "v_mfma_f32_16x16x32_bf16 v[72:75], v[244:247], v[164:167], v[72:75]\n"
      "v_fmaak_f32 v158, v17, v158, 0x3727c5ac\n"
      "v_fma_f32 v158, -v253, v253, v158\n"
      "v_max_f32 v158, 0x3727c5ac, v158\n"
      "v_mfma_f32_16x16x32_bf16 v[76:79], a[212:215], v[164:167], v[76:79]\n"
      "v_rsq_f32 v175, v158\n"
      "v_mfma_f32_16x16x32_bf16 v[80:83], a[204:207], v[164:167], v[80:83]\n"
      "s_mov_b32 s77, s83\n"
      "v_mov_b32 v163, 0\n"
      "v_fmac_f32 v36, v252, v68\n"
      "v_fmac_f32 v37, v252, v69\n"
      "v_fmac_f32 v38, v252, v70\n"
      "v_fmac_f32 v39, v252, v71\n"
      "v_mul_f32 v36, v174, v36\n"
      "v_mul_f32 v37, v174, v37\n"
      "v_mul_f32 v38, v174, v38\n"
      "v_mul_f32 v39, v174, v39\n"
      "v_fmac_f32 v41, v252, v73\n"
      "v_cvt_pk_bf16_f32 v92, v36, v37\n"
      "v_cvt_pk_bf16_f32 v93, v38, v39\n"
      "v_fmac_f32 v44, v252, v76\n"
      "v_mul_f32 v41, v174, v41\n"
      "v_mul_f32 v44, v174, v44\n"
      "v_fmac_f32 v45, v252, v77\n"
      "v_fmac_f32 v46, v252, v78\n"
      "v_cvt_pk_bf16_f32 v94, v41, 0\n"
      "v_mul_f32 v45, v174, v45\n"
      "v_mul_f32 v46, v174, v46\n"
      "v_fmac_f32 v47, v252, v79\n"
      "v_mul_f32 v47, v174, v47\n"
      "v_fmac_f32 v42, v252, v74\n"
      "v_cvt_pk_bf16_f32 v160, v44, v45\n"
      "v_fmac_f32 v48, v252, v80\n"
      "v_mul_f32 v42, v174, v42\n"
      "v_cvt_pk_bf16_f32 v161, v46, v47\n"
      "v_fmac_f32 v49, v252, v81\n"
      "v_fmac_f32 v50, v252, v82\n"
      "v_cvt_pk_bf16_f32 v162, v42, 0\n"
      "v_fmac_f32 v51, v252, v83\n"
      "v_fmac_f32 v40, v252, v72\n"
      "v_cvt_pk_bf16_f32 v84, v48, v49\n"
      "v_cvt_pk_bf16_f32 v85, v50, v51\n"
      "v_cvt_pk_bf16_f32 v86, v40, 0\n"
      "v_mul_f32 v150, 0x3eae1a93, v174\n"
      "v_mfma_f32_16x16x32_bf16 v[132:135], v[160:163], v[248:251], 0\n"
      "v_mfma_f32_16x16x32_bf16 v[136:139], v[160:163], v[12:15], 0\n"
      "v_mfma_f32_16x16x32_bf16 v[120:123], v[92:95], v[84:87], v[104:107]\n"
      "s_nop 5\n"
      "v_cvt_pk_bf16_f32 v96, v132, v133\n"
      "v_cvt_pk_bf16_f32 v97, v134, v135\n"
      "v_cvt_pk_bf16_f32 v100, v136, v137\n"
      "v_cvt_pk_bf16_f32 v101, v138, v139\n"
      "v_max_f32 v152, v120, v121\n"
      "v_max_f32 v153, v122, v123\n"
      ".Lqkv_loop0:\n"
      "v_mfma_f32_16x16x32_bf16 v[60:63], a[176:179], v[20:23], v[60:63]\n"
      "ds_read_b128 v[146:149], v3 offset:0\n"
      "v_writelane_b32 v8, s76, 0\n"
      "v_mfma_f32_16x16x32_bf16 v[60:63], a[188:191], v[24:27], v[60:63]\n"
      "s_waitcnt vmcnt(8)\n"
      "v_cmp_ne_u32 vcc, v16, v18\n"
      "ds_read_b64 v[140:141], v9 offset:18432\n"
      "v_mfma_f32_16x16x32_bf16 v[64:67], a[168:171], v[20:23], v[64:67]\n"
      "s_add_u32 m0, s77, 0\n"
      "ds_read_b64 v[142:143], v9 offset:18944\n"
      "ds_read_b64 v[144:145], v9 offset:19456\n"
      "v_mfma_f32_16x16x32_bf16 v[64:67], a[180:183], v[24:27], v[64:67]\n"
      "s_waitcnt lgkmcnt(5)\n"
      "s_cbranch_vccnz .Lsl1\n"
      ".Lbk1:\n"
      "v_mul_f32 v151, 0x3eae1a93, v175\n"
      "v_mfma_f32_16x16x32_bf16 v[52:55], a[196:199], v[28:31], v[52:55]\n"
      "s_waitcnt lgkmcnt(4)\n"
      "ds_read_b128 v[168:171], v3 offset:1024\n"
      "v_mfma_f32_16x16x32_bf16 v[52:55], a[208:211], v[32:35], v[52:55]\n"
      "s_cmp_eq_u32 s66, s80\n"
      "buffer_load_dword v18, v5, s[36:39], s72 offen sc1\n"
      "v_mfma_f32_16x16x32_bf16 v[56:59], v[240:243], v[28:31], v[56:59]\n"
      "s_cbranch_scc1 .Ltg0\n"
      ".Ltb0:\n"
      "v_mfma_f32_16x16x32_bf16 v[56:59], v[244:247], v[32:35], v[56:59]\n"
      "buffer_load_dwordx4 v2, s[36:39], s69 offen sc1 lds\n"
      "v_mfma_f32_16x16x32_bf16 v[60:63], a[200:203], v[28:31], v[60:63]\n"
      "ds_read_b128 v[20:23], v3 offset:2048\n"
      "v_fmac_f32 v52, v253, v68\n"
      "v_mfma_f32_16x16x32_bf16 v[60:63], a[212:215], v[32:35], v[60:63]\n"
      "v_mul_f32 v52, v175, v52\n"
      "v_fmac_f32 v53, v253, v69\n"
      "v_mfma_f32_16x16x32_bf16 v[64:67], a[192:195], v[28:31], v[64:67]\n"
      "v_mul_f32 v53, v175, v53\n"
      "v_fmac_f32 v54, v253, v70\n"
      "v_mfma_f32_16x16x32_bf16 v[64:67], a[204:207], v[32:35], v[64:67]\n"
      "s_waitcnt lgkmcnt(5)\n"
      "buffer_load_dwordx4 v2, s[36:39], s69 offen offset:1024 sc1 lds\n"
      "v_mfma_f32_16x16x32_bf16 v[36:39], a[4:7], v[146:149], 0\n"
      "s_waitcnt lgkmcnt(1)\n"
      "ds_read_b128 v[24:27], v3 offset:3072\n"
      "v_mul_f32 v54, v175, v54\n"
      "v_mfma_f32_16x16x32_bf16 v[36:39], a[16:19], v[168:171], v[36:39]\n"
      "v_fmac_f32 v55, v253, v71\n"
      "v_fmac_f32 v57, v253, v73\n"
      "v_mfma_f32_16x16x32_bf16 v[40:43], v[176:179], v[146:149], 0\n"
      "v_mul_f32 v55, v175, v55\n"
      "v_mul_f32 v57, v175, v57\n"
      "v_mfma_f32_16x16x32_bf16 v[40:43], v[180:183], v[168:171], v[40:43]\n"
      "buffer_load_dwordx4 v2, s[36:39], s69 offen offset:2048 sc1 lds\n"
      "v_mfma_f32_16x16x32_bf16 v[44:47], a[8:11], v[146:149], 0\n"
      "ds_read_b128 v[28:31], v3 offset:4096\n"
      "v_cvt_pk_bf16_f32 v92, v52, v53\n"
      "v_mfma_f32_16x16x32_bf16 v[44:47], a[20:23], v[168:171], v[44:47]\n"
      "v_cvt_pk_bf16_f32 v93, v54, v55\n"
      "v_cvt_pk_bf16_f32 v94, v57, 0\n"
      "v_mfma_f32_16x16x32_bf16 v[48:51], a[0:3], v[146:149], 0\n"
      "v_fmac_f32 v60, v253, v76\n"
      "v_fmac_f32 v61, v253, v77\n"
      "v_mfma_f32_16x16x32_bf16 v[48:51], a[12:15], v[168:171], v[48:51]\n"
      "s_waitcnt lgkmcnt(2)\n"
      "buffer_load_dwordx4 v2, s[36:39], s69 offen offset:3072 sc1 lds\n"
      "v_mfma_f32_16x16x32_bf16 v[36:39], a[28:31], v[20:23], v[36:39]\n"
      "s_waitcnt lgkmcnt(1)\n"
      "ds_read_b128 v[32:35], v3 offset:5120\n"
      "v_mul_f32 v60, v175, v60\n"
      "v_mfma_f32_16x16x32_bf16 v[36:39], a[40:43], v[24:27], v[36:39]\n"
      "s_add_u32 m0, s78, 0\n"
      "v_mul_f32 v61, v175, v61\n"
      "v_fmac_f32 v62, v253, v78\n"
      "v_mfma_f32_16x16x32_bf16 v[40:43], v[184:187], v[20:23], v[40:43]\n"
      "v_mul_f32 v62, v175, v62\n"
      "v_fmac_f32 v63, v253, v79\n"
      "v_mfma_f32_16x16x32_bf16 v[124:127], v[92:95], v[84:87], v[108:111]\n"
      "buffer_load_dwordx4 v4, s[36:39], s69 offen sc1 lds\n"
      "v_mfma_f32_16x16x32_bf16 v[40:43], v[188:191], v[24:27], v[40:43]\n"
      "s_mov_b32 s71, s72\n"
      "v_mul_f32 v63, v175, v63\n"
      "v_fmac_f32 v58, v253, v74\n"
      "v_mfma_f32_16x16x32_bf16 v[44:47], a[32:35], v[20:23], v[44:47]\n"
      "s_cmp_le_i32 s66, 61\n"
      "ds_read_b128 v[146:149], v3 offset:6144\n"
      "v_mul_f32 v58, v175, v58\n"
      "v_mfma_f32_16x16x32_bf16 v[44:47], a[44:47], v[24:27], v[44:47]\n"
      "s_cselect_b32 s69, s70, 0x40000000\n"
      "v_max3_f32 v154, v152, v124, v125\n"
      "v_max3_f32 v155, v153, v126, v127\n"
      "v_mfma_f32_16x16x32_bf16 v[48:51], a[24:27], v[20:23], v[48:51]\n"
      "s_sub_u32 s48, s66, 1\n"
      "v_max_f32 v156, v154, v155\n"
      "v_mov_b32 v157, v156\n"
      "v_mfma_f32_16x16x32_bf16 v[48:51], a[36:39], v[24:27], v[48:51]\n"
      "s_waitcnt lgkmcnt(2)\n"
      "v_permlane32_swap_b32 v156, v157\n"
      "v_cvt_pk_bf16_f32 v160, v60, v61\n"
      "v_mfma_f32_16x16x32_bf16 v[36:39], a[52:55], v[28:31], v[36:39]\n"
      "s_waitcnt lgkmcnt(1)\n"
      "ds_read_b128 v[168:171], v3 offset:7168\n"
      "v_max_f32 v156, v156, v157\n"
      "v_mfma_f32_16x16x32_bf16 v[36:39], a[64:67], v[32:35], v[36:39]\n"
      "s_cmp_eq_u32 s66, 33\n"
      "v_mov_b32 v157, v156\n"
      "v_cvt_pk_bf16_f32 v161, v62, v63\n"
      "v_mfma_f32_16x16x32_bf16 v[40:43], v[192:195], v[28:31], v[40:43]\n"
      "s_cselect_b32 s48, 99, s48\n"
      "v_permlane16_swap_b32 v156, v157\n"
      "v_cvt_pk_bf16_f32 v162, v58, 0\n"
      "v_mfma_f32_16x16x32_bf16 v[40:43], v[196:199], v[32:35], v[40:43]\n"
      "s_cmp_lt_u32 s48, 65\n"
      "v_max_f32 v156, v156, v157\n"
      "v_mul_f32 v156, v156, v150\n"
      "v_mfma_f32_16x16x32_bf16 v[44:47], a[56:59], v[28:31], v[44:47]\n"
      "s_cselect_b32 s75, s74, 0x40000000\n"
      "ds_read_b128 v[20:23], v3 offset:8192\n"
      "v_fma_f32 v154, v154, v150, -v156\n"
      "v_mfma_f32_16x16x32_bf16 v[44:47], a[68:71], v[32:35], v[44:47]\n"
      "s_cselect_b32 s48, 20480, 0\n"
      "v_fma_f32 v155, v155, v150, -v156\n"
      "v_fmac_f32 v64, v253, v80\n"
      "v_mfma_f32_16x16x32_bf16 v[48:51], a[48:51], v[28:31], v[48:51]\n"
      "s_addc_u32 s76, s76, 0\n"
      "v_exp_f32 v154, v154\n"
      "v_mfma_f32_16x16x32_bf16 v[48:51], a[60:63], v[32:35], v[48:51]\n"
      "s_waitcnt lgkmcnt(2)\n"
      "v_exp_f32 v155, v155\n"
      "v_mfma_f32_16x16x32_bf16 v[36:39], a[76:79], v[146:149], v[36:39]\n"
      "s_waitcnt lgkmcnt(1)\n"
      "ds_read_b128 v[24:27], v3 offset:9216\n"
      "v_add_f32 v156, v154, v155\n"
      "v_mfma_f32_16x16x32_bf16 v[36:39], a[88:91], v[168:171], v[36:39]\n"
      "s_add_u32 s74, s74, s48\n"
      "v_mov_b32 v157, v156\n"
      "v_fmac_f32 v65, v253, v81\n"
      "v_mfma_f32_16x16x32_bf16 v[40:43], v[200:203], v[146:149], v[40:43]\n"
      "s_waitcnt vmcnt(6)\n"
      "v_permlane32_swap_b32 v156, v157\n"
      "s_barrier\n"
      "v_mfma_f32_16x16x32_bf16 v[40:43], v[204:207], v[168:171], v[40:43]\n"
      "buffer_store_dword v8, v7, s[36:39], 0 offen sc1\n"
      "v_mfma_f32_16x16x32_bf16 v[44:47], a[80:83], v[146:149], v[44:47]\n"
      "s_cmp_eq_u32 s73, 128\n"
      "ds_read_b128 v[28:31], v3 offset:10240\n"
      "v_add_f32 v156, v156, v157\n"
      "v_mfma_f32_16x16x32_bf16 v[44:47], a[92:95], v[168:171], v[44:47]\n"
      "s_subb_u32 s48, s73, 0\n"
      "v_mov_b32 v157, v156\n"
      "v_fmac_f32 v66, v253, v82\n"
      "v_mfma_f32_16x16x32_bf16 v[48:51], a[72:75], v[146:149], v[48:51]\n"
      "s_min_u32 s48, s48, s67\n"
      "v_permlane16_swap_b32 v156, v157\n"
      "v_fmac_f32 v67, v253, v83\n"
      "v_mfma_f32_16x16x32_bf16 v[48:51], a[84:87], v[168:171], v[48:51]\n"
      "s_mul_i32 s72, s48, 1536\n"
      "v_add_f32 v156, v156, v157\n"
      "v_fmac_f32 v56, v253, v72\n"
      "v_mfma_f32_16x16x32_bf16 v[132:135], v[160:163], v[248:251], 0\n"
      "s_add_u32 s72, s72, 82042880\n"
      "v_rcp_f32 v156, v156\n"
      "v_mfma_f32_16x16x32_bf16 v[136:139], v[160:163], v[12:15], 0\n"
      "s_waitcnt lgkmcnt(2)\n"
      "v_mul_f32 v154, v154, v156\n"
      "v_mul_f32 v155, v155, v156\n"
      "v_mfma_f32_16x16x32_bf16 v[36:39], a[100:103], v[20:23], v[36:39]\n"
      "s_waitcnt lgkmcnt(1)\n"
      "ds_read_b128 v[32:35], v3 offset:11264\n"
      "v_cvt_pk_bf16_f32 v154, v154, v154\n"
      "v_mfma_f32_16x16x32_bf16 v[36:39], a[112:115], v[24:27], v[36:39]\n"
      "s_mul_i32 s70, s48, 20480\n"
      "v_cvt_pk_bf16_f32 v155, v155, v155\n"
      "v_and_b32 v128, v154, v116\n"
      "v_mfma_f32_16x16x32_bf16 v[40:43], v[208:211], v[20:23], v[40:43]\n"
      "s_add_u32 s70, s70, s83\n"
      "v_and_b32 v129, v155, v117\n"
      "v_and_b32 v130, v154, v118\n"
      "v_mfma_f32_16x16x32_bf16 v[40:43], v[212:215], v[24:27], v[40:43]\n"
      "s_cmp_eq_u32 s66, 27\n"
      "v_and_b32 v131, v155, v119\n"
      "v_cvt_pk_bf16_f32 v98, v132, v133\n"
      "v_mfma_f32_16x16x32_bf16 v[44:47], a[104:107], v[20:23], v[44:47]\n"
      "s_cselect_b32 s48, 97, 1\n"
      "ds_read_b128 v[146:149], v3 offset:12288\n"
      "v_cvt_pk_bf16_f32 v99, v134, v135\n"
      "v_mfma_f32_16x16x32_bf16 v[44:47], a[116:119], v[24:27], v[44:47]\n"
      "s_add_u32 s73, s73, s48\n"
      "v_cvt_pk_bf16_f32 v102, v136, v137\n"
      "v_cvt_pk_bf16_f32 v103, v138, v139\n"
      "v_mfma_f32_16x16x32_bf16 v[48:51], a[96:99], v[20:23], v[48:51]\n"
      "s_add_u32 s66, s66, 1\n"
      "v_cvt_pk_bf16_f32 v88, v64, v65\n"
      "v_cvt_pk_bf16_f32 v89, v66, v67\n"
      "v_mfma_f32_16x16x32_bf16 v[48:51], a[108:111], v[24:27], v[48:51]\n"
      "v_cvt_pk_bf16_f32 v90, v56, 0\n"
      "v_add_f32 v140, v140, v142\n"
      "v_mfma_f32_16x16x32_bf16 v[132:135], v[96:99], v[128:131], 0\n"
      "v_add_f32 v141, v141, v143\n"
      "v_add_f32 v140, v140, v144\n"
      "v_mfma_f32_16x16x32_bf16 v[136:139], v[100:103], v[128:131], 0\n"
      "s_waitcnt lgkmcnt(2)\n"
      "v_add_f32 v141, v141, v145\n"
      "v_mfma_f32_16x16x32_bf16 v[36:39], a[124:127], v[28:31], v[36:39]\n"
      "s_waitcnt lgkmcnt(1)\n"
      "ds_read_b128 v[168:171], v3 offset:13312\n"
      "v_permlane32_swap_b32 v140, v141\n"
      "v_mfma_f32_16x16x32_bf16 v[36:39], a[136:139], v[32:35], v[36:39]\n"
      "v_cvt_pk_bf16_f32 v164, v132, v133\n"
      "v_cvt_pk_bf16_f32 v165, v134, v135\n"
      "v_mfma_f32_16x16x32_bf16 v[40:43], v[216:219], v[28:31], v[40:43]\n"
      "buffer_store_dwordx2 v[164:165], v11, s[36:39], s75 offen sc1\n"
      "v_mfma_f32_16x16x32_bf16 v[40:43], v[220:223], v[32:35], v[40:43]\n"
      "v_cvt_pk_bf16_f32 v166, v136, v137\n"
      "v_add_f32 v140, v140, v141\n"
      "v_mfma_f32_16x16x32_bf16 v[44:47], a[128:131], v[28:31], v[44:47]\n"
      "ds_read_b128 v[20:23], v3 offset:14336\n"
      "v_mov_b32 v142, v140\n"
      "v_mfma_f32_16x16x32_bf16 v[44:47], a[140:143], v[32:35], v[44:47]\n"
      "s_nop 0\n"
      "v_permlane16_swap_b32 v140, v142\n"
      "v_mfma_f32_16x16x32_bf16 v[48:51], a[120:123], v[28:31], v[48:51]\n"
      "v_add_f32 v140, v140, v142\n"
      "v_mfma_f32_16x16x32_bf16 v[48:51], a[132:135], v[32:35], v[48:51]\n"
      "s_waitcnt lgkmcnt(2)\n"
      "buffer_store_dword v166, v6, s[36:39], s75 offen sc1\n"
      "v_mfma_f32_16x16x32_bf16 v[36:39], a[148:151], v[146:149], v[36:39]\n"
      "s_waitcnt lgkmcnt(1)\n"
      "ds_read_b128 v[24:27], v3 offset:15360\n"
      "v_mov_b32 v252, v140\n"
      "v_mfma_f32_16x16x32_bf16 v[36:39], a[160:163], v[168:171], v[36:39]\n"
      "s_nop 0\n"
      "v_permlane32_swap_b32 v252, v140\n"
      "v_mfma_f32_16x16x32_bf16 v[120:123], v[92:95], v[88:91], v[104:107]\n"
      "v_mul_f32 v252, 0xbae38e39, v252\n"
      "v_mfma_f32_16x16x32_bf16 v[40:43], v[224:227], v[146:149], v[40:43]\n"
      "v_fmaak_f32 v140, v17, v140, 0x3727c5ac\n"
      "v_mfma_f32_16x16x32_bf16 v[40:43], v[228:231], v[168:171], v[40:43]\n"
      "v_fma_f32 v140, -v252, v252, v140\n"
      "v_mfma_f32_16x16x32_bf16 v[44:47], a[152:155], v[146:149], v[44:47]\n"
      "ds_read_b128 v[28:31], v3 offset:16384\n"
      "s_nop 0\n"
      "v_max_f32 v152, v120, v121\n"
      "v_mfma_f32_16x16x32_bf16 v[44:47], a[164:167], v[168:171], v[44:47]\n"
      "v_max_f32 v153, v122, v123\n"
      "v_max_f32 v140, 0x3727c5ac, v140\n"
      "v_mfma_f32_16x16x32_bf16 v[48:51], a[144:147], v[146:149], v[48:51]\n"
      "v_rsq_f32 v174, v140\n"
      "v_mfma_f32_16x16x32_bf16 v[48:51], a[156:159], v[168:171], v[48:51]\n"
      "s_waitcnt lgkmcnt(2)\n"
      "v_mfma_f32_16x16x32_bf16 v[36:39], a[172:175], v[20:23], v[36:39]\n"
      "s_waitcnt lgkmcnt(1)\n"
      "ds_read_b128 v[32:35], v3 offset:17408\n"
      "v_mfma_f32_16x16x32_bf16 v[36:39], a[184:187], v[24:27], v[36:39]\n"
      "v_mfma_f32_16x16x32_bf16 v[40:43], v[232:235], v[20:23], v[40:43]\n"
      "v_mfma_f32_16x16x32_bf16 v[40:43], v[236:239], v[24:27], v[40:43]\n"
      ".Lmq00:\n"
      "v_mfma_f32_16x16x32_bf16 v[44:47], a[176:179], v[20:23], v[44:47]\n"
      "s_add_u32 m0, s77, 20480\n"
      "ds_read_b128 v[146:149], v3 offset:20480\n"
      "v_writelane_b32 v8, s76, 0\n"
      "v_mfma_f32_16x16x32_bf16 v[44:47], a[188:191], v[24:27], v[44:47]\n"
      "s_waitcnt vmcnt(8)\n"
      "v_cmp_ne_u32 vcc, v16, v18\n"
      "ds_read_b64 v[140:141], v9 offset:38912\n"
      "v_mfma_f32_16x16x32_bf16 v[48:51], a[168:171], v[20:23], v[48:51]\n"
      "s_cbranch_vccnz .Lsl2\n"
      ".Lbk2:\n"
      "buffer_load_dword v18, v5, s[36:39], s72 offen sc1\n"
      "v_mfma_f32_16x16x32_bf16 v[48:51], a[180:183], v[24:27], v[48:51]\n"
      "s_waitcnt lgkmcnt(3)\n"
      "ds_read_b64 v[142:143], v9 offset:39424\n"
      "ds_read_b64 v[144:145], v9 offset:39936\n"
      "v_mfma_f32_16x16x32_bf16 v[36:39], a[196:199], v[28:31], v[36:39]\n"
      "s_waitcnt lgkmcnt(4)\n"
      "ds_read_b128 v[168:171], v3 offset:21504\n"
      "v_mul_f32 v150, 0x3eae1a93, v174\n"
      "v_mfma_f32_16x16x32_bf16 v[36:39], a[208:211], v[32:35], v[36:39]\n"
      "s_cmp_eq_u32 s66, s80\n"
      "buffer_load_dwordx4 v2, s[36:39], s69 offen sc1 lds\n"
      "v_mfma_f32_16x16x32_bf16 v[40:43], v[240:243], v[28:31], v[40:43]\n"
      "s_cbranch_scc1 .Ltg1\n"
      ".Ltb1:\n"
      "v_mfma_f32_16x16x32_bf16 v[40:43], v[244:247], v[32:35], v[40:43]\n"
      "v_mfma_f32_16x16x32_bf16 v[44:47], a[200:203], v[28:31], v[44:47]\n"
      "ds_read_b128 v[20:23], v3 offset:22528\n"
      "s_nop 0\n"
      "v_fmac_f32 v36, v252, v68\n"
      "v_mfma_f32_16x16x32_bf16 v[44:47], a[212:215], v[32:35], v[44:47]\n"
      "buffer_load_dwordx4 v2, s[36:39], s69 offen offset:1024 sc1 lds\n"
      "v_mfma_f32_16x16x32_bf16 v[48:51], a[192:195], v[28:31], v[48:51]\n"
      "v_mul_f32 v36, v174, v36\n"
      "v_fmac_f32 v37, v252, v69\n"
      "v_mfma_f32_16x16x32_bf16 v[48:51], a[204:207], v[32:35], v[48:51]\n"
      "s_waitcnt lgkmcnt(5)\n"
      "v_mul_f32 v37, v174, v37\n"
      "v_fmac_f32 v38, v252, v70\n"
      "v_mfma_f32_16x16x32_bf16 v[52:55], a[4:7], v[146:149], 0\n"
      "s_waitcnt lgkmcnt(1)\n"
      "ds_read_b128 v[24:27], v3 offset:23552\n"
      "v_mul_f32 v38, v174, v38\n"
      "v_mfma_f32_16x16x32_bf16 v[52:55], a[16:19], v[168:171], v[52:55]\n"
      "buffer_load_dwordx4 v2, s[36:39], s69 offen offset:2048 sc1 lds\n"
      "v_mfma_f32_16x16x32_bf16 v[56:59], v[176:179], v[146:149], 0\n"
      "v_fmac_f32 v39, v252, v71\n"
      "v_fmac_f32 v41, v252, v73\n"
      "v_mfma_f32_16x16x32_bf16 v[56:59], v[180:183], v[168:171], v[56:59]\n"
      "v_mul_f32 v39, v174, v39\n"
      "v_mul_f32 v41, v174, v41\n"
      "v_mfma_f32_16x16x32_bf16 v[60:63], a[8:11], v[146:149], 0\n"
      "ds_read_b128 v[28:31], v3 offset:24576\n"
      "v_cvt_pk_bf16_f32 v92, v36, v37\n"
      "v_mfma_f32_16x16x32_bf16 v[60:63], a[20:23], v[168:171], v[60:63]\n"
      "buffer_load_dwordx4 v2, s[36:39], s69 offen offset:3072 sc1 lds\n"
      "v_mfma_f32_16x16x32_bf16 v[64:67], a[0:3], v[146:149], 0\n"
      "s_add_u32 m0, s78, 20480\n"
      "v_cvt_pk_bf16_f32 v93, v38, v39\n"
      "v_cvt_pk_bf16_f32 v94, v41, 0\n"
      "v_mfma_f32_16x16x32_bf16 v[64:67], a[12:15], v[168:171], v[64:67]\n"
      "s_waitcnt lgkmcnt(2)\n"
      "v_fmac_f32 v44, v252, v76\n"
      "v_fmac_f32 v45, v252, v77\n"
      "v_mfma_f32_16x16x32_bf16 v[52:55], a[28:31], v[20:23], v[52:55]\n"
      "s_waitcnt lgkmcnt(1)\n"
      "ds_read_b128 v[32:35], v3 offset:25600\n"
      "v_mul_f32 v44, v174, v44\n"
      "v_mfma_f32_16x16x32_bf16 v[52:55], a[40:43], v[24:27], v[52:55]\n"
      "buffer_load_dwordx4 v4, s[36:39], s69 offen sc1 lds\n"
      "v_mfma_f32_16x16x32_bf16 v[56:59], v[184:187], v[20:23], v[56:59]\n"
      "s_mov_b32 s71, s72\n"
      "v_mul_f32 v45, v174, v45\n"
      "v_fmac_f32 v46, v252, v78\n"
      "v_mfma_f32_16x16x32_bf16 v[124:127], v[92:95], v[88:91], v[108:111]\n"
      "s_cmp_le_i32 s66, 61\n"
      "v_mul_f32 v46, v174, v46\n"
      "v_fmac_f32 v47, v252, v79\n"
      "v_mfma_f32_16x16x32_bf16 v[56:59], v[188:191], v[24:27], v[56:59]\n"
      "s_cselect_b32 s69, s70, 0x40000000\n"
      "v_mul_f32 v47, v174, v47\n"
      "v_fmac_f32 v42, v252, v74\n"
      "v_mfma_f32_16x16x32_bf16 v[60:63], a[32:35], v[20:23], v[60:63]\n"
      "ds_read_b128 v[146:149], v3 offset:26624\n"
      "v_mul_f32 v42, v174, v42\n"
      "v_mfma_f32_16x16x32_bf16 v[60:63], a[44:47], v[24:27], v[60:63]\n"
      "v_max3_f32 v154, v152, v124, v125\n"
      "v_max3_f32 v155, v153, v126, v127\n"
      "v_mfma_f32_16x16x32_bf16 v[64:67], a[24:27], v[20:23], v[64:67]\n"
      "v_max_f32 v156, v154, v155\n"
      "v_mov_b32 v157, v156\n"
      "v_mfma_f32_16x16x32_bf16 v[64:67], a[36:39], v[24:27], v[64:67]\n"
      "s_waitcnt lgkmcnt(2)\n"
      "v_permlane32_swap_b32 v156, v157\n"
      "v_cvt_pk_bf16_f32 v160, v44, v45\n"
      "v_mfma_f32_16x16x32_bf16 v[52:55], a[52:55], v[28:31], v[52:55]\n"
      "s_waitcnt lgkmcnt(1)\n"
      "ds_read_b128 v[168:171], v3 offset:27648\n"
      "v_max_f32 v156, v156, v157\n"
      "v_mfma_f32_16x16x32_bf16 v[52:55], a[64:67], v[32:35], v[52:55]\n"
      "v_mov_b32 v157, v156\n"
      "v_cvt_pk_bf16_f32 v161, v46, v47\n"
      "v_mfma_f32_16x16x32_bf16 v[56:59], v[192:195], v[28:31], v[56:59]\n"
      "v_permlane16_swap_b32 v156, v157\n"
      "v_cvt_pk_bf16_f32 v162, v42, 0\n"
      "v_mfma_f32_16x16x32_bf16 v[56:59], v[196:199], v[32:35], v[56:59]\n"
      "v_max_f32 v156, v156, v157\n"
      "v_mul_f32 v156, v156, v151\n"
      "v_mfma_f32_16x16x32_bf16 v[60:63], a[56:59], v[28:31], v[60:63]\n"
      "ds_read_b128 v[20:23], v3 offset:28672\n"
      "v_fma_f32 v154, v154, v151, -v156\n"
      "v_mfma_f32_16x16x32_bf16 v[60:63], a[68:71], v[32:35], v[60:63]\n"
      "v_fma_f32 v155, v155, v151, -v156\n"
      "v_fmac_f32 v48, v252, v80\n"
      "v_mfma_f32_16x16x32_bf16 v[64:67], a[48:51], v[28:31], v[64:67]\n"
      "v_exp_f32 v154, v154\n"
      "v_mfma_f32_16x16x32_bf16 v[64:67], a[60:63], v[32:35], v[64:67]\n"
      "s_waitcnt lgkmcnt(2)\n"
      "v_exp_f32 v155, v155\n"
      "v_mfma_f32_16x16x32_bf16 v[52:55], a[76:79], v[146:149], v[52:55]\n"
      "s_waitcnt lgkmcnt(1)\n"
      "ds_read_b128 v[24:27], v3 offset:29696\n"
      "v_add_f32 v156, v154, v155\n"
      "v_mfma_f32_16x16x32_bf16 v[52:55], a[88:91], v[168:171], v[52:55]\n"
      "v_mov_b32 v157, v156\n"
      "v_fmac_f32 v49, v252, v81\n"
      "v_mfma_f32_16x16x32_bf16 v[56:59], v[200:203], v[146:149], v[56:59]\n"
      "s_waitcnt vmcnt(6)\n"
      "v_permlane32_swap_b32 v156, v157\n"
      "s_barrier\n"
      "v_mfma_f32_16x16x32_bf16 v[56:59], v[204:207], v[168:171], v[56:59]\n"
      "buffer_store_dword v8, v7, s[36:39], 0 offen sc1\n"
      "v_mfma_f32_16x16x32_bf16 v[60:63], a[80:83], v[146:149], v[60:63]\n"
      "s_cmp_ge_i32 s66, 66\n"
      "ds_read_b128 v[28:31], v3 offset:30720\n"
      "v_add_f32 v156, v156, v157\n"
      "v_mfma_f32_16x16x32_bf16 v[60:63], a[92:95], v[168:171], v[60:63]\n"
      "s_cbranch_scc1 .Lqkv_x\n"
      "v_mov_b32 v157, v156\n"
      "v_fmac_f32 v50, v252, v82\n"
      "v_mfma_f32_16x16x32_bf16 v[64:67], a[72:75], v[146:149], v[64:67]\n"
      "s_sub_u32 s48, s66, 1\n"
      "v_permlane16_swap_b32 v156, v157\n"
      "v_fmac_f32 v51, v252, v83\n"
      "v_mfma_f32_16x16x32_bf16 v[64:67], a[84:87], v[168:171], v[64:67]\n"
      "s_cmp_eq_u32 s66, 33\n"
      "v_add_f32 v156, v156, v157\n"
      "v_fmac_f32 v40, v252, v72\n"
      "v_mfma_f32_16x16x32_bf16 v[132:135], v[160:163], v[248:251], 0\n"
      "s_cselect_b32 s48, 99, s48\n"
      "v_rcp_f32 v156, v156\n"
      "v_mfma_f32_16x16x32_bf16 v[136:139], v[160:163], v[12:15], 0\n"
      "s_waitcnt lgkmcnt(2)\n"
      "v_mul_f32 v154, v154, v156\n"
      "v_mul_f32 v155, v155, v156\n"
      "v_mfma_f32_16x16x32_bf16 v[52:55], a[100:103], v[20:23], v[52:55]\n"
      "s_waitcnt lgkmcnt(1)\n"
      "ds_read_b128 v[32:35], v3 offset:31744\n"
      "v_cvt_pk_bf16_f32 v154, v154, v154\n"
      "v_mfma_f32_16x16x32_bf16 v[52:55], a[112:115], v[24:27], v[52:55]\n"
      "s_cmp_lt_u32 s48, 65\n"
      "v_cvt_pk_bf16_f32 v155, v155, v155\n"
      "v_and_b32 v128, v154, v118\n"
      "v_mfma_f32_16x16x32_bf16 v[56:59], v[208:211], v[20:23], v[56:59]\n"
      "s_cselect_b32 s75, s74, 0x40000000\n"
      "v_and_b32 v129, v155, v119\n"
      "v_and_b32 v130, v154, v116\n"
      "v_mfma_f32_16x16x32_bf16 v[56:59], v[212:215], v[24:27], v[56:59]\n"
      "s_cselect_b32 s48, 20480, 0\n"
      "v_and_b32 v131, v155, v117\n"
      "v_cvt_pk_bf16_f32 v96, v132, v133\n"
      "v_mfma_f32_16x16x32_bf16 v[60:63], a[104:107], v[20:23], v[60:63]\n"
      "s_addc_u32 s76, s76, 0\n"
      "ds_read_b128 v[146:149], v3 offset:32768\n"
      "v_cvt_pk_bf16_f32 v97, v134, v135\n"
      "v_mfma_f32_16x16x32_bf16 v[60:63], a[116:119], v[24:27], v[60:63]\n"
      "s_add_u32 s74, s74, s48\n"
      "v_cvt_pk_bf16_f32 v100, v136, v137\n"
      "v_cvt_pk_bf16_f32 v101, v138, v139\n"
      "v_mfma_f32_16x16x32_bf16 v[64:67], a[96:99], v[20:23], v[64:67]\n"
      "s_cmp_eq_u32 s73, 128\n"
      "v_cvt_pk_bf16_f32 v84, v48, v49\n"
      "v_cvt_pk_bf16_f32 v85, v50, v51\n"
      "v_mfma_f32_16x16x32_bf16 v[64:67], a[108:111], v[24:27], v[64:67]\n"
      "s_subb_u32 s48, s73, 0\n"
      "v_cvt_pk_bf16_f32 v86, v40, 0\n"
      "v_mfma_f32_16x16x32_bf16 v[132:135], v[96:99], v[128:131], 0\n"
      "s_min_u32 s48, s48, s67\n"
      "v_add_f32 v140, v140, v142\n"
      "v_mfma_f32_16x16x32_bf16 v[136:139], v[100:103], v[128:131], 0\n"
      "s_waitcnt lgkmcnt(2)\n"
      "v_add_f32 v141, v141, v143\n"
      "v_add_f32 v140, v140, v144\n"
      "v_mfma_f32_16x16x32_bf16 v[52:55], a[124:127], v[28:31], v[52:55]\n"
      "s_waitcnt lgkmcnt(1)\n"
      "ds_read_b128 v[168:171], v3 offset:33792\n"
      "v_add_f32 v141, v141, v145\n"
      "v_mfma_f32_16x16x32_bf16 v[52:55], a[136:139], v[32:35], v[52:55]\n"
      "s_mul_i32 s72, s48, 1536\n"
      "v_cvt_pk_bf16_f32 v164, v132, v133\n"
      "v_cvt_pk_bf16_f32 v165, v134, v135\n"
      "v_mfma_f32_16x16x32_bf16 v[56:59], v[216:219], v[28:31], v[56:59]\n"
      "s_add_u32 s72, s72, 82042880\n"
      "buffer_store_dwordx2 v[164:165], v11, s[36:39], s75 offen sc1\n"
      "v_mfma_f32_16x16x32_bf16 v[56:59], v[220:223], v[32:35], v[56:59]\n"
      "s_mul_i32 s70, s48, 20480\n"
      "v_cvt_pk_bf16_f32 v166, v136, v137\n"
      "v_permlane32_swap_b32 v140, v141\n"
      "v_mfma_f32_16x16x32_bf16 v[60:63], a[128:131], v[28:31], v[60:63]\n"
      "s_add_u32 s70, s70, s83\n"
      "ds_read_b128 v[20:23], v3 offset:34816\n"
      "v_add_f32 v140, v140, v141\n"
      "v_mfma_f32_16x16x32_bf16 v[60:63], a[140:143], v[32:35], v[60:63]\n"
      "s_cmp_eq_u32 s66, 27\n"
      "v_mov_b32 v142, v140\n"
      "v_mfma_f32_16x16x32_bf16 v[64:67], a[120:123], v[28:31], v[64:67]\n"
      "s_cselect_b32 s48, 97, 1\n"
      "v_permlane16_swap_b32 v140, v142\n"
      "v_mfma_f32_16x16x32_bf16 v[64:67], a[132:135], v[32:35], v[64:67]\n"
      "s_waitcnt lgkmcnt(2)\n"
      "buffer_store_dword v166, v6, s[36:39], s75 offen sc1\n"
      "v_mfma_f32_16x16x32_bf16 v[52:55], a[148:151], v[146:149], v[52:55]\n"
      "s_waitcnt lgkmcnt(1)\n"
      "ds_read_b128 v[24:27], v3 offset:35840\n"
      "v_add_f32 v140, v140, v142\n"
      "v_mfma_f32_16x16x32_bf16 v[52:55], a[160:163], v[168:171], v[52:55]\n"
      "s_add_u32 s73, s73, s48\n"
      "v_mov_b32 v253, v140\n"
      "v_mfma_f32_16x16x32_bf16 v[120:123], v[92:95], v[84:87], v[104:107]\n"
      "s_add_u32 s66, s66, 1\n"
      "v_permlane32_swap_b32 v253, v140\n"
      "v_mfma_f32_16x16x32_bf16 v[56:59], v[224:227], v[146:149], v[56:59]\n"
      "s_xor_b32 s48, s79, 40960\n"
      "v_mul_f32 v253, 0xbae38e39, v253\n"
      "v_mfma_f32_16x16x32_bf16 v[56:59], v[228:231], v[168:171], v[56:59]\n"
      "s_sub_u32 s49, s79, s48\n"
      "v_fmaak_f32 v140, v17, v140, 0x3727c5ac\n"
      "v_mfma_f32_16x16x32_bf16 v[60:63], a[152:155], v[146:149], v[60:63]\n"
      "s_mov_b32 s79, s48\n"
      "ds_read_b128 v[28:31], v3 offset:36864\n"
      "v_max_f32 v152, v120, v121\n"
      "v_mfma_f32_16x16x32_bf16 v[60:63], a[164:167], v[168:171], v[60:63]\n"
      "s_add_u32 s77, s77, s49\n"
      "v_max_f32 v153, v122, v123\n"
      "v_fma_f32 v140, -v253, v253, v140\n"
      "v_mfma_f32_16x16x32_bf16 v[64:67], a[144:147], v[146:149], v[64:67]\n"
      "s_add_u32 s78, s78, s49\n"
      "v_max_f32 v140, 0x3727c5ac, v140\n"
      "v_mfma_f32_16x16x32_bf16 v[64:67], a[156:159], v[168:171], v[64:67]\n"
      "s_waitcnt lgkmcnt(2)\n"
      "v_add_u32 v9, s79, v10\n"
      "v_mfma_f32_16x16x32_bf16 v[52:55], a[172:175], v[20:23], v[52:55]\n"
      "s_waitcnt lgkmcnt(1)\n"
      "ds_read_b128 v[32:35], v3 offset:37888\n"
      "v_mfma_f32_16x16x32_bf16 v[52:55], a[184:187], v[24:27], v[52:55]\n"
      "v_add_u32 v3, s79, v2\n"
      "v_mfma_f32_16x16x32_bf16 v[56:59], v[232:235], v[20:23], v[56:59]\n"
      "v_rsq_f32 v175, v140\n"
      "v_mfma_f32_16x16x32_bf16 v[56:59], v[236:239], v[24:27], v[56:59]\n"
      ".Lmq10:\n"
      "s_branch .Lqkv_loop0\n"
      ".Lqkv_x:\n"
      "s_cmp_lt_u32 s34, 8\n"
      "s_cbranch_scc0 .Lexit\n"
      "s_getpc_b64 vcc\n"
      "s_add_u32 vcc_lo, vcc_lo, .Lbeg-.\n"
      "s_addc_u32 vcc_hi, vcc_hi, -1\n"
      "s_lshl_b32 s50, s28, 13\n"
      "v_lshlrev_b32 v60, 7, v1\n"
      "v_add_u32 v60, s50, v60\n"
      "v_add_u32 v61, 0, v60\n"
      "v_min_u32 v61, .Lfin-.Lbeg-4, v61\n"
      "global_load_dword v62, v61, vcc\n"
      "v_add_u32 v61, 32768, v60\n"
      "v_min_u32 v61, .Lfin-.Lbeg-4, v61\n"
      "global_load_dword v63, v61, vcc\n"
      "v_add_u32 v61, 65536, v60\n"
      "v_min_u32 v61, .Lfin-.Lbeg-4, v61\n"
      "global_load_dword v64, v61, vcc\n"
      "s_or_b32 s48, s34, s28\n"
      "s_cmp_lg_u32 s48, 0\n"
      "s_cbranch_scc1 .Lexit\n"
      "s_waitcnt vmcnt(0)\n"
      "v_lshlrev_b32 v20, 7, v1\n"
      "s_mov_b32 s56, 0x4000\n"
      ".Lqkv_y:\n"
      "s_mov_b32 s48, 81911808\n"
      "buffer_load_dwordx2 v[24:25], v20, s[36:39], s48 offen offset:8 sc1\n"
      "s_mov_b32 s48, 81920000\n"
      "buffer_load_dwordx2 v[26:27], v20, s[36:39], s48 offen offset:8 sc1\n"
      "s_mov_b32 s48, 81928192\n"
      "buffer_load_dwordx2 v[28:29], v20, s[36:39], s48 offen offset:8 sc1\n"
      "s_mov_b32 s48, 81936384\n"
      "buffer_load_dwordx2 v[30:31], v20, s[36:39], s48 offen offset:8 sc1\n"
      "s_mov_b32 s48, 81944576\n"
      "buffer_load_dwordx2 v[32:33], v20, s[36:39], s48 offen offset:8 sc1\n"
      "s_mov_b32 s48, 81952768\n"
      "buffer_load_dwordx2 v[34:35], v20, s[36:39], s48 offen offset:8 sc1\n"
      "s_mov_b32 s48, 81960960\n"
      "buffer_load_dwordx2 v[36:37], v20, s[36:39], s48 offen offset:8 sc1\n"
      "s_mov_b32 s48, 81969152\n"
      "buffer_load_dwordx2 v[38:39], v20, s[36:39], s48 offen offset:8 sc1\n"
      "s_mov_b32 s48, 81977344\n"
      "buffer_load_dwordx2 v[40:41], v20, s[36:39], s48 offen offset:8 sc1\n"
      "s_mov_b32 s48, 81985536\n"
      "buffer_load_dwordx2 v[42:43], v20, s[36:39], s48 offen offset:8 sc1\n"
      "s_mov_b32 s48, 81993728\n"
      "buffer_load_dwordx2 v[44:45], v20, s[36:39], s48 offen offset:8 sc1\n"
      "s_mov_b32 s48, 82001920\n"
      "buffer_load_dwordx2 v[46:47], v20, s[36:39], s48 offen offset:8 sc1\n"
      "s_mov_b32 s48, 82010112\n"
      "buffer_load_dwordx2 v[48:49], v20, s[36:39], s48 offen offset:8 sc1\n"
      "s_mov_b32 s48, 82018304\n"
      "buffer_load_dwordx2 v[50:51], v20, s[36:39], s48 offen offset:8 sc1\n"
      "s_mov_b32 s48, 82026496\n"
      "buffer_load_dwordx2 v[52:53], v20, s[36:39], s48 offen offset:8 sc1\n"
      "s_mov_b32 s48, 82034688\n"
      "buffer_load_dwordx2 v[54:55], v20, s[36:39], s48 offen offset:8 sc1\n"
      "s_mov_b64 s[50:51], 0\n"
      "s_waitcnt vmcnt(0)\n"
      "v_cmp_ne_u64 vcc, s[98:99], v[24:25]\n"
      "s_nop 0\n"
      "s_or_b64 s[50:51], s[50:51], vcc\n"
      "v_cmp_ne_u64 vcc, s[98:99], v[26:27]\n"
      "s_nop 0\n"
      "s_or_b64 s[50:51], s[50:51], vcc\n"
      "v_cmp_ne_u64 vcc, s[98:99], v[28:29]\n"
      "s_nop 0\n"
      "s_or_b64 s[50:51], s[50:51], vcc\n"
      "v_cmp_ne_u64 vcc, s[98:99], v[30:31]\n"
      "s_nop 0\n"
      "s_or_b64 s[50:51], s[50:51], vcc\n"
      "v_cmp_ne_u64 vcc, s[98:99], v[32:33]\n"
      "s_nop 0\n"
      "s_or_b64 s[50:51], s[50:51], vcc\n"
      "v_cmp_ne_u64 vcc, s[98:99], v[34:35]\n"
      "s_nop 0\n"
      "s_or_b64 s[50:51], s[50:51], vcc\n"
      "v_cmp_ne_u64 vcc, s[98:99], v[36:37]\n"
      "s_nop 0\n"
      "s_or_b64 s[50:51], s[50:51], vcc\n"
      "v_cmp_ne_u64 vcc, s[98:99], v[38:39]\n"
      "s_nop 0\n"
      "s_or_b64 s[50:51], s[50:51], vcc\n"
      "v_cmp_ne_u64 vcc, s[98:99], v[40:41]\n"
      "s_nop 0\n"
      "s_or_b64 s[50:51], s[50:51], vcc\n"
      "v_cmp_ne_u64 vcc, s[98:99], v[42:43]\n"
      "s_nop 0\n"
      "s_or_b64 s[50:51], s[50:51], vcc\n"
      "v_cmp_ne_u64 vcc, s[98:99], v[44:45]\n"
      "s_nop 0\n"
      "s_or_b64 s[50:51], s[50:51], vcc\n"
      "v_cmp_ne_u64 vcc, s[98:99], v[46:47]\n"
      "s_nop 0\n"
      "s_or_b64 s[50:51], s[50:51], vcc\n"
      "v_cmp_ne_u64 vcc, s[98:99], v[48:49]\n"
      "s_nop 0\n"
      "s_or_b64 s[50:51], s[50:51], vcc\n"
      "v_cmp_ne_u64 vcc, s[98:99], v[50:51]\n"
      "s_nop 0\n"
      "s_or_b64 s[50:51], s[50:51], vcc\n"
      "v_cmp_ne_u64 vcc, s[98:99], v[52:53]\n"
      "s_nop 0\n"
      "s_or_b64 s[50:51], s[50:51], vcc\n"
      "v_cmp_ne_u64 vcc, s[98:99], v[54:55]\n"
      "s_nop 0\n"
      "s_or_b64 s[50:51], s[50:51], vcc\n"
      "s_cbranch_scc0 .Lqkv_z\n"
      "s_sub_u32 s56, s56, 1\n"
      "s_cbranch_scc0 .Lqkv_y\n"
      ".Lqkv_z:\n"
      "v_mov_b32 v22, s98\n"
      "v_mov_b32 v23, s99\n"
      "s_mov_b32 s48, 82439168\n"
      "buffer_store_dwordx2 v[22:23], off, s[36:39], s48 sc1\n"
      "s_branch .Lexit\n"
      ".Ldn:\n"
      "s_sub_u32 s49, s49, 10\n"
      "s_mul_i32 s50, s49, 43\n"
      "s_lshr_b32 s50, s50, 7\n"
      "s_mul_i32 s51, s50, 3\n"
      "s_sub_u32 s31, s49, s51\n"
      "s_mul_i32 s51, s48, 3\n"
      "s_add_u32 s50, s50, s51\n"
      "s_lshr_b32 s30, s50, 2\n"
      "s_and_b32 s29, s50, 3\n"
      "v_lshrrev_b32 v160, 4, v1\n"
      "v_and_b32 v161, 15, v1\n"
      "s_mul_i32 s80, s31, 192\n"
      "s_mul_i32 s50, s28, 48\n"
      "s_add_u32 s80, s80, s50\n"
      "s_and_b32 s52, s28, 1\n"
      "s_lshl_b32 s50, s52, 4\n"
      "s_add_u32 s50, s50, s80\n"
      "s_lshl_b32 s51, s52, 5\n"
      "s_sub_u32 s51, s80, s51\n"
      "s_add_u32 s51, s51, 32\n"
      "v_mul_u32_u24 v162, 1152, v161\n"
      "v_lshl_add_u32 v13, v160, 4, v162\n"
      "v_lshl_add_u32 v14, v160, 3, v162\n"
      "s_lshl_b32 s53, s50, 1\n"
      "v_add_u32 v13, s53, v13\n"
      "s_lshl_b32 s53, s51, 1\n"
      "v_add_u32 v14, s53, v14\n"
      "s_lshl_b32 s53, s50, 5\n"
      "v_add_u32 v12, s53, v2\n"
      "v_lshrrev_b32 v162, 1, v160\n"
      "v_lshl_add_u32 v162, v162, 4, v161\n"
      "v_lshlrev_b32 v162, 4, v162\n"
      "v_and_b32 v163, 1, v160\n"
      "v_lshl_add_u32 v162, v163, 3, v162\n"
      "s_lshl_b32 s53, s51, 5\n"
      "v_add_u32 v11, s53, v162\n"
      "s_lshl_b32 s53, s31, 2\n"
      "s_add_u32 s53, s53, s28\n"
      "s_lshl_b32 s53, s53, 7\n"
      "s_add_u32 s53, s53, 18432\n"
      "v_lshrrev_b32 v162, 5, v1\n"
      "v_lshlrev_b32 v162, 2, v162\n"
      "v_lshl_add_u32 v162, v161, 3, v162\n"
      "v_add_u32 v15, s53, v162\n"
      "s_lshl_b32 s81, s29, 5\n"
      "s_sub_u32 s81, 96, s81\n"
      "s_mul_i32 s50, s29, 18\n"
      "s_mul_i32 s51, s31, 6\n"
      "s_add_u32 s50, s50, s51\n"
      "s_add_u32 s50, s50, s30\n"
      "s_add_u32 s50, s50, 184\n"
      "s_lshl_b32 s50, s50, 2\n"
      "s_add_u32 s50, s50, s28\n"
      "s_lshl_b32 s50, s50, 7\n"
      "s_add_u32 s50, s50, 81911808\n"
      "v_lshlrev_b32 v7, 2, v1\n"
      "v_lshrrev_b32 v162, 5, v1\n"
      "v_lshl_or_b32 v7, v162, 30, v7\n"
      "v_add_u32 v7, s50, v7\n"
      "s_waitcnt lgkmcnt(0)\n"
      "s_mov_b32 s36, s20\n"
      "s_and_b32 s37, s21, 0xffff\n"
      "s_mov_b32 s38, 0x4eaf200\n"
      "s_mov_b32 s39, 0x20000\n"
      "s_mov_b32 s98, 82439168\n"
      "buffer_load_dwordx2 v[252:253], off, s[36:39], s98 sc1\n"
      "s_mov_b32 s40, s8\n"
      "s_and_b32 s41, s9, 0xffff\n"
      "s_mov_b32 s42, 0x240000\n"
      "s_mov_b32 s43, 0x20000\n"
      "s_add_u32 s50, s81, s30\n"
      "s_sub_u32 s51, 127, s50\n"
      "s_mul_i32 s51, s51, 18432\n"
      "s_mul_i32 s50, s50, 20480\n"
      "s_add_u32 s50, s50, 0\n"
      "s_mov_b32 s54, 0x3f803f80\n"
      "buffer_load_dwordx4 v[20:23], v13, s[40:43], s51 offen\n"
      "buffer_load_dwordx2 v[24:25], v14, s[40:43], s51 offen\n"
      "s_sub_u32 s51, s51, 110592\n"
      "buffer_load_dwordx4 v[26:29], v13, s[40:43], s51 offen\n"
      "buffer_load_dwordx2 v[30:31], v14, s[40:43], s51 offen\n"
      "s_sub_u32 s51, s51, 110592\n"
      "buffer_load_dwordx4 v[32:35], v13, s[40:43], s51 offen\n"
      "buffer_load_dwordx2 v[36:37], v14, s[40:43], s51 offen\n"
      "s_sub_u32 s51, s51, 110592\n"
      "buffer_load_dwordx4 v[38:41], v13, s[40:43], s51 offen\n"
      "buffer_load_dwordx2 v[42:43], v14, s[40:43], s51 offen\n"
      "s_sub_u32 s51, s51, 110592\n"
      "buffer_load_dwordx4 v[44:47], v13, s[40:43], s51 offen\n"
      "buffer_load_dwordx2 v[48:49], v14, s[40:43], s51 offen\n"
      "s_sub_u32 s51, s51, 110592\n"
      "s_cmp_lt_u32 s30, 2\n"
      "s_cselect_b32 s51, s51, 0x40000000\n"
      "buffer_load_dwordx4 v[50:53], v13, s[40:43], s51 offen\n"
      "buffer_load_dwordx2 v[54:55], v14, s[40:43], s51 offen\n"
      "s_sub_u32 s51, s51, 110592\n"
      "v_lshrrev_b32 v162, 3, v1\n"
      "v_mul_u32_u24 v162, 9216, v162\n"
      "v_and_b32 v163, 7, v1\n"
      "v_lshl_add_u32 v162, v163, 4, v162\n"
      "s_cmp_lt_u32 s30, 2\n"
      "s_cselect_b32 s53, 48, 40\n"
      "v_mov_b32 v164, 0x40000000\n"
      "v_cmp_gt_u32 vcc, s53, v1\n"
      "s_nop 1\n"
      "v_cndmask_b32 v162, v164, v162, vcc\n"
      "s_add_u32 s55, s81, s30\n"
      "s_mul_i32 s55, s55, 1536\n"
      "s_lshl_b32 s65, s31, 2\n"
      "s_add_u32 s65, s65, s28\n"
      "s_lshl_b32 s65, s65, 7\n"
      "s_add_u32 s65, s65, 82042880\n"
      "s_add_u32 s55, s55, s65\n"
      "v_cmp_gt_u32 vcc, 8, v1\n"
      "s_nop 1\n"
      "v_cndmask_b32 v163, v164, v162, vcc\n"
      "v_cndmask_b32 v162, v162, v164, vcc\n"
      "s_waitcnt vmcnt(12)\n"
      "v_readfirstlane_b32 s98, v252\n"
      "v_readfirstlane_b32 s99, v253\n"
      "v_mov_b32 v8, 0\n"
      "s_add_u32 s98, s98, 1\n"
      "s_addc_u32 s99, s99, 0\n"
      "v_writelane_b32 v8, s98, 2\n"
      "v_writelane_b32 v8, s99, 3\n"
      "buffer_store_dword v8, v7, s[36:39], 0 offen sc1\n"
      "v_mov_b32 v100, s98\n"
      "v_mov_b32 v101, s99\n"
      "v_mov_b32 v102, s98\n"
      "v_mov_b32 v103, s99\n"
      "s_waitcnt vmcnt(11)\n"
      "buffer_store_dwordx4 v[20:23], v12, s[36:39], s50 offen sc1\n"
      "buffer_store_dwordx2 v[24:25], v11, s[36:39], s50 offen sc1\n"
      "v_dot2_f32_bf16 v60, v20, s54, 0\n"
      "v_dot2_f32_bf16 v61, v20, v20, 0\n"
      "v_dot2_f32_bf16 v60, v21, s54, v60\n"
      "v_dot2_f32_bf16 v61, v21, v21, v61\n"
      "v_dot2_f32_bf16 v60, v22, s54, v60\n"
      "v_dot2_f32_bf16 v61, v22, v22, v61\n"
      "v_dot2_f32_bf16 v60, v23, s54, v60\n"
      "v_dot2_f32_bf16 v61, v23, v23, v61\n"
      "v_dot2_f32_bf16 v60, v24, s54, v60\n"
      "v_dot2_f32_bf16 v61, v24, v24, v61\n"
      "v_dot2_f32_bf16 v60, v25, s54, v60\n"
      "v_dot2_f32_bf16 v61, v25, v25, v61\n"
      "s_nop 2\n"
      "v_permlane32_swap_b32 v60, v61\n"
      "v_add_f32 v60, v60, v61\n"
      "v_mov_b32 v62, v60\n"
      "s_nop 1\n"
      "v_permlane16_swap_b32 v60, v62\n"
      "v_add_f32 v60, v60, v62\n"
      "buffer_store_dword v60, v15, s[36:39], s50 offen sc1\n"
      "s_add_u32 s50, s50, 122880\n"
      "s_waitcnt vmcnt(12)\n"
      "buffer_store_dwordx4 v[26:29], v12, s[36:39], s50 offen sc1\n"
      "buffer_store_dwordx2 v[30:31], v11, s[36:39], s50 offen sc1\n"
      "v_dot2_f32_bf16 v64, v26, s54, 0\n"
      "v_dot2_f32_bf16 v65, v26, v26, 0\n"
      "v_dot2_f32_bf16 v64, v27, s54, v64\n"
      "v_dot2_f32_bf16 v65, v27, v27, v65\n"
      "v_dot2_f32_bf16 v64, v28, s54, v64\n"
      "v_dot2_f32_bf16 v65, v28, v28, v65\n"
      "v_dot2_f32_bf16 v64, v29, s54, v64\n"
      "v_dot2_f32_bf16 v65, v29, v29, v65\n"
      "v_dot2_f32_bf16 v64, v30, s54, v64\n"
      "v_dot2_f32_bf16 v65, v30, v30, v65\n"
      "v_dot2_f32_bf16 v64, v31, s54, v64\n"
      "v_dot2_f32_bf16 v65, v31, v31, v65\n"
      "s_nop 2\n"
      "v_permlane32_swap_b32 v64, v65\n"
      "v_add_f32 v64, v64, v65\n"
      "v_mov_b32 v66, v64\n"
      "s_nop 1\n"
      "v_permlane16_swap_b32 v64, v66\n"
      "v_add_f32 v64, v64, v66\n"
      "buffer_store_dword v64, v15, s[36:39], s50 offen sc1\n"
      "s_add_u32 s50, s50, 122880\n"
      "s_waitcnt vmcnt(13)\n"
      "buffer_store_dwordx4 v[32:35], v12, s[36:39], s50 offen sc1\n"
      "buffer_store_dwordx2 v[36:37], v11, s[36:39], s50 offen sc1\n"
      "v_dot2_f32_bf16 v68, v32, s54, 0\n"
      "v_dot2_f32_bf16 v69, v32, v32, 0\n"
      "v_dot2_f32_bf16 v68, v33, s54, v68\n"
      "v_dot2_f32_bf16 v69, v33, v33, v69\n"
      "v_dot2_f32_bf16 v68, v34, s54, v68\n"
      "v_dot2_f32_bf16 v69, v34, v34, v69\n"
      "v_dot2_f32_bf16 v68, v35, s54, v68\n"
      "v_dot2_f32_bf16 v69, v35, v35, v69\n"
      "v_dot2_f32_bf16 v68, v36, s54, v68\n"
      "v_dot2_f32_bf16 v69, v36, v36, v69\n"
      "v_dot2_f32_bf16 v68, v37, s54, v68\n"
      "v_dot2_f32_bf16 v69, v37, v37, v69\n"
      "s_nop 2\n"
      "v_permlane32_swap_b32 v68, v69\n"
      "v_add_f32 v68, v68, v69\n"
      "v_mov_b32 v70, v68\n"
      "s_nop 1\n"
      "v_permlane16_swap_b32 v68, v70\n"
      "v_add_f32 v68, v68, v70\n"
      "buffer_store_dword v68, v15, s[36:39], s50 offen sc1\n"
      "s_add_u32 s50, s50, 122880\n"
      "s_waitcnt vmcnt(6)\n"
      "buffer_store_dwordx4 v[100:103], v163, s[36:39], s55 offen sc1\n"
      "buffer_store_dwordx4 v[38:41], v12, s[36:39], s50 offen sc1\n"
      "buffer_store_dwordx2 v[42:43], v11, s[36:39], s50 offen sc1\n"
      "v_dot2_f32_bf16 v72, v38, s54, 0\n"
      "v_dot2_f32_bf16 v73, v38, v38, 0\n"
      "v_dot2_f32_bf16 v72, v39, s54, v72\n"
      "v_dot2_f32_bf16 v73, v39, v39, v73\n"
      "v_dot2_f32_bf16 v72, v40, s54, v72\n"
      "v_dot2_f32_bf16 v73, v40, v40, v73\n"
      "v_dot2_f32_bf16 v72, v41, s54, v72\n"
      "v_dot2_f32_bf16 v73, v41, v41, v73\n"
      "v_dot2_f32_bf16 v72, v42, s54, v72\n"
      "v_dot2_f32_bf16 v73, v42, v42, v73\n"
      "v_dot2_f32_bf16 v72, v43, s54, v72\n"
      "v_dot2_f32_bf16 v73, v43, v43, v73\n"
      "s_nop 2\n"
      "v_permlane32_swap_b32 v72, v73\n"
      "v_add_f32 v72, v72, v73\n"
      "v_mov_b32 v74, v72\n"
      "s_nop 1\n"
      "v_permlane16_swap_b32 v72, v74\n"
      "v_add_f32 v72, v72, v74\n"
      "buffer_store_dword v72, v15, s[36:39], s50 offen sc1\n"
      "s_add_u32 s50, s50, 122880\n"
      "buffer_store_dwordx4 v[44:47], v12, s[36:39], s50 offen sc1\n"
      "buffer_store_dwordx2 v[48:49], v11, s[36:39], s50 offen sc1\n"
      "v_dot2_f32_bf16 v76, v44, s54, 0\n"
      "v_dot2_f32_bf16 v77, v44, v44, 0\n"
      "v_dot2_f32_bf16 v76, v45, s54, v76\n"
      "v_dot2_f32_bf16 v77, v45, v45, v77\n"
      "v_dot2_f32_bf16 v76, v46, s54, v76\n"
      "v_dot2_f32_bf16 v77, v46, v46, v77\n"
      "v_dot2_f32_bf16 v76, v47, s54, v76\n"
      "v_dot2_f32_bf16 v77, v47, v47, v77\n"
      "v_dot2_f32_bf16 v76, v48, s54, v76\n"
      "v_dot2_f32_bf16 v77, v48, v48, v77\n"
      "v_dot2_f32_bf16 v76, v49, s54, v76\n"
      "v_dot2_f32_bf16 v77, v49, v49, v77\n"
      "s_nop 2\n"
      "v_permlane32_swap_b32 v76, v77\n"
      "v_add_f32 v76, v76, v77\n"
      "v_mov_b32 v78, v76\n"
      "s_nop 1\n"
      "v_permlane16_swap_b32 v76, v78\n"
      "v_add_f32 v76, v76, v78\n"
      "buffer_store_dword v76, v15, s[36:39], s50 offen sc1\n"
      "s_add_u32 s50, s50, 122880\n"
      "s_cmp_lt_u32 s30, 2\n"
      "s_cselect_b32 s50, s50, 0x40000000\n"
      "buffer_store_dwordx4 v[50:53], v12, s[36:39], s50 offen sc1\n"
      "buffer_store_dwordx2 v[54:55], v11, s[36:39], s50 offen sc1\n"
      "v_dot2_f32_bf16 v80, v50, s54, 0\n"
      "v_dot2_f32_bf16 v81, v50, v50, 0\n"
      "v_dot2_f32_bf16 v80, v51, s54, v80\n"
      "v_dot2_f32_bf16 v81, v51, v51, v81\n"
      "v_dot2_f32_bf16 v80, v52, s54, v80\n"
      "v_dot2_f32_bf16 v81, v52, v52, v81\n"
      "v_dot2_f32_bf16 v80, v53, s54, v80\n"
      "v_dot2_f32_bf16 v81, v53, v53, v81\n"
      "v_dot2_f32_bf16 v80, v54, s54, v80\n"
      "v_dot2_f32_bf16 v81, v54, v54, v81\n"
      "v_dot2_f32_bf16 v80, v55, s54, v80\n"
      "v_dot2_f32_bf16 v81, v55, v55, v81\n"
      "s_nop 2\n"
      "v_permlane32_swap_b32 v80, v81\n"
      "v_add_f32 v80, v80, v81\n"
      "v_mov_b32 v82, v80\n"
      "s_nop 1\n"
      "v_permlane16_swap_b32 v80, v82\n"
      "v_add_f32 v80, v80, v82\n"
      "buffer_store_dword v80, v15, s[36:39], s50 offen sc1\n"
      "s_add_u32 s50, s50, 122880\n"
      "s_waitcnt vmcnt(0) lgkmcnt(0)\n"
      "buffer_store_dwordx4 v[100:103], v162, s[36:39], s55 offen sc1\n"
      "s_mov_b32 s40, s14\n"
      "s_and_b32 s41, s15, 0xffff\n"
      "s_mov_b32 s42, 0x510000\n"
      "s_mov_b32 s43, 0x20000\n"
      "s_mov_b32 s44, s16\n"
      "s_and_b32 s45, s17, 0xffff\n"
      "s_mov_b32 s46, 0x510000\n"
      "s_mov_b32 s47, 0x20000\n"
      "s_mov_b32 s44, s18\n"
      "s_and_b32 s45, s19, 0xffff\n"
      "s_mov_b32 s46, 0x510000\n"
      "s_mov_b32 s47, 0x20000\n"
      "v_add_u32 v162, 0, v1\n"
      "v_mul_u32_u24 v163, 171, v162\n"
      "v_lshrrev_b32 v163, 12, v163\n"
      "v_mul_u32_u24 v163, 768, v163\n"
      "v_lshl_add_u32 v20, v1, 4, v163\n"
      "v_add_u32 v162, 64, v1\n"
      "v_mul_u32_u24 v163, 171, v162\n"
      "v_lshrrev_b32 v163, 12, v163\n"
      "v_mul_u32_u24 v163, 768, v163\n"
      "v_lshl_add_u32 v21, v1, 4, v163\n"
      "v_add_u32 v162, 128, v1\n"
      "v_mul_u32_u24 v163, 171, v162\n"
      "v_lshrrev_b32 v163, 12, v163\n"
      "v_mul_u32_u24 v163, 768, v163\n"
      "v_lshl_add_u32 v22, v1, 4, v163\n"
      "v_bfe_u32 v162, v1, 2, 2\n"
      "v_lshl_add_u32 v162, v160, 3, v162\n"
      "v_mul_u32_u24 v162, 384, v162\n"
      "s_mul_i32 s50, s28, 96\n"
      "v_add_u32 v162, s50, v162\n"
      "v_and_b32 v163, 3, v1\n"
      "v_lshl_add_u32 v23, v163, 4, v162\n"
      "v_lshl_add_u32 v24, v163, 3, v162\n"
      "s_lshl_b32 s50, s52, 5\n"
      "v_add_u32 v23, s50, v23\n"
      "s_lshl_b32 s50, s52, 6\n"
      "s_sub_u32 s50, 64, s50\n"
      "v_add_u32 v24, s50, v24\n"
      "v_add_u32 v25, 0x10000, v23\n"
      "v_add_u32 v26, 0x10000, v24\n"
      "v_add_u32 v27, 0x20000, v23\n"
      "v_add_u32 v28, 0x20000, v24\n"
      "s_mul_i32 s73, s28, 3072\n"
      "s_mul_i32 s51, s30, 884736\n"
      "s_mul_i32 s50, s31, 384\n"
      "s_add_u32 s51, s51, s50\n"
      "s_mul_i32 s50, s28, 9216\n"
      "s_add_u32 s51, s51, s50\n"
      "s_sleep 16\n"
      "s_sleep 16\n"
      "s_sleep 16\n"
      "s_sleep 16\n"
      "s_sleep 16\n"
      "s_sleep 16\n"
      "s_add_u32 m0, s73, 0\n"
      "s_add_u32 s50, s51, 0\n"
      "buffer_load_dwordx4 v20, s[44:47], s50 offen lds\n"
      "buffer_load_dwordx4 v21, s[44:47], s50 offen offset:1024 lds\n"
      "buffer_load_dwordx4 v22, s[44:47], s50 offen offset:2048 lds\n"
      "s_add_u32 m0, s73, 12288\n"
      "s_add_u32 s50, s51, 36864\n"
      "buffer_load_dwordx4 v20, s[44:47], s50 offen lds\n"
      "buffer_load_dwordx4 v21, s[44:47], s50 offen offset:1024 lds\n"
      "buffer_load_dwordx4 v22, s[44:47], s50 offen offset:2048 lds\n"
      "s_add_u32 m0, s73, 24576\n"
      "s_add_u32 s50, s51, 73728\n"
      "buffer_load_dwordx4 v20, s[44:47], s50 offen lds\n"
      "buffer_load_dwordx4 v21, s[44:47], s50 offen offset:1024 lds\n"
      "buffer_load_dwordx4 v22, s[44:47], s50 offen offset:2048 lds\n"
      "s_add_u32 m0, s73, 36864\n"
      "s_add_u32 s50, s51, 110592\n"
      "buffer_load_dwordx4 v20, s[44:47], s50 offen lds\n"
      "buffer_load_dwordx4 v21, s[44:47], s50 offen offset:1024 lds\n"
      "buffer_load_dwordx4 v22, s[44:47], s50 offen offset:2048 lds\n"
      "s_add_u32 m0, s73, 49152\n"
      "s_add_u32 s50, s51, 147456\n"
      "buffer_load_dwordx4 v20, s[44:47], s50 offen lds\n"
      "buffer_load_dwordx4 v21, s[44:47], s50 offen offset:1024 lds\n"
      "buffer_load_dwordx4 v22, s[44:47], s50 offen offset:2048 lds\n"
      "s_add_u32 m0, s73, 61440\n"
      "s_add_u32 s50, s51, 184320\n"
      "buffer_load_dwordx4 v20, s[44:47], s50 offen lds\n"
      "buffer_load_dwordx4 v21, s[44:47], s50 offen offset:1024 lds\n"
      "buffer_load_dwordx4 v22, s[44:47], s50 offen offset:2048 lds\n"
      "s_add_u32 m0, s73, 73728\n"
      "s_add_u32 s50, s51, 221184\n"
      "buffer_load_dwordx4 v20, s[44:47], s50 offen lds\n"
      "buffer_load_dwordx4 v21, s[44:47], s50 offen offset:1024 lds\n"
      "buffer_load_dwordx4 v22, s[44:47], s50 offen offset:2048 lds\n"
      "s_add_u32 m0, s73, 86016\n"
      "s_add_u32 s50, s51, 258048\n"
      "buffer_load_dwordx4 v20, s[44:47], s50 offen lds\n"
      "buffer_load_dwordx4 v21, s[44:47], s50 offen offset:1024 lds\n"
      "buffer_load_dwordx4 v22, s[44:47], s50 offen offset:2048 lds\n"
      "s_add_u32 m0, s73, 98304\n"
      "s_add_u32 s50, s51, 294912\n"
      "buffer_load_dwordx4 v20, s[44:47], s50 offen lds\n"
      "buffer_load_dwordx4 v21, s[44:47], s50 offen offset:1024 lds\n"
      "buffer_load_dwordx4 v22, s[44:47], s50 offen offset:2048 lds\n"
      "s_add_u32 m0, s73, 110592\n"
      "s_add_u32 s50, s51, 331776\n"
      "buffer_load_dwordx4 v20, s[44:47], s50 offen lds\n"
      "buffer_load_dwordx4 v21, s[44:47], s50 offen offset:1024 lds\n"
      "buffer_load_dwordx4 v22, s[44:47], s50 offen offset:2048 lds\n"
      "s_add_u32 m0, s73, 122880\n"
      "s_add_u32 s50, s51, 368640\n"
      "buffer_load_dwordx4 v20, s[44:47], s50 offen lds\n"
      "buffer_load_dwordx4 v21, s[44:47], s50 offen offset:1024 lds\n"
      "buffer_load_dwordx4 v22, s[44:47], s50 offen offset:2048 lds\n"
      "s_add_u32 m0, s73, 135168\n"
      "s_add_u32 s50, s51, 405504\n"
      "buffer_load_dwordx4 v20, s[44:47], s50 offen lds\n"
      "buffer_load_dwordx4 v21, s[44:47], s50 offen offset:1024 lds\n"
      "buffer_load_dwordx4 v22, s[44:47], s50 offen offset:2048 lds\n"
      "s_waitcnt vmcnt(30)\n"
      "s_barrier\n"
      "ds_read_b64_tr_b16 a[0:1], v23 offset:0\n"
      "ds_read_b64_tr_b16 a[2:3], v23 offset:1536\n"
      "ds_read_b64_tr_b16 a[4:5], v23 offset:8\n"
      "ds_read_b64_tr_b16 a[6:7], v23 offset:1544\n"
      "ds_read_b64_tr_b16 a[8:9], v24 offset:0\n"
      "ds_read_b64_tr_b16 a[10:11], v24 offset:1536\n"
      "ds_read_b64_tr_b16 a[12:13], v23 offset:12288\n"
      "ds_read_b64_tr_b16 a[14:15], v23 offset:13824\n"
      "ds_read_b64_tr_b16 a[16:17], v23 offset:12296\n"
      "ds_read_b64_tr_b16 a[18:19], v23 offset:13832\n"
      "ds_read_b64_tr_b16 a[20:21], v24 offset:12288\n"
      "ds_read_b64_tr_b16 a[22:23], v24 offset:13824\n"
      "s_waitcnt vmcnt(24) lgkmcnt(0)\n"
      "s_barrier\n"
      "s_add_u32 m0, s73, 0\n"
      "s_add_u32 s50, s51, 442368\n"
      "buffer_load_dwordx4 v20, s[44:47], s50 offen lds\n"
      "buffer_load_dwordx4 v21, s[44:47], s50 offen offset:1024 lds\n"
      "buffer_load_dwordx4 v22, s[44:47], s50 offen offset:2048 lds\n"
      "s_add_u32 m0, s73, 12288\n"
      "s_add_u32 s50, s51, 479232\n"
      "buffer_load_dwordx4 v20, s[44:47], s50 offen lds\n"
      "buffer_load_dwordx4 v21, s[44:47], s50 offen offset:1024 lds\n"
      "buffer_load_dwordx4 v22, s[44:47], s50 offen offset:2048 lds\n"
      "ds_read_b64_tr_b16 a[24:25], v23 offset:24576\n"
      "ds_read_b64_tr_b16 a[26:27], v23 offset:26112\n"
      "ds_read_b64_tr_b16 a[28:29], v23 offset:24584\n"
      "ds_read_b64_tr_b16 a[30:31], v23 offset:26120\n"
      "ds_read_b64_tr_b16 a[32:33], v24 offset:24576\n"
      "ds_read_b64_tr_b16 a[34:35], v24 offset:26112\n"
      "ds_read_b64_tr_b16 a[36:37], v23 offset:36864\n"
      "ds_read_b64_tr_b16 a[38:39], v23 offset:38400\n"
      "ds_read_b64_tr_b16 a[40:41], v23 offset:36872\n"
      "ds_read_b64_tr_b16 a[42:43], v23 offset:38408\n"
      "ds_read_b64_tr_b16 a[44:45], v24 offset:36864\n"
      "ds_read_b64_tr_b16 a[46:47], v24 offset:38400\n"
      "s_waitcnt vmcnt(24) lgkmcnt(0)\n"
      "s_barrier\n"
      "s_add_u32 m0, s73, 24576\n"
      "s_add_u32 s50, s51, 516096\n"
      "buffer_load_dwordx4 v20, s[44:47], s50 offen lds\n"
      "buffer_load_dwordx4 v21, s[44:47], s50 offen offset:1024 lds\n"
      "buffer_load_dwordx4 v22, s[44:47], s50 offen offset:2048 lds\n"
      "s_add_u32 m0, s73, 36864\n"
      "s_add_u32 s50, s51, 552960\n"
      "buffer_load_dwordx4 v20, s[44:47], s50 offen lds\n"
      "buffer_load_dwordx4 v21, s[44:47], s50 offen offset:1024 lds\n"
      "buffer_load_dwordx4 v22, s[44:47], s50 offen offset:2048 lds\n"
      "ds_read_b64_tr_b16 a[48:49], v23 offset:49152\n"
      "ds_read_b64_tr_b16 a[50:51], v23 offset:50688\n"
      "ds_read_b64_tr_b16 a[52:53], v23 offset:49160\n"
      "ds_read_b64_tr_b16 a[54:55], v23 offset:50696\n"
      "ds_read_b64_tr_b16 a[56:57], v24 offset:49152\n"
      "ds_read_b64_tr_b16 a[58:59], v24 offset:50688\n"
      "ds_read_b64_tr_b16 a[60:61], v23 offset:61440\n"
      "ds_read_b64_tr_b16 a[62:63], v23 offset:62976\n"
      "ds_read_b64_tr_b16 a[64:65], v23 offset:61448\n"
      "ds_read_b64_tr_b16 a[66:67], v23 offset:62984\n"
      "ds_read_b64_tr_b16 a[68:69], v24 offset:61440\n"
      "ds_read_b64_tr_b16 a[70:71], v24 offset:62976\n"
      "s_waitcnt vmcnt(24) lgkmcnt(0)\n"
      "s_barrier\n"
      "s_add_u32 m0, s73, 49152\n"
      "s_add_u32 s50, s51, 589824\n"
      "buffer_load_dwordx4 v20, s[44:47], s50 offen lds\n"
      "buffer_load_dwordx4 v21, s[44:47], s50 offen offset:1024 lds\n"
      "buffer_load_dwordx4 v22, s[44:47], s50 offen offset:2048 lds\n"
      "s_add_u32 m0, s73, 61440\n"
      "s_add_u32 s50, s51, 626688\n"
      "buffer_load_dwordx4 v20, s[44:47], s50 offen lds\n"
      "buffer_load_dwordx4 v21, s[44:47], s50 offen offset:1024 lds\n"
      "buffer_load_dwordx4 v22, s[44:47], s50 offen offset:2048 lds\n"
      "ds_read_b64_tr_b16 a[72:73], v25 offset:8192\n"
      "ds_read_b64_tr_b16 a[74:75], v25 offset:9728\n"
      "ds_read_b64_tr_b16 a[76:77], v25 offset:8200\n"
      "ds_read_b64_tr_b16 a[78:79], v25 offset:9736\n"
      "ds_read_b64_tr_b16 a[80:81], v26 offset:8192\n"
      "ds_read_b64_tr_b16 a[82:83], v26 offset:9728\n"
      "ds_read_b64_tr_b16 a[84:85], v25 offset:20480\n"
      "ds_read_b64_tr_b16 a[86:87], v25 offset:22016\n"
      "ds_read_b64_tr_b16 a[88:89], v25 offset:20488\n"
      "ds_read_b64_tr_b16 a[90:91], v25 offset:22024\n"
      "ds_read_b64_tr_b16 a[92:93], v26 offset:20480\n"
      "ds_read_b64_tr_b16 a[94:95], v26 offset:22016\n"
      "s_waitcnt vmcnt(24) lgkmcnt(0)\n"
      "s_barrier\n"
      "s_add_u32 m0, s73, 73728\n"
      "s_add_u32 s50, s51, 663552\n"
      "buffer_load_dwordx4 v20, s[44:47], s50 offen lds\n"
      "buffer_load_dwordx4 v21, s[44:47], s50 offen offset:1024 lds\n"
      "buffer_load_dwordx4 v22, s[44:47], s50 offen offset:2048 lds\n"
      "s_add_u32 m0, s73, 86016\n"
      "s_add_u32 s50, s51, 700416\n"
      "buffer_load_dwordx4 v20, s[44:47], s50 offen lds\n"
      "buffer_load_dwordx4 v21, s[44:47], s50 offen offset:1024 lds\n"
      "buffer_load_dwordx4 v22, s[44:47], s50 offen offset:2048 lds\n"
      "ds_read_b64_tr_b16 a[96:97], v25 offset:32768\n"
      "ds_read_b64_tr_b16 a[98:99], v25 offset:34304\n"
      "ds_read_b64_tr_b16 a[100:101], v25 offset:32776\n"
      "ds_read_b64_tr_b16 a[102:103], v25 offset:34312\n"
      "ds_read_b64_tr_b16 a[104:105], v26 offset:32768\n"
      "ds_read_b64_tr_b16 a[106:107], v26 offset:34304\n"
      "ds_read_b64_tr_b16 a[108:109], v25 offset:45056\n"
      "ds_read_b64_tr_b16 a[110:111], v25 offset:46592\n"
      "ds_read_b64_tr_b16 a[112:113], v25 offset:45064\n"
      "ds_read_b64_tr_b16 a[114:115], v25 offset:46600\n"
      "ds_read_b64_tr_b16 a[116:117], v26 offset:45056\n"
      "ds_read_b64_tr_b16 a[118:119], v26 offset:46592\n"
      "s_waitcnt vmcnt(24) lgkmcnt(0)\n"
      "s_barrier\n"
      "s_add_u32 m0, s73, 98304\n"
      "s_add_u32 s50, s51, 737280\n"
      "buffer_load_dwordx4 v20, s[44:47], s50 offen lds\n"
      "buffer_load_dwordx4 v21, s[44:47], s50 offen offset:1024 lds\n"
      "buffer_load_dwordx4 v22, s[44:47], s50 offen offset:2048 lds\n"
      "s_add_u32 m0, s73, 110592\n"
      "s_add_u32 s50, s51, 774144\n"
      "buffer_load_dwordx4 v20, s[44:47], s50 offen lds\n"
      "buffer_load_dwordx4 v21, s[44:47], s50 offen offset:1024 lds\n"
      "buffer_load_dwordx4 v22, s[44:47], s50 offen offset:2048 lds\n"
      "ds_read_b64_tr_b16 a[120:121], v25 offset:57344\n"
      "ds_read_b64_tr_b16 a[122:123], v25 offset:58880\n"
      "ds_read_b64_tr_b16 a[124:125], v25 offset:57352\n"
      "ds_read_b64_tr_b16 a[126:127], v25 offset:58888\n"
      "ds_read_b64_tr_b16 a[128:129], v26 offset:57344\n"
      "ds_read_b64_tr_b16 a[130:131], v26 offset:58880\n"
      "ds_read_b64_tr_b16 a[132:133], v27 offset:4096\n"
      "ds_read_b64_tr_b16 a[134:135], v27 offset:5632\n"
      "ds_read_b64_tr_b16 a[136:137], v27 offset:4104\n"
      "ds_read_b64_tr_b16 a[138:139], v27 offset:5640\n"
      "ds_read_b64_tr_b16 a[140:141], v28 offset:4096\n"
      "ds_read_b64_tr_b16 a[142:143], v28 offset:5632\n"
      "s_waitcnt vmcnt(24) lgkmcnt(0)\n"
      "s_barrier\n"
      "s_add_u32 m0, s73, 122880\n"
      "s_add_u32 s50, s51, 811008\n"
      "buffer_load_dwordx4 v20, s[44:47], s50 offen lds\n"
      "buffer_load_dwordx4 v21, s[44:47], s50 offen offset:1024 lds\n"
      "buffer_load_dwordx4 v22, s[44:47], s50 offen offset:2048 lds\n"
      "s_add_u32 m0, s73, 135168\n"
      "s_add_u32 s50, s51, 847872\n"
      "buffer_load_dwordx4 v20, s[44:47], s50 offen lds\n"
      "buffer_load_dwordx4 v21, s[44:47], s50 offen offset:1024 lds\n"
      "buffer_load_dwordx4 v22, s[44:47], s50 offen offset:2048 lds\n"
      "ds_read_b64_tr_b16 a[144:145], v23 offset:0\n"
      "ds_read_b64_tr_b16 a[146:147], v23 offset:1536\n"
      "ds_read_b64_tr_b16 a[148:149], v23 offset:8\n"
      "ds_read_b64_tr_b16 a[150:151], v23 offset:1544\n"
      "ds_read_b64_tr_b16 a[152:153], v24 offset:0\n"
      "ds_read_b64_tr_b16 a[154:155], v24 offset:1536\n"
      "ds_read_b64_tr_b16 a[156:157], v23 offset:12288\n"
      "ds_read_b64_tr_b16 a[158:159], v23 offset:13824\n"
      "ds_read_b64_tr_b16 a[160:161], v23 offset:12296\n"
      "ds_read_b64_tr_b16 a[162:163], v23 offset:13832\n"
      "ds_read_b64_tr_b16 a[164:165], v24 offset:12288\n"
      "ds_read_b64_tr_b16 a[166:167], v24 offset:13824\n"
      "s_waitcnt vmcnt(24) lgkmcnt(0)\n"
      "s_barrier\n"
      "ds_read_b64_tr_b16 a[168:169], v23 offset:24576\n"
      "ds_read_b64_tr_b16 a[170:171], v23 offset:26112\n"
      "ds_read_b64_tr_b16 a[172:173], v23 offset:24584\n"
      "ds_read_b64_tr_b16 a[174:175], v23 offset:26120\n"
      "ds_read_b64_tr_b16 a[176:177], v24 offset:24576\n"
      "ds_read_b64_tr_b16 a[178:179], v24 offset:26112\n"
      "ds_read_b64_tr_b16 a[180:181], v23 offset:36864\n"
      "ds_read_b64_tr_b16 a[182:183], v23 offset:38400\n"
      "ds_read_b64_tr_b16 a[184:185], v23 offset:36872\n"
      "ds_read_b64_tr_b16 a[186:187], v23 offset:38408\n"
      "ds_read_b64_tr_b16 a[188:189], v24 offset:36864\n"
      "ds_read_b64_tr_b16 a[190:191], v24 offset:38400\n"
      "s_waitcnt vmcnt(18) lgkmcnt(0)\n"
      "s_barrier\n"
      "ds_read_b64_tr_b16 a[192:193], v23 offset:49152\n"
      "ds_read_b64_tr_b16 a[194:195], v23 offset:50688\n"
      "ds_read_b64_tr_b16 a[196:197], v23 offset:49160\n"
      "ds_read_b64_tr_b16 a[198:199], v23 offset:50696\n"
      "ds_read_b64_tr_b16 a[200:201], v24 offset:49152\n"
      "ds_read_b64_tr_b16 a[202:203], v24 offset:50688\n"
      "ds_read_b64_tr_b16 a[204:205], v23 offset:61440\n"
      "ds_read_b64_tr_b16 a[206:207], v23 offset:62976\n"
      "ds_read_b64_tr_b16 a[208:209], v23 offset:61448\n"
      "ds_read_b64_tr_b16 a[210:211], v23 offset:62984\n"
      "ds_read_b64_tr_b16 a[212:213], v24 offset:61440\n"
      "ds_read_b64_tr_b16 a[214:215], v24 offset:62976\n"
      "s_waitcnt vmcnt(12) lgkmcnt(0)\n"
      "s_barrier\n"
      "ds_read_b64_tr_b16 a[216:217], v25 offset:8192\n"
      "ds_read_b64_tr_b16 a[218:219], v25 offset:9728\n"
      "ds_read_b64_tr_b16 a[220:221], v25 offset:8200\n"
      "ds_read_b64_tr_b16 a[222:223], v25 offset:9736\n"
      "ds_read_b64_tr_b16 a[224:225], v26 offset:8192\n"
      "ds_read_b64_tr_b16 a[226:227], v26 offset:9728\n"
      "ds_read_b64_tr_b16 a[228:229], v25 offset:20480\n"
      "ds_read_b64_tr_b16 a[230:231], v25 offset:22016\n"
      "ds_read_b64_tr_b16 a[232:233], v25 offset:20488\n"
      "ds_read_b64_tr_b16 a[234:235], v25 offset:22024\n"
      "ds_read_b64_tr_b16 a[236:237], v26 offset:20480\n"
      "ds_read_b64_tr_b16 a[238:239], v26 offset:22016\n"
      "s_waitcnt vmcnt(6) lgkmcnt(0)\n"
      "s_barrier\n"
      "ds_read_b64_tr_b16 a[240:241], v25 offset:32768\n"
      "ds_read_b64_tr_b16 a[242:243], v25 offset:34304\n"
      "ds_read_b64_tr_b16 a[244:245], v25 offset:32776\n"
      "ds_read_b64_tr_b16 a[246:247], v25 offset:34312\n"
      "ds_read_b64_tr_b16 a[248:249], v26 offset:32768\n"
      "ds_read_b64_tr_b16 a[250:251], v26 offset:34304\n"
      "ds_read_b64_tr_b16 a[252:253], v25 offset:45056\n"
      "ds_read_b64_tr_b16 a[254:255], v25 offset:46592\n"
      "ds_read_b64_tr_b16 v[216:217], v25 offset:45064\n"
      "ds_read_b64_tr_b16 v[218:219], v25 offset:46600\n"
      "ds_read_b64_tr_b16 v[220:221], v26 offset:45056\n"
      "ds_read_b64_tr_b16 v[222:223], v26 offset:46592\n"
      "s_waitcnt vmcnt(0) lgkmcnt(0)\n"
      "s_barrier\n"
      "ds_read_b64_tr_b16 v[224:225], v25 offset:57344\n"
      "ds_read_b64_tr_b16 v[226:227], v25 offset:58880\n"
      "ds_read_b64_tr_b16 v[228:229], v25 offset:57352\n"
      "ds_read_b64_tr_b16 v[230:231], v25 offset:58888\n"
      "ds_read_b64_tr_b16 v[232:233], v26 offset:57344\n"
      "ds_read_b64_tr_b16 v[234:235], v26 offset:58880\n"
      "ds_read_b64_tr_b16 v[236:237], v27 offset:4096\n"
      "ds_read_b64_tr_b16 v[238:239], v27 offset:5632\n"
      "ds_read_b64_tr_b16 v[240:241], v27 offset:4104\n"
      "ds_read_b64_tr_b16 v[242:243], v27 offset:5640\n"
      "ds_read_b64_tr_b16 v[244:245], v28 offset:4096\n"
      "ds_read_b64_tr_b16 v[246:247], v28 offset:5632\n"
      "s_mov_b32 s40, s22\n"
      "s_and_b32 s41, s23, 0xffff\n"
      "s_mov_b32 s42, 0x240000\n"
      "s_mov_b32 s43, 0x20000\n"
      "s_mov_b32 s66, -1\n"
      "s_mul_i32 s68, s29, 9437184\n"
      "s_mul_i32 s50, s30, 24576\n"
      "s_mul_i32 s73, s28, 6144\n"
      "s_add_u32 s68, s68, s50\n"
      "s_add_u32 s68, s68, s73\n"
      "s_add_u32 s68, s68, 15851520\n"
      "s_mul_i32 s70, s29, 7077888\n"
      "s_mul_i32 s50, s31, 36864\n"
      "s_add_u32 s70, s70, s50\n"
      "s_mul_i32 s50, s30, 6144\n"
      "s_add_u32 s70, s70, s50\n"
      "s_add_u32 s70, s70, 53600256\n"
      "s_mul_i32 s50, s28, 1536\n"
      "v_add_u32 v18, s50, v2\n"
      "v_lshlrev_b32 v118, 3, v1\n"
      "v_add_u32 v118, s50, v118\n"
      "s_mov_b32 s50, -30720\n"
      "s_cmp_lt_u32 s30, 5\n"
      "s_cselect_b32 s82, 6144, s50\n"
      "s_mov_b32 s50, -24576\n"
      "s_cmp_lt_u32 s30, 4\n"
      "s_cselect_b32 s83, 12288, s50\n"
      "s_mov_b32 s50, -18432\n"
      "s_cmp_lt_u32 s30, 3\n"
      "s_cselect_b32 s84, 18432, s50\n"
      "s_mov_b32 s50, -12288\n"
      "s_cmp_lt_u32 s30, 2\n"
      "s_cselect_b32 s85, 24576, s50\n"
      "s_mov_b32 s50, -6144\n"
      "s_cmp_lt_u32 s30, 1\n"
      "s_cselect_b32 s86, 30720, s50\n"
      "v_mov_b32 v3, v2\n"
      "v_add_u32 v4, 49152, v2\n"
      "v_add_u32 v119, 98304, v2\n"
      "v_add_u32 v19, 4096, v2\n"
      "v_min_u32 v162, 5, v1\n"
      "v_lshlrev_b32 v162, 7, v162\n"
      "s_mul_i32 s50, s29, 36\n"
      "s_mul_i32 s51, s30, 6\n"
      "s_add_u32 s50, s50, s51\n"
      "s_lshl_b32 s50, s50, 9\n"
      "s_mul_i32 s51, s28, 768\n"
      "s_add_u32 s50, s50, s51\n"
      "s_add_u32 s50, s50, 81932288\n"
      "v_add_u32 v5, s50, v162\n"
      "v_and_b32 v5, 0xfffffe7f, v5\n"
      "s_mul_i32 s50, s29, 18\n"
      "s_mul_i32 s51, s31, 6\n"
      "s_add_u32 s50, s50, s51\n"
      "s_lshl_b32 s50, s50, 2\n"
      "s_add_u32 s50, s50, s28\n"
      "s_lshl_b32 s50, s50, 7\n"
      "s_add_u32 s50, s50, 82006016\n"
      "v_min_u32 v162, 4, v1\n"
      "v_cmp_le_u32 vcc, s30, v162\n"
      "s_nop 1\n"
      "v_addc_co_u32 v162, vcc, 0, v162, vcc\n"
      "v_lshlrev_b32 v162, 9, v162\n"
      "v_add_u32 v9, s50, v162\n"
      "v_lshlrev_b32 v162, 2, v1\n"
      "v_cmp_gt_u32 vcc, 32, v1\n"
      "s_nop 1\n"
      "v_cndmask_b32 v16, v164, v162, vcc\n"
      "v_mov_b32 v6, 0\n"
      "v_mov_b32 v10, 0\n"
      "v_mov_b32 v17, 0\n"
      "s_mov_b32 s74, 0\n"
      "s_mov_b32 s88, -1\n"
      "s_mov_b32 s75, 0x40000000\n"
      "s_mov_b32 s76, 0x40000000\n"
      "s_mov_b32 s77, 0x40000000\n"
      "s_mov_b32 s78, 0x40000000\n"
      "s_mov_b32 s79, 0x40000000\n"
      "s_add_u32 s65, s65, 198144\n"
      "s_sub_u32 s50, 8, s30\n"
      "s_cmp_ge_u32 s50, 6\n"
      "s_cselect_b32 s51, 6, 0\n"
      "s_sub_u32 s50, s50, s51\n"
      "s_cmp_lt_u32 s50, 2\n"
      "s_cselect_b32 s51, 48, 40\n"
      "v_lshrrev_b32 v162, 3, v1\n"
      "v_mul_u32_u24 v162, 9216, v162\n"
      "v_and_b32 v163, 7, v1\n"
      "v_lshl_add_u32 v162, v163, 4, v162\n"
      "v_cmp_gt_u32 vcc, s51, v1\n"
      "s_add_u32 s50, s50, s81\n"
      "s_mul_i32 s50, s50, 1536\n"
      "v_cndmask_b32 v162, v164, v162, vcc\n"
      "s_add_u32 s50, s50, s65\n"
      "s_not_b32 s51, s98\n"
      "v_mov_b32 v100, s51\n"
      "v_mov_b32 v101, s51\n"
      "v_mov_b32 v102, s51\n"
      "v_mov_b32 v103, s51\n"
      "buffer_store_dwordx4 v[100:103], v162, s[36:39], s50 offen sc1\n"
      "s_mov_b32 s56, 0x20000\n"
      ".Lsp3:\n"
      "buffer_load_dwordx2 v[20:21], v5, s[36:39], 0 offen offset:8 sc1\n"
      "s_waitcnt vmcnt(0)\n"
      "v_cmp_ne_u64 vcc, s[98:99], v[20:21]\n"
      "s_cbranch_vccz .Lbk3\n"
      "s_sleep 4\n"
      "s_sub_u32 s56, s56, 1\n"
      "s_cbranch_scc0 .Lsp3\n"
      ".Lbk3:\n"
      "s_mov_b32 s56, 0x20000\n"
      ".Lsp4:\n"
      "buffer_load_dwordx2 v[20:21], v9, s[36:39], 0 offen offset:8 sc1\n"
      "s_waitcnt vmcnt(0)\n"
      "v_cmp_ne_u64 vcc, s[98:99], v[20:21]\n"
      "s_cbranch_vccz .Lbk4\n"
      "s_sleep 4\n"
      "s_sub_u32 s56, s56, 1\n"
      "s_cbranch_scc0 .Lsp4\n"
      ".Lbk4:\n"
      "s_waitcnt vmcnt(0) lgkmcnt(0)\n"
      "s_barrier\n"
      "s_mov_b32 s71, 0x40000000\n"
      "s_add_u32 s50, s30, 1\n"
      "s_mov_b32 s56, 0x20000\n"
      ".Lsp5:\n"
      "v_cmp_gt_u32 vcc, 1, v6\n"
      "s_cbranch_vccz .Lbk5\n"
      "s_sleep 4\n"
      "buffer_load_dword v6, v5, s[36:39], 0 offen sc1\n"
      "s_waitcnt vmcnt(0)\n"
      "s_sub_u32 s56, s56, 1\n"
      "s_cbranch_scc0 .Lsp5\n"
      ".Lbk5:\n"
      "s_cmp_ge_u32 s50, 6\n"
      "s_cselect_b32 s51, 6, 0\n"
      "s_sub_u32 s51, s50, s51\n"
      "s_mul_i32 s51, s51, 24576\n"
      "v_add_u32 v162, s51, v2\n"
      "s_add_u32 m0, s73, s51\n"
      "s_add_u32 s50, s50, 1\n"
      "buffer_load_dwordx4 v2, s[36:39], s68 offen sc1 lds\n"
      "buffer_load_dwordx4 v2, s[36:39], s68 offen offset:1024 sc1 lds\n"
      "buffer_load_dwordx4 v2, s[36:39], s68 offen offset:2048 sc1 lds\n"
      "buffer_load_dwordx4 v2, s[36:39], s68 offen offset:3072 sc1 lds\n"
      "s_add_u32 m0, m0, 4096\n"
      "s_nop 0\n"
      "buffer_load_dwordx4 v19, s[36:39], s68 offen sc1 lds\n"
      "buffer_load_dwordx4 v19, s[36:39], s68 offen offset:1024 sc1 lds\n"
      "s_mov_b32 s56, 0x20000\n"
      ".Lsp6:\n"
      "v_cmp_gt_u32 vcc, 2, v6\n"
      "s_cbranch_vccz .Lbk6\n"
      "s_sleep 4\n"
      "buffer_load_dword v6, v5, s[36:39], 0 offen sc1\n"
      "s_waitcnt vmcnt(0)\n"
      "s_sub_u32 s56, s56, 1\n"
      "s_cbranch_scc0 .Lsp6\n"
      ".Lbk6:\n"
      "s_cmp_ge_u32 s50, 6\n"
      "s_cselect_b32 s51, 6, 0\n"
      "s_sub_u32 s51, s50, s51\n"
      "s_mul_i32 s51, s51, 24576\n"
      "s_add_u32 s68, s68, 147456\n"
      "s_add_u32 m0, s73, s51\n"
      "s_add_u32 s50, s50, 1\n"
      "buffer_load_dwordx4 v2, s[36:39], s68 offen sc1 lds\n"
      "buffer_load_dwordx4 v2, s[36:39], s68 offen offset:1024 sc1 lds\n"
      "buffer_load_dwordx4 v2, s[36:39], s68 offen offset:2048 sc1 lds\n"
      "buffer_load_dwordx4 v2, s[36:39], s68 offen offset:3072 sc1 lds\n"
      "s_add_u32 m0, m0, 4096\n"
      "s_nop 0\n"
      "buffer_load_dwordx4 v19, s[36:39], s68 offen sc1 lds\n"
      "buffer_load_dwordx4 v19, s[36:39], s68 offen offset:1024 sc1 lds\n"
      "s_waitcnt vmcnt(6)\n"
      "s_barrier\n"
      "ds_read_b128 v[20:23], v162 offset:0\n"
      "ds_read_b128 v[24:27], v162 offset:1024\n"
      "ds_read_b128 v[28:31], v162 offset:2048\n"
      "ds_read_b128 v[32:35], v162 offset:3072\n"
      "buffer_load_dword v6, v5, s[36:39], 0 offen sc1\n"
      "s_mov_b32 s87, 0x40000000\n"
      "buffer_load_dword v163, off, s[36:39], s87\n"
      "buffer_load_dword v164, off, s[36:39], s87\n"
      "buffer_load_dword v165, off, s[36:39], s87\n"
      "buffer_load_dword v166, off, s[36:39], s87\n"
      "s_waitcnt lgkmcnt(0)\n"
      "s_cmp_eq_u32 s30, 1\n"
      "s_cbranch_scc1 .Ldn_e1\n"
      "s_cmp_eq_u32 s30, 2\n"
      "s_cbranch_scc1 .Ldn_e2\n"
      "s_cmp_eq_u32 s30, 3\n"
      "s_cbranch_scc1 .Ldn_e3\n"
      "s_cmp_eq_u32 s30, 4\n"
      "s_cbranch_scc1 .Ldn_e4\n"
      "s_cmp_eq_u32 s30, 5\n"
      "s_cbranch_scc1 .Ldn_e5\n"
      "s_branch .Ldn_e0\n"
      ".Ldn_loop:\n"
      "v_mfma_f32_16x16x32_bf16 v[48:51], a[232:235], v[24:27], v[48:51]\n"
      "ds_read_b128 v[40:43], v3 offset:23552\n"
      "v_mfma_f32_16x16x32_bf16 v[52:55], a[224:227], v[20:23], v[52:55]\n"
      "s_barrier\n"
      "v_mfma_f32_16x16x32_bf16 v[52:55], a[236:239], v[24:27], v[52:55]\n"
      "s_waitcnt vmcnt(7) lgkmcnt(3)\n"
      "buffer_store_dword v8, v7, s[36:39], 0 offen sc1\n"
      "v_mfma_f32_16x16x32_bf16 v[44:47], a[240:243], v[28:31], v[44:47]\n"
      "s_waitcnt lgkmcnt(2)\n"
      "ds_read_b128 v[20:23], v3 offset:24576\n"
      "v_mfma_f32_16x16x32_bf16 v[44:47], a[252:255], v[32:35], v[44:47]\n"
      "s_add_u32 s87, s75, s82\n"
      "v_mfma_f32_16x16x32_bf16 v[48:51], a[244:247], v[28:31], v[48:51]\n"
      "s_cmp_lt_u32 s66, 64\n"
      "buffer_load_dwordx4 v[80:83], v18, s[36:39], s87 offen sc1\n"
      "v_mfma_f32_16x16x32_bf16 v[48:51], v[216:219], v[32:35], v[48:51]\n"
      "s_cselect_b32 s71, s70, 0x40000000\n"
      "ds_read_b128 v[24:27], v3 offset:25600\n"
      "v_mfma_f32_16x16x32_bf16 v[52:55], a[248:251], v[28:31], v[52:55]\n"
      "s_add_u32 s70, s70, 110592\n"
      "v_mfma_f32_16x16x32_bf16 v[52:55], v[220:223], v[32:35], v[52:55]\n"
      "s_waitcnt lgkmcnt(3)\n"
      "v_mfma_f32_16x16x32_bf16 v[44:47], v[224:227], v[36:39], v[44:47]\n"
      "s_waitcnt lgkmcnt(2)\n"
      "ds_read_b128 v[28:31], v3 offset:26624\n"
      "v_mfma_f32_16x16x32_bf16 v[44:47], v[236:239], v[40:43], v[44:47]\n"
      "buffer_load_dwordx2 v[84:85], v118, s[36:39], s87 offen offset:1024 sc1\n"
      "v_mfma_f32_16x16x32_bf16 v[48:51], v[228:231], v[36:39], v[48:51]\n"
      "v_mfma_f32_16x16x32_bf16 v[48:51], v[240:243], v[40:43], v[48:51]\n"
      "ds_read_b128 v[32:35], v3 offset:27648\n"
      "v_mfma_f32_16x16x32_bf16 v[52:55], v[232:235], v[36:39], v[52:55]\n"
      "v_mfma_f32_16x16x32_bf16 v[52:55], v[244:247], v[40:43], v[52:55]\n"
      "s_waitcnt lgkmcnt(3)\n"
      ".Ldn_e0:\n"
      "v_mfma_f32_16x16x32_bf16 v[56:59], a[0:3], v[20:23], 0\n"
      "s_waitcnt lgkmcnt(2)\n"
      "ds_read_b128 v[36:39], v3 offset:28672\n"
      "v_cvt_pk_bf16_f32 v112, v44, v45\n"
      "v_mfma_f32_16x16x32_bf16 v[56:59], a[12:15], v[24:27], v[56:59]\n"
      "s_add_u32 s67, s66, 4\n"
      "v_cvt_pk_bf16_f32 v113, v46, v47\n"
      "v_mfma_f32_16x16x32_bf16 v[60:63], a[4:7], v[20:23], 0\n"
      "s_min_i32 s67, s67, 64\n"
      "v_cvt_pk_bf16_f32 v114, v48, v49\n"
      "v_cvt_pk_bf16_f32 v115, v50, v51\n"
      "v_mfma_f32_16x16x32_bf16 v[60:63], a[16:19], v[24:27], v[60:63]\n"
      "s_max_i32 s67, s67, 0\n"
      "ds_read_b128 v[40:43], v3 offset:29696\n"
      "v_mfma_f32_16x16x32_bf16 v[64:67], a[8:11], v[20:23], 0\n"
      "s_add_u32 s68, s68, 147456\n"
      "buffer_store_dwordx4 v[112:115], v18, s[36:39], s71 offen sc1\n"
      "v_mfma_f32_16x16x32_bf16 v[64:67], a[20:23], v[24:27], v[64:67]\n"
      "s_waitcnt lgkmcnt(3)\n"
      "v_cvt_pk_bf16_f32 v116, v52, v53\n"
      "v_cvt_pk_bf16_f32 v117, v54, v55\n"
      "v_mfma_f32_16x16x32_bf16 v[56:59], a[24:27], v[28:31], v[56:59]\n"
      "s_waitcnt lgkmcnt(2)\n"
      "ds_read_b128 v[20:23], v3 offset:30720\n"
      "v_mfma_f32_16x16x32_bf16 v[56:59], a[36:39], v[32:35], v[56:59]\n"
      "s_add_u32 s48, s66, 3\n"
      "v_mfma_f32_16x16x32_bf16 v[60:63], a[28:31], v[28:31], v[60:63]\n"
      "s_cmp_lt_u32 s48, 64\n"
      "v_mfma_f32_16x16x32_bf16 v[60:63], a[40:43], v[32:35], v[60:63]\n"
      "s_cselect_b32 s69, s68, 0x40000000\n"
      "ds_read_b128 v[24:27], v3 offset:31744\n"
      "v_mfma_f32_16x16x32_bf16 v[64:67], a[32:35], v[28:31], v[64:67]\n"
      "s_add_u32 s66, s66, 1\n"
      "buffer_store_dwordx2 v[116:117], v118, s[36:39], s71 offen offset:1024 sc1\n"
      "v_mfma_f32_16x16x32_bf16 v[64:67], a[44:47], v[32:35], v[64:67]\n"
      "s_waitcnt lgkmcnt(3)\n"
      "v_mfma_f32_16x16x32_bf16 v[56:59], a[48:51], v[36:39], v[56:59]\n"
      "s_waitcnt lgkmcnt(2)\n"
      "ds_read_b128 v[28:31], v3 offset:32768\n"
      "v_mfma_f32_16x16x32_bf16 v[56:59], a[60:63], v[40:43], v[56:59]\n"
      "s_max_i32 s72, s66, 0\n"
      "v_mfma_f32_16x16x32_bf16 v[60:63], a[52:55], v[36:39], v[60:63]\n"
      "s_add_u32 s87, s75, s83\n"
      "v_writelane_b32 v8, s72, 0\n"
      "v_mfma_f32_16x16x32_bf16 v[60:63], a[64:67], v[40:43], v[60:63]\n"
      "ds_read_b128 v[32:35], v3 offset:33792\n"
      "v_mfma_f32_16x16x32_bf16 v[64:67], a[56:59], v[36:39], v[64:67]\n"
      "buffer_load_dwordx4 v[86:89], v18, s[36:39], s87 offen sc1\n"
      "v_mfma_f32_16x16x32_bf16 v[64:67], a[68:71], v[40:43], v[64:67]\n"
      "s_waitcnt lgkmcnt(3)\n"
      "v_mfma_f32_16x16x32_bf16 v[56:59], a[72:75], v[20:23], v[56:59]\n"
      "s_waitcnt lgkmcnt(2)\n"
      "ds_read_b128 v[36:39], v3 offset:34816\n"
      "v_mfma_f32_16x16x32_bf16 v[56:59], a[84:87], v[24:27], v[56:59]\n"
      "v_mfma_f32_16x16x32_bf16 v[60:63], a[76:79], v[20:23], v[60:63]\n"
      "buffer_load_dwordx2 v[90:91], v118, s[36:39], s87 offen offset:1024 sc1\n"
      "v_mfma_f32_16x16x32_bf16 v[60:63], a[88:91], v[24:27], v[60:63]\n"
      "s_add_u32 s87, s75, s84\n"
      "ds_read_b128 v[40:43], v3 offset:35840\n"
      "v_mfma_f32_16x16x32_bf16 v[64:67], a[80:83], v[20:23], v[64:67]\n"
      "s_add_u32 m0, s73, 73728\n"
      "v_mfma_f32_16x16x32_bf16 v[64:67], a[92:95], v[24:27], v[64:67]\n"
      "s_waitcnt lgkmcnt(3)\n"
      "v_mfma_f32_16x16x32_bf16 v[56:59], a[96:99], v[28:31], v[56:59]\n"
      "s_waitcnt lgkmcnt(2)\n"
      "ds_read_b128 v[20:23], v3 offset:36864\n"
      "v_mfma_f32_16x16x32_bf16 v[56:59], a[108:111], v[32:35], v[56:59]\n"
      "buffer_load_dwordx4 v[92:95], v18, s[36:39], s87 offen sc1\n"
      "v_mfma_f32_16x16x32_bf16 v[60:63], a[100:103], v[28:31], v[60:63]\n"
      "v_mfma_f32_16x16x32_bf16 v[60:63], a[112:115], v[32:35], v[60:63]\n"
      "ds_read_b128 v[24:27], v3 offset:37888\n"
      "v_mfma_f32_16x16x32_bf16 v[64:67], a[104:107], v[28:31], v[64:67]\n"
      "v_mfma_f32_16x16x32_bf16 v[64:67], a[116:119], v[32:35], v[64:67]\n"
      "s_waitcnt lgkmcnt(3)\n"
      "v_mfma_f32_16x16x32_bf16 v[56:59], a[120:123], v[36:39], v[56:59]\n"
      "s_waitcnt lgkmcnt(2)\n"
      "ds_read_b128 v[28:31], v3 offset:38912\n"
      "v_mfma_f32_16x16x32_bf16 v[56:59], a[132:135], v[40:43], v[56:59]\n"
      "v_mfma_f32_16x16x32_bf16 v[60:63], a[124:127], v[36:39], v[60:63]\n"
      "v_mfma_f32_16x16x32_bf16 v[60:63], a[136:139], v[40:43], v[60:63]\n"
      "ds_read_b128 v[32:35], v3 offset:39936\n"
      "v_mfma_f32_16x16x32_bf16 v[64:67], a[128:131], v[36:39], v[64:67]\n"
      "v_mfma_f32_16x16x32_bf16 v[64:67], a[140:143], v[40:43], v[64:67]\n"
      "s_waitcnt vmcnt(14) lgkmcnt(3)\n"
      "v_cmp_gt_u32 vcc, s67, v6\n"
      "v_mfma_f32_16x16x32_bf16 v[56:59], a[144:147], v[20:23], v[56:59]\n"
      "s_waitcnt lgkmcnt(2)\n"
      "ds_read_b128 v[36:39], v3 offset:40960\n"
      "v_mfma_f32_16x16x32_bf16 v[56:59], a[156:159], v[24:27], v[56:59]\n"
      "s_cbranch_vccnz .Lsl7\n"
      ".Lbk7:\n"
      "buffer_load_dword v6, v5, s[36:39], 0 offen sc1\n"
      "v_mfma_f32_16x16x32_bf16 v[60:63], a[148:151], v[20:23], v[60:63]\n"
      "v_mfma_f32_16x16x32_bf16 v[60:63], a[160:163], v[24:27], v[60:63]\n"
      "ds_read_b128 v[40:43], v3 offset:41984\n"
      "v_mfma_f32_16x16x32_bf16 v[64:67], a[152:155], v[20:23], v[64:67]\n"
      "buffer_load_dwordx4 v2, s[36:39], s69 offen sc1 lds\n"
      "v_mfma_f32_16x16x32_bf16 v[64:67], a[164:167], v[24:27], v[64:67]\n"
      "s_waitcnt lgkmcnt(3)\n"
      "v_mfma_f32_16x16x32_bf16 v[56:59], a[168:171], v[28:31], v[56:59]\n"
      "s_waitcnt lgkmcnt(2)\n"
      "ds_read_b128 v[20:23], v3 offset:43008\n"
      "v_mfma_f32_16x16x32_bf16 v[56:59], a[180:183], v[32:35], v[56:59]\n"
      "buffer_load_dwordx4 v2, s[36:39], s69 offen offset:1024 sc1 lds\n"
      "v_mfma_f32_16x16x32_bf16 v[60:63], a[172:175], v[28:31], v[60:63]\n"
      "v_mfma_f32_16x16x32_bf16 v[60:63], a[184:187], v[32:35], v[60:63]\n"
      "ds_read_b128 v[24:27], v3 offset:44032\n"
      "v_mfma_f32_16x16x32_bf16 v[64:67], a[176:179], v[28:31], v[64:67]\n"
      "buffer_load_dwordx4 v2, s[36:39], s69 offen offset:2048 sc1 lds\n"
      "v_mfma_f32_16x16x32_bf16 v[64:67], a[188:191], v[32:35], v[64:67]\n"
      "s_waitcnt lgkmcnt(3)\n"
      "v_mfma_f32_16x16x32_bf16 v[56:59], a[192:195], v[36:39], v[56:59]\n"
      "s_waitcnt lgkmcnt(2)\n"
      "ds_read_b128 v[28:31], v3 offset:45056\n"
      "v_mfma_f32_16x16x32_bf16 v[56:59], a[204:207], v[40:43], v[56:59]\n"
      "buffer_load_dwordx4 v2, s[36:39], s69 offen offset:3072 sc1 lds\n"
      "v_mfma_f32_16x16x32_bf16 v[60:63], a[196:199], v[36:39], v[60:63]\n"
      "s_waitcnt vmcnt(11)\n"
      "s_add_u32 m0, s73, 77824\n"
      "v_lshlrev_b32 v120, 16, v80\n"
      "v_mfma_f32_16x16x32_bf16 v[60:63], a[208:211], v[40:43], v[60:63]\n"
      "s_cmp_lt_i32 s66, 63\n"
      "ds_read_b128 v[32:35], v3 offset:46080\n"
      "v_add_f32 v168, v168, v120\n"
      "v_mfma_f32_16x16x32_bf16 v[64:67], a[200:203], v[36:39], v[64:67]\n"
      "buffer_load_dwordx4 v19, s[36:39], s69 offen sc1 lds\n"
      "v_mfma_f32_16x16x32_bf16 v[64:67], a[212:215], v[40:43], v[64:67]\n"
      "s_waitcnt lgkmcnt(3)\n"
      "v_and_b32 v121, 0xffff0000, v80\n"
      "v_lshlrev_b32 v122, 16, v81\n"
      "v_mfma_f32_16x16x32_bf16 v[56:59], a[216:219], v[20:23], v[56:59]\n"
      "s_waitcnt lgkmcnt(2)\n"
      "ds_read_b128 v[36:39], v3 offset:47104\n"
      "v_add_f32 v169, v169, v121\n"
      "v_mfma_f32_16x16x32_bf16 v[56:59], a[228:231], v[24:27], v[56:59]\n"
      "buffer_load_dwordx4 v19, s[36:39], s69 offen offset:1024 sc1 lds\n"
      "v_mfma_f32_16x16x32_bf16 v[60:63], a[220:223], v[20:23], v[60:63]\n"
      "v_add_f32 v170, v170, v122\n"
      "v_and_b32 v123, 0xffff0000, v81\n"
      ".Lmd0:\n"
      "s_cbranch_scc0 .Ldn_t1\n"
      "v_mfma_f32_16x16x32_bf16 v[60:63], a[232:235], v[24:27], v[60:63]\n"
      "ds_read_b128 v[40:43], v3 offset:48128\n"
      "v_add_f32 v171, v171, v123\n"
      "v_mfma_f32_16x16x32_bf16 v[64:67], a[224:227], v[20:23], v[64:67]\n"
      "v_lshlrev_b32 v120, 16, v82\n"
      "s_barrier\n"
      "v_mfma_f32_16x16x32_bf16 v[64:67], a[236:239], v[24:27], v[64:67]\n"
      "s_waitcnt vmcnt(10) lgkmcnt(3)\n"
      "buffer_store_dword v8, v7, s[36:39], 0 offen sc1\n"
      "v_mfma_f32_16x16x32_bf16 v[56:59], a[240:243], v[28:31], v[56:59]\n"
      "s_waitcnt lgkmcnt(2)\n"
      "ds_read_b128 v[20:23], v4 offset:0\n"
      "v_add_f32 v172, v172, v120\n"
      "v_mfma_f32_16x16x32_bf16 v[56:59], a[252:255], v[32:35], v[56:59]\n"
      "buffer_load_dwordx2 v[96:97], v118, s[36:39], s87 offen offset:1024 sc1\n"
      "v_mfma_f32_16x16x32_bf16 v[60:63], a[244:247], v[28:31], v[60:63]\n"
      "v_and_b32 v121, 0xffff0000, v82\n"
      "v_lshlrev_b32 v122, 16, v83\n"
      "v_mfma_f32_16x16x32_bf16 v[60:63], v[216:219], v[32:35], v[60:63]\n"
      "ds_read_b128 v[24:27], v4 offset:1024\n"
      "v_add_f32 v173, v173, v121\n"
      "v_mfma_f32_16x16x32_bf16 v[64:67], a[248:251], v[28:31], v[64:67]\n"
      "v_add_f32 v174, v174, v122\n"
      "v_and_b32 v123, 0xffff0000, v83\n"
      "v_mfma_f32_16x16x32_bf16 v[64:67], v[220:223], v[32:35], v[64:67]\n"
      "s_waitcnt lgkmcnt(3)\n"
      "v_add_f32 v175, v175, v123\n"
      "v_lshlrev_b32 v120, 16, v84\n"
      "v_mfma_f32_16x16x32_bf16 v[56:59], v[224:227], v[36:39], v[56:59]\n"
      "s_waitcnt lgkmcnt(2)\n"
      "ds_read_b128 v[28:31], v4 offset:2048\n"
      "v_add_f32 v176, v176, v120\n"
      "v_mfma_f32_16x16x32_bf16 v[56:59], v[236:239], v[40:43], v[56:59]\n"
      "s_add_u32 s87, s75, s85\n"
      "v_and_b32 v121, 0xffff0000, v84\n"
      "v_lshlrev_b32 v122, 16, v85\n"
      "v_mfma_f32_16x16x32_bf16 v[60:63], v[228:231], v[36:39], v[60:63]\n"
      "s_cmp_lt_u32 s66, 64\n"
      "buffer_load_dwordx4 v[98:101], v18, s[36:39], s87 offen sc1\n"
      "v_mfma_f32_16x16x32_bf16 v[60:63], v[240:243], v[40:43], v[60:63]\n"
      "s_cselect_b32 s71, s70, 0x40000000\n"
      "ds_read_b128 v[32:35], v4 offset:3072\n"
      "v_add_f32 v177, v177, v121\n"
      "v_mfma_f32_16x16x32_bf16 v[64:67], v[232:235], v[36:39], v[64:67]\n"
      "s_add_u32 s70, s70, 110592\n"
      "v_add_f32 v178, v178, v122\n"
      "v_and_b32 v123, 0xffff0000, v85\n"
      "v_mfma_f32_16x16x32_bf16 v[64:67], v[244:247], v[40:43], v[64:67]\n"
      "s_waitcnt lgkmcnt(3)\n"
      "v_add_f32 v179, v179, v123\n"
      ".Ldn_e1:\n"
      "v_mfma_f32_16x16x32_bf16 v[44:47], a[0:3], v[20:23], 0\n"
      "s_waitcnt lgkmcnt(2)\n"
      "ds_read_b128 v[36:39], v4 offset:4096\n"
      "v_cvt_pk_bf16_f32 v112, v56, v57\n"
      "v_mfma_f32_16x16x32_bf16 v[44:47], a[12:15], v[24:27], v[44:47]\n"
      "s_add_u32 s67, s66, 4\n"
      "v_cvt_pk_bf16_f32 v113, v58, v59\n"
      "v_mfma_f32_16x16x32_bf16 v[48:51], a[4:7], v[20:23], 0\n"
      "s_min_i32 s67, s67, 64\n"
      "v_cvt_pk_bf16_f32 v114, v60, v61\n"
      "v_cvt_pk_bf16_f32 v115, v62, v63\n"
      "v_mfma_f32_16x16x32_bf16 v[48:51], a[16:19], v[24:27], v[48:51]\n"
      "s_max_i32 s67, s67, 0\n"
      "ds_read_b128 v[40:43], v4 offset:5120\n"
      "v_mfma_f32_16x16x32_bf16 v[52:55], a[8:11], v[20:23], 0\n"
      "s_add_u32 s68, s68, 147456\n"
      "buffer_store_dwordx4 v[112:115], v18, s[36:39], s71 offen sc1\n"
      "v_mfma_f32_16x16x32_bf16 v[52:55], a[20:23], v[24:27], v[52:55]\n"
      "s_waitcnt lgkmcnt(3)\n"
      "v_cvt_pk_bf16_f32 v116, v64, v65\n"
      "v_cvt_pk_bf16_f32 v117, v66, v67\n"
      "v_mfma_f32_16x16x32_bf16 v[44:47], a[24:27], v[28:31], v[44:47]\n"
      "s_waitcnt vmcnt(13) lgkmcnt(2)\n"
      "ds_read_b128 v[20:23], v4 offset:6144\n"
      "v_lshlrev_b32 v120, 16, v86\n"
      "v_mfma_f32_16x16x32_bf16 v[44:47], a[36:39], v[32:35], v[44:47]\n"
      "s_add_u32 s48, s66, 3\n"
      "v_add_f32 v168, v168, v120\n"
      "v_and_b32 v121, 0xffff0000, v86\n"
      "v_mfma_f32_16x16x32_bf16 v[48:51], a[28:31], v[28:31], v[48:51]\n"
      "s_cmp_lt_u32 s48, 64\n"
      "v_add_f32 v169, v169, v121\n"
      "v_lshlrev_b32 v122, 16, v87\n"
      "v_mfma_f32_16x16x32_bf16 v[48:51], a[40:43], v[32:35], v[48:51]\n"
      "s_cselect_b32 s69, s68, 0x40000000\n"
      "ds_read_b128 v[24:27], v4 offset:7168\n"
      "v_add_f32 v170, v170, v122\n"
      "v_mfma_f32_16x16x32_bf16 v[52:55], a[32:35], v[28:31], v[52:55]\n"
      "s_add_u32 s66, s66, 1\n"
      "buffer_store_dwordx2 v[116:117], v118, s[36:39], s71 offen offset:1024 sc1\n"
      "v_mfma_f32_16x16x32_bf16 v[52:55], a[44:47], v[32:35], v[52:55]\n"
      "s_waitcnt lgkmcnt(3)\n"
      "v_and_b32 v123, 0xffff0000, v87\n"
      "v_lshlrev_b32 v120, 16, v88\n"
      "v_mfma_f32_16x16x32_bf16 v[44:47], a[48:51], v[36:39], v[44:47]\n"
      "s_waitcnt lgkmcnt(2)\n"
      "ds_read_b128 v[28:31], v4 offset:8192\n"
      "v_add_f32 v171, v171, v123\n"
      "v_mfma_f32_16x16x32_bf16 v[44:47], a[60:63], v[40:43], v[44:47]\n"
      "s_max_i32 s72, s66, 0\n"
      "v_add_f32 v172, v172, v120\n"
      "v_and_b32 v121, 0xffff0000, v88\n"
      "v_mfma_f32_16x16x32_bf16 v[48:51], a[52:55], v[36:39], v[48:51]\n"
      "v_writelane_b32 v8, s72, 0\n"
      "v_add_f32 v173, v173, v121\n"
      "v_mfma_f32_16x16x32_bf16 v[48:51], a[64:67], v[40:43], v[48:51]\n"
      "ds_read_b128 v[32:35], v4 offset:9216\n"
      "v_lshlrev_b32 v122, 16, v89\n"
      "v_mfma_f32_16x16x32_bf16 v[52:55], a[56:59], v[36:39], v[52:55]\n"
      "buffer_load_dwordx2 v[102:103], v118, s[36:39], s87 offen offset:1024 sc1\n"
      "v_mfma_f32_16x16x32_bf16 v[52:55], a[68:71], v[40:43], v[52:55]\n"
      "s_waitcnt lgkmcnt(3)\n"
      "v_add_f32 v174, v174, v122\n"
      "v_and_b32 v123, 0xffff0000, v89\n"
      "v_mfma_f32_16x16x32_bf16 v[44:47], a[72:75], v[20:23], v[44:47]\n"
      "s_waitcnt lgkmcnt(2)\n"
      "ds_read_b128 v[36:39], v4 offset:10240\n"
      "v_add_f32 v175, v175, v123\n"
      "v_mfma_f32_16x16x32_bf16 v[44:47], a[84:87], v[24:27], v[44:47]\n"
      "s_waitcnt vmcnt(14)\n"
      "s_add_u32 s87, s75, s86\n"
      "v_lshlrev_b32 v120, 16, v90\n"
      "v_mfma_f32_16x16x32_bf16 v[48:51], a[76:79], v[20:23], v[48:51]\n"
      "s_add_u32 m0, s73, 98304\n"
      "buffer_load_dwordx4 v[104:107], v18, s[36:39], s87 offen sc1\n"
      "v_mfma_f32_16x16x32_bf16 v[48:51], a[88:91], v[24:27], v[48:51]\n"
      "ds_read_b128 v[40:43], v4 offset:11264\n"
      "v_add_f32 v176, v176, v120\n"
      "v_mfma_f32_16x16x32_bf16 v[52:55], a[80:83], v[20:23], v[52:55]\n"
      "v_and_b32 v121, 0xffff0000, v90\n"
      "v_lshlrev_b32 v122, 16, v91\n"
      "v_mfma_f32_16x16x32_bf16 v[52:55], a[92:95], v[24:27], v[52:55]\n"
      "s_waitcnt lgkmcnt(3)\n"
      "v_add_f32 v177, v177, v121\n"
      "v_add_f32 v178, v178, v122\n"
      "v_mfma_f32_16x16x32_bf16 v[44:47], a[96:99], v[28:31], v[44:47]\n"
      "s_waitcnt lgkmcnt(2)\n"
      "ds_read_b128 v[20:23], v4 offset:12288\n"
      "v_and_b32 v123, 0xffff0000, v91\n"
      "v_mfma_f32_16x16x32_bf16 v[44:47], a[108:111], v[32:35], v[44:47]\n"
      "buffer_load_dwordx2 v[108:109], v118, s[36:39], s87 offen offset:1024 sc1\n"
      "v_mfma_f32_16x16x32_bf16 v[48:51], a[100:103], v[28:31], v[48:51]\n"
      "s_waitcnt vmcnt(15)\n"
      "v_add_f32 v179, v179, v123\n"
      "v_lshlrev_b32 v120, 16, v92\n"
      "v_mfma_f32_16x16x32_bf16 v[48:51], a[112:115], v[32:35], v[48:51]\n"
      "ds_read_b128 v[24:27], v4 offset:13312\n"
      "v_add_f32 v168, v168, v120\n"
      "v_mfma_f32_16x16x32_bf16 v[52:55], a[104:107], v[28:31], v[52:55]\n"
      "v_and_b32 v121, 0xffff0000, v92\n"
      "v_lshlrev_b32 v122, 16, v93\n"
      "v_mfma_f32_16x16x32_bf16 v[52:55], a[116:119], v[32:35], v[52:55]\n"
      "s_waitcnt lgkmcnt(3)\n"
      "v_add_f32 v169, v169, v121\n"
      "v_add_f32 v170, v170, v122\n"
      "v_mfma_f32_16x16x32_bf16 v[44:47], a[120:123], v[36:39], v[44:47]\n"
      "s_waitcnt lgkmcnt(2)\n"
      "ds_read_b128 v[28:31], v4 offset:14336\n"
      "v_and_b32 v123, 0xffff0000, v93\n"
      "v_mfma_f32_16x16x32_bf16 v[44:47], a[132:135], v[40:43], v[44:47]\n"
      "v_add_f32 v171, v171, v123\n"
      "v_lshlrev_b32 v120, 16, v94\n"
      "v_mfma_f32_16x16x32_bf16 v[48:51], a[124:127], v[36:39], v[48:51]\n"
      "v_add_f32 v172, v172, v120\n"
      "v_and_b32 v121, 0xffff0000, v94\n"
      "v_mfma_f32_16x16x32_bf16 v[48:51], a[136:139], v[40:43], v[48:51]\n"
      "ds_read_b128 v[32:35], v4 offset:15360\n"
      "v_add_f32 v173, v173, v121\n"
      "v_mfma_f32_16x16x32_bf16 v[52:55], a[128:131], v[36:39], v[52:55]\n"
      "v_lshlrev_b32 v122, 16, v95\n"
      "v_and_b32 v123, 0xffff0000, v95\n"
      "v_mfma_f32_16x16x32_bf16 v[52:55], a[140:143], v[40:43], v[52:55]\n"
      "s_waitcnt vmcnt(14) lgkmcnt(3)\n"
      "v_cmp_gt_u32 vcc, s67, v6\n"
      "v_add_f32 v174, v174, v122\n"
      "v_mfma_f32_16x16x32_bf16 v[44:47], a[144:147], v[20:23], v[44:47]\n"
      "s_waitcnt lgkmcnt(2)\n"
      "ds_read_b128 v[36:39], v4 offset:16384\n"
      "v_add_f32 v175, v175, v123\n"
      "v_mfma_f32_16x16x32_bf16 v[44:47], a[156:159], v[24:27], v[44:47]\n"
      "s_cbranch_vccnz .Lsl8\n"
      ".Lbk8:\n"
      "buffer_load_dword v6, v5, s[36:39], 0 offen sc1\n"
      "v_mfma_f32_16x16x32_bf16 v[48:51], a[148:151], v[20:23], v[48:51]\n"
      "v_mfma_f32_16x16x32_bf16 v[48:51], a[160:163], v[24:27], v[48:51]\n"
      "ds_read_b128 v[40:43], v4 offset:17408\n"
      "v_mfma_f32_16x16x32_bf16 v[52:55], a[152:155], v[20:23], v[52:55]\n"
      "buffer_load_dwordx4 v2, s[36:39], s69 offen sc1 lds\n"
      "v_mfma_f32_16x16x32_bf16 v[52:55], a[164:167], v[24:27], v[52:55]\n"
      "s_waitcnt lgkmcnt(3)\n"
      "v_mfma_f32_16x16x32_bf16 v[44:47], a[168:171], v[28:31], v[44:47]\n"
      "s_waitcnt lgkmcnt(2)\n"
      "ds_read_b128 v[20:23], v4 offset:18432\n"
      "v_mfma_f32_16x16x32_bf16 v[44:47], a[180:183], v[32:35], v[44:47]\n"
      "buffer_load_dwordx4 v2, s[36:39], s69 offen offset:1024 sc1 lds\n"
      "v_mfma_f32_16x16x32_bf16 v[48:51], a[172:175], v[28:31], v[48:51]\n"
      "v_mfma_f32_16x16x32_bf16 v[48:51], a[184:187], v[32:35], v[48:51]\n"
      "ds_read_b128 v[24:27], v4 offset:19456\n"
      "v_mfma_f32_16x16x32_bf16 v[52:55], a[176:179], v[28:31], v[52:55]\n"
      "buffer_load_dwordx4 v2, s[36:39], s69 offen offset:2048 sc1 lds\n"
      "v_mfma_f32_16x16x32_bf16 v[52:55], a[188:191], v[32:35], v[52:55]\n"
      "s_waitcnt lgkmcnt(3)\n"
      "v_mfma_f32_16x16x32_bf16 v[44:47], a[192:195], v[36:39], v[44:47]\n"
      "s_waitcnt lgkmcnt(2)\n"
      "ds_read_b128 v[28:31], v4 offset:20480\n"
      "v_mfma_f32_16x16x32_bf16 v[44:47], a[204:207], v[40:43], v[44:47]\n"
      "buffer_load_dwordx4 v2, s[36:39], s69 offen offset:3072 sc1 lds\n"
      "v_mfma_f32_16x16x32_bf16 v[48:51], a[196:199], v[36:39], v[48:51]\n"
      "s_waitcnt vmcnt(11)\n"
      "s_add_u32 m0, s73, 102400\n"
      "v_lshlrev_b32 v120, 16, v96\n"
      "v_mfma_f32_16x16x32_bf16 v[48:51], a[208:211], v[40:43], v[48:51]\n"
      "s_cmp_lt_i32 s66, 63\n"
      "ds_read_b128 v[32:35], v4 offset:21504\n"
      "v_add_f32 v176, v176, v120\n"
      "v_mfma_f32_16x16x32_bf16 v[52:55], a[200:203], v[36:39], v[52:55]\n"
      "buffer_load_dwordx4 v19, s[36:39], s69 offen sc1 lds\n"
      "v_mfma_f32_16x16x32_bf16 v[52:55], a[212:215], v[40:43], v[52:55]\n"
      "s_waitcnt lgkmcnt(3)\n"
      "v_and_b32 v121, 0xffff0000, v96\n"
      "v_lshlrev_b32 v122, 16, v97\n"
      "v_mfma_f32_16x16x32_bf16 v[44:47], a[216:219], v[20:23], v[44:47]\n"
      "s_waitcnt lgkmcnt(2)\n"
      "ds_read_b128 v[36:39], v4 offset:22528\n"
      "v_add_f32 v177, v177, v121\n"
      "v_mfma_f32_16x16x32_bf16 v[44:47], a[228:231], v[24:27], v[44:47]\n"
      "buffer_load_dwordx4 v19, s[36:39], s69 offen offset:1024 sc1 lds\n"
      "v_mfma_f32_16x16x32_bf16 v[48:51], a[220:223], v[20:23], v[48:51]\n"
      "v_add_f32 v178, v178, v122\n"
      "v_and_b32 v123, 0xffff0000, v97\n"
      ".Lmd1:\n"
      "s_cbranch_scc0 .Ldn_t2\n"
      "v_mfma_f32_16x16x32_bf16 v[48:51], a[232:235], v[24:27], v[48:51]\n"
      "s_cmp_lt_u32 s66, 64\n"
      "ds_read_b128 v[40:43], v4 offset:23552\n"
      "v_add_f32 v179, v179, v123\n"
      "v_mfma_f32_16x16x32_bf16 v[52:55], a[224:227], v[20:23], v[52:55]\n"
      "s_waitcnt vmcnt(12)\n"
      "v_lshlrev_b32 v120, 16, v98\n"
      "s_barrier\n"
      "v_mfma_f32_16x16x32_bf16 v[52:55], a[236:239], v[24:27], v[52:55]\n"
      "s_waitcnt vmcnt(10) lgkmcnt(3)\n"
      "buffer_store_dword v8, v7, s[36:39], 0 offen sc1\n"
      "v_mfma_f32_16x16x32_bf16 v[44:47], a[240:243], v[28:31], v[44:47]\n"
      "s_waitcnt lgkmcnt(2)\n"
      "ds_read_b128 v[20:23], v4 offset:24576\n"
      "v_add_f32 v168, v168, v120\n"
      "v_mfma_f32_16x16x32_bf16 v[44:47], a[252:255], v[32:35], v[44:47]\n"
      "s_cselect_b32 s71, s70, 0x40000000\n"
      "v_and_b32 v121, 0xffff0000, v98\n"
      "v_lshlrev_b32 v122, 16, v99\n"
      "v_mfma_f32_16x16x32_bf16 v[48:51], a[244:247], v[28:31], v[48:51]\n"
      "s_add_u32 s70, s70, 110592\n"
      "v_add_f32 v169, v169, v121\n"
      "v_add_f32 v170, v170, v122\n"
      "v_mfma_f32_16x16x32_bf16 v[48:51], v[216:219], v[32:35], v[48:51]\n"
      "ds_read_b128 v[24:27], v4 offset:25600\n"
      "v_and_b32 v123, 0xffff0000, v99\n"
      "v_mfma_f32_16x16x32_bf16 v[52:55], a[248:251], v[28:31], v[52:55]\n"
      "v_add_f32 v171, v171, v123\n"
      "v_lshlrev_b32 v120, 16, v100\n"
      "v_mfma_f32_16x16x32_bf16 v[52:55], v[220:223], v[32:35], v[52:55]\n"
      "s_waitcnt lgkmcnt(3)\n"
      "v_add_f32 v172, v172, v120\n"
      "v_and_b32 v121, 0xffff0000, v100\n"
      "v_mfma_f32_16x16x32_bf16 v[44:47], v[224:227], v[36:39], v[44:47]\n"
      "s_waitcnt lgkmcnt(2)\n"
      "ds_read_b128 v[28:31], v4 offset:26624\n"
      "v_add_f32 v173, v173, v121\n"
      "v_mfma_f32_16x16x32_bf16 v[44:47], v[236:239], v[40:43], v[44:47]\n"
      "v_lshlrev_b32 v122, 16, v101\n"
      "v_and_b32 v123, 0xffff0000, v101\n"
      "v_mfma_f32_16x16x32_bf16 v[48:51], v[228:231], v[36:39], v[48:51]\n"
      "v_add_f32 v174, v174, v122\n"
      "v_add_f32 v175, v175, v123\n"
      "v_mfma_f32_16x16x32_bf16 v[48:51], v[240:243], v[40:43], v[48:51]\n"
      "ds_read_b128 v[32:35], v4 offset:27648\n"
      "v_mfma_f32_16x16x32_bf16 v[52:55], v[232:235], v[36:39], v[52:55]\n"
      "v_mfma_f32_16x16x32_bf16 v[52:55], v[244:247], v[40:43], v[52:55]\n"
      "s_waitcnt lgkmcnt(3)\n"
      ".Ldn_e2:\n"
      "v_mfma_f32_16x16x32_bf16 v[68:71], a[0:3], v[20:23], 0\n"
      "s_waitcnt lgkmcnt(2)\n"
      "ds_read_b128 v[36:39], v4 offset:28672\n"
      "v_cvt_pk_bf16_f32 v112, v44, v45\n"
      "v_mfma_f32_16x16x32_bf16 v[68:71], a[12:15], v[24:27], v[68:71]\n"
      "s_add_u32 s67, s66, 4\n"
      "v_cvt_pk_bf16_f32 v113, v46, v47\n"
      "v_mfma_f32_16x16x32_bf16 v[72:75], a[4:7], v[20:23], 0\n"
      "s_min_i32 s67, s67, 64\n"
      "v_cvt_pk_bf16_f32 v114, v48, v49\n"
      "v_cvt_pk_bf16_f32 v115, v50, v51\n"
      "v_mfma_f32_16x16x32_bf16 v[72:75], a[16:19], v[24:27], v[72:75]\n"
      "s_max_i32 s67, s67, 0\n"
      "ds_read_b128 v[40:43], v4 offset:29696\n"
      "v_mfma_f32_16x16x32_bf16 v[76:79], a[8:11], v[20:23], 0\n"
      "s_add_u32 s68, s68, 147456\n"
      "buffer_store_dwordx4 v[112:115], v18, s[36:39], s71 offen sc1\n"
      "v_mfma_f32_16x16x32_bf16 v[76:79], a[20:23], v[24:27], v[76:79]\n"
      "s_waitcnt lgkmcnt(3)\n"
      "v_cvt_pk_bf16_f32 v116, v52, v53\n"
      "v_cvt_pk_bf16_f32 v117, v54, v55\n"
      "v_mfma_f32_16x16x32_bf16 v[68:71], a[24:27], v[28:31], v[68:71]\n"
      "s_waitcnt vmcnt(11) lgkmcnt(2)\n"
      "ds_read_b128 v[20:23], v4 offset:30720\n"
      "v_lshlrev_b32 v120, 16, v102\n"
      "v_mfma_f32_16x16x32_bf16 v[68:71], a[36:39], v[32:35], v[68:71]\n"
      "s_add_u32 s48, s66, 3\n"
      "v_add_f32 v176, v176, v120\n"
      "v_and_b32 v121, 0xffff0000, v102\n"
      "v_mfma_f32_16x16x32_bf16 v[72:75], a[28:31], v[28:31], v[72:75]\n"
      "s_cmp_lt_u32 s48, 64\n"
      "v_add_f32 v177, v177, v121\n"
      "v_lshlrev_b32 v122, 16, v103\n"
      "v_mfma_f32_16x16x32_bf16 v[72:75], a[40:43], v[32:35], v[72:75]\n"
      "s_cselect_b32 s69, s68, 0x40000000\n"
      "ds_read_b128 v[24:27], v4 offset:31744\n"
      "v_add_f32 v178, v178, v122\n"
      "v_mfma_f32_16x16x32_bf16 v[76:79], a[32:35], v[28:31], v[76:79]\n"
      "s_add_u32 s66, s66, 1\n"
      "buffer_store_dwordx2 v[116:117], v118, s[36:39], s71 offen offset:1024 sc1\n"
      "v_mfma_f32_16x16x32_bf16 v[76:79], a[44:47], v[32:35], v[76:79]\n"
      "s_waitcnt vmcnt(11) lgkmcnt(3)\n"
      "v_and_b32 v123, 0xffff0000, v103\n"
      "v_lshlrev_b32 v120, 16, v104\n"
      "v_mfma_f32_16x16x32_bf16 v[68:71], a[48:51], v[36:39], v[68:71]\n"
      "s_waitcnt lgkmcnt(2)\n"
      "ds_read_b128 v[28:31], v4 offset:32768\n"
      "v_add_f32 v179, v179, v123\n"
      "v_mfma_f32_16x16x32_bf16 v[68:71], a[60:63], v[40:43], v[68:71]\n"
      "s_max_i32 s72, s66, 0\n"
      "v_add_f32 v168, v168, v120\n"
      "v_and_b32 v121, 0xffff0000, v104\n"
      "v_mfma_f32_16x16x32_bf16 v[72:75], a[52:55], v[36:39], v[72:75]\n"
      "s_add_u32 m0, s73, 122880\n"
      "v_writelane_b32 v8, s72, 0\n"
      "v_add_f32 v169, v169, v121\n"
      "v_mfma_f32_16x16x32_bf16 v[72:75], a[64:67], v[40:43], v[72:75]\n"
      "ds_read_b128 v[32:35], v4 offset:33792\n"
      "v_lshlrev_b32 v122, 16, v105\n"
      "v_mfma_f32_16x16x32_bf16 v[76:79], a[56:59], v[36:39], v[76:79]\n"
      "v_add_f32 v170, v170, v122\n"
      "v_and_b32 v123, 0xffff0000, v105\n"
      "v_mfma_f32_16x16x32_bf16 v[76:79], a[68:71], v[40:43], v[76:79]\n"
      "s_waitcnt lgkmcnt(3)\n"
      "v_add_f32 v171, v171, v123\n"
      "v_lshlrev_b32 v120, 16, v106\n"
      "v_mfma_f32_16x16x32_bf16 v[68:71], a[72:75], v[20:23], v[68:71]\n"
      "s_waitcnt lgkmcnt(2)\n"
      "ds_read_b128 v[36:39], v4 offset:34816\n"
      "v_add_f32 v172, v172, v120\n"
      "v_mfma_f32_16x16x32_bf16 v[68:71], a[84:87], v[24:27], v[68:71]\n"
      "v_and_b32 v121, 0xffff0000, v106\n"
      "v_lshlrev_b32 v122, 16, v107\n"
      "v_mfma_f32_16x16x32_bf16 v[72:75], a[76:79], v[20:23], v[72:75]\n"
      "v_add_f32 v173, v173, v121\n"
      "v_add_f32 v174, v174, v122\n"
      "v_mfma_f32_16x16x32_bf16 v[72:75], a[88:91], v[24:27], v[72:75]\n"
      "ds_read_b128 v[40:43], v4 offset:35840\n"
      "v_and_b32 v123, 0xffff0000, v107\n"
      "v_mfma_f32_16x16x32_bf16 v[76:79], a[80:83], v[20:23], v[76:79]\n"
      "s_waitcnt vmcnt(10)\n"
      "v_add_f32 v175, v175, v123\n"
      "v_lshlrev_b32 v120, 16, v108\n"
      "v_mfma_f32_16x16x32_bf16 v[76:79], a[92:95], v[24:27], v[76:79]\n"
      "s_waitcnt lgkmcnt(3)\n"
      "v_add_f32 v176, v176, v120\n"
      "v_and_b32 v121, 0xffff0000, v108\n"
      "v_mfma_f32_16x16x32_bf16 v[68:71], a[96:99], v[28:31], v[68:71]\n"
      "s_waitcnt lgkmcnt(2)\n"
      "ds_read_b128 v[20:23], v4 offset:36864\n"
      "v_add_f32 v177, v177, v121\n"
      "v_mfma_f32_16x16x32_bf16 v[68:71], a[108:111], v[32:35], v[68:71]\n"
      "v_lshlrev_b32 v122, 16, v109\n"
      "v_and_b32 v123, 0xffff0000, v109\n"
      "v_mfma_f32_16x16x32_bf16 v[72:75], a[100:103], v[28:31], v[72:75]\n"
      "v_add_f32 v178, v178, v122\n"
      "v_add_f32 v179, v179, v123\n"
      "v_mfma_f32_16x16x32_bf16 v[72:75], a[112:115], v[32:35], v[72:75]\n"
      "ds_read_b128 v[24:27], v4 offset:37888\n"
      "v_cvt_pk_bf16_f32 v146, v168, v169\n"
      "v_mfma_f32_16x16x32_bf16 v[76:79], a[104:107], v[28:31], v[76:79]\n"
      "v_cvt_pk_bf16_f32 v147, v170, v171\n"
      "v_cvt_pk_bf16_f32 v148, v172, v173\n"
      "v_mfma_f32_16x16x32_bf16 v[76:79], a[116:119], v[32:35], v[76:79]\n"
      "s_waitcnt lgkmcnt(3)\n"
      "v_cvt_pk_bf16_f32 v149, v174, v175\n"
      "v_cvt_pk_bf16_f32 v150, v176, v177\n"
      "v_mfma_f32_16x16x32_bf16 v[68:71], a[120:123], v[36:39], v[68:71]\n"
      "s_waitcnt lgkmcnt(2)\n"
      "ds_read_b128 v[28:31], v4 offset:38912\n"
      "v_cvt_pk_bf16_f32 v151, v178, v179\n"
      "v_mfma_f32_16x16x32_bf16 v[68:71], a[132:135], v[40:43], v[68:71]\n"
      "v_mul_f32 v153, v168, v168\n"
      "v_add_f32 v152, v168, v169\n"
      "v_mfma_f32_16x16x32_bf16 v[72:75], a[124:127], v[36:39], v[72:75]\n"
      "v_fmac_f32 v153, v169, v169\n"
      "v_add_f32 v152, v152, v170\n"
      "v_mfma_f32_16x16x32_bf16 v[72:75], a[136:139], v[40:43], v[72:75]\n"
      "ds_read_b128 v[32:35], v4 offset:39936\n"
      "v_fmac_f32 v153, v170, v170\n"
      "v_mfma_f32_16x16x32_bf16 v[76:79], a[128:131], v[36:39], v[76:79]\n"
      "v_add_f32 v152, v152, v171\n"
      "v_fmac_f32 v153, v171, v171\n"
      "v_mfma_f32_16x16x32_bf16 v[76:79], a[140:143], v[40:43], v[76:79]\n"
      "s_waitcnt vmcnt(9) lgkmcnt(3)\n"
      "v_cmp_gt_u32 vcc, s67, v6\n"
      "v_add_f32 v152, v152, v172\n"
      "v_mfma_f32_16x16x32_bf16 v[68:71], a[144:147], v[20:23], v[68:71]\n"
      "s_waitcnt lgkmcnt(2)\n"
      "ds_read_b128 v[36:39], v4 offset:40960\n"
      "v_fmac_f32 v153, v172, v172\n"
      "v_mfma_f32_16x16x32_bf16 v[68:71], a[156:159], v[24:27], v[68:71]\n"
      "s_cbranch_vccnz .Lsl9\n"
      ".Lbk9:\n"
      "buffer_load_dword v6, v5, s[36:39], 0 offen sc1\n"
      "v_mfma_f32_16x16x32_bf16 v[72:75], a[148:151], v[20:23], v[72:75]\n"
      "v_add_f32 v152, v152, v173\n"
      "v_fmac_f32 v153, v173, v173\n"
      "v_mfma_f32_16x16x32_bf16 v[72:75], a[160:163], v[24:27], v[72:75]\n"
      "ds_read_b128 v[40:43], v4 offset:41984\n"
      "v_add_f32 v152, v152, v174\n"
      "v_mfma_f32_16x16x32_bf16 v[76:79], a[152:155], v[20:23], v[76:79]\n"
      "buffer_load_dwordx4 v2, s[36:39], s69 offen sc1 lds\n"
      "v_mfma_f32_16x16x32_bf16 v[76:79], a[164:167], v[24:27], v[76:79]\n"
      "s_waitcnt lgkmcnt(3)\n"
      "v_fmac_f32 v153, v174, v174\n"
      "v_add_f32 v152, v152, v175\n"
      "v_mfma_f32_16x16x32_bf16 v[68:71], a[168:171], v[28:31], v[68:71]\n"
      "s_waitcnt lgkmcnt(2)\n"
      "ds_read_b128 v[20:23], v4 offset:43008\n"
      "v_fmac_f32 v153, v175, v175\n"
      "v_mfma_f32_16x16x32_bf16 v[68:71], a[180:183], v[32:35], v[68:71]\n"
      "buffer_load_dwordx4 v2, s[36:39], s69 offen offset:1024 sc1 lds\n"
      "v_mfma_f32_16x16x32_bf16 v[72:75], a[172:175], v[28:31], v[72:75]\n"
      "v_add_f32 v152, v152, v176\n"
      "v_fmac_f32 v153, v176, v176\n"
      "v_mfma_f32_16x16x32_bf16 v[72:75], a[184:187], v[32:35], v[72:75]\n"
      "ds_read_b128 v[24:27], v4 offset:44032\n"
      "v_add_f32 v152, v152, v177\n"
      "v_mfma_f32_16x16x32_bf16 v[76:79], a[176:179], v[28:31], v[76:79]\n"
      "buffer_load_dwordx4 v2, s[36:39], s69 offen offset:2048 sc1 lds\n"
      "v_mfma_f32_16x16x32_bf16 v[76:79], a[188:191], v[32:35], v[76:79]\n"
      "s_waitcnt lgkmcnt(3)\n"
      "v_fmac_f32 v153, v177, v177\n"
      "v_add_f32 v152, v152, v178\n"
      "v_mfma_f32_16x16x32_bf16 v[68:71], a[192:195], v[36:39], v[68:71]\n"
      "s_waitcnt lgkmcnt(2)\n"
      "ds_read_b128 v[28:31], v4 offset:45056\n"
      "v_fmac_f32 v153, v178, v178\n"
      "v_mfma_f32_16x16x32_bf16 v[68:71], a[204:207], v[40:43], v[68:71]\n"
      "buffer_load_dwordx4 v2, s[36:39], s69 offen offset:3072 sc1 lds\n"
      "v_mfma_f32_16x16x32_bf16 v[72:75], a[196:199], v[36:39], v[72:75]\n"
      "s_add_u32 m0, s73, 126976\n"
      "v_add_f32 v152, v152, v179\n"
      "v_fmac_f32 v153, v179, v179\n"
      "v_mfma_f32_16x16x32_bf16 v[72:75], a[208:211], v[40:43], v[72:75]\n"
      "s_cmp_lt_i32 s66, 63\n"
      "ds_read_b128 v[32:35], v4 offset:46080\n"
      "v_permlane32_swap_b32 v152, v153\n"
      "v_mfma_f32_16x16x32_bf16 v[76:79], a[200:203], v[36:39], v[76:79]\n"
      "buffer_load_dwordx4 v19, s[36:39], s69 offen sc1 lds\n"
      "v_mfma_f32_16x16x32_bf16 v[76:79], a[212:215], v[40:43], v[76:79]\n"
      "s_waitcnt lgkmcnt(3)\n"
      "v_add_f32 v152, v152, v153\n"
      "v_mfma_f32_16x16x32_bf16 v[68:71], a[216:219], v[20:23], v[68:71]\n"
      "s_waitcnt lgkmcnt(2)\n"
      "ds_read_b128 v[36:39], v4 offset:47104\n"
      "v_mov_b32 v154, v152\n"
      "v_mfma_f32_16x16x32_bf16 v[68:71], a[228:231], v[24:27], v[68:71]\n"
      "buffer_load_dwordx4 v19, s[36:39], s69 offen offset:1024 sc1 lds\n"
      "v_mfma_f32_16x16x32_bf16 v[72:75], a[220:223], v[20:23], v[72:75]\n"
      "v_permlane16_swap_b32 v152, v154\n"
      ".Lmd2:\n"
      "s_cbranch_scc0 .Ldn_t3\n"
      "v_mfma_f32_16x16x32_bf16 v[72:75], a[232:235], v[24:27], v[72:75]\n"
      "s_cmp_lt_u32 s66, 64\n"
      "ds_read_b128 v[40:43], v4 offset:48128\n"
      "v_add_f32 v152, v152, v154\n"
      "v_mfma_f32_16x16x32_bf16 v[76:79], a[224:227], v[20:23], v[76:79]\n"
      "s_waitcnt vmcnt(10)\n"
      "s_barrier\n"
      "v_mfma_f32_16x16x32_bf16 v[76:79], a[236:239], v[24:27], v[76:79]\n"
      "s_waitcnt vmcnt(7) lgkmcnt(3)\n"
      "buffer_store_dword v8, v7, s[36:39], 0 offen sc1\n"
      "v_mfma_f32_16x16x32_bf16 v[68:71], a[240:243], v[28:31], v[68:71]\n"
      "s_waitcnt lgkmcnt(2)\n"
      "ds_read_b128 v[20:23], v119 offset:0\n"
      "v_mfma_f32_16x16x32_bf16 v[68:71], a[252:255], v[32:35], v[68:71]\n"
      "s_cselect_b32 s71, s70, 0x40000000\n"
      "buffer_store_dwordx4 v[146:149], v12, s[36:39], s77 offen sc1\n"
      "v_mfma_f32_16x16x32_bf16 v[72:75], a[244:247], v[28:31], v[72:75]\n"
      "s_add_u32 s70, s70, 110592\n"
      "v_mfma_f32_16x16x32_bf16 v[72:75], v[216:219], v[32:35], v[72:75]\n"
      "s_mov_b32 s75, s71\n"
      "ds_read_b128 v[24:27], v119 offset:1024\n"
      "v_mfma_f32_16x16x32_bf16 v[76:79], a[248:251], v[28:31], v[76:79]\n"
      "s_mov_b32 s88, s66\n"
      "v_mfma_f32_16x16x32_bf16 v[76:79], v[220:223], v[32:35], v[76:79]\n"
      "s_waitcnt lgkmcnt(3)\n"
      "v_mfma_f32_16x16x32_bf16 v[68:71], v[224:227], v[36:39], v[68:71]\n"
      "s_waitcnt lgkmcnt(2)\n"
      "ds_read_b128 v[28:31], v119 offset:2048\n"
      "v_mfma_f32_16x16x32_bf16 v[68:71], v[236:239], v[40:43], v[68:71]\n"
      "buffer_store_dwordx2 v[150:151], v11, s[36:39], s77 offen sc1\n"
      "v_mfma_f32_16x16x32_bf16 v[72:75], v[228:231], v[36:39], v[72:75]\n"
      "v_mfma_f32_16x16x32_bf16 v[72:75], v[240:243], v[40:43], v[72:75]\n"
      "ds_read_b128 v[32:35], v119 offset:3072\n"
      "v_mfma_f32_16x16x32_bf16 v[76:79], v[232:235], v[36:39], v[76:79]\n"
      "buffer_store_dword v152, v15, s[36:39], s77 offen sc1\n"
      "v_mfma_f32_16x16x32_bf16 v[76:79], v[244:247], v[40:43], v[76:79]\n"
      "s_waitcnt lgkmcnt(3)\n"
      ".Ldn_e3:\n"
      "v_mfma_f32_16x16x32_bf16 v[44:47], a[0:3], v[20:23], 0\n"
      "s_waitcnt lgkmcnt(2)\n"
      "ds_read_b128 v[36:39], v119 offset:4096\n"
      "v_mfma_f32_16x16x32_bf16 v[44:47], a[12:15], v[24:27], v[44:47]\n"
      "s_add_u32 s67, s66, 4\n"
      "buffer_store_dwordx4 v[146:149], v13, s[40:43], s78 offen\n"
      "v_mfma_f32_16x16x32_bf16 v[48:51], a[4:7], v[20:23], 0\n"
      "s_min_i32 s67, s67, 64\n"
      "v_mfma_f32_16x16x32_bf16 v[48:51], a[16:19], v[24:27], v[48:51]\n"
      "s_max_i32 s67, s67, 0\n"
      "ds_read_b128 v[40:43], v119 offset:5120\n"
      "v_mfma_f32_16x16x32_bf16 v[52:55], a[8:11], v[20:23], 0\n"
      "s_add_u32 s68, s68, 147456\n"
      "v_mfma_f32_16x16x32_bf16 v[52:55], a[20:23], v[24:27], v[52:55]\n"
      "s_waitcnt lgkmcnt(3)\n"
      "v_mfma_f32_16x16x32_bf16 v[44:47], a[24:27], v[28:31], v[44:47]\n"
      "s_waitcnt lgkmcnt(2)\n"
      "ds_read_b128 v[20:23], v119 offset:6144\n"
      "v_mfma_f32_16x16x32_bf16 v[44:47], a[36:39], v[32:35], v[44:47]\n"
      "s_add_u32 s48, s66, 3\n"
      "buffer_store_dwordx2 v[150:151], v14, s[40:43], s78 offen\n"
      "v_mfma_f32_16x16x32_bf16 v[48:51], a[28:31], v[28:31], v[48:51]\n"
      "s_cmp_lt_u32 s48, 64\n"
      "v_mfma_f32_16x16x32_bf16 v[48:51], a[40:43], v[32:35], v[48:51]\n"
      "s_cselect_b32 s69, s68, 0x40000000\n"
      "ds_read_b128 v[24:27], v119 offset:7168\n"
      "v_mfma_f32_16x16x32_bf16 v[52:55], a[32:35], v[28:31], v[52:55]\n"
      "s_add_u32 s66, s66, 1\n"
      "v_mfma_f32_16x16x32_bf16 v[52:55], a[44:47], v[32:35], v[52:55]\n"
      "s_waitcnt lgkmcnt(3)\n"
      "v_mfma_f32_16x16x32_bf16 v[44:47], a[48:51], v[36:39], v[44:47]\n"
      "s_waitcnt lgkmcnt(2)\n"
      "ds_read_b128 v[28:31], v119 offset:8192\n"
      "v_mfma_f32_16x16x32_bf16 v[44:47], a[60:63], v[40:43], v[44:47]\n"
      "s_max_i32 s72, s66, 0\n"
      "v_mfma_f32_16x16x32_bf16 v[48:51], a[52:55], v[36:39], v[48:51]\n"
      "s_add_u32 m0, s73, 0\n"
      "v_writelane_b32 v8, s72, 0\n"
      "v_mfma_f32_16x16x32_bf16 v[48:51], a[64:67], v[40:43], v[48:51]\n"
      "ds_read_b128 v[32:35], v119 offset:9216\n"
      "v_mfma_f32_16x16x32_bf16 v[52:55], a[56:59], v[36:39], v[52:55]\n"
      "v_mfma_f32_16x16x32_bf16 v[52:55], a[68:71], v[40:43], v[52:55]\n"
      "s_waitcnt lgkmcnt(3)\n"
      "v_mfma_f32_16x16x32_bf16 v[44:47], a[72:75], v[20:23], v[44:47]\n"
      "s_waitcnt lgkmcnt(2)\n"
      "ds_read_b128 v[36:39], v119 offset:10240\n"
      "v_mfma_f32_16x16x32_bf16 v[44:47], a[84:87], v[24:27], v[44:47]\n"
      "v_mfma_f32_16x16x32_bf16 v[48:51], a[76:79], v[20:23], v[48:51]\n"
      "v_mfma_f32_16x16x32_bf16 v[48:51], a[88:91], v[24:27], v[48:51]\n"
      "ds_read_b128 v[40:43], v119 offset:11264\n"
      "v_mfma_f32_16x16x32_bf16 v[52:55], a[80:83], v[20:23], v[52:55]\n"
      "v_mfma_f32_16x16x32_bf16 v[52:55], a[92:95], v[24:27], v[52:55]\n"
      "s_waitcnt lgkmcnt(3)\n"
      "v_mfma_f32_16x16x32_bf16 v[44:47], a[96:99], v[28:31], v[44:47]\n"
      "s_waitcnt lgkmcnt(2)\n"
      "ds_read_b128 v[20:23], v119 offset:12288\n"
      "v_mfma_f32_16x16x32_bf16 v[44:47], a[108:111], v[32:35], v[44:47]\n"
      "v_mfma_f32_16x16x32_bf16 v[48:51], a[100:103], v[28:31], v[48:51]\n"
      "v_mfma_f32_16x16x32_bf16 v[48:51], a[112:115], v[32:35], v[48:51]\n"
      "ds_read_b128 v[24:27], v119 offset:13312\n"
      "v_mfma_f32_16x16x32_bf16 v[52:55], a[104:107], v[28:31], v[52:55]\n"
      "v_mfma_f32_16x16x32_bf16 v[52:55], a[116:119], v[32:35], v[52:55]\n"
      "s_waitcnt lgkmcnt(3)\n"
      "v_mfma_f32_16x16x32_bf16 v[44:47], a[120:123], v[36:39], v[44:47]\n"
      "s_waitcnt lgkmcnt(2)\n"
      "ds_read_b128 v[28:31], v119 offset:14336\n"
      "v_mfma_f32_16x16x32_bf16 v[44:47], a[132:135], v[40:43], v[44:47]\n"
      "v_mfma_f32_16x16x32_bf16 v[48:51], a[124:127], v[36:39], v[48:51]\n"
      "v_mfma_f32_16x16x32_bf16 v[48:51], a[136:139], v[40:43], v[48:51]\n"
      "ds_read_b128 v[32:35], v119 offset:15360\n"
      "v_mfma_f32_16x16x32_bf16 v[52:55], a[128:131], v[36:39], v[52:55]\n"
      "v_mfma_f32_16x16x32_bf16 v[52:55], a[140:143], v[40:43], v[52:55]\n"
      "s_waitcnt vmcnt(12) lgkmcnt(3)\n"
      "v_cmp_gt_u32 vcc, s67, v6\n"
      "v_mfma_f32_16x16x32_bf16 v[44:47], a[144:147], v[20:23], v[44:47]\n"
      "s_waitcnt lgkmcnt(2)\n"
      "ds_read_b128 v[36:39], v119 offset:16384\n"
      "v_mfma_f32_16x16x32_bf16 v[44:47], a[156:159], v[24:27], v[44:47]\n"
      "s_cbranch_vccnz .Lsl10\n"
      ".Lbk10:\n"
      "buffer_load_dword v6, v5, s[36:39], 0 offen sc1\n"
      "v_mfma_f32_16x16x32_bf16 v[48:51], a[148:151], v[20:23], v[48:51]\n"
      "s_add_u32 s48, s88, 1\n"
      "v_mfma_f32_16x16x32_bf16 v[48:51], a[160:163], v[24:27], v[48:51]\n"
      "s_cmp_lt_u32 s88, 64\n"
      "ds_read_b128 v[40:43], v119 offset:17408\n"
      "v_mfma_f32_16x16x32_bf16 v[52:55], a[152:155], v[20:23], v[52:55]\n"
      "s_cselect_b32 s74, s48, 0\n"
      "buffer_load_dwordx4 v2, s[36:39], s69 offen sc1 lds\n"
      "v_mfma_f32_16x16x32_bf16 v[52:55], a[164:167], v[24:27], v[52:55]\n"
      "s_waitcnt lgkmcnt(3)\n"
      "v_mfma_f32_16x16x32_bf16 v[44:47], a[168:171], v[28:31], v[44:47]\n"
      "s_waitcnt lgkmcnt(2)\n"
      "ds_read_b128 v[20:23], v119 offset:18432\n"
      "v_mfma_f32_16x16x32_bf16 v[44:47], a[180:183], v[32:35], v[44:47]\n"
      "s_mul_i32 s48, s29, 65\n"
      "buffer_load_dwordx4 v2, s[36:39], s69 offen offset:1024 sc1 lds\n"
      "v_mfma_f32_16x16x32_bf16 v[48:51], a[172:175], v[28:31], v[48:51]\n"
      "s_add_u32 s48, s48, s88\n"
      "v_mfma_f32_16x16x32_bf16 v[48:51], a[184:187], v[32:35], v[48:51]\n"
      "s_mul_i32 s48, s48, 20480\n"
      "ds_read_b128 v[24:27], v119 offset:19456\n"
      "v_mfma_f32_16x16x32_bf16 v[52:55], a[176:179], v[28:31], v[52:55]\n"
      "s_add_u32 s48, s48, 10526720\n"
      "buffer_load_dwordx4 v2, s[36:39], s69 offen offset:2048 sc1 lds\n"
      "v_mfma_f32_16x16x32_bf16 v[52:55], a[188:191], v[32:35], v[52:55]\n"
      "s_waitcnt lgkmcnt(3)\n"
      "v_mfma_f32_16x16x32_bf16 v[44:47], a[192:195], v[36:39], v[44:47]\n"
      "s_waitcnt lgkmcnt(2)\n"
      "ds_read_b128 v[28:31], v119 offset:20480\n"
      "v_mfma_f32_16x16x32_bf16 v[44:47], a[204:207], v[40:43], v[44:47]\n"
      "s_cmp_lt_u32 s88, 64\n"
      "buffer_load_dwordx4 v2, s[36:39], s69 offen offset:3072 sc1 lds\n"
      "v_mfma_f32_16x16x32_bf16 v[48:51], a[196:199], v[36:39], v[48:51]\n"
      "s_cselect_b32 s76, s48, 0x40000000\n"
      "v_mfma_f32_16x16x32_bf16 v[48:51], a[208:211], v[40:43], v[48:51]\n"
      "s_add_u32 m0, s73, 4096\n"
      "ds_read_b128 v[32:35], v119 offset:21504\n"
      "v_mfma_f32_16x16x32_bf16 v[52:55], a[200:203], v[36:39], v[52:55]\n"
      "s_cmp_lt_i32 s66, 63\n"
      "buffer_load_dwordx4 v19, s[36:39], s69 offen sc1 lds\n"
      "v_mfma_f32_16x16x32_bf16 v[52:55], a[212:215], v[40:43], v[52:55]\n"
      "s_waitcnt vmcnt(8) lgkmcnt(3)\n"
      "buffer_store_dword v8, v16, s[36:39], s79 offen sc1\n"
      "v_mfma_f32_16x16x32_bf16 v[44:47], a[216:219], v[20:23], v[44:47]\n"
      "s_waitcnt lgkmcnt(2)\n"
      "ds_read_b128 v[36:39], v119 offset:22528\n"
      "v_mfma_f32_16x16x32_bf16 v[44:47], a[228:231], v[24:27], v[44:47]\n"
      "v_mfma_f32_16x16x32_bf16 v[48:51], a[220:223], v[20:23], v[48:51]\n"
      "buffer_load_dwordx4 v19, s[36:39], s69 offen offset:1024 sc1 lds\n"
      ".Lmd3:\n"
      "s_cbranch_scc0 .Ldn_t4\n"
      "v_mfma_f32_16x16x32_bf16 v[48:51], a[232:235], v[24:27], v[48:51]\n"
      "s_cmp_lt_u32 s66, 64\n"
      "ds_read_b128 v[40:43], v119 offset:23552\n"
      "v_mfma_f32_16x16x32_bf16 v[52:55], a[224:227], v[20:23], v[52:55]\n"
      "s_barrier\n"
      "v_mfma_f32_16x16x32_bf16 v[52:55], a[236:239], v[24:27], v[52:55]\n"
      "s_waitcnt lgkmcnt(3)\n"
      "buffer_store_dword v8, v7, s[36:39], 0 offen sc1\n"
      "v_mfma_f32_16x16x32_bf16 v[44:47], a[240:243], v[28:31], v[44:47]\n"
      "s_waitcnt lgkmcnt(2)\n"
      "ds_read_b128 v[20:23], v119 offset:24576\n"
      "v_mfma_f32_16x16x32_bf16 v[44:47], a[252:255], v[32:35], v[44:47]\n"
      "s_cselect_b32 s71, s70, 0x40000000\n"
      "v_mfma_f32_16x16x32_bf16 v[48:51], a[244:247], v[28:31], v[48:51]\n"
      "s_add_u32 s70, s70, 110592\n"
      "v_mfma_f32_16x16x32_bf16 v[48:51], v[216:219], v[32:35], v[48:51]\n"
      "ds_read_b128 v[24:27], v119 offset:25600\n"
      "v_mfma_f32_16x16x32_bf16 v[52:55], a[248:251], v[28:31], v[52:55]\n"
      "v_mfma_f32_16x16x32_bf16 v[52:55], v[220:223], v[32:35], v[52:55]\n"
      "s_waitcnt lgkmcnt(3)\n"
      "v_mfma_f32_16x16x32_bf16 v[44:47], v[224:227], v[36:39], v[44:47]\n"
      "s_waitcnt lgkmcnt(2)\n"
      "ds_read_b128 v[28:31], v119 offset:26624\n"
      "v_mfma_f32_16x16x32_bf16 v[44:47], v[236:239], v[40:43], v[44:47]\n"
      "v_mfma_f32_16x16x32_bf16 v[48:51], v[228:231], v[36:39], v[48:51]\n"
      "v_mfma_f32_16x16x32_bf16 v[48:51], v[240:243], v[40:43], v[48:51]\n"
      "ds_read_b128 v[32:35], v119 offset:27648\n"
      "v_mfma_f32_16x16x32_bf16 v[52:55], v[232:235], v[36:39], v[52:55]\n"
      "v_mfma_f32_16x16x32_bf16 v[52:55], v[244:247], v[40:43], v[52:55]\n"
      "s_waitcnt lgkmcnt(3)\n"
      ".Ldn_e4:\n"
      "v_mfma_f32_16x16x32_bf16 v[56:59], a[0:3], v[20:23], 0\n"
      "s_waitcnt lgkmcnt(2)\n"
      "ds_read_b128 v[36:39], v119 offset:28672\n"
      "v_cvt_pk_bf16_f32 v112, v44, v45\n"
      "v_mfma_f32_16x16x32_bf16 v[56:59], a[12:15], v[24:27], v[56:59]\n"
      "s_add_u32 s67, s66, 4\n"
      "v_cvt_pk_bf16_f32 v113, v46, v47\n"
      "v_mfma_f32_16x16x32_bf16 v[60:63], a[4:7], v[20:23], 0\n"
      "s_min_i32 s67, s67, 64\n"
      "v_cvt_pk_bf16_f32 v114, v48, v49\n"
      "v_cvt_pk_bf16_f32 v115, v50, v51\n"
      "v_mfma_f32_16x16x32_bf16 v[60:63], a[16:19], v[24:27], v[60:63]\n"
      "s_max_i32 s67, s67, 0\n"
      "ds_read_b128 v[40:43], v119 offset:29696\n"
      "v_mfma_f32_16x16x32_bf16 v[64:67], a[8:11], v[20:23], 0\n"
      "s_add_u32 s68, s68, 147456\n"
      "buffer_store_dwordx4 v[112:115], v18, s[36:39], s71 offen sc1\n"
      "v_mfma_f32_16x16x32_bf16 v[64:67], a[20:23], v[24:27], v[64:67]\n"
      "s_waitcnt lgkmcnt(3)\n"
      "v_cvt_pk_bf16_f32 v116, v52, v53\n"
      "v_cvt_pk_bf16_f32 v117, v54, v55\n"
      "v_mfma_f32_16x16x32_bf16 v[56:59], a[24:27], v[28:31], v[56:59]\n"
      "s_waitcnt lgkmcnt(2)\n"
      "ds_read_b128 v[20:23], v119 offset:30720\n"
      "v_mfma_f32_16x16x32_bf16 v[56:59], a[36:39], v[32:35], v[56:59]\n"
      "s_add_u32 s48, s66, 3\n"
      "v_mfma_f32_16x16x32_bf16 v[60:63], a[28:31], v[28:31], v[60:63]\n"
      "s_cmp_lt_u32 s48, 64\n"
      "v_mfma_f32_16x16x32_bf16 v[60:63], a[40:43], v[32:35], v[60:63]\n"
      "s_cselect_b32 s69, s68, 0x40000000\n"
      "ds_read_b128 v[24:27], v119 offset:31744\n"
      "v_mfma_f32_16x16x32_bf16 v[64:67], a[32:35], v[28:31], v[64:67]\n"
      "s_add_u32 s66, s66, 1\n"
      "buffer_store_dwordx2 v[116:117], v118, s[36:39], s71 offen offset:1024 sc1\n"
      "v_mfma_f32_16x16x32_bf16 v[64:67], a[44:47], v[32:35], v[64:67]\n"
      "s_waitcnt lgkmcnt(3)\n"
      "v_mfma_f32_16x16x32_bf16 v[56:59], a[48:51], v[36:39], v[56:59]\n"
      "s_waitcnt lgkmcnt(2)\n"
      "ds_read_b128 v[28:31], v119 offset:32768\n"
      "v_mfma_f32_16x16x32_bf16 v[56:59], a[60:63], v[40:43], v[56:59]\n"
      "s_max_i32 s72, s66, 0\n"
      "v_mfma_f32_16x16x32_bf16 v[60:63], a[52:55], v[36:39], v[60:63]\n"
      "s_add_u32 s48, s81, s88\n"
      "v_writelane_b32 v8, s72, 0\n"
      "v_mfma_f32_16x16x32_bf16 v[60:63], a[64:67], v[40:43], v[60:63]\n"
      "s_sub_u32 s48, 159, s48\n"
      "ds_read_b128 v[32:35], v119 offset:33792\n"
      "v_mfma_f32_16x16x32_bf16 v[64:67], a[56:59], v[36:39], v[64:67]\n"
      "s_mul_i32 s48, s48, 18432\n"
      "buffer_load_dwordx4 v[140:143], v12, s[36:39], s76 offen sc1\n"
      "v_mfma_f32_16x16x32_bf16 v[64:67], a[68:71], v[40:43], v[64:67]\n"
      "s_waitcnt lgkmcnt(3)\n"
      "v_mfma_f32_16x16x32_bf16 v[56:59], a[72:75], v[20:23], v[56:59]\n"
      "s_waitcnt lgkmcnt(2)\n"
      "ds_read_b128 v[36:39], v119 offset:34816\n"
      "v_mfma_f32_16x16x32_bf16 v[56:59], a[84:87], v[24:27], v[56:59]\n"
      "s_sub_u32 s49, s88, 32\n"
      "v_mfma_f32_16x16x32_bf16 v[60:63], a[76:79], v[20:23], v[60:63]\n"
      "s_cmp_lt_u32 s49, 32\n"
      "buffer_load_dwordx2 v[144:145], v11, s[36:39], s76 offen sc1\n"
      "v_mfma_f32_16x16x32_bf16 v[60:63], a[88:91], v[24:27], v[60:63]\n"
      "s_cselect_b32 s78, s48, 0x40000000\n"
      "ds_read_b128 v[40:43], v119 offset:35840\n"
      "v_mfma_f32_16x16x32_bf16 v[64:67], a[80:83], v[20:23], v[64:67]\n"
      "s_add_u32 m0, s73, 24576\n"
      "v_mfma_f32_16x16x32_bf16 v[64:67], a[92:95], v[24:27], v[64:67]\n"
      "s_waitcnt lgkmcnt(3)\n"
      "v_mfma_f32_16x16x32_bf16 v[56:59], a[96:99], v[28:31], v[56:59]\n"
      "s_waitcnt lgkmcnt(2)\n"
      "ds_read_b128 v[20:23], v119 offset:36864\n"
      "v_mfma_f32_16x16x32_bf16 v[56:59], a[108:111], v[32:35], v[56:59]\n"
      "v_mfma_f32_16x16x32_bf16 v[60:63], a[100:103], v[28:31], v[60:63]\n"
      "v_mfma_f32_16x16x32_bf16 v[60:63], a[112:115], v[32:35], v[60:63]\n"
      "ds_read_b128 v[24:27], v119 offset:37888\n"
      "v_mfma_f32_16x16x32_bf16 v[64:67], a[104:107], v[28:31], v[64:67]\n"
      "v_mfma_f32_16x16x32_bf16 v[64:67], a[116:119], v[32:35], v[64:67]\n"
      "s_waitcnt lgkmcnt(3)\n"
      "v_mfma_f32_16x16x32_bf16 v[56:59], a[120:123], v[36:39], v[56:59]\n"
      "s_waitcnt lgkmcnt(2)\n"
      "ds_read_b128 v[28:31], v119 offset:38912\n"
      "v_mfma_f32_16x16x32_bf16 v[56:59], a[132:135], v[40:43], v[56:59]\n"
      "v_mfma_f32_16x16x32_bf16 v[60:63], a[124:127], v[36:39], v[60:63]\n"
      "v_mfma_f32_16x16x32_bf16 v[60:63], a[136:139], v[40:43], v[60:63]\n"
      "ds_read_b128 v[32:35], v119 offset:39936\n"
      "v_mfma_f32_16x16x32_bf16 v[64:67], a[128:131], v[36:39], v[64:67]\n"
      "v_mfma_f32_16x16x32_bf16 v[64:67], a[140:143], v[40:43], v[64:67]\n"
      "s_waitcnt vmcnt(12) lgkmcnt(3)\n"
      "v_cmp_gt_u32 vcc, s67, v6\n"
      "v_mfma_f32_16x16x32_bf16 v[56:59], a[144:147], v[20:23], v[56:59]\n"
      "s_waitcnt lgkmcnt(2)\n"
      "ds_read_b128 v[36:39], v119 offset:40960\n"
      "v_mfma_f32_16x16x32_bf16 v[56:59], a[156:159], v[24:27], v[56:59]\n"
      "s_cbranch_vccnz .Lsl11\n"
      ".Lbk11:\n"
      "buffer_load_dword v6, v5, s[36:39], 0 offen sc1\n"
      "v_mfma_f32_16x16x32_bf16 v[60:63], a[148:151], v[20:23], v[60:63]\n"
      "v_mfma_f32_16x16x32_bf16 v[60:63], a[160:163], v[24:27], v[60:63]\n"
      "ds_read_b128 v[40:43], v119 offset:41984\n"
      "v_mfma_f32_16x16x32_bf16 v[64:67], a[152:155], v[20:23], v[64:67]\n"
      "buffer_load_dwordx4 v2, s[36:39], s69 offen sc1 lds\n"
      "v_mfma_f32_16x16x32_bf16 v[64:67], a[164:167], v[24:27], v[64:67]\n"
      "s_waitcnt lgkmcnt(3)\n"
      "v_mfma_f32_16x16x32_bf16 v[56:59], a[168:171], v[28:31], v[56:59]\n"
      "s_waitcnt lgkmcnt(2)\n"
      "ds_read_b128 v[20:23], v119 offset:43008\n"
      "v_mfma_f32_16x16x32_bf16 v[56:59], a[180:183], v[32:35], v[56:59]\n"
      "buffer_load_dwordx4 v2, s[36:39], s69 offen offset:1024 sc1 lds\n"
      "v_mfma_f32_16x16x32_bf16 v[60:63], a[172:175], v[28:31], v[60:63]\n"
      "v_mfma_f32_16x16x32_bf16 v[60:63], a[184:187], v[32:35], v[60:63]\n"
      "ds_read_b128 v[24:27], v119 offset:44032\n"
      "v_mfma_f32_16x16x32_bf16 v[64:67], a[176:179], v[28:31], v[64:67]\n"
      "buffer_load_dwordx4 v2, s[36:39], s69 offen offset:2048 sc1 lds\n"
      "v_mfma_f32_16x16x32_bf16 v[64:67], a[188:191], v[32:35], v[64:67]\n"
      "s_waitcnt lgkmcnt(3)\n"
      "v_mfma_f32_16x16x32_bf16 v[56:59], a[192:195], v[36:39], v[56:59]\n"
      "s_waitcnt lgkmcnt(2)\n"
      "ds_read_b128 v[28:31], v119 offset:45056\n"
      "v_mfma_f32_16x16x32_bf16 v[56:59], a[204:207], v[40:43], v[56:59]\n"
      "buffer_load_dwordx4 v2, s[36:39], s69 offen offset:3072 sc1 lds\n"
      "v_mfma_f32_16x16x32_bf16 v[60:63], a[196:199], v[36:39], v[60:63]\n"
      "s_add_u32 m0, s73, 28672\n"
      "v_mfma_f32_16x16x32_bf16 v[60:63], a[208:211], v[40:43], v[60:63]\n"
      "s_cmp_lt_i32 s66, 63\n"
      "ds_read_b128 v[32:35], v119 offset:46080\n"
      "v_mfma_f32_16x16x32_bf16 v[64:67], a[200:203], v[36:39], v[64:67]\n"
      "buffer_load_dwordx4 v19, s[36:39], s69 offen sc1 lds\n"
      "v_mfma_f32_16x16x32_bf16 v[64:67], a[212:215], v[40:43], v[64:67]\n"
      "s_waitcnt lgkmcnt(3)\n"
      "v_mfma_f32_16x16x32_bf16 v[56:59], a[216:219], v[20:23], v[56:59]\n"
      "s_waitcnt lgkmcnt(2)\n"
      "ds_read_b128 v[36:39], v119 offset:47104\n"
      "v_mfma_f32_16x16x32_bf16 v[56:59], a[228:231], v[24:27], v[56:59]\n"
      "buffer_load_dwordx4 v19, s[36:39], s69 offen offset:1024 sc1 lds\n"
      "v_mfma_f32_16x16x32_bf16 v[60:63], a[220:223], v[20:23], v[60:63]\n"
      ".Lmd4:\n"
      "s_cbranch_scc0 .Ldn_t5\n"
      "v_mfma_f32_16x16x32_bf16 v[60:63], a[232:235], v[24:27], v[60:63]\n"
      "s_cmp_lt_u32 s66, 64\n"
      "ds_read_b128 v[40:43], v119 offset:48128\n"
      "v_mfma_f32_16x16x32_bf16 v[64:67], a[224:227], v[20:23], v[64:67]\n"
      "s_waitcnt vmcnt(12)\n"
      "s_barrier\n"
      "v_mfma_f32_16x16x32_bf16 v[64:67], a[236:239], v[24:27], v[64:67]\n"
      "s_waitcnt vmcnt(9) lgkmcnt(3)\n"
      "buffer_store_dword v8, v7, s[36:39], 0 offen sc1\n"
      "v_mfma_f32_16x16x32_bf16 v[56:59], a[240:243], v[28:31], v[56:59]\n"
      "s_waitcnt lgkmcnt(2)\n"
      "ds_read_b128 v[20:23], v3 offset:0\n"
      "v_mfma_f32_16x16x32_bf16 v[56:59], a[252:255], v[32:35], v[56:59]\n"
      "s_cselect_b32 s71, s70, 0x40000000\n"
      "v_mfma_f32_16x16x32_bf16 v[60:63], a[244:247], v[28:31], v[60:63]\n"
      "s_add_u32 s70, s70, 110592\n"
      "v_mfma_f32_16x16x32_bf16 v[60:63], v[216:219], v[32:35], v[60:63]\n"
      "ds_read_b128 v[24:27], v3 offset:1024\n"
      "v_mfma_f32_16x16x32_bf16 v[64:67], a[248:251], v[28:31], v[64:67]\n"
      "v_mfma_f32_16x16x32_bf16 v[64:67], v[220:223], v[32:35], v[64:67]\n"
      "s_waitcnt lgkmcnt(3)\n"
      "v_mfma_f32_16x16x32_bf16 v[56:59], v[224:227], v[36:39], v[56:59]\n"
      "s_waitcnt lgkmcnt(2)\n"
      "ds_read_b128 v[28:31], v3 offset:2048\n"
      "v_mfma_f32_16x16x32_bf16 v[56:59], v[236:239], v[40:43], v[56:59]\n"
      "v_mfma_f32_16x16x32_bf16 v[60:63], v[228:231], v[36:39], v[60:63]\n"
      "v_mfma_f32_16x16x32_bf16 v[60:63], v[240:243], v[40:43], v[60:63]\n"
      "ds_read_b128 v[32:35], v3 offset:3072\n"
      "v_mfma_f32_16x16x32_bf16 v[64:67], v[232:235], v[36:39], v[64:67]\n"
      "buffer_load_dword v10, v9, s[36:39], 0 offen sc1\n"
      "v_mfma_f32_16x16x32_bf16 v[64:67], v[244:247], v[40:43], v[64:67]\n"
      "s_waitcnt lgkmcnt(3)\n"
      ".Ldn_e5:\n"
      "v_mfma_f32_16x16x32_bf16 v[44:47], a[0:3], v[20:23], 0\n"
      "s_waitcnt lgkmcnt(2)\n"
      "ds_read_b128 v[36:39], v3 offset:4096\n"
      "v_cvt_pk_bf16_f32 v112, v56, v57\n"
      "v_mfma_f32_16x16x32_bf16 v[44:47], a[12:15], v[24:27], v[44:47]\n"
      "s_add_u32 s67, s66, 4\n"
      "v_cvt_pk_bf16_f32 v113, v58, v59\n"
      "v_mfma_f32_16x16x32_bf16 v[48:51], a[4:7], v[20:23], 0\n"
      "s_min_i32 s67, s67, 64\n"
      "v_cvt_pk_bf16_f32 v114, v60, v61\n"
      "v_cvt_pk_bf16_f32 v115, v62, v63\n"
      "v_mfma_f32_16x16x32_bf16 v[48:51], a[16:19], v[24:27], v[48:51]\n"
      "s_max_i32 s67, s67, 0\n"
      "ds_read_b128 v[40:43], v3 offset:5120\n"
      "v_mfma_f32_16x16x32_bf16 v[52:55], a[8:11], v[20:23], 0\n"
      "s_add_u32 s68, s68, 147456\n"
      "buffer_store_dwordx4 v[112:115], v18, s[36:39], s71 offen sc1\n"
      "v_mfma_f32_16x16x32_bf16 v[52:55], a[20:23], v[24:27], v[52:55]\n"
      "s_waitcnt lgkmcnt(3)\n"
      "v_cvt_pk_bf16_f32 v116, v64, v65\n"
      "v_cvt_pk_bf16_f32 v117, v66, v67\n"
      "v_mfma_f32_16x16x32_bf16 v[44:47], a[24:27], v[28:31], v[44:47]\n"
      "s_waitcnt vmcnt(10) lgkmcnt(2)\n"
      "ds_read_b128 v[20:23], v3 offset:6144\n"
      "v_lshlrev_b32 v168, 16, v140\n"
      "v_mfma_f32_16x16x32_bf16 v[44:47], a[36:39], v[32:35], v[44:47]\n"
      "s_add_u32 s48, s66, 3\n"
      "v_add_f32 v168, v68, v168\n"
      "v_and_b32 v169, 0xffff0000, v140\n"
      "v_mfma_f32_16x16x32_bf16 v[48:51], a[28:31], v[28:31], v[48:51]\n"
      "s_cmp_lt_u32 s48, 64\n"
      "v_add_f32 v169, v69, v169\n"
      "v_lshlrev_b32 v170, 16, v141\n"
      "v_mfma_f32_16x16x32_bf16 v[48:51], a[40:43], v[32:35], v[48:51]\n"
      "s_cselect_b32 s69, s68, 0x40000000\n"
      "ds_read_b128 v[24:27], v3 offset:7168\n"
      "v_add_f32 v170, v70, v170\n"
      "v_mfma_f32_16x16x32_bf16 v[52:55], a[32:35], v[28:31], v[52:55]\n"
      "s_add_u32 s66, s66, 1\n"
      "buffer_store_dwordx2 v[116:117], v118, s[36:39], s71 offen offset:1024 sc1\n"
      "v_mfma_f32_16x16x32_bf16 v[52:55], a[44:47], v[32:35], v[52:55]\n"
      "s_waitcnt lgkmcnt(3)\n"
      "v_and_b32 v171, 0xffff0000, v141\n"
      "v_lshlrev_b32 v172, 16, v142\n"
      "v_mfma_f32_16x16x32_bf16 v[44:47], a[48:51], v[36:39], v[44:47]\n"
      "s_waitcnt lgkmcnt(2)\n"
      "ds_read_b128 v[28:31], v3 offset:8192\n"
      "v_add_f32 v171, v71, v171\n"
      "v_mfma_f32_16x16x32_bf16 v[44:47], a[60:63], v[40:43], v[44:47]\n"
      "s_max_i32 s72, s66, 0\n"
      "v_add_f32 v172, v72, v172\n"
      "v_and_b32 v173, 0xffff0000, v142\n"
      "v_mfma_f32_16x16x32_bf16 v[48:51], a[52:55], v[36:39], v[48:51]\n"
      "s_add_u32 s48, s81, s88\n"
      "v_writelane_b32 v8, s72, 0\n"
      "v_add_f32 v173, v73, v173\n"
      "v_mfma_f32_16x16x32_bf16 v[48:51], a[64:67], v[40:43], v[48:51]\n"
      "s_mul_i32 s49, s48, 20480\n"
      "ds_read_b128 v[32:35], v3 offset:9216\n"
      "v_lshlrev_b32 v174, 16, v143\n"
      "v_mfma_f32_16x16x32_bf16 v[52:55], a[56:59], v[36:39], v[52:55]\n"
      "s_add_u32 s49, s49, 2641920\n"
      "v_add_f32 v174, v74, v174\n"
      "v_and_b32 v175, 0xffff0000, v143\n"
      "v_mfma_f32_16x16x32_bf16 v[52:55], a[68:71], v[40:43], v[52:55]\n"
      "s_waitcnt lgkmcnt(3)\n"
      "v_add_f32 v175, v75, v175\n"
      "v_lshlrev_b32 v176, 16, v144\n"
      "v_mfma_f32_16x16x32_bf16 v[44:47], a[72:75], v[20:23], v[44:47]\n"
      "s_waitcnt lgkmcnt(2)\n"
      "ds_read_b128 v[36:39], v3 offset:10240\n"
      "v_add_f32 v176, v76, v176\n"
      "v_mfma_f32_16x16x32_bf16 v[44:47], a[84:87], v[24:27], v[44:47]\n"
      "s_mul_i32 s50, s48, 1536\n"
      "v_and_b32 v177, 0xffff0000, v144\n"
      "v_lshlrev_b32 v178, 16, v145\n"
      "v_mfma_f32_16x16x32_bf16 v[48:51], a[76:79], v[20:23], v[48:51]\n"
      "s_add_u32 s50, s50, s65\n"
      "v_add_f32 v177, v77, v177\n"
      "v_add_f32 v178, v78, v178\n"
      "v_mfma_f32_16x16x32_bf16 v[48:51], a[88:91], v[24:27], v[48:51]\n"
      "s_cmp_lt_u32 s88, 32\n"
      "ds_read_b128 v[40:43], v3 offset:11264\n"
      "v_and_b32 v179, 0xffff0000, v145\n"
      "v_mfma_f32_16x16x32_bf16 v[52:55], a[80:83], v[20:23], v[52:55]\n"
      "s_cselect_b32 s77, s49, 0x40000000\n"
      "v_add_f32 v179, v79, v179\n"
      "v_mfma_f32_16x16x32_bf16 v[52:55], a[92:95], v[24:27], v[52:55]\n"
      "s_waitcnt lgkmcnt(3)\n"
      "v_mfma_f32_16x16x32_bf16 v[44:47], a[96:99], v[28:31], v[44:47]\n"
      "s_waitcnt lgkmcnt(2)\n"
      "ds_read_b128 v[20:23], v3 offset:12288\n"
      "v_mfma_f32_16x16x32_bf16 v[44:47], a[108:111], v[32:35], v[44:47]\n"
      "s_cselect_b32 s79, s50, 0x40000000\n"
      "v_mfma_f32_16x16x32_bf16 v[48:51], a[100:103], v[28:31], v[48:51]\n"
      "s_add_u32 m0, s73, 49152\n"
      "v_mfma_f32_16x16x32_bf16 v[48:51], a[112:115], v[32:35], v[48:51]\n"
      "ds_read_b128 v[24:27], v3 offset:13312\n"
      "v_mfma_f32_16x16x32_bf16 v[52:55], a[104:107], v[28:31], v[52:55]\n"
      "v_mfma_f32_16x16x32_bf16 v[52:55], a[116:119], v[32:35], v[52:55]\n"
      "s_waitcnt lgkmcnt(3)\n"
      "v_mfma_f32_16x16x32_bf16 v[44:47], a[120:123], v[36:39], v[44:47]\n"
      "s_waitcnt lgkmcnt(2)\n"
      "ds_read_b128 v[28:31], v3 offset:14336\n"
      "v_mfma_f32_16x16x32_bf16 v[44:47], a[132:135], v[40:43], v[44:47]\n"
      "v_mfma_f32_16x16x32_bf16 v[48:51], a[124:127], v[36:39], v[48:51]\n"
      "v_mfma_f32_16x16x32_bf16 v[48:51], a[136:139], v[40:43], v[48:51]\n"
      "ds_read_b128 v[32:35], v3 offset:15360\n"
      "v_mfma_f32_16x16x32_bf16 v[52:55], a[128:131], v[36:39], v[52:55]\n"
      "v_mfma_f32_16x16x32_bf16 v[52:55], a[140:143], v[40:43], v[52:55]\n"
      "s_waitcnt vmcnt(10) lgkmcnt(3)\n"
      "v_cmp_gt_u32 vcc, s67, v6\n"
      "v_mfma_f32_16x16x32_bf16 v[44:47], a[144:147], v[20:23], v[44:47]\n"
      "s_waitcnt lgkmcnt(2)\n"
      "ds_read_b128 v[36:39], v3 offset:16384\n"
      "v_mfma_f32_16x16x32_bf16 v[44:47], a[156:159], v[24:27], v[44:47]\n"
      "s_cbranch_vccnz .Lsl12\n"
      ".Lbk12:\n"
      "buffer_load_dword v6, v5, s[36:39], 0 offen sc1\n"
      "v_mfma_f32_16x16x32_bf16 v[48:51], a[148:151], v[20:23], v[48:51]\n"
      "v_mfma_f32_16x16x32_bf16 v[48:51], a[160:163], v[24:27], v[48:51]\n"
      "ds_read_b128 v[40:43], v3 offset:17408\n"
      "v_mfma_f32_16x16x32_bf16 v[52:55], a[152:155], v[20:23], v[52:55]\n"
      "buffer_load_dwordx4 v2, s[36:39], s69 offen sc1 lds\n"
      "v_mfma_f32_16x16x32_bf16 v[52:55], a[164:167], v[24:27], v[52:55]\n"
      "s_waitcnt lgkmcnt(3)\n"
      "v_mfma_f32_16x16x32_bf16 v[44:47], a[168:171], v[28:31], v[44:47]\n"
      "s_waitcnt lgkmcnt(2)\n"
      "ds_read_b128 v[20:23], v3 offset:18432\n"
      "v_mfma_f32_16x16x32_bf16 v[44:47], a[180:183], v[32:35], v[44:47]\n"
      "buffer_load_dwordx4 v2, s[36:39], s69 offen offset:1024 sc1 lds\n"
      "v_mfma_f32_16x16x32_bf16 v[48:51], a[172:175], v[28:31], v[48:51]\n"
      "v_mfma_f32_16x16x32_bf16 v[48:51], a[184:187], v[32:35], v[48:51]\n"
      "ds_read_b128 v[24:27], v3 offset:19456\n"
      "v_mfma_f32_16x16x32_bf16 v[52:55], a[176:179], v[28:31], v[52:55]\n"
      "buffer_load_dwordx4 v2, s[36:39], s69 offen offset:2048 sc1 lds\n"
      "v_mfma_f32_16x16x32_bf16 v[52:55], a[188:191], v[32:35], v[52:55]\n"
      "s_waitcnt lgkmcnt(3)\n"
      "v_mfma_f32_16x16x32_bf16 v[44:47], a[192:195], v[36:39], v[44:47]\n"
      "s_waitcnt lgkmcnt(2)\n"
      "ds_read_b128 v[28:31], v3 offset:20480\n"
      "v_mfma_f32_16x16x32_bf16 v[44:47], a[204:207], v[40:43], v[44:47]\n"
      "buffer_load_dwordx4 v2, s[36:39], s69 offen offset:3072 sc1 lds\n"
      "v_mfma_f32_16x16x32_bf16 v[48:51], a[196:199], v[36:39], v[48:51]\n"
      "s_add_u32 m0, s73, 53248\n"
      "v_mfma_f32_16x16x32_bf16 v[48:51], a[208:211], v[40:43], v[48:51]\n"
      "ds_read_b128 v[32:35], v3 offset:21504\n"
      "v_mfma_f32_16x16x32_bf16 v[52:55], a[200:203], v[36:39], v[52:55]\n"
      "s_waitcnt vmcnt(7)\n"
      "v_cmp_gt_u32 vcc, s74, v10\n"
      "v_mfma_f32_16x16x32_bf16 v[52:55], a[212:215], v[40:43], v[52:55]\n"
      "s_waitcnt lgkmcnt(3)\n"
      "s_cbranch_vccnz .Lsl13\n"
      ".Lbk13:\n"
      "buffer_load_dwordx4 v19, s[36:39], s69 offen sc1 lds\n"
      "v_mfma_f32_16x16x32_bf16 v[44:47], a[216:219], v[20:23], v[44:47]\n"
      "s_waitcnt lgkmcnt(2)\n"
      "s_cmp_lt_i32 s66, 63\n"
      "ds_read_b128 v[36:39], v3 offset:22528\n"
      "v_mfma_f32_16x16x32_bf16 v[44:47], a[228:231], v[24:27], v[44:47]\n"
      "v_mfma_f32_16x16x32_bf16 v[48:51], a[220:223], v[20:23], v[48:51]\n"
      "buffer_load_dwordx4 v19, s[36:39], s69 offen offset:1024 sc1 lds\n"
      ".Lmd5:\n"
      "s_cbranch_scc1 .Ldn_loop\n"
      ".Ldn_t0:\n"
      "s_waitcnt lgkmcnt(0)\n"
      "v_mfma_f32_16x16x32_bf16 v[48:51], a[232:235], v[24:27], v[48:51]\n"
      "ds_read_b128 v[40:43], v3 offset:23552\n"
      "v_mfma_f32_16x16x32_bf16 v[52:55], a[224:227], v[20:23], v[52:55]\n"
      "s_add_u32 s48, s88, 1\n"
      "v_mfma_f32_16x16x32_bf16 v[52:55], a[236:239], v[24:27], v[52:55]\n"
      "s_waitcnt vmcnt(0)\n"
      "buffer_store_dword v8, v7, s[36:39], 0 offen sc1\n"
      "v_mfma_f32_16x16x32_bf16 v[44:47], a[240:243], v[28:31], v[44:47]\n"
      "v_mfma_f32_16x16x32_bf16 v[44:47], a[252:255], v[32:35], v[44:47]\n"
      "s_cmp_lt_u32 s88, 64\n"
      "v_writelane_b32 v8, 64, 0\n"
      "v_mfma_f32_16x16x32_bf16 v[48:51], a[244:247], v[28:31], v[48:51]\n"
      "s_cselect_b32 s74, s48, 0\n"
      "v_mfma_f32_16x16x32_bf16 v[48:51], v[216:219], v[32:35], v[48:51]\n"
      "s_mul_i32 s48, s29, 65\n"
      "v_mfma_f32_16x16x32_bf16 v[52:55], a[248:251], v[28:31], v[52:55]\n"
      "s_add_u32 s48, s48, s88\n"
      "v_mfma_f32_16x16x32_bf16 v[52:55], v[220:223], v[32:35], v[52:55]\n"
      "v_mfma_f32_16x16x32_bf16 v[44:47], v[224:227], v[36:39], v[44:47]\n"
      "s_waitcnt lgkmcnt(0)\n"
      "v_mfma_f32_16x16x32_bf16 v[44:47], v[236:239], v[40:43], v[44:47]\n"
      "s_mul_i32 s48, s48, 20480\n"
      "v_mfma_f32_16x16x32_bf16 v[48:51], v[228:231], v[36:39], v[48:51]\n"
      "s_add_u32 s48, s48, 10526720\n"
      "v_mfma_f32_16x16x32_bf16 v[48:51], v[240:243], v[40:43], v[48:51]\n"
      "s_cmp_lt_u32 s88, 64\n"
      "v_mfma_f32_16x16x32_bf16 v[52:55], v[232:235], v[36:39], v[52:55]\n"
      "s_cselect_b32 s76, s48, 0x40000000\n"
      "v_mfma_f32_16x16x32_bf16 v[52:55], v[244:247], v[40:43], v[52:55]\n"
      "s_add_u32 s48, s81, s88\n"
      "buffer_load_dwordx4 v[140:143], v12, s[36:39], s76 offen sc1\n"
      "s_sub_u32 s48, 159, s48\n"
      "v_cvt_pk_bf16_f32 v112, v44, v45\n"
      "v_cvt_pk_bf16_f32 v113, v46, v47\n"
      "s_mul_i32 s48, s48, 18432\n"
      "buffer_load_dwordx2 v[144:145], v11, s[36:39], s76 offen sc1\n"
      "s_sub_u32 s49, s88, 32\n"
      "v_cvt_pk_bf16_f32 v114, v48, v49\n"
      "v_cvt_pk_bf16_f32 v115, v50, v51\n"
      "s_cmp_lt_u32 s49, 32\n"
      "buffer_store_dwordx4 v[112:115], v18, s[36:39], s70 offen sc1\n"
      "s_cselect_b32 s78, s48, 0x40000000\n"
      "v_cvt_pk_bf16_f32 v116, v52, v53\n"
      "v_cvt_pk_bf16_f32 v117, v54, v55\n"
      "buffer_store_dwordx2 v[116:117], v118, s[36:39], s70 offen offset:1024 sc1\n"
      "s_branch .Ldn_r\n"
      ".Ldn_t1:\n"
      "s_waitcnt lgkmcnt(0)\n"
      "v_mfma_f32_16x16x32_bf16 v[60:63], a[232:235], v[24:27], v[60:63]\n"
      "ds_read_b128 v[40:43], v3 offset:48128\n"
      "v_mfma_f32_16x16x32_bf16 v[64:67], a[224:227], v[20:23], v[64:67]\n"
      "s_add_u32 s48, s88, 1\n"
      "v_mfma_f32_16x16x32_bf16 v[64:67], a[236:239], v[24:27], v[64:67]\n"
      "s_waitcnt vmcnt(0)\n"
      "buffer_store_dword v8, v7, s[36:39], 0 offen sc1\n"
      "v_mfma_f32_16x16x32_bf16 v[56:59], a[240:243], v[28:31], v[56:59]\n"
      "v_mfma_f32_16x16x32_bf16 v[56:59], a[252:255], v[32:35], v[56:59]\n"
      "s_cmp_lt_u32 s88, 64\n"
      "v_writelane_b32 v8, 64, 0\n"
      "v_mfma_f32_16x16x32_bf16 v[60:63], a[244:247], v[28:31], v[60:63]\n"
      "s_cselect_b32 s74, s48, 0\n"
      "v_mfma_f32_16x16x32_bf16 v[60:63], v[216:219], v[32:35], v[60:63]\n"
      "s_mul_i32 s48, s29, 65\n"
      "v_mfma_f32_16x16x32_bf16 v[64:67], a[248:251], v[28:31], v[64:67]\n"
      "s_add_u32 s48, s48, s88\n"
      "v_mfma_f32_16x16x32_bf16 v[64:67], v[220:223], v[32:35], v[64:67]\n"
      "v_mfma_f32_16x16x32_bf16 v[56:59], v[224:227], v[36:39], v[56:59]\n"
      "s_waitcnt lgkmcnt(0)\n"
      "v_mfma_f32_16x16x32_bf16 v[56:59], v[236:239], v[40:43], v[56:59]\n"
      "s_mul_i32 s48, s48, 20480\n"
      "v_mfma_f32_16x16x32_bf16 v[60:63], v[228:231], v[36:39], v[60:63]\n"
      "s_add_u32 s48, s48, 10526720\n"
      "v_mfma_f32_16x16x32_bf16 v[60:63], v[240:243], v[40:43], v[60:63]\n"
      "s_cmp_lt_u32 s88, 64\n"
      "v_mfma_f32_16x16x32_bf16 v[64:67], v[232:235], v[36:39], v[64:67]\n"
      "s_cselect_b32 s76, s48, 0x40000000\n"
      "v_mfma_f32_16x16x32_bf16 v[64:67], v[244:247], v[40:43], v[64:67]\n"
      "s_add_u32 s48, s81, s88\n"
      "buffer_load_dwordx4 v[140:143], v12, s[36:39], s76 offen sc1\n"
      "s_sub_u32 s48, 159, s48\n"
      "v_cvt_pk_bf16_f32 v112, v56, v57\n"
      "v_cvt_pk_bf16_f32 v113, v58, v59\n"
      "s_mul_i32 s48, s48, 18432\n"
      "buffer_load_dwordx2 v[144:145], v11, s[36:39], s76 offen sc1\n"
      "s_sub_u32 s49, s88, 32\n"
      "v_cvt_pk_bf16_f32 v114, v60, v61\n"
      "v_cvt_pk_bf16_f32 v115, v62, v63\n"
      "s_cmp_lt_u32 s49, 32\n"
      "buffer_store_dwordx4 v[112:115], v18, s[36:39], s70 offen sc1\n"
      "s_cselect_b32 s78, s48, 0x40000000\n"
      "v_cvt_pk_bf16_f32 v116, v64, v65\n"
      "v_cvt_pk_bf16_f32 v117, v66, v67\n"
      "buffer_store_dwordx2 v[116:117], v118, s[36:39], s70 offen offset:1024 sc1\n"
      "s_branch .Ldn_r\n"
      ".Ldn_t2:\n"
      "s_waitcnt lgkmcnt(0)\n"
      "v_mfma_f32_16x16x32_bf16 v[48:51], a[232:235], v[24:27], v[48:51]\n"
      "ds_read_b128 v[40:43], v4 offset:23552\n"
      "v_mfma_f32_16x16x32_bf16 v[52:55], a[224:227], v[20:23], v[52:55]\n"
      "s_add_u32 s48, s88, 1\n"
      "v_mfma_f32_16x16x32_bf16 v[52:55], a[236:239], v[24:27], v[52:55]\n"
      "s_waitcnt vmcnt(0)\n"
      "buffer_store_dword v8, v7, s[36:39], 0 offen sc1\n"
      "v_mfma_f32_16x16x32_bf16 v[44:47], a[240:243], v[28:31], v[44:47]\n"
      "v_mfma_f32_16x16x32_bf16 v[44:47], a[252:255], v[32:35], v[44:47]\n"
      "s_cmp_lt_u32 s88, 64\n"
      "v_writelane_b32 v8, 64, 0\n"
      "v_mfma_f32_16x16x32_bf16 v[48:51], a[244:247], v[28:31], v[48:51]\n"
      "s_cselect_b32 s74, s48, 0\n"
      "v_mfma_f32_16x16x32_bf16 v[48:51], v[216:219], v[32:35], v[48:51]\n"
      "s_mul_i32 s48, s29, 65\n"
      "v_mfma_f32_16x16x32_bf16 v[52:55], a[248:251], v[28:31], v[52:55]\n"
      "s_add_u32 s48, s48, s88\n"
      "v_mfma_f32_16x16x32_bf16 v[52:55], v[220:223], v[32:35], v[52:55]\n"
      "v_mfma_f32_16x16x32_bf16 v[44:47], v[224:227], v[36:39], v[44:47]\n"
      "s_waitcnt lgkmcnt(0)\n"
      "v_mfma_f32_16x16x32_bf16 v[44:47], v[236:239], v[40:43], v[44:47]\n"
      "s_mul_i32 s48, s48, 20480\n"
      "v_mfma_f32_16x16x32_bf16 v[48:51], v[228:231], v[36:39], v[48:51]\n"
      "s_add_u32 s48, s48, 10526720\n"
      "v_mfma_f32_16x16x32_bf16 v[48:51], v[240:243], v[40:43], v[48:51]\n"
      "s_cmp_lt_u32 s88, 64\n"
      "v_mfma_f32_16x16x32_bf16 v[52:55], v[232:235], v[36:39], v[52:55]\n"
      "s_cselect_b32 s76, s48, 0x40000000\n"
      "v_mfma_f32_16x16x32_bf16 v[52:55], v[244:247], v[40:43], v[52:55]\n"
      "s_add_u32 s48, s81, s88\n"
      "buffer_load_dwordx4 v[140:143], v12, s[36:39], s76 offen sc1\n"
      "s_sub_u32 s48, 159, s48\n"
      "v_cvt_pk_bf16_f32 v112, v44, v45\n"
      "v_cvt_pk_bf16_f32 v113, v46, v47\n"
      "s_mul_i32 s48, s48, 18432\n"
      "buffer_load_dwordx2 v[144:145], v11, s[36:39], s76 offen sc1\n"
      "s_sub_u32 s49, s88, 32\n"
      "v_cvt_pk_bf16_f32 v114, v48, v49\n"
      "v_cvt_pk_bf16_f32 v115, v50, v51\n"
      "s_cmp_lt_u32 s49, 32\n"
      "buffer_store_dwordx4 v[112:115], v18, s[36:39], s70 offen sc1\n"
      "s_cselect_b32 s78, s48, 0x40000000\n"
      "v_cvt_pk_bf16_f32 v116, v52, v53\n"
      "v_cvt_pk_bf16_f32 v117, v54, v55\n"
      "buffer_store_dwordx2 v[116:117], v118, s[36:39], s70 offen offset:1024 sc1\n"
      "s_branch .Ldn_r\n"
      ".Ldn_t3:\n"
      "s_waitcnt lgkmcnt(0)\n"
      "v_mfma_f32_16x16x32_bf16 v[72:75], a[232:235], v[24:27], v[72:75]\n"
      "buffer_store_dwordx4 v[146:149], v13, s[40:43], s78 offen\n"
      "ds_read_b128 v[40:43], v4 offset:48128\n"
      "v_mfma_f32_16x16x32_bf16 v[76:79], a[224:227], v[20:23], v[76:79]\n"
      "s_mov_b32 s75, s70\n"
      "v_mfma_f32_16x16x32_bf16 v[76:79], a[236:239], v[24:27], v[76:79]\n"
      "s_waitcnt vmcnt(0)\n"
      "buffer_store_dword v8, v7, s[36:39], 0 offen sc1\n"
      "v_mfma_f32_16x16x32_bf16 v[68:71], a[240:243], v[28:31], v[68:71]\n"
      "v_mfma_f32_16x16x32_bf16 v[68:71], a[252:255], v[32:35], v[68:71]\n"
      "s_mov_b32 s88, 63\n"
      "buffer_store_dwordx2 v[150:151], v14, s[40:43], s78 offen\n"
      "v_writelane_b32 v8, 64, 0\n"
      "v_mfma_f32_16x16x32_bf16 v[72:75], a[244:247], v[28:31], v[72:75]\n"
      "s_add_u32 s48, s88, 1\n"
      "v_mfma_f32_16x16x32_bf16 v[72:75], v[216:219], v[32:35], v[72:75]\n"
      "s_cmp_lt_u32 s88, 64\n"
      "v_mfma_f32_16x16x32_bf16 v[76:79], a[248:251], v[28:31], v[76:79]\n"
      "s_cselect_b32 s74, s48, 0\n"
      "v_mfma_f32_16x16x32_bf16 v[76:79], v[220:223], v[32:35], v[76:79]\n"
      "v_mfma_f32_16x16x32_bf16 v[68:71], v[224:227], v[36:39], v[68:71]\n"
      "s_waitcnt lgkmcnt(0)\n"
      "v_mfma_f32_16x16x32_bf16 v[68:71], v[236:239], v[40:43], v[68:71]\n"
      "s_mul_i32 s48, s29, 65\n"
      "v_mfma_f32_16x16x32_bf16 v[72:75], v[228:231], v[36:39], v[72:75]\n"
      "s_add_u32 s48, s48, s88\n"
      "v_mfma_f32_16x16x32_bf16 v[72:75], v[240:243], v[40:43], v[72:75]\n"
      "s_mul_i32 s48, s48, 20480\n"
      "v_mfma_f32_16x16x32_bf16 v[76:79], v[232:235], v[36:39], v[76:79]\n"
      "s_add_u32 s48, s48, 10526720\n"
      "v_mfma_f32_16x16x32_bf16 v[76:79], v[244:247], v[40:43], v[76:79]\n"
      "s_cmp_lt_u32 s88, 64\n"
      "s_cselect_b32 s76, s48, 0x40000000\n"
      "s_add_u32 s48, s81, s88\n"
      "buffer_load_dwordx4 v[140:143], v12, s[36:39], s76 offen sc1\n"
      "s_sub_u32 s48, 159, s48\n"
      "s_mul_i32 s48, s48, 18432\n"
      "buffer_load_dwordx2 v[144:145], v11, s[36:39], s76 offen sc1\n"
      "s_sub_u32 s49, s88, 32\n"
      "s_cmp_lt_u32 s49, 32\n"
      "s_cselect_b32 s78, s48, 0x40000000\n"
      "s_branch .Ldn_r\n"
      ".Ldn_t4:\n"
      "s_waitcnt lgkmcnt(0)\n"
      "v_mfma_f32_16x16x32_bf16 v[48:51], a[232:235], v[24:27], v[48:51]\n"
      "ds_read_b128 v[40:43], v119 offset:23552\n"
      "v_mfma_f32_16x16x32_bf16 v[52:55], a[224:227], v[20:23], v[52:55]\n"
      "s_add_u32 s48, s88, 1\n"
      "v_mfma_f32_16x16x32_bf16 v[52:55], a[236:239], v[24:27], v[52:55]\n"
      "s_waitcnt vmcnt(0)\n"
      "buffer_store_dword v8, v7, s[36:39], 0 offen sc1\n"
      "v_mfma_f32_16x16x32_bf16 v[44:47], a[240:243], v[28:31], v[44:47]\n"
      "v_mfma_f32_16x16x32_bf16 v[44:47], a[252:255], v[32:35], v[44:47]\n"
      "s_cmp_lt_u32 s88, 64\n"
      "v_writelane_b32 v8, 64, 0\n"
      "v_mfma_f32_16x16x32_bf16 v[48:51], a[244:247], v[28:31], v[48:51]\n"
      "s_cselect_b32 s74, s48, 0\n"
      "v_mfma_f32_16x16x32_bf16 v[48:51], v[216:219], v[32:35], v[48:51]\n"
      "s_mul_i32 s48, s29, 65\n"
      "v_mfma_f32_16x16x32_bf16 v[52:55], a[248:251], v[28:31], v[52:55]\n"
      "s_add_u32 s48, s48, s88\n"
      "v_mfma_f32_16x16x32_bf16 v[52:55], v[220:223], v[32:35], v[52:55]\n"
      "v_mfma_f32_16x16x32_bf16 v[44:47], v[224:227], v[36:39], v[44:47]\n"
      "s_waitcnt lgkmcnt(0)\n"
      "v_mfma_f32_16x16x32_bf16 v[44:47], v[236:239], v[40:43], v[44:47]\n"
      "s_mul_i32 s48, s48, 20480\n"
      "v_mfma_f32_16x16x32_bf16 v[48:51], v[228:231], v[36:39], v[48:51]\n"
      "s_add_u32 s48, s48, 10526720\n"
      "v_mfma_f32_16x16x32_bf16 v[48:51], v[240:243], v[40:43], v[48:51]\n"
      "s_cmp_lt_u32 s88, 64\n"
      "v_mfma_f32_16x16x32_bf16 v[52:55], v[232:235], v[36:39], v[52:55]\n"
      "s_cselect_b32 s76, s48, 0x40000000\n"
      "v_mfma_f32_16x16x32_bf16 v[52:55], v[244:247], v[40:43], v[52:55]\n"
      "s_add_u32 s48, s81, s88\n"
      "buffer_load_dwordx4 v[140:143], v12, s[36:39], s76 offen sc1\n"
      "s_sub_u32 s48, 159, s48\n"
      "v_cvt_pk_bf16_f32 v112, v44, v45\n"
      "v_cvt_pk_bf16_f32 v113, v46, v47\n"
      "s_mul_i32 s48, s48, 18432\n"
      "buffer_load_dwordx2 v[144:145], v11, s[36:39], s76 offen sc1\n"
      "s_sub_u32 s49, s88, 32\n"
      "v_cvt_pk_bf16_f32 v114, v48, v49\n"
      "v_cvt_pk_bf16_f32 v115, v50, v51\n"
      "s_cmp_lt_u32 s49, 32\n"
      "buffer_store_dwordx4 v[112:115], v18, s[36:39], s70 offen sc1\n"
      "s_cselect_b32 s78, s48, 0x40000000\n"
      "v_cvt_pk_bf16_f32 v116, v52, v53\n"
      "v_cvt_pk_bf16_f32 v117, v54, v55\n"
      "buffer_store_dwordx2 v[116:117], v118, s[36:39], s70 offen offset:1024 sc1\n"
      "s_branch .Ldn_r\n"
      ".Ldn_t5:\n"
      "s_waitcnt lgkmcnt(0)\n"
      "v_mfma_f32_16x16x32_bf16 v[60:63], a[232:235], v[24:27], v[60:63]\n"
      "ds_read_b128 v[40:43], v119 offset:48128\n"
      "v_mfma_f32_16x16x32_bf16 v[64:67], a[224:227], v[20:23], v[64:67]\n"
      "s_add_u32 s48, s88, 1\n"
      "v_mfma_f32_16x16x32_bf16 v[64:67], a[236:239], v[24:27], v[64:67]\n"
      "s_waitcnt vmcnt(0)\n"
      "buffer_store_dword v8, v7, s[36:39], 0 offen sc1\n"
      "v_mfma_f32_16x16x32_bf16 v[56:59], a[240:243], v[28:31], v[56:59]\n"
      "v_mfma_f32_16x16x32_bf16 v[56:59], a[252:255], v[32:35], v[56:59]\n"
      "s_cmp_lt_u32 s88, 64\n"
      "v_writelane_b32 v8, 64, 0\n"
      "v_mfma_f32_16x16x32_bf16 v[60:63], a[244:247], v[28:31], v[60:63]\n"
      "s_cselect_b32 s74, s48, 0\n"
      "v_mfma_f32_16x16x32_bf16 v[60:63], v[216:219], v[32:35], v[60:63]\n"
      "s_mul_i32 s48, s29, 65\n"
      "v_mfma_f32_16x16x32_bf16 v[64:67], a[248:251], v[28:31], v[64:67]\n"
      "s_add_u32 s48, s48, s88\n"
      "v_mfma_f32_16x16x32_bf16 v[64:67], v[220:223], v[32:35], v[64:67]\n"
      "v_mfma_f32_16x16x32_bf16 v[56:59], v[224:227], v[36:39], v[56:59]\n"
      "s_waitcnt lgkmcnt(0)\n"
      "v_mfma_f32_16x16x32_bf16 v[56:59], v[236:239], v[40:43], v[56:59]\n"
      "s_mul_i32 s48, s48, 20480\n"
      "v_mfma_f32_16x16x32_bf16 v[60:63], v[228:231], v[36:39], v[60:63]\n"
      "s_add_u32 s48, s48, 10526720\n"
      "v_mfma_f32_16x16x32_bf16 v[60:63], v[240:243], v[40:43], v[60:63]\n"
      "s_cmp_lt_u32 s88, 64\n"
      "v_mfma_f32_16x16x32_bf16 v[64:67], v[232:235], v[36:39], v[64:67]\n"
      "s_cselect_b32 s76, s48, 0x40000000\n"
      "v_mfma_f32_16x16x32_bf16 v[64:67], v[244:247], v[40:43], v[64:67]\n"
      "s_add_u32 s48, s81, s88\n"
      "buffer_load_dwordx4 v[140:143], v12, s[36:39], s76 offen sc1\n"
      "s_sub_u32 s48, 159, s48\n"
      "v_cvt_pk_bf16_f32 v112, v56, v57\n"
      "v_cvt_pk_bf16_f32 v113, v58, v59\n"
      "s_mul_i32 s48, s48, 18432\n"
      "buffer_load_dwordx2 v[144:145], v11, s[36:39], s76 offen sc1\n"
      "s_sub_u32 s49, s88, 32\n"
      "v_cvt_pk_bf16_f32 v114, v60, v61\n"
      "v_cvt_pk_bf16_f32 v115, v62, v63\n"
      "s_cmp_lt_u32 s49, 32\n"
      "buffer_store_dwordx4 v[112:115], v18, s[36:39], s70 offen sc1\n"
      "s_cselect_b32 s78, s48, 0x40000000\n"
      "v_cvt_pk_bf16_f32 v116, v64, v65\n"
      "v_cvt_pk_bf16_f32 v117, v66, v67\n"
      "buffer_store_dwordx2 v[116:117], v118, s[36:39], s70 offen offset:1024 sc1\n"
      ".Ldn_r:\n"
      "s_waitcnt vmcnt(0) lgkmcnt(0)\n"
      "buffer_store_dword v8, v7, s[36:39], 0 offen sc1\n"
      "v_lshlrev_b32 v168, 16, v140\n"
      "v_and_b32 v169, 0xffff0000, v140\n"
      "v_lshlrev_b32 v170, 16, v141\n"
      "v_and_b32 v171, 0xffff0000, v141\n"
      "v_add_f32 v168, v68, v168\n"
      "v_add_f32 v169, v69, v169\n"
      "v_add_f32 v170, v70, v170\n"
      "v_add_f32 v171, v71, v171\n"
      "v_lshlrev_b32 v172, 16, v142\n"
      "v_and_b32 v173, 0xffff0000, v142\n"
      "v_lshlrev_b32 v174, 16, v143\n"
      "v_and_b32 v175, 0xffff0000, v143\n"
      "v_add_f32 v172, v72, v172\n"
      "v_add_f32 v173, v73, v173\n"
      "v_add_f32 v174, v74, v174\n"
      "v_add_f32 v175, v75, v175\n"
      "v_lshlrev_b32 v176, 16, v144\n"
      "v_and_b32 v177, 0xffff0000, v144\n"
      "v_lshlrev_b32 v178, 16, v145\n"
      "v_and_b32 v179, 0xffff0000, v145\n"
      "v_add_f32 v176, v76, v176\n"
      "v_add_f32 v177, v77, v177\n"
      "v_add_f32 v178, v78, v178\n"
      "v_add_f32 v179, v79, v179\n"
      "v_cmp_gt_u32 vcc, s74, v10\n"
      "s_cbranch_vccnz .Lsl14\n"
      ".Lbk14:\n"
      "s_add_u32 s87, s75, s82\n"
      "buffer_load_dwordx4 v[80:83], v18, s[36:39], s87 offen sc1\n"
      "buffer_load_dwordx2 v[84:85], v118, s[36:39], s87 offen offset:1024 sc1\n"
      "s_add_u32 s87, s75, s83\n"
      "buffer_load_dwordx4 v[86:89], v18, s[36:39], s87 offen sc1\n"
      "buffer_load_dwordx2 v[90:91], v118, s[36:39], s87 offen offset:1024 sc1\n"
      "s_add_u32 s87, s75, s84\n"
      "buffer_load_dwordx4 v[92:95], v18, s[36:39], s87 offen sc1\n"
      "buffer_load_dwordx2 v[96:97], v118, s[36:39], s87 offen offset:1024 sc1\n"
      "s_add_u32 s87, s75, s85\n"
      "buffer_load_dwordx4 v[98:101], v18, s[36:39], s87 offen sc1\n"
      "buffer_load_dwordx2 v[102:103], v118, s[36:39], s87 offen offset:1024 sc1\n"
      "s_add_u32 s87, s75, s86\n"
      "buffer_load_dwordx4 v[104:107], v18, s[36:39], s87 offen sc1\n"
      "buffer_load_dwordx2 v[108:109], v118, s[36:39], s87 offen offset:1024 sc1\n"
      "s_waitcnt vmcnt(9)\n"
      "v_lshlrev_b32 v120, 16, v80\n"
      "v_and_b32 v121, 0xffff0000, v80\n"
      "v_lshlrev_b32 v122, 16, v81\n"
      "v_add_f32 v168, v168, v120\n"
      "v_add_f32 v169, v169, v121\n"
      "v_add_f32 v170, v170, v122\n"
      "v_and_b32 v123, 0xffff0000, v81\n"
      "v_add_f32 v171, v171, v123\n"
      "v_lshlrev_b32 v120, 16, v82\n"
      "v_and_b32 v121, 0xffff0000, v82\n"
      "v_lshlrev_b32 v122, 16, v83\n"
      "v_add_f32 v172, v172, v120\n"
      "v_add_f32 v173, v173, v121\n"
      "v_add_f32 v174, v174, v122\n"
      "v_and_b32 v123, 0xffff0000, v83\n"
      "s_waitcnt vmcnt(8)\n"
      "v_add_f32 v175, v175, v123\n"
      "v_lshlrev_b32 v120, 16, v84\n"
      "v_and_b32 v121, 0xffff0000, v84\n"
      "v_add_f32 v176, v176, v120\n"
      "v_add_f32 v177, v177, v121\n"
      "v_lshlrev_b32 v122, 16, v85\n"
      "v_and_b32 v123, 0xffff0000, v85\n"
      "s_waitcnt vmcnt(7)\n"
      "v_add_f32 v178, v178, v122\n"
      "v_add_f32 v179, v179, v123\n"
      "v_lshlrev_b32 v120, 16, v86\n"
      "v_add_f32 v168, v168, v120\n"
      "v_and_b32 v121, 0xffff0000, v86\n"
      "v_lshlrev_b32 v122, 16, v87\n"
      "v_and_b32 v123, 0xffff0000, v87\n"
      "v_add_f32 v169, v169, v121\n"
      "v_add_f32 v170, v170, v122\n"
      "v_add_f32 v171, v171, v123\n"
      "v_lshlrev_b32 v120, 16, v88\n"
      "v_add_f32 v172, v172, v120\n"
      "v_and_b32 v121, 0xffff0000, v88\n"
      "v_lshlrev_b32 v122, 16, v89\n"
      "v_and_b32 v123, 0xffff0000, v89\n"
      "v_add_f32 v173, v173, v121\n"
      "v_add_f32 v174, v174, v122\n"
      "v_add_f32 v175, v175, v123\n"
      "s_waitcnt vmcnt(6)\n"
      "v_lshlrev_b32 v120, 16, v90\n"
      "v_and_b32 v121, 0xffff0000, v90\n"
      "v_lshlrev_b32 v122, 16, v91\n"
      "v_add_f32 v176, v176, v120\n"
      "v_add_f32 v177, v177, v121\n"
      "v_add_f32 v178, v178, v122\n"
      "v_and_b32 v123, 0xffff0000, v91\n"
      "s_waitcnt vmcnt(5)\n"
      "v_add_f32 v179, v179, v123\n"
      "v_lshlrev_b32 v120, 16, v92\n"
      "v_and_b32 v121, 0xffff0000, v92\n"
      "v_add_f32 v168, v168, v120\n"
      "v_add_f32 v169, v169, v121\n"
      "v_lshlrev_b32 v122, 16, v93\n"
      "v_and_b32 v123, 0xffff0000, v93\n"
      "v_add_f32 v170, v170, v122\n"
      "v_add_f32 v171, v171, v123\n"
      "v_lshlrev_b32 v120, 16, v94\n"
      "v_and_b32 v121, 0xffff0000, v94\n"
      "v_add_f32 v172, v172, v120\n"
      "v_add_f32 v173, v173, v121\n"
      "v_lshlrev_b32 v122, 16, v95\n"
      "v_and_b32 v123, 0xffff0000, v95\n"
      "s_waitcnt vmcnt(4)\n"
      "v_add_f32 v174, v174, v122\n"
      "v_add_f32 v175, v175, v123\n"
      "v_lshlrev_b32 v120, 16, v96\n"
      "v_add_f32 v176, v176, v120\n"
      "v_and_b32 v121, 0xffff0000, v96\n"
      "v_lshlrev_b32 v122, 16, v97\n"
      "v_and_b32 v123, 0xffff0000, v97\n"
      "v_add_f32 v177, v177, v121\n"
      "v_add_f32 v178, v178, v122\n"
      "v_add_f32 v179, v179, v123\n"
      "s_waitcnt vmcnt(3)\n"
      "v_lshlrev_b32 v120, 16, v98\n"
      "v_and_b32 v121, 0xffff0000, v98\n"
      "v_lshlrev_b32 v122, 16, v99\n"
      "v_add_f32 v168, v168, v120\n"
      "v_add_f32 v169, v169, v121\n"
      "v_add_f32 v170, v170, v122\n"
      "v_and_b32 v123, 0xffff0000, v99\n"
      "v_add_f32 v171, v171, v123\n"
      "v_lshlrev_b32 v120, 16, v100\n"
      "v_and_b32 v121, 0xffff0000, v100\n"
      "v_lshlrev_b32 v122, 16, v101\n"
      "v_add_f32 v172, v172, v120\n"
      "v_add_f32 v173, v173, v121\n"
      "v_add_f32 v174, v174, v122\n"
      "v_and_b32 v123, 0xffff0000, v101\n"
      "s_waitcnt vmcnt(2)\n"
      "v_add_f32 v175, v175, v123\n"
      "v_lshlrev_b32 v120, 16, v102\n"
      "v_and_b32 v121, 0xffff0000, v102\n"
      "v_add_f32 v176, v176, v120\n"
      "v_add_f32 v177, v177, v121\n"
      "v_lshlrev_b32 v122, 16, v103\n"
      "v_and_b32 v123, 0xffff0000, v103\n"
      "s_waitcnt vmcnt(1)\n"
      "v_add_f32 v178, v178, v122\n"
      "v_add_f32 v179, v179, v123\n"
      "v_lshlrev_b32 v120, 16, v104\n"
      "v_add_f32 v168, v168, v120\n"
      "v_and_b32 v121, 0xffff0000, v104\n"
      "v_lshlrev_b32 v122, 16, v105\n"
      "v_and_b32 v123, 0xffff0000, v105\n"
      "v_add_f32 v169, v169, v121\n"
      "v_add_f32 v170, v170, v122\n"
      "v_add_f32 v171, v171, v123\n"
      "v_lshlrev_b32 v120, 16, v106\n"
      "v_add_f32 v172, v172, v120\n"
      "v_and_b32 v121, 0xffff0000, v106\n"
      "v_lshlrev_b32 v122, 16, v107\n"
      "v_and_b32 v123, 0xffff0000, v107\n"
      "v_add_f32 v173, v173, v121\n"
      "v_add_f32 v174, v174, v122\n"
      "v_add_f32 v175, v175, v123\n"
      "v_cvt_pk_bf16_f32 v146, v168, v169\n"
      "s_waitcnt vmcnt(0)\n"
      "v_lshlrev_b32 v120, 16, v108\n"
      "v_and_b32 v121, 0xffff0000, v108\n"
      "v_lshlrev_b32 v122, 16, v109\n"
      "v_add_f32 v176, v176, v120\n"
      "v_add_f32 v177, v177, v121\n"
      "v_add_f32 v178, v178, v122\n"
      "v_and_b32 v123, 0xffff0000, v109\n"
      "v_add_f32 v179, v179, v123\n"
      "v_cvt_pk_bf16_f32 v147, v170, v171\n"
      "v_cvt_pk_bf16_f32 v148, v172, v173\n"
      "v_cvt_pk_bf16_f32 v149, v174, v175\n"
      "buffer_store_dwordx4 v[146:149], v13, s[40:43], s78 offen\n"
      "v_cvt_pk_bf16_f32 v150, v176, v177\n"
      "v_cvt_pk_bf16_f32 v151, v178, v179\n"
      "buffer_store_dwordx2 v[150:151], v14, s[40:43], s78 offen\n"
      "s_branch .Lexit\n"
      ".Lgu:\n"
      "s_sub_u32 s49, s49, 19\n"
      "s_mul_i32 s50, s49, 43\n"
      "s_lshr_b32 s50, s50, 8\n"
      "s_mul_i32 s51, s50, 6\n"
      "s_sub_u32 s49, s49, s51\n"
      "s_bfe_u32 s51, s48, 0x10001\n"
      "s_lshl_b32 s51, s51, 1\n"
      "s_lshr_b32 s52, s48, 2\n"
      "s_or_b32 s51, s51, s52\n"
      "s_lshr_b32 s52, s48, 1\n"
      "s_add_u32 s52, s52, 3\n"
      "s_and_b32 s52, s52, 3\n"
      "s_add_u32 s29, s50, s51\n"
      "s_add_u32 s29, s29, 2\n"
      "s_and_b32 s29, s29, 3\n"
      "s_mov_b32 s30, s52\n"
      "s_bitcmp1_b32 s48, 0\n"
      "s_cbranch_scc1 .Lgu_m\n"
      "s_cmp_eq_u32 s50, 2\n"
      "s_cbranch_scc0 .Lgu_m0\n"
      "s_mov_b32 s49, 5\n"
      "s_add_u32 s29, s51, 1\n"
      "s_and_b32 s29, s29, 3\n"
      "s_branch .Lgu_m\n"
      ".Lgu_m0:\n"
      "s_and_b32 s53, s48, 2\n"
      "s_sub_u32 s29, 2, s53\n"
      "s_add_u32 s29, s29, s50\n"
      "s_lshr_b32 s53, s48, 1\n"
      "s_xor_b32 s53, s53, s48\n"
      "s_bfe_u32 s53, s53, 0x10001\n"
      "s_add_u32 s30, s53, 4\n"
      ".Lgu_m:\n"
      "s_mul_i32 s30, s30, 6\n"
      "s_add_u32 s30, s30, s49\n"
      "v_lshrrev_b32 v20, 4, v1\n"
      "v_and_b32 v21, 15, v1\n"
      "v_lshlrev_b32 v22, 2, v20\n"
      "v_xor_b32 v21, v21, v22\n"
      "v_mul_u32_u24 v22, 9216, v20\n"
      "v_lshl_add_u32 v10, v21, 4, v22\n"
      "v_bfe_u32 v22, v1, 2, 2\n"
      "v_lshl_add_u32 v20, v20, 3, v22\n"
      "v_lshlrev_b32 v20, 8, v20\n"
      "v_xor_b32 v22, s28, v22\n"
      "v_lshl_add_u32 v20, v22, 6, v20\n"
      "v_and_b32 v21, 3, v1\n"
      "v_lshl_add_u32 v11, v21, 4, v20\n"
      "v_add_u32 v66, 0x10000, v11\n"
      "v_add_u32 v67, 0x20000, v11\n"
      "s_lshl_b32 s73, s28, 11\n"
      "s_lshl_b32 s78, s30, 8\n"
      "s_mul_i32 s50, s28, 73728\n"
      "s_add_u32 s78, s78, s50\n"
      "v_mov_b32 v12, 0x3f803f80\n"
      "v_mov_b32 v13, 0x3f803f80\n"
      "v_mov_b32 v14, 0x3f803f80\n"
      "v_mov_b32 v15, 0x3f803f80\n"
      "s_mul_i32 s51, s29, 36\n"
      "s_add_u32 s51, s51, s30\n"
      "s_add_u32 s51, s51, 40\n"
      "s_lshl_b32 s51, s51, 2\n"
      "s_add_u32 s51, s51, s28\n"
      "s_lshl_b32 s51, s51, 7\n"
      "s_add_u32 s51, s51, 81911808\n"
      "v_lshlrev_b32 v7, 2, v1\n"
      "v_lshrrev_b32 v20, 5, v1\n"
      "v_lshl_or_b32 v7, v20, 30, v7\n"
      "v_add_u32 v7, s51, v7\n"
      "s_waitcnt lgkmcnt(0)\n"
      "s_mov_b32 s36, s20\n"
      "s_and_b32 s37, s21, 0xffff\n"
      "s_mov_b32 s38, 0x4eaf200\n"
      "s_mov_b32 s39, 0x20000\n"
      "s_mov_b32 s98, 82439168\n"
      "buffer_load_dwordx2 v[252:253], off, s[36:39], s98 sc1\n"
      "s_waitcnt vmcnt(0)\n"
      "v_readfirstlane_b32 s98, v252\n"
      "v_readfirstlane_b32 s99, v253\n"
      "v_mov_b32 v8, 0\n"
      "s_add_u32 s98, s98, 1\n"
      "s_addc_u32 s99, s99, 0\n"
      "v_writelane_b32 v8, s98, 2\n"
      "v_writelane_b32 v8, s99, 3\n"
      "buffer_store_dword v8, v7, s[36:39], 0 offen sc1\n"
      "s_mov_b32 s40, s14\n"
      "s_and_b32 s41, s15, 0xffff\n"
      "s_mov_b32 s42, 0x510000\n"
      "s_mov_b32 s43, 0x20000\n"
      "s_mov_b32 s44, s16\n"
      "s_and_b32 s45, s17, 0xffff\n"
      "s_mov_b32 s46, 0x510000\n"
      "s_mov_b32 s47, 0x20000\n"
      "s_add_u32 m0, s73, 0\n"
      "s_add_u32 s50, s78, 0\n"
      "buffer_load_dwordx4 v10, s[40:43], s50 offen lds\n"
      "s_add_u32 m0, s73, 1024\n"
      "s_add_u32 s50, s78, 36864\n"
      "buffer_load_dwordx4 v10, s[40:43], s50 offen lds\n"
      "s_add_u32 m0, s73, 8192\n"
      "s_add_u32 s50, s78, 294912\n"
      "buffer_load_dwordx4 v10, s[40:43], s50 offen lds\n"
      "s_add_u32 m0, s73, 9216\n"
      "s_add_u32 s50, s78, 331776\n"
      "buffer_load_dwordx4 v10, s[40:43], s50 offen lds\n"
      "s_add_u32 m0, s73, 16384\n"
      "s_add_u32 s50, s78, 589824\n"
      "buffer_load_dwordx4 v10, s[40:43], s50 offen lds\n"
      "s_add_u32 m0, s73, 17408\n"
      "s_add_u32 s50, s78, 626688\n"
      "buffer_load_dwordx4 v10, s[40:43], s50 offen lds\n"
      "s_add_u32 m0, s73, 24576\n"
      "s_add_u32 s50, s78, 884736\n"
      "buffer_load_dwordx4 v10, s[40:43], s50 offen lds\n"
      "s_add_u32 m0, s73, 25600\n"
      "s_add_u32 s50, s78, 921600\n"
      "buffer_load_dwordx4 v10, s[40:43], s50 offen lds\n"
      "s_add_u32 m0, s73, 32768\n"
      "s_add_u32 s50, s78, 1179648\n"
      "buffer_load_dwordx4 v10, s[40:43], s50 offen lds\n"
      "s_add_u32 m0, s73, 33792\n"
      "s_add_u32 s50, s78, 1216512\n"
      "buffer_load_dwordx4 v10, s[40:43], s50 offen lds\n"
      "s_add_u32 m0, s73, 40960\n"
      "s_add_u32 s50, s78, 1474560\n"
      "buffer_load_dwordx4 v10, s[40:43], s50 offen lds\n"
      "s_add_u32 m0, s73, 41984\n"
      "s_add_u32 s50, s78, 1511424\n"
      "buffer_load_dwordx4 v10, s[40:43], s50 offen lds\n"
      "s_add_u32 m0, s73, 49152\n"
      "s_add_u32 s50, s78, 1769472\n"
      "buffer_load_dwordx4 v10, s[40:43], s50 offen lds\n"
      "s_add_u32 m0, s73, 50176\n"
      "s_add_u32 s50, s78, 1806336\n"
      "buffer_load_dwordx4 v10, s[40:43], s50 offen lds\n"
      "s_add_u32 m0, s73, 57344\n"
      "s_add_u32 s50, s78, 2064384\n"
      "buffer_load_dwordx4 v10, s[40:43], s50 offen lds\n"
      "s_add_u32 m0, s73, 58368\n"
      "s_add_u32 s50, s78, 2101248\n"
      "buffer_load_dwordx4 v10, s[40:43], s50 offen lds\n"
      "s_add_u32 m0, s73, 65536\n"
      "s_add_u32 s50, s78, 2359296\n"
      "buffer_load_dwordx4 v10, s[40:43], s50 offen lds\n"
      "s_add_u32 m0, s73, 66560\n"
      "s_add_u32 s50, s78, 2396160\n"
      "buffer_load_dwordx4 v10, s[40:43], s50 offen lds\n"
      "v_mov_b32 v96, 0\n"
      "v_mov_b32 v97, 0\n"
      "v_mov_b32 v98, 0\n"
      "v_mov_b32 v99, 0\n"
      "v_mov_b32 v100, 0\n"
      "v_mov_b32 v101, 0\n"
      "v_mov_b32 v20, 0x3f80\n"
      "v_mov_b32 v21, 0x3f800000\n"
      "s_mov_b32 vcc_lo, 0x100001\n"
      "s_mov_b32 vcc_hi, 0x10000100\n"
      "v_cndmask_b32 v98, v98, v20, vcc\n"
      "s_mov_b32 vcc_lo, 0x200002\n"
      "s_mov_b32 vcc_hi, 0x20000200\n"
      "v_cndmask_b32 v98, v98, v21, vcc\n"
      "s_mov_b32 vcc_lo, 0x400004\n"
      "s_mov_b32 vcc_hi, 0x40000400\n"
      "v_cndmask_b32 v99, v99, v20, vcc\n"
      "s_mov_b32 vcc_lo, 0x800008\n"
      "s_mov_b32 vcc_hi, 0x80000800\n"
      "v_cndmask_b32 v99, v99, v21, vcc\n"
      "s_mov_b32 s66, -1\n"
      "s_mov_b32 s75, 0\n"
      "s_mul_i32 s68, s29, 1331200\n"
      "s_lshl_b32 s50, s28, 12\n"
      "s_add_u32 s68, s68, s50\n"
      "s_add_u32 s69, s68, 10526720\n"
      "s_add_u32 s68, s69, 40960\n"
      "s_mov_b32 s79, s50\n"
      "s_lshl_b32 s74, s28, 10\n"
      "s_add_u32 s74, s74, 16384\n"
      "s_sub_u32 s51, s74, s50\n"
      "v_add_u32 v4, s51, v2\n"
      "s_cmp_lt_u32 s28, 3\n"
      "s_cselect_b32 s51, 64, 0\n"
      "v_mov_b32 v40, 0x40000000\n"
      "v_cmp_gt_u32 vcc, s51, v1\n"
      "s_nop 1\n"
      "v_cndmask_b32 v4, v40, v4, vcc\n"
      "s_mul_i32 s70, s29, 9437184\n"
      "s_lshl_b32 s51, s30, 2\n"
      "s_add_u32 s51, s51, s28\n"
      "s_lshl_b32 s51, s51, 10\n"
      "s_add_u32 s70, s70, s51\n"
      "s_add_u32 s70, s70, 15704064\n"
      "v_and_b32 v41, 4, v1\n"
      "s_lshl_b32 s51, s29, 10\n"
      "s_add_u32 s51, s51, 81928192\n"
      "v_lshlrev_b32 v41, 7, v41\n"
      "v_add_u32 v5, s51, v41\n"
      "v_mov_b32 v6, 0\n"
      "v_mov_b32 v83, 0\n"
      "v_lshlrev_b32 v80, 3, v1\n"
      "v_mov_b32 v9, v80\n"
      "v_mov_b32 v3, v2\n"
      "v_mov_b32 v81, 0x3ae38e39\n"
      "s_mov_b32 s71, 0x40000000\n"
      "s_waitcnt vmcnt(12)\n"
      "s_barrier\n"
      "ds_read_b64_tr_b16 a[0:1], v11 offset:0\n"
      "ds_read_b64_tr_b16 a[2:3], v11 offset:1024\n"
      "ds_read_b64_tr_b16 a[4:5], v11 offset:8\n"
      "ds_read_b64_tr_b16 a[6:7], v11 offset:1032\n"
      "ds_read_b64_tr_b16 a[8:9], v11 offset:8192\n"
      "ds_read_b64_tr_b16 a[10:11], v11 offset:9216\n"
      "ds_read_b64_tr_b16 a[12:13], v11 offset:8200\n"
      "ds_read_b64_tr_b16 a[14:15], v11 offset:9224\n"
      "ds_read_b64_tr_b16 a[16:17], v11 offset:16384\n"
      "ds_read_b64_tr_b16 a[18:19], v11 offset:17408\n"
      "ds_read_b64_tr_b16 a[20:21], v11 offset:16392\n"
      "ds_read_b64_tr_b16 a[22:23], v11 offset:17416\n"
      "s_add_u32 m0, s73, 73728\n"
      "s_add_u32 s50, s78, 2654208\n"
      "buffer_load_dwordx4 v10, s[40:43], s50 offen lds\n"
      "s_add_u32 m0, s73, 74752\n"
      "s_add_u32 s50, s78, 2691072\n"
      "buffer_load_dwordx4 v10, s[40:43], s50 offen lds\n"
      "s_add_u32 m0, s73, 81920\n"
      "s_add_u32 s50, s78, 2949120\n"
      "buffer_load_dwordx4 v10, s[40:43], s50 offen lds\n"
      "s_add_u32 m0, s73, 82944\n"
      "s_add_u32 s50, s78, 2985984\n"
      "buffer_load_dwordx4 v10, s[40:43], s50 offen lds\n"
      "s_add_u32 m0, s73, 90112\n"
      "s_add_u32 s50, s78, 3244032\n"
      "buffer_load_dwordx4 v10, s[40:43], s50 offen lds\n"
      "s_add_u32 m0, s73, 91136\n"
      "s_add_u32 s50, s78, 3280896\n"
      "buffer_load_dwordx4 v10, s[40:43], s50 offen lds\n"
      "s_waitcnt vmcnt(12) lgkmcnt(0)\n"
      "s_barrier\n"
      "ds_read_b64_tr_b16 a[24:25], v11 offset:24576\n"
      "ds_read_b64_tr_b16 a[26:27], v11 offset:25600\n"
      "ds_read_b64_tr_b16 a[28:29], v11 offset:24584\n"
      "ds_read_b64_tr_b16 a[30:31], v11 offset:25608\n"
      "ds_read_b64_tr_b16 a[32:33], v11 offset:32768\n"
      "ds_read_b64_tr_b16 a[34:35], v11 offset:33792\n"
      "ds_read_b64_tr_b16 a[36:37], v11 offset:32776\n"
      "ds_read_b64_tr_b16 a[38:39], v11 offset:33800\n"
      "ds_read_b64_tr_b16 a[40:41], v11 offset:40960\n"
      "ds_read_b64_tr_b16 a[42:43], v11 offset:41984\n"
      "ds_read_b64_tr_b16 a[44:45], v11 offset:40968\n"
      "ds_read_b64_tr_b16 a[46:47], v11 offset:41992\n"
      "v_mfma_f32_16x16x32_bf16 v[16:19], v[12:15], a[0:3], 0\n"
      "v_mfma_f32_16x16x32_bf16 v[20:23], v[12:15], a[4:7], 0\n"
      "v_mfma_f32_16x16x32_bf16 v[16:19], v[12:15], a[8:11], v[16:19]\n"
      "v_mfma_f32_16x16x32_bf16 v[20:23], v[12:15], a[12:15], v[20:23]\n"
      "v_mfma_f32_16x16x32_bf16 v[16:19], v[12:15], a[16:19], v[16:19]\n"
      "v_mfma_f32_16x16x32_bf16 v[20:23], v[12:15], a[20:23], v[20:23]\n"
      "s_add_u32 m0, s73, 98304\n"
      "s_add_u32 s50, s78, 3538944\n"
      "buffer_load_dwordx4 v10, s[40:43], s50 offen lds\n"
      "s_add_u32 m0, s73, 99328\n"
      "s_add_u32 s50, s78, 3575808\n"
      "buffer_load_dwordx4 v10, s[40:43], s50 offen lds\n"
      "s_add_u32 m0, s73, 106496\n"
      "s_add_u32 s50, s78, 3833856\n"
      "buffer_load_dwordx4 v10, s[40:43], s50 offen lds\n"
      "s_add_u32 m0, s73, 107520\n"
      "s_add_u32 s50, s78, 3870720\n"
      "buffer_load_dwordx4 v10, s[40:43], s50 offen lds\n"
      "s_add_u32 m0, s73, 114688\n"
      "s_add_u32 s50, s78, 4128768\n"
      "buffer_load_dwordx4 v10, s[40:43], s50 offen lds\n"
      "s_add_u32 m0, s73, 115712\n"
      "s_add_u32 s50, s78, 4165632\n"
      "buffer_load_dwordx4 v10, s[40:43], s50 offen lds\n"
      "s_waitcnt vmcnt(12) lgkmcnt(0)\n"
      "s_barrier\n"
      "ds_read_b64_tr_b16 a[48:49], v11 offset:49152\n"
      "ds_read_b64_tr_b16 a[50:51], v11 offset:50176\n"
      "ds_read_b64_tr_b16 a[52:53], v11 offset:49160\n"
      "ds_read_b64_tr_b16 a[54:55], v11 offset:50184\n"
      "ds_read_b64_tr_b16 a[56:57], v11 offset:57344\n"
      "ds_read_b64_tr_b16 a[58:59], v11 offset:58368\n"
      "ds_read_b64_tr_b16 a[60:61], v11 offset:57352\n"
      "ds_read_b64_tr_b16 a[62:63], v11 offset:58376\n"
      "ds_read_b64_tr_b16 a[64:65], v66 offset:0\n"
      "ds_read_b64_tr_b16 a[66:67], v66 offset:1024\n"
      "ds_read_b64_tr_b16 a[68:69], v66 offset:8\n"
      "ds_read_b64_tr_b16 a[70:71], v66 offset:1032\n"
      "v_mfma_f32_16x16x32_bf16 v[16:19], v[12:15], a[24:27], v[16:19]\n"
      "v_mfma_f32_16x16x32_bf16 v[20:23], v[12:15], a[28:31], v[20:23]\n"
      "v_mfma_f32_16x16x32_bf16 v[16:19], v[12:15], a[32:35], v[16:19]\n"
      "v_mfma_f32_16x16x32_bf16 v[20:23], v[12:15], a[36:39], v[20:23]\n"
      "v_mfma_f32_16x16x32_bf16 v[16:19], v[12:15], a[40:43], v[16:19]\n"
      "v_mfma_f32_16x16x32_bf16 v[20:23], v[12:15], a[44:47], v[20:23]\n"
      "s_add_u32 m0, s73, 122880\n"
      "s_add_u32 s50, s78, 4423680\n"
      "buffer_load_dwordx4 v10, s[40:43], s50 offen lds\n"
      "s_add_u32 m0, s73, 123904\n"
      "s_add_u32 s50, s78, 4460544\n"
      "buffer_load_dwordx4 v10, s[40:43], s50 offen lds\n"
      "s_add_u32 m0, s73, 131072\n"
      "s_add_u32 s50, s78, 4718592\n"
      "buffer_load_dwordx4 v10, s[40:43], s50 offen lds\n"
      "s_add_u32 m0, s73, 132096\n"
      "s_add_u32 s50, s78, 4755456\n"
      "buffer_load_dwordx4 v10, s[40:43], s50 offen lds\n"
      "s_add_u32 m0, s73, 139264\n"
      "s_add_u32 s50, s78, 5013504\n"
      "buffer_load_dwordx4 v10, s[40:43], s50 offen lds\n"
      "s_add_u32 m0, s73, 140288\n"
      "s_add_u32 s50, s78, 5050368\n"
      "buffer_load_dwordx4 v10, s[40:43], s50 offen lds\n"
      "s_waitcnt vmcnt(12) lgkmcnt(0)\n"
      "s_barrier\n"
      "ds_read_b64_tr_b16 a[72:73], v66 offset:8192\n"
      "ds_read_b64_tr_b16 a[74:75], v66 offset:9216\n"
      "ds_read_b64_tr_b16 a[76:77], v66 offset:8200\n"
      "ds_read_b64_tr_b16 a[78:79], v66 offset:9224\n"
      "ds_read_b64_tr_b16 a[80:81], v66 offset:16384\n"
      "ds_read_b64_tr_b16 a[82:83], v66 offset:17408\n"
      "ds_read_b64_tr_b16 a[84:85], v66 offset:16392\n"
      "ds_read_b64_tr_b16 a[86:87], v66 offset:17416\n"
      "ds_read_b64_tr_b16 a[88:89], v66 offset:24576\n"
      "ds_read_b64_tr_b16 a[90:91], v66 offset:25600\n"
      "ds_read_b64_tr_b16 a[92:93], v66 offset:24584\n"
      "ds_read_b64_tr_b16 a[94:95], v66 offset:25608\n"
      "v_mfma_f32_16x16x32_bf16 v[16:19], v[12:15], a[48:51], v[16:19]\n"
      "v_mfma_f32_16x16x32_bf16 v[20:23], v[12:15], a[52:55], v[20:23]\n"
      "v_mfma_f32_16x16x32_bf16 v[16:19], v[12:15], a[56:59], v[16:19]\n"
      "v_mfma_f32_16x16x32_bf16 v[20:23], v[12:15], a[60:63], v[20:23]\n"
      "v_mfma_f32_16x16x32_bf16 v[16:19], v[12:15], a[64:67], v[16:19]\n"
      "v_mfma_f32_16x16x32_bf16 v[20:23], v[12:15], a[68:71], v[20:23]\n"
      "s_add_u32 m0, s73, 0\n"
      "s_add_u32 s50, s78, 0\n"
      "buffer_load_dwordx4 v10, s[44:47], s50 offen lds\n"
      "s_add_u32 m0, s73, 1024\n"
      "s_add_u32 s50, s78, 36864\n"
      "buffer_load_dwordx4 v10, s[44:47], s50 offen lds\n"
      "s_add_u32 m0, s73, 8192\n"
      "s_add_u32 s50, s78, 294912\n"
      "buffer_load_dwordx4 v10, s[44:47], s50 offen lds\n"
      "s_add_u32 m0, s73, 9216\n"
      "s_add_u32 s50, s78, 331776\n"
      "buffer_load_dwordx4 v10, s[44:47], s50 offen lds\n"
      "s_add_u32 m0, s73, 16384\n"
      "s_add_u32 s50, s78, 589824\n"
      "buffer_load_dwordx4 v10, s[44:47], s50 offen lds\n"
      "s_add_u32 m0, s73, 17408\n"
      "s_add_u32 s50, s78, 626688\n"
      "buffer_load_dwordx4 v10, s[44:47], s50 offen lds\n"
      "s_waitcnt vmcnt(12) lgkmcnt(0)\n"
      "s_barrier\n"
      "ds_read_b64_tr_b16 a[96:97], v66 offset:32768\n"
      "ds_read_b64_tr_b16 a[98:99], v66 offset:33792\n"
      "ds_read_b64_tr_b16 a[100:101], v66 offset:32776\n"
      "ds_read_b64_tr_b16 a[102:103], v66 offset:33800\n"
      "ds_read_b64_tr_b16 a[104:105], v66 offset:40960\n"
      "ds_read_b64_tr_b16 a[106:107], v66 offset:41984\n"
      "ds_read_b64_tr_b16 a[108:109], v66 offset:40968\n"
      "ds_read_b64_tr_b16 a[110:111], v66 offset:41992\n"
      "ds_read_b64_tr_b16 a[112:113], v66 offset:49152\n"
      "ds_read_b64_tr_b16 a[114:115], v66 offset:50176\n"
      "ds_read_b64_tr_b16 a[116:117], v66 offset:49160\n"
      "ds_read_b64_tr_b16 a[118:119], v66 offset:50184\n"
      "v_mfma_f32_16x16x32_bf16 v[16:19], v[12:15], a[72:75], v[16:19]\n"
      "v_mfma_f32_16x16x32_bf16 v[20:23], v[12:15], a[76:79], v[20:23]\n"
      "v_mfma_f32_16x16x32_bf16 v[16:19], v[12:15], a[80:83], v[16:19]\n"
      "v_mfma_f32_16x16x32_bf16 v[20:23], v[12:15], a[84:87], v[20:23]\n"
      "v_mfma_f32_16x16x32_bf16 v[16:19], v[12:15], a[88:91], v[16:19]\n"
      "v_mfma_f32_16x16x32_bf16 v[20:23], v[12:15], a[92:95], v[20:23]\n"
      "s_add_u32 m0, s73, 24576\n"
      "s_add_u32 s50, s78, 884736\n"
      "buffer_load_dwordx4 v10, s[44:47], s50 offen lds\n"
      "s_add_u32 m0, s73, 25600\n"
      "s_add_u32 s50, s78, 921600\n"
      "buffer_load_dwordx4 v10, s[44:47], s50 offen lds\n"
      "s_add_u32 m0, s73, 32768\n"
      "s_add_u32 s50, s78, 1179648\n"
      "buffer_load_dwordx4 v10, s[44:47], s50 offen lds\n"
      "s_add_u32 m0, s73, 33792\n"
      "s_add_u32 s50, s78, 1216512\n"
      "buffer_load_dwordx4 v10, s[44:47], s50 offen lds\n"
      "s_add_u32 m0, s73, 40960\n"
      "s_add_u32 s50, s78, 1474560\n"
      "buffer_load_dwordx4 v10, s[44:47], s50 offen lds\n"
      "s_add_u32 m0, s73, 41984\n"
      "s_add_u32 s50, s78, 1511424\n"
      "buffer_load_dwordx4 v10, s[44:47], s50 offen lds\n"
      "s_waitcnt vmcnt(12) lgkmcnt(0)\n"
      "s_barrier\n"
      "ds_read_b64_tr_b16 a[120:121], v66 offset:57344\n"
      "ds_read_b64_tr_b16 a[122:123], v66 offset:58368\n"
      "ds_read_b64_tr_b16 a[124:125], v66 offset:57352\n"
      "ds_read_b64_tr_b16 a[126:127], v66 offset:58376\n"
      "ds_read_b64_tr_b16 a[128:129], v67 offset:0\n"
      "ds_read_b64_tr_b16 a[130:131], v67 offset:1024\n"
      "ds_read_b64_tr_b16 a[132:133], v67 offset:8\n"
      "ds_read_b64_tr_b16 a[134:135], v67 offset:1032\n"
      "ds_read_b64_tr_b16 a[136:137], v67 offset:8192\n"
      "ds_read_b64_tr_b16 a[138:139], v67 offset:9216\n"
      "ds_read_b64_tr_b16 a[140:141], v67 offset:8200\n"
      "ds_read_b64_tr_b16 a[142:143], v67 offset:9224\n"
      "v_mfma_f32_16x16x32_bf16 v[16:19], v[12:15], a[96:99], v[16:19]\n"
      "v_mfma_f32_16x16x32_bf16 v[20:23], v[12:15], a[100:103], v[20:23]\n"
      "v_mfma_f32_16x16x32_bf16 v[16:19], v[12:15], a[104:107], v[16:19]\n"
      "v_mfma_f32_16x16x32_bf16 v[20:23], v[12:15], a[108:111], v[20:23]\n"
      "v_mfma_f32_16x16x32_bf16 v[16:19], v[12:15], a[112:115], v[16:19]\n"
      "v_mfma_f32_16x16x32_bf16 v[20:23], v[12:15], a[116:119], v[20:23]\n"
      "s_add_u32 m0, s73, 49152\n"
      "s_add_u32 s50, s78, 1769472\n"
      "buffer_load_dwordx4 v10, s[44:47], s50 offen lds\n"
      "s_add_u32 m0, s73, 50176\n"
      "s_add_u32 s50, s78, 1806336\n"
      "buffer_load_dwordx4 v10, s[44:47], s50 offen lds\n"
      "s_add_u32 m0, s73, 57344\n"
      "s_add_u32 s50, s78, 2064384\n"
      "buffer_load_dwordx4 v10, s[44:47], s50 offen lds\n"
      "s_add_u32 m0, s73, 58368\n"
      "s_add_u32 s50, s78, 2101248\n"
      "buffer_load_dwordx4 v10, s[44:47], s50 offen lds\n"
      "s_add_u32 m0, s73, 65536\n"
      "s_add_u32 s50, s78, 2359296\n"
      "buffer_load_dwordx4 v10, s[44:47], s50 offen lds\n"
      "s_add_u32 m0, s73, 66560\n"
      "s_add_u32 s50, s78, 2396160\n"
      "buffer_load_dwordx4 v10, s[44:47], s50 offen lds\n"
      "s_waitcnt vmcnt(12) lgkmcnt(0)\n"
      "s_barrier\n"
      "ds_read_b64_tr_b16 v[104:105], v11 offset:0\n"
      "ds_read_b64_tr_b16 v[106:107], v11 offset:1024\n"
      "ds_read_b64_tr_b16 v[108:109], v11 offset:8\n"
      "ds_read_b64_tr_b16 v[110:111], v11 offset:1032\n"
      "ds_read_b64_tr_b16 v[112:113], v11 offset:8192\n"
      "ds_read_b64_tr_b16 v[114:115], v11 offset:9216\n"
      "ds_read_b64_tr_b16 v[116:117], v11 offset:8200\n"
      "ds_read_b64_tr_b16 v[118:119], v11 offset:9224\n"
      "ds_read_b64_tr_b16 v[120:121], v11 offset:16384\n"
      "ds_read_b64_tr_b16 v[122:123], v11 offset:17408\n"
      "ds_read_b64_tr_b16 v[124:125], v11 offset:16392\n"
      "ds_read_b64_tr_b16 v[126:127], v11 offset:17416\n"
      "v_mfma_f32_16x16x32_bf16 v[16:19], v[12:15], a[120:123], v[16:19]\n"
      "v_mfma_f32_16x16x32_bf16 v[20:23], v[12:15], a[124:127], v[20:23]\n"
      "v_mfma_f32_16x16x32_bf16 v[16:19], v[12:15], a[128:131], v[16:19]\n"
      "v_mfma_f32_16x16x32_bf16 v[20:23], v[12:15], a[132:135], v[20:23]\n"
      "v_mfma_f32_16x16x32_bf16 v[16:19], v[12:15], a[136:139], v[16:19]\n"
      "v_mfma_f32_16x16x32_bf16 v[20:23], v[12:15], a[140:143], v[20:23]\n"
      "s_add_u32 m0, s73, 73728\n"
      "s_add_u32 s50, s78, 2654208\n"
      "buffer_load_dwordx4 v10, s[44:47], s50 offen lds\n"
      "s_add_u32 m0, s73, 74752\n"
      "s_add_u32 s50, s78, 2691072\n"
      "buffer_load_dwordx4 v10, s[44:47], s50 offen lds\n"
      "s_add_u32 m0, s73, 81920\n"
      "s_add_u32 s50, s78, 2949120\n"
      "buffer_load_dwordx4 v10, s[44:47], s50 offen lds\n"
      "s_add_u32 m0, s73, 82944\n"
      "s_add_u32 s50, s78, 2985984\n"
      "buffer_load_dwordx4 v10, s[44:47], s50 offen lds\n"
      "s_add_u32 m0, s73, 90112\n"
      "s_add_u32 s50, s78, 3244032\n"
      "buffer_load_dwordx4 v10, s[44:47], s50 offen lds\n"
      "s_add_u32 m0, s73, 91136\n"
      "s_add_u32 s50, s78, 3280896\n"
      "buffer_load_dwordx4 v10, s[44:47], s50 offen lds\n"
      "v_mul_f32 v16, 0xbae38e39, v16\n"
      "v_mul_f32 v17, 0xbae38e39, v17\n"
      "v_mul_f32 v18, 0xbae38e39, v18\n"
      "v_mul_f32 v19, 0xbae38e39, v19\n"
      "v_mul_f32 v20, 0xbae38e39, v20\n"
      "v_mul_f32 v21, 0xbae38e39, v21\n"
      "v_mul_f32 v22, 0xbae38e39, v22\n"
      "v_mul_f32 v23, 0xbae38e39, v23\n"
      "v_mfma_f32_16x16x32_bf16 v[32:35], v[12:15], a[136:139], 0\n"
      "v_mfma_f32_16x16x32_bf16 v[36:39], v[12:15], a[140:143], 0\n"
      "v_mfma_f32_16x16x32_bf16 v[48:51], v[98:101], a[0:3], v[16:19]\n"
      "v_mfma_f32_16x16x32_bf16 v[52:55], v[96:99], a[0:3], v[16:19]\n"
      "v_mfma_f32_16x16x32_bf16 v[68:71], v[98:101], a[4:7], v[20:23]\n"
      "v_mfma_f32_16x16x32_bf16 v[72:75], v[96:99], a[4:7], v[20:23]\n"
      "v_mfma_f32_16x16x32_bf16 v[84:87], v[98:101], a[8:11], v[16:19]\n"
      "s_nop 3\n"
      "v_cvt_pk_bf16_f32 v48, v48, v49\n"
      "v_cvt_pk_bf16_f32 v50, v50, v51\n"
      "v_cvt_pk_bf16_f32 v52, v52, v53\n"
      "v_mfma_f32_16x16x32_bf16 v[88:91], v[96:99], a[8:11], v[16:19]\n"
      "v_cvt_pk_bf16_f32 v54, v54, v55\n"
      "v_accvgpr_write_b32 a0, v48\n"
      "v_accvgpr_write_b32 a1, v50\n"
      "v_accvgpr_write_b32 a2, v52\n"
      "v_accvgpr_write_b32 a3, v54\n"
      "v_mfma_f32_16x16x32_bf16 v[48:51], v[98:101], a[12:15], v[20:23]\n"
      "v_cvt_pk_bf16_f32 v68, v68, v69\n"
      "v_cvt_pk_bf16_f32 v70, v70, v71\n"
      "v_cvt_pk_bf16_f32 v72, v72, v73\n"
      "v_mfma_f32_16x16x32_bf16 v[52:55], v[96:99], a[12:15], v[20:23]\n"
      "v_cvt_pk_bf16_f32 v74, v74, v75\n"
      "v_accvgpr_write_b32 a4, v68\n"
      "v_accvgpr_write_b32 a5, v70\n"
      "v_mfma_f32_16x16x32_bf16 v[32:35], v[12:15], a[0:3], v[32:35]\n"
      "v_accvgpr_write_b32 a6, v72\n"
      "v_accvgpr_write_b32 a7, v74\n"
      "v_mfma_f32_16x16x32_bf16 v[68:71], v[98:101], a[16:19], v[16:19]\n"
      "v_cvt_pk_bf16_f32 v84, v84, v85\n"
      "v_cvt_pk_bf16_f32 v86, v86, v87\n"
      "v_cvt_pk_bf16_f32 v88, v88, v89\n"
      "v_mfma_f32_16x16x32_bf16 v[72:75], v[96:99], a[16:19], v[16:19]\n"
      "v_cvt_pk_bf16_f32 v90, v90, v91\n"
      "v_accvgpr_write_b32 a8, v84\n"
      "v_accvgpr_write_b32 a9, v86\n"
      "v_mfma_f32_16x16x32_bf16 v[36:39], v[12:15], a[4:7], v[36:39]\n"
      "v_accvgpr_write_b32 a10, v88\n"
      "v_accvgpr_write_b32 a11, v90\n"
      "v_mfma_f32_16x16x32_bf16 v[84:87], v[98:101], a[20:23], v[20:23]\n"
      "v_cvt_pk_bf16_f32 v48, v48, v49\n"
      "v_cvt_pk_bf16_f32 v50, v50, v51\n"
      "v_cvt_pk_bf16_f32 v52, v52, v53\n"
      "v_mfma_f32_16x16x32_bf16 v[88:91], v[96:99], a[20:23], v[20:23]\n"
      "v_cvt_pk_bf16_f32 v54, v54, v55\n"
      "v_accvgpr_write_b32 a12, v48\n"
      "v_accvgpr_write_b32 a13, v50\n"
      "v_mfma_f32_16x16x32_bf16 v[32:35], v[12:15], a[8:11], v[32:35]\n"
      "v_accvgpr_write_b32 a14, v52\n"
      "v_accvgpr_write_b32 a15, v54\n"
      "v_mfma_f32_16x16x32_bf16 v[48:51], v[98:101], a[24:27], v[16:19]\n"
      "v_cvt_pk_bf16_f32 v68, v68, v69\n"
      "v_cvt_pk_bf16_f32 v70, v70, v71\n"
      "v_cvt_pk_bf16_f32 v72, v72, v73\n"
      "v_mfma_f32_16x16x32_bf16 v[52:55], v[96:99], a[24:27], v[16:19]\n"
      "v_cvt_pk_bf16_f32 v74, v74, v75\n"
      "v_accvgpr_write_b32 a16, v68\n"
      "v_accvgpr_write_b32 a17, v70\n"
      "v_mfma_f32_16x16x32_bf16 v[36:39], v[12:15], a[12:15], v[36:39]\n"
      "v_accvgpr_write_b32 a18, v72\n"
      "v_accvgpr_write_b32 a19, v74\n"
      "s_waitcnt vmcnt(12) lgkmcnt(0)\n"
      "s_barrier\n"
      "ds_read_b64_tr_b16 v[128:129], v11 offset:24576\n"
      "ds_read_b64_tr_b16 v[130:131], v11 offset:25600\n"
      "ds_read_b64_tr_b16 v[132:133], v11 offset:24584\n"
      "ds_read_b64_tr_b16 v[134:135], v11 offset:25608\n"
      "ds_read_b64_tr_b16 v[136:137], v11 offset:32768\n"
      "ds_read_b64_tr_b16 v[138:139], v11 offset:33792\n"
      "ds_read_b64_tr_b16 v[140:141], v11 offset:32776\n"
      "ds_read_b64_tr_b16 v[142:143], v11 offset:33800\n"
      "ds_read_b64_tr_b16 v[144:145], v11 offset:40960\n"
      "ds_read_b64_tr_b16 v[146:147], v11 offset:41984\n"
      "ds_read_b64_tr_b16 v[148:149], v11 offset:40968\n"
      "ds_read_b64_tr_b16 v[150:151], v11 offset:41992\n"
      "v_mfma_f32_16x16x32_bf16 v[24:27], v[12:15], v[104:107], 0\n"
      "v_mfma_f32_16x16x32_bf16 v[28:31], v[12:15], v[108:111], 0\n"
      "v_mfma_f32_16x16x32_bf16 v[24:27], v[12:15], v[112:115], v[24:27]\n"
      "v_mfma_f32_16x16x32_bf16 v[28:31], v[12:15], v[116:119], v[28:31]\n"
      "v_mfma_f32_16x16x32_bf16 v[24:27], v[12:15], v[120:123], v[24:27]\n"
      "v_mfma_f32_16x16x32_bf16 v[28:31], v[12:15], v[124:127], v[28:31]\n"
      "s_add_u32 m0, s73, 98304\n"
      "s_add_u32 s50, s78, 3538944\n"
      "buffer_load_dwordx4 v10, s[44:47], s50 offen lds\n"
      "s_add_u32 m0, s73, 99328\n"
      "s_add_u32 s50, s78, 3575808\n"
      "buffer_load_dwordx4 v10, s[44:47], s50 offen lds\n"
      "s_add_u32 m0, s73, 106496\n"
      "s_add_u32 s50, s78, 3833856\n"
      "buffer_load_dwordx4 v10, s[44:47], s50 offen lds\n"
      "s_add_u32 m0, s73, 107520\n"
      "s_add_u32 s50, s78, 3870720\n"
      "buffer_load_dwordx4 v10, s[44:47], s50 offen lds\n"
      "s_add_u32 m0, s73, 114688\n"
      "s_add_u32 s50, s78, 4128768\n"
      "buffer_load_dwordx4 v10, s[44:47], s50 offen lds\n"
      "s_add_u32 m0, s73, 115712\n"
      "s_add_u32 s50, s78, 4165632\n"
      "buffer_load_dwordx4 v10, s[44:47], s50 offen lds\n"
      "v_mfma_f32_16x16x32_bf16 v[68:71], v[98:101], a[28:31], v[20:23]\n"
      "v_cvt_pk_bf16_f32 v84, v84, v85\n"
      "v_cvt_pk_bf16_f32 v86, v86, v87\n"
      "v_cvt_pk_bf16_f32 v88, v88, v89\n"
      "v_mfma_f32_16x16x32_bf16 v[72:75], v[96:99], a[28:31], v[20:23]\n"
      "v_cvt_pk_bf16_f32 v90, v90, v91\n"
      "v_accvgpr_write_b32 a20, v84\n"
      "v_accvgpr_write_b32 a21, v86\n"
      "v_mfma_f32_16x16x32_bf16 v[32:35], v[12:15], a[16:19], v[32:35]\n"
      "v_accvgpr_write_b32 a22, v88\n"
      "v_accvgpr_write_b32 a23, v90\n"
      "v_mfma_f32_16x16x32_bf16 v[84:87], v[98:101], a[32:35], v[16:19]\n"
      "v_cvt_pk_bf16_f32 v48, v48, v49\n"
      "v_cvt_pk_bf16_f32 v50, v50, v51\n"
      "v_cvt_pk_bf16_f32 v52, v52, v53\n"
      "v_mfma_f32_16x16x32_bf16 v[88:91], v[96:99], a[32:35], v[16:19]\n"
      "v_cvt_pk_bf16_f32 v54, v54, v55\n"
      "v_accvgpr_write_b32 a24, v48\n"
      "v_accvgpr_write_b32 a25, v50\n"
      "v_mfma_f32_16x16x32_bf16 v[36:39], v[12:15], a[20:23], v[36:39]\n"
      "v_accvgpr_write_b32 a26, v52\n"
      "v_accvgpr_write_b32 a27, v54\n"
      "v_mfma_f32_16x16x32_bf16 v[48:51], v[98:101], a[36:39], v[20:23]\n"
      "v_cvt_pk_bf16_f32 v68, v68, v69\n"
      "v_cvt_pk_bf16_f32 v70, v70, v71\n"
      "v_cvt_pk_bf16_f32 v72, v72, v73\n"
      "v_mfma_f32_16x16x32_bf16 v[52:55], v[96:99], a[36:39], v[20:23]\n"
      "v_cvt_pk_bf16_f32 v74, v74, v75\n"
      "v_accvgpr_write_b32 a28, v68\n"
      "v_accvgpr_write_b32 a29, v70\n"
      "v_mfma_f32_16x16x32_bf16 v[32:35], v[12:15], a[24:27], v[32:35]\n"
      "v_accvgpr_write_b32 a30, v72\n"
      "v_accvgpr_write_b32 a31, v74\n"
      "v_mfma_f32_16x16x32_bf16 v[68:71], v[98:101], a[40:43], v[16:19]\n"
      "v_cvt_pk_bf16_f32 v84, v84, v85\n"
      "v_cvt_pk_bf16_f32 v86, v86, v87\n"
      "v_cvt_pk_bf16_f32 v88, v88, v89\n"
      "v_mfma_f32_16x16x32_bf16 v[72:75], v[96:99], a[40:43], v[16:19]\n"
      "v_cvt_pk_bf16_f32 v90, v90, v91\n"
      "v_accvgpr_write_b32 a32, v84\n"
      "v_accvgpr_write_b32 a33, v86\n"
      "v_mfma_f32_16x16x32_bf16 v[36:39], v[12:15], a[28:31], v[36:39]\n"
      "v_accvgpr_write_b32 a34, v88\n"
      "v_accvgpr_write_b32 a35, v90\n"
      "v_mfma_f32_16x16x32_bf16 v[84:87], v[98:101], a[44:47], v[20:23]\n"
      "v_cvt_pk_bf16_f32 v48, v48, v49\n"
      "v_cvt_pk_bf16_f32 v50, v50, v51\n"
      "v_cvt_pk_bf16_f32 v52, v52, v53\n"
      "v_mfma_f32_16x16x32_bf16 v[88:91], v[96:99], a[44:47], v[20:23]\n"
      "v_cvt_pk_bf16_f32 v54, v54, v55\n"
      "v_accvgpr_write_b32 a36, v48\n"
      "v_accvgpr_write_b32 a37, v50\n"
      "v_mfma_f32_16x16x32_bf16 v[32:35], v[12:15], a[32:35], v[32:35]\n"
      "v_accvgpr_write_b32 a38, v52\n"
      "v_accvgpr_write_b32 a39, v54\n"
      "v_mfma_f32_16x16x32_bf16 v[48:51], v[98:101], a[48:51], v[16:19]\n"
      "v_cvt_pk_bf16_f32 v68, v68, v69\n"
      "v_cvt_pk_bf16_f32 v70, v70, v71\n"
      "v_cvt_pk_bf16_f32 v72, v72, v73\n"
      "v_mfma_f32_16x16x32_bf16 v[52:55], v[96:99], a[48:51], v[16:19]\n"
      "v_cvt_pk_bf16_f32 v74, v74, v75\n"
      "v_accvgpr_write_b32 a40, v68\n"
      "v_accvgpr_write_b32 a41, v70\n"
      "v_mfma_f32_16x16x32_bf16 v[36:39], v[12:15], a[36:39], v[36:39]\n"
      "v_accvgpr_write_b32 a42, v72\n"
      "v_accvgpr_write_b32 a43, v74\n"
      "v_mfma_f32_16x16x32_bf16 v[68:71], v[98:101], a[52:55], v[20:23]\n"
      "v_cvt_pk_bf16_f32 v84, v84, v85\n"
      "v_cvt_pk_bf16_f32 v86, v86, v87\n"
      "v_cvt_pk_bf16_f32 v88, v88, v89\n"
      "v_mfma_f32_16x16x32_bf16 v[72:75], v[96:99], a[52:55], v[20:23]\n"
      "v_cvt_pk_bf16_f32 v90, v90, v91\n"
      "v_accvgpr_write_b32 a44, v84\n"
      "v_accvgpr_write_b32 a45, v86\n"
      "v_mfma_f32_16x16x32_bf16 v[32:35], v[12:15], a[40:43], v[32:35]\n"
      "v_accvgpr_write_b32 a46, v88\n"
      "v_accvgpr_write_b32 a47, v90\n"
      "v_mfma_f32_16x16x32_bf16 v[84:87], v[98:101], a[56:59], v[16:19]\n"
      "v_cvt_pk_bf16_f32 v48, v48, v49\n"
      "v_cvt_pk_bf16_f32 v50, v50, v51\n"
      "v_cvt_pk_bf16_f32 v52, v52, v53\n"
      "v_mfma_f32_16x16x32_bf16 v[88:91], v[96:99], a[56:59], v[16:19]\n"
      "v_cvt_pk_bf16_f32 v54, v54, v55\n"
      "v_accvgpr_write_b32 a48, v48\n"
      "v_accvgpr_write_b32 a49, v50\n"
      "v_mfma_f32_16x16x32_bf16 v[36:39], v[12:15], a[44:47], v[36:39]\n"
      "v_accvgpr_write_b32 a50, v52\n"
      "v_accvgpr_write_b32 a51, v54\n"
      "s_waitcnt vmcnt(12) lgkmcnt(0)\n"
      "s_barrier\n"
      "ds_read_b64_tr_b16 v[152:153], v11 offset:49152\n"
      "ds_read_b64_tr_b16 v[154:155], v11 offset:50176\n"
      "ds_read_b64_tr_b16 v[156:157], v11 offset:49160\n"
      "ds_read_b64_tr_b16 v[158:159], v11 offset:50184\n"
      "ds_read_b64_tr_b16 v[160:161], v11 offset:57344\n"
      "ds_read_b64_tr_b16 v[162:163], v11 offset:58368\n"
      "ds_read_b64_tr_b16 v[164:165], v11 offset:57352\n"
      "ds_read_b64_tr_b16 v[166:167], v11 offset:58376\n"
      "ds_read_b64_tr_b16 v[168:169], v66 offset:0\n"
      "ds_read_b64_tr_b16 v[170:171], v66 offset:1024\n"
      "ds_read_b64_tr_b16 v[172:173], v66 offset:8\n"
      "ds_read_b64_tr_b16 v[174:175], v66 offset:1032\n"
      "v_mfma_f32_16x16x32_bf16 v[24:27], v[12:15], v[128:131], v[24:27]\n"
      "v_mfma_f32_16x16x32_bf16 v[28:31], v[12:15], v[132:135], v[28:31]\n"
      "v_mfma_f32_16x16x32_bf16 v[24:27], v[12:15], v[136:139], v[24:27]\n"
      "v_mfma_f32_16x16x32_bf16 v[28:31], v[12:15], v[140:143], v[28:31]\n"
      "v_mfma_f32_16x16x32_bf16 v[24:27], v[12:15], v[144:147], v[24:27]\n"
      "v_mfma_f32_16x16x32_bf16 v[28:31], v[12:15], v[148:151], v[28:31]\n"
      "s_add_u32 m0, s73, 122880\n"
      "s_add_u32 s50, s78, 4423680\n"
      "buffer_load_dwordx4 v10, s[44:47], s50 offen lds\n"
      "s_add_u32 m0, s73, 123904\n"
      "s_add_u32 s50, s78, 4460544\n"
      "buffer_load_dwordx4 v10, s[44:47], s50 offen lds\n"
      "s_add_u32 m0, s73, 131072\n"
      "s_add_u32 s50, s78, 4718592\n"
      "buffer_load_dwordx4 v10, s[44:47], s50 offen lds\n"
      "s_add_u32 m0, s73, 132096\n"
      "s_add_u32 s50, s78, 4755456\n"
      "buffer_load_dwordx4 v10, s[44:47], s50 offen lds\n"
      "s_add_u32 m0, s73, 139264\n"
      "s_add_u32 s50, s78, 5013504\n"
      "buffer_load_dwordx4 v10, s[44:47], s50 offen lds\n"
      "s_add_u32 m0, s73, 140288\n"
      "s_add_u32 s50, s78, 5050368\n"
      "buffer_load_dwordx4 v10, s[44:47], s50 offen lds\n"
      "v_mfma_f32_16x16x32_bf16 v[48:51], v[98:101], a[60:63], v[20:23]\n"
      "v_cvt_pk_bf16_f32 v68, v68, v69\n"
      "v_cvt_pk_bf16_f32 v70, v70, v71\n"
      "v_cvt_pk_bf16_f32 v72, v72, v73\n"
      "v_mfma_f32_16x16x32_bf16 v[52:55], v[96:99], a[60:63], v[20:23]\n"
      "v_cvt_pk_bf16_f32 v74, v74, v75\n"
      "v_accvgpr_write_b32 a52, v68\n"
      "v_accvgpr_write_b32 a53, v70\n"
      "v_mfma_f32_16x16x32_bf16 v[32:35], v[12:15], a[48:51], v[32:35]\n"
      "v_accvgpr_write_b32 a54, v72\n"
      "v_accvgpr_write_b32 a55, v74\n"
      "v_mfma_f32_16x16x32_bf16 v[68:71], v[98:101], a[64:67], v[16:19]\n"
      "v_cvt_pk_bf16_f32 v84, v84, v85\n"
      "v_cvt_pk_bf16_f32 v86, v86, v87\n"
      "v_cvt_pk_bf16_f32 v88, v88, v89\n"
      "v_mfma_f32_16x16x32_bf16 v[72:75], v[96:99], a[64:67], v[16:19]\n"
      "v_cvt_pk_bf16_f32 v90, v90, v91\n"
      "v_accvgpr_write_b32 a56, v84\n"
      "v_accvgpr_write_b32 a57, v86\n"
      "v_mfma_f32_16x16x32_bf16 v[36:39], v[12:15], a[52:55], v[36:39]\n"
      "v_accvgpr_write_b32 a58, v88\n"
      "v_accvgpr_write_b32 a59, v90\n"
      "v_mfma_f32_16x16x32_bf16 v[84:87], v[98:101], a[68:71], v[20:23]\n"
      "v_cvt_pk_bf16_f32 v48, v48, v49\n"
      "v_cvt_pk_bf16_f32 v50, v50, v51\n"
      "v_cvt_pk_bf16_f32 v52, v52, v53\n"
      "v_mfma_f32_16x16x32_bf16 v[88:91], v[96:99], a[68:71], v[20:23]\n"
      "v_cvt_pk_bf16_f32 v54, v54, v55\n"
      "v_accvgpr_write_b32 a60, v48\n"
      "v_accvgpr_write_b32 a61, v50\n"
      "v_mfma_f32_16x16x32_bf16 v[32:35], v[12:15], a[56:59], v[32:35]\n"
      "v_accvgpr_write_b32 a62, v52\n"
      "v_accvgpr_write_b32 a63, v54\n"
      "v_mfma_f32_16x16x32_bf16 v[48:51], v[98:101], a[72:75], v[16:19]\n"
      "v_cvt_pk_bf16_f32 v68, v68, v69\n"
      "v_cvt_pk_bf16_f32 v70, v70, v71\n"
      "v_cvt_pk_bf16_f32 v72, v72, v73\n"
      "v_mfma_f32_16x16x32_bf16 v[52:55], v[96:99], a[72:75], v[16:19]\n"
      "v_cvt_pk_bf16_f32 v74, v74, v75\n"
      "v_accvgpr_write_b32 a64, v68\n"
      "v_accvgpr_write_b32 a65, v70\n"
      "v_mfma_f32_16x16x32_bf16 v[36:39], v[12:15], a[60:63], v[36:39]\n"
      "v_accvgpr_write_b32 a66, v72\n"
      "v_accvgpr_write_b32 a67, v74\n"
      "v_mfma_f32_16x16x32_bf16 v[68:71], v[98:101], a[76:79], v[20:23]\n"
      "v_cvt_pk_bf16_f32 v84, v84, v85\n"
      "v_cvt_pk_bf16_f32 v86, v86, v87\n"
      "v_cvt_pk_bf16_f32 v88, v88, v89\n"
      "v_mfma_f32_16x16x32_bf16 v[72:75], v[96:99], a[76:79], v[20:23]\n"
      "v_cvt_pk_bf16_f32 v90, v90, v91\n"
      "v_accvgpr_write_b32 a68, v84\n"
      "v_accvgpr_write_b32 a69, v86\n"
      "v_mfma_f32_16x16x32_bf16 v[32:35], v[12:15], a[64:67], v[32:35]\n"
      "v_accvgpr_write_b32 a70, v88\n"
      "v_accvgpr_write_b32 a71, v90\n"
      "v_mfma_f32_16x16x32_bf16 v[84:87], v[98:101], a[80:83], v[16:19]\n"
      "v_cvt_pk_bf16_f32 v48, v48, v49\n"
      "v_cvt_pk_bf16_f32 v50, v50, v51\n"
      "v_cvt_pk_bf16_f32 v52, v52, v53\n"
      "v_mfma_f32_16x16x32_bf16 v[88:91], v[96:99], a[80:83], v[16:19]\n"
      "v_cvt_pk_bf16_f32 v54, v54, v55\n"
      "v_accvgpr_write_b32 a72, v48\n"
      "v_accvgpr_write_b32 a73, v50\n"
      "v_mfma_f32_16x16x32_bf16 v[36:39], v[12:15], a[68:71], v[36:39]\n"
      "v_accvgpr_write_b32 a74, v52\n"
      "v_accvgpr_write_b32 a75, v54\n"
      "v_mfma_f32_16x16x32_bf16 v[48:51], v[98:101], a[84:87], v[20:23]\n"
      "v_cvt_pk_bf16_f32 v68, v68, v69\n"
      "v_cvt_pk_bf16_f32 v70, v70, v71\n"
      "v_cvt_pk_bf16_f32 v72, v72, v73\n"
      "v_mfma_f32_16x16x32_bf16 v[52:55], v[96:99], a[84:87], v[20:23]\n"
      "v_cvt_pk_bf16_f32 v74, v74, v75\n"
      "v_accvgpr_write_b32 a76, v68\n"
      "v_accvgpr_write_b32 a77, v70\n"
      "v_mfma_f32_16x16x32_bf16 v[32:35], v[12:15], a[72:75], v[32:35]\n"
      "v_accvgpr_write_b32 a78, v72\n"
      "v_accvgpr_write_b32 a79, v74\n"
      "v_mfma_f32_16x16x32_bf16 v[68:71], v[98:101], a[88:91], v[16:19]\n"
      "v_cvt_pk_bf16_f32 v84, v84, v85\n"
      "v_cvt_pk_bf16_f32 v86, v86, v87\n"
      "v_cvt_pk_bf16_f32 v88, v88, v89\n"
      "v_mfma_f32_16x16x32_bf16 v[72:75], v[96:99], a[88:91], v[16:19]\n"
      "v_cvt_pk_bf16_f32 v90, v90, v91\n"
      "v_accvgpr_write_b32 a80, v84\n"
      "v_accvgpr_write_b32 a81, v86\n"
      "v_mfma_f32_16x16x32_bf16 v[36:39], v[12:15], a[76:79], v[36:39]\n"
      "v_accvgpr_write_b32 a82, v88\n"
      "v_accvgpr_write_b32 a83, v90\n"
      "s_waitcnt vmcnt(12) lgkmcnt(0)\n"
      "s_barrier\n"
      "ds_read_b64_tr_b16 v[176:177], v66 offset:8192\n"
      "ds_read_b64_tr_b16 v[178:179], v66 offset:9216\n"
      "ds_read_b64_tr_b16 v[180:181], v66 offset:8200\n"
      "ds_read_b64_tr_b16 v[182:183], v66 offset:9224\n"
      "ds_read_b64_tr_b16 v[184:185], v66 offset:16384\n"
      "ds_read_b64_tr_b16 v[186:187], v66 offset:17408\n"
      "ds_read_b64_tr_b16 v[188:189], v66 offset:16392\n"
      "ds_read_b64_tr_b16 v[190:191], v66 offset:17416\n"
      "ds_read_b64_tr_b16 v[192:193], v66 offset:24576\n"
      "ds_read_b64_tr_b16 v[194:195], v66 offset:25600\n"
      "ds_read_b64_tr_b16 v[196:197], v66 offset:24584\n"
      "ds_read_b64_tr_b16 v[198:199], v66 offset:25608\n"
      "v_mfma_f32_16x16x32_bf16 v[24:27], v[12:15], v[152:155], v[24:27]\n"
      "v_mfma_f32_16x16x32_bf16 v[28:31], v[12:15], v[156:159], v[28:31]\n"
      "v_mfma_f32_16x16x32_bf16 v[24:27], v[12:15], v[160:163], v[24:27]\n"
      "v_mfma_f32_16x16x32_bf16 v[28:31], v[12:15], v[164:167], v[28:31]\n"
      "v_mfma_f32_16x16x32_bf16 v[24:27], v[12:15], v[168:171], v[24:27]\n"
      "v_mfma_f32_16x16x32_bf16 v[28:31], v[12:15], v[172:175], v[28:31]\n"
      "v_mfma_f32_16x16x32_bf16 v[84:87], v[98:101], a[92:95], v[20:23]\n"
      "v_cvt_pk_bf16_f32 v48, v48, v49\n"
      "v_cvt_pk_bf16_f32 v50, v50, v51\n"
      "v_cvt_pk_bf16_f32 v52, v52, v53\n"
      "v_mfma_f32_16x16x32_bf16 v[88:91], v[96:99], a[92:95], v[20:23]\n"
      "v_cvt_pk_bf16_f32 v54, v54, v55\n"
      "v_accvgpr_write_b32 a84, v48\n"
      "v_accvgpr_write_b32 a85, v50\n"
      "v_mfma_f32_16x16x32_bf16 v[32:35], v[12:15], a[80:83], v[32:35]\n"
      "v_accvgpr_write_b32 a86, v52\n"
      "v_accvgpr_write_b32 a87, v54\n"
      "v_mfma_f32_16x16x32_bf16 v[48:51], v[98:101], a[96:99], v[16:19]\n"
      "v_cvt_pk_bf16_f32 v68, v68, v69\n"
      "v_cvt_pk_bf16_f32 v70, v70, v71\n"
      "v_cvt_pk_bf16_f32 v72, v72, v73\n"
      "v_mfma_f32_16x16x32_bf16 v[52:55], v[96:99], a[96:99], v[16:19]\n"
      "v_cvt_pk_bf16_f32 v74, v74, v75\n"
      "v_accvgpr_write_b32 a88, v68\n"
      "v_accvgpr_write_b32 a89, v70\n"
      "v_mfma_f32_16x16x32_bf16 v[36:39], v[12:15], a[84:87], v[36:39]\n"
      "v_accvgpr_write_b32 a90, v72\n"
      "v_accvgpr_write_b32 a91, v74\n"
      "v_mfma_f32_16x16x32_bf16 v[68:71], v[98:101], a[100:103], v[20:23]\n"
      "v_cvt_pk_bf16_f32 v84, v84, v85\n"
      "v_cvt_pk_bf16_f32 v86, v86, v87\n"
      "v_cvt_pk_bf16_f32 v88, v88, v89\n"
      "v_mfma_f32_16x16x32_bf16 v[72:75], v[96:99], a[100:103], v[20:23]\n"
      "v_cvt_pk_bf16_f32 v90, v90, v91\n"
      "v_accvgpr_write_b32 a92, v84\n"
      "v_accvgpr_write_b32 a93, v86\n"
      "v_mfma_f32_16x16x32_bf16 v[32:35], v[12:15], a[88:91], v[32:35]\n"
      "v_accvgpr_write_b32 a94, v88\n"
      "v_accvgpr_write_b32 a95, v90\n"
      "v_mfma_f32_16x16x32_bf16 v[84:87], v[98:101], a[104:107], v[16:19]\n"
      "v_cvt_pk_bf16_f32 v48, v48, v49\n"
      "v_cvt_pk_bf16_f32 v50, v50, v51\n"
      "v_cvt_pk_bf16_f32 v52, v52, v53\n"
      "v_mfma_f32_16x16x32_bf16 v[88:91], v[96:99], a[104:107], v[16:19]\n"
      "v_cvt_pk_bf16_f32 v54, v54, v55\n"
      "v_accvgpr_write_b32 a96, v48\n"
      "v_accvgpr_write_b32 a97, v50\n"
      "v_mfma_f32_16x16x32_bf16 v[36:39], v[12:15], a[92:95], v[36:39]\n"
      "v_accvgpr_write_b32 a98, v52\n"
      "v_accvgpr_write_b32 a99, v54\n"
      "v_mfma_f32_16x16x32_bf16 v[48:51], v[98:101], a[108:111], v[20:23]\n"
      "v_cvt_pk_bf16_f32 v68, v68, v69\n"
      "v_cvt_pk_bf16_f32 v70, v70, v71\n"
      "v_cvt_pk_bf16_f32 v72, v72, v73\n"
      "v_mfma_f32_16x16x32_bf16 v[52:55], v[96:99], a[108:111], v[20:23]\n"
      "v_cvt_pk_bf16_f32 v74, v74, v75\n"
      "v_accvgpr_write_b32 a100, v68\n"
      "v_accvgpr_write_b32 a101, v70\n"
      "v_mfma_f32_16x16x32_bf16 v[32:35], v[12:15], a[96:99], v[32:35]\n"
      "v_accvgpr_write_b32 a102, v72\n"
      "v_accvgpr_write_b32 a103, v74\n"
      "s_nop 1\n"
      "v_mfma_f32_16x16x32_bf16 v[36:39], v[12:15], a[100:103], v[36:39]\n"
      "v_cvt_pk_bf16_f32 v84, v84, v85\n"
      "v_cvt_pk_bf16_f32 v86, v86, v87\n"
      "v_cvt_pk_bf16_f32 v88, v88, v89\n"
      "v_cvt_pk_bf16_f32 v90, v90, v91\n"
      "v_accvgpr_write_b32 a104, v84\n"
      "v_accvgpr_write_b32 a105, v86\n"
      "v_accvgpr_write_b32 a106, v88\n"
      "v_accvgpr_write_b32 a107, v90\n"
      "s_nop 1\n"
      "v_mfma_f32_16x16x32_bf16 v[32:35], v[12:15], a[104:107], v[32:35]\n"
      "v_cvt_pk_bf16_f32 v48, v48, v49\n"
      "v_cvt_pk_bf16_f32 v50, v50, v51\n"
      "v_cvt_pk_bf16_f32 v52, v52, v53\n"
      "v_cvt_pk_bf16_f32 v54, v54, v55\n"
      "v_accvgpr_write_b32 a108, v48\n"
      "v_accvgpr_write_b32 a109, v50\n"
      "v_accvgpr_write_b32 a110, v52\n"
      "v_accvgpr_write_b32 a111, v54\n"
      "s_nop 1\n"
      "v_mfma_f32_16x16x32_bf16 v[36:39], v[12:15], a[108:111], v[36:39]\n"
      "s_waitcnt vmcnt(6) lgkmcnt(0)\n"
      "s_barrier\n"
      "ds_read_b64_tr_b16 v[200:201], v66 offset:32768\n"
      "ds_read_b64_tr_b16 v[202:203], v66 offset:33792\n"
      "ds_read_b64_tr_b16 v[204:205], v66 offset:32776\n"
      "ds_read_b64_tr_b16 v[206:207], v66 offset:33800\n"
      "ds_read_b64_tr_b16 v[208:209], v66 offset:40960\n"
      "ds_read_b64_tr_b16 v[210:211], v66 offset:41984\n"
      "ds_read_b64_tr_b16 v[212:213], v66 offset:40968\n"
      "ds_read_b64_tr_b16 v[214:215], v66 offset:41992\n"
      "ds_read_b64_tr_b16 v[216:217], v66 offset:49152\n"
      "ds_read_b64_tr_b16 v[218:219], v66 offset:50176\n"
      "ds_read_b64_tr_b16 v[220:221], v66 offset:49160\n"
      "ds_read_b64_tr_b16 v[222:223], v66 offset:50184\n"
      "v_mfma_f32_16x16x32_bf16 v[24:27], v[12:15], v[176:179], v[24:27]\n"
      "v_mfma_f32_16x16x32_bf16 v[28:31], v[12:15], v[180:183], v[28:31]\n"
      "v_mfma_f32_16x16x32_bf16 v[24:27], v[12:15], v[184:187], v[24:27]\n"
      "v_mfma_f32_16x16x32_bf16 v[28:31], v[12:15], v[188:191], v[28:31]\n"
      "v_mfma_f32_16x16x32_bf16 v[24:27], v[12:15], v[192:195], v[24:27]\n"
      "v_mfma_f32_16x16x32_bf16 v[28:31], v[12:15], v[196:199], v[28:31]\n"
      "v_mfma_f32_16x16x32_bf16 v[16:19], v[12:15], a[112:115], v[32:35]\n"
      "v_mfma_f32_16x16x32_bf16 v[20:23], v[12:15], a[116:119], v[36:39]\n"
      "v_mfma_f32_16x16x32_bf16 v[16:19], v[12:15], a[120:123], v[16:19]\n"
      "v_mfma_f32_16x16x32_bf16 v[20:23], v[12:15], a[124:127], v[20:23]\n"
      "v_mfma_f32_16x16x32_bf16 v[16:19], v[12:15], a[128:131], v[16:19]\n"
      "v_mfma_f32_16x16x32_bf16 v[20:23], v[12:15], a[132:135], v[20:23]\n"
      "s_nop 6\n"
      "v_mul_f32 v16, 0xbc000000, v16\n"
      "v_mul_f32 v17, 0xbc000000, v17\n"
      "v_mul_f32 v18, 0xbc000000, v18\n"
      "v_mul_f32 v19, 0xbc000000, v19\n"
      "v_mul_f32 v20, 0xbc000000, v20\n"
      "v_mul_f32 v21, 0xbc000000, v21\n"
      "v_mul_f32 v22, 0xbc000000, v22\n"
      "v_mul_f32 v23, 0xbc000000, v23\n"
      "v_mfma_f32_16x16x32_bf16 v[68:71], v[98:101], a[112:115], v[16:19]\n"
      "v_mfma_f32_16x16x32_bf16 v[72:75], v[96:99], a[112:115], v[16:19]\n"
      "v_mfma_f32_16x16x32_bf16 v[84:87], v[98:101], a[116:119], v[20:23]\n"
      "v_mfma_f32_16x16x32_bf16 v[88:91], v[96:99], a[116:119], v[20:23]\n"
      "v_mfma_f32_16x16x32_bf16 v[48:51], v[98:101], a[120:123], v[16:19]\n"
      "s_nop 3\n"
      "v_cvt_pk_bf16_f32 v68, v68, v69\n"
      "v_cvt_pk_bf16_f32 v70, v70, v71\n"
      "v_cvt_pk_bf16_f32 v72, v72, v73\n"
      "v_mfma_f32_16x16x32_bf16 v[52:55], v[96:99], a[120:123], v[16:19]\n"
      "v_cvt_pk_bf16_f32 v74, v74, v75\n"
      "v_accvgpr_write_b32 a112, v68\n"
      "v_accvgpr_write_b32 a113, v70\n"
      "v_accvgpr_write_b32 a114, v72\n"
      "v_accvgpr_write_b32 a115, v74\n"
      "v_mfma_f32_16x16x32_bf16 v[68:71], v[98:101], a[124:127], v[20:23]\n"
      "v_cvt_pk_bf16_f32 v84, v84, v85\n"
      "v_cvt_pk_bf16_f32 v86, v86, v87\n"
      "v_cvt_pk_bf16_f32 v88, v88, v89\n"
      "v_mfma_f32_16x16x32_bf16 v[72:75], v[96:99], a[124:127], v[20:23]\n"
      "v_cvt_pk_bf16_f32 v90, v90, v91\n"
      "v_accvgpr_write_b32 a116, v84\n"
      "v_accvgpr_write_b32 a117, v86\n"
      "v_mfma_f32_16x16x32_bf16 v[32:35], v[12:15], a[112:115], v[32:35]\n"
      "v_accvgpr_write_b32 a118, v88\n"
      "v_accvgpr_write_b32 a119, v90\n"
      "v_mfma_f32_16x16x32_bf16 v[84:87], v[98:101], a[128:131], v[16:19]\n"
      "v_cvt_pk_bf16_f32 v48, v48, v49\n"
      "v_cvt_pk_bf16_f32 v50, v50, v51\n"
      "v_cvt_pk_bf16_f32 v52, v52, v53\n"
      "v_mfma_f32_16x16x32_bf16 v[88:91], v[96:99], a[128:131], v[16:19]\n"
      "v_cvt_pk_bf16_f32 v54, v54, v55\n"
      "v_accvgpr_write_b32 a120, v48\n"
      "v_accvgpr_write_b32 a121, v50\n"
      "v_mfma_f32_16x16x32_bf16 v[36:39], v[12:15], a[116:119], v[36:39]\n"
      "v_accvgpr_write_b32 a122, v52\n"
      "v_accvgpr_write_b32 a123, v54\n"
      "v_mfma_f32_16x16x32_bf16 v[48:51], v[98:101], a[132:135], v[20:23]\n"
      "v_cvt_pk_bf16_f32 v68, v68, v69\n"
      "v_cvt_pk_bf16_f32 v70, v70, v71\n"
      "v_cvt_pk_bf16_f32 v72, v72, v73\n"
      "v_mfma_f32_16x16x32_bf16 v[52:55], v[96:99], a[132:135], v[20:23]\n"
      "v_cvt_pk_bf16_f32 v74, v74, v75\n"
      "v_accvgpr_write_b32 a124, v68\n"
      "v_accvgpr_write_b32 a125, v70\n"
      "v_mfma_f32_16x16x32_bf16 v[32:35], v[12:15], a[120:123], v[32:35]\n"
      "v_accvgpr_write_b32 a126, v72\n"
      "v_accvgpr_write_b32 a127, v74\n"
      "s_nop 1\n"
      "v_mfma_f32_16x16x32_bf16 v[36:39], v[12:15], a[124:127], v[36:39]\n"
      "v_cvt_pk_bf16_f32 v84, v84, v85\n"
      "v_cvt_pk_bf16_f32 v86, v86, v87\n"
      "v_cvt_pk_bf16_f32 v88, v88, v89\n"
      "v_cvt_pk_bf16_f32 v90, v90, v91\n"
      "v_accvgpr_write_b32 a128, v84\n"
      "v_accvgpr_write_b32 a129, v86\n"
      "v_accvgpr_write_b32 a130, v88\n"
      "v_accvgpr_write_b32 a131, v90\n"
      "s_waitcnt vmcnt(0) lgkmcnt(0)\n"
      "s_barrier\n"
      "ds_read_b64_tr_b16 v[224:225], v66 offset:57344\n"
      "ds_read_b64_tr_b16 v[226:227], v66 offset:58368\n"
      "ds_read_b64_tr_b16 v[228:229], v66 offset:57352\n"
      "ds_read_b64_tr_b16 v[230:231], v66 offset:58376\n"
      "ds_read_b64_tr_b16 v[232:233], v67 offset:0\n"
      "ds_read_b64_tr_b16 v[234:235], v67 offset:1024\n"
      "ds_read_b64_tr_b16 v[236:237], v67 offset:8\n"
      "ds_read_b64_tr_b16 v[238:239], v67 offset:1032\n"
      "ds_read_b64_tr_b16 v[240:241], v67 offset:8192\n"
      "ds_read_b64_tr_b16 v[242:243], v67 offset:9216\n"
      "ds_read_b64_tr_b16 v[244:245], v67 offset:8200\n"
      "ds_read_b64_tr_b16 v[246:247], v67 offset:9224\n"
      "v_mfma_f32_16x16x32_bf16 v[24:27], v[12:15], v[200:203], v[24:27]\n"
      "v_mfma_f32_16x16x32_bf16 v[28:31], v[12:15], v[204:207], v[28:31]\n"
      "v_mfma_f32_16x16x32_bf16 v[24:27], v[12:15], v[208:211], v[24:27]\n"
      "v_mfma_f32_16x16x32_bf16 v[28:31], v[12:15], v[212:215], v[28:31]\n"
      "v_mfma_f32_16x16x32_bf16 v[24:27], v[12:15], v[216:219], v[24:27]\n"
      "v_mfma_f32_16x16x32_bf16 v[28:31], v[12:15], v[220:223], v[28:31]\n"
      "buffer_load_dwordx2 v[44:45], v5, s[36:39], 0 offen offset:8 sc1\n"
      "v_mfma_f32_16x16x32_bf16 v[32:35], v[12:15], a[128:131], v[32:35]\n"
      "v_cvt_pk_bf16_f32 v48, v48, v49\n"
      "v_cvt_pk_bf16_f32 v50, v50, v51\n"
      "v_cvt_pk_bf16_f32 v52, v52, v53\n"
      "v_cvt_pk_bf16_f32 v54, v54, v55\n"
      "v_accvgpr_write_b32 a132, v48\n"
      "v_accvgpr_write_b32 a133, v50\n"
      "v_accvgpr_write_b32 a134, v52\n"
      "v_accvgpr_write_b32 a135, v54\n"
      "s_nop 1\n"
      "v_mfma_f32_16x16x32_bf16 v[36:39], v[12:15], a[132:135], v[36:39]\n"
      "v_mul_f32 v16, 0xbd000000, v32\n"
      "v_mul_f32 v17, 0xbd000000, v33\n"
      "v_mul_f32 v18, 0xbd000000, v34\n"
      "v_mul_f32 v19, 0xbd000000, v35\n"
      "s_nop 3\n"
      "v_mul_f32 v20, 0xbd000000, v36\n"
      "v_mul_f32 v21, 0xbd000000, v37\n"
      "v_mul_f32 v22, 0xbd000000, v38\n"
      "v_mul_f32 v23, 0xbd000000, v39\n"
      "v_mfma_f32_16x16x32_bf16 v[68:71], v[98:101], a[136:139], v[16:19]\n"
      "v_mfma_f32_16x16x32_bf16 v[72:75], v[96:99], a[136:139], v[16:19]\n"
      "v_mfma_f32_16x16x32_bf16 v[84:87], v[98:101], a[140:143], v[20:23]\n"
      "v_mfma_f32_16x16x32_bf16 v[88:91], v[96:99], a[140:143], v[20:23]\n"
      "s_nop 4\n"
      "v_cvt_pk_bf16_f32 v68, v68, v69\n"
      "v_cvt_pk_bf16_f32 v70, v70, v71\n"
      "v_cvt_pk_bf16_f32 v72, v72, v73\n"
      "v_cvt_pk_bf16_f32 v74, v74, v75\n"
      "v_accvgpr_write_b32 a136, v68\n"
      "v_accvgpr_write_b32 a137, v70\n"
      "v_accvgpr_write_b32 a138, v72\n"
      "v_accvgpr_write_b32 a139, v74\n"
      "v_cvt_pk_bf16_f32 v84, v84, v85\n"
      "v_cvt_pk_bf16_f32 v86, v86, v87\n"
      "v_cvt_pk_bf16_f32 v88, v88, v89\n"
      "v_cvt_pk_bf16_f32 v90, v90, v91\n"
      "v_accvgpr_write_b32 a140, v84\n"
      "v_accvgpr_write_b32 a141, v86\n"
      "v_accvgpr_write_b32 a142, v88\n"
      "v_accvgpr_write_b32 a143, v90\n"
      "s_waitcnt lgkmcnt(0)\n"
      "s_barrier\n"
      "v_mfma_f32_16x16x32_bf16 v[24:27], v[12:15], v[224:227], v[24:27]\n"
      "v_mfma_f32_16x16x32_bf16 v[28:31], v[12:15], v[228:231], v[28:31]\n"
      "v_mfma_f32_16x16x32_bf16 v[24:27], v[12:15], v[232:235], v[24:27]\n"
      "v_mfma_f32_16x16x32_bf16 v[28:31], v[12:15], v[236:239], v[28:31]\n"
      "v_mfma_f32_16x16x32_bf16 v[24:27], v[12:15], v[240:243], v[24:27]\n"
      "v_mfma_f32_16x16x32_bf16 v[28:31], v[12:15], v[244:247], v[28:31]\n"
      "s_nop 6\n"
      "v_mul_f32 v24, 0xbae38e39, v24\n"
      "v_mul_f32 v25, 0xbae38e39, v25\n"
      "v_mul_f32 v26, 0xbae38e39, v26\n"
      "v_mul_f32 v27, 0xbae38e39, v27\n"
      "v_mul_f32 v28, 0xbae38e39, v28\n"
      "v_mul_f32 v29, 0xbae38e39, v29\n"
      "v_mul_f32 v30, 0xbae38e39, v30\n"
      "v_mul_f32 v31, 0xbae38e39, v31\n"
      "v_mfma_f32_16x16x32_bf16 v[32:35], v[12:15], v[240:243], 0\n"
      "v_mfma_f32_16x16x32_bf16 v[36:39], v[12:15], v[244:247], 0\n"
      "v_mfma_f32_16x16x32_bf16 v[48:51], v[98:101], v[104:107], v[24:27]\n"
      "v_mfma_f32_16x16x32_bf16 v[52:55], v[96:99], v[104:107], v[24:27]\n"
      "v_mfma_f32_16x16x32_bf16 v[68:71], v[98:101], v[108:111], v[28:31]\n"
      "v_mfma_f32_16x16x32_bf16 v[72:75], v[96:99], v[108:111], v[28:31]\n"
      "v_mfma_f32_16x16x32_bf16 v[84:87], v[98:101], v[112:115], v[24:27]\n"
      "s_nop 3\n"
      "v_cvt_pk_bf16_f32 v104, v48, v49\n"
      "v_cvt_pk_bf16_f32 v105, v50, v51\n"
      "v_mfma_f32_16x16x32_bf16 v[88:91], v[96:99], v[112:115], v[24:27]\n"
      "v_cvt_pk_bf16_f32 v106, v52, v53\n"
      "v_cvt_pk_bf16_f32 v107, v54, v55\n"
      "v_mfma_f32_16x16x32_bf16 v[48:51], v[98:101], v[116:119], v[28:31]\n"
      "v_cvt_pk_bf16_f32 v108, v68, v69\n"
      "v_cvt_pk_bf16_f32 v109, v70, v71\n"
      "v_mfma_f32_16x16x32_bf16 v[52:55], v[96:99], v[116:119], v[28:31]\n"
      "v_cvt_pk_bf16_f32 v110, v72, v73\n"
      "v_cvt_pk_bf16_f32 v111, v74, v75\n"
      "v_mfma_f32_16x16x32_bf16 v[32:35], v[12:15], v[104:107], v[32:35]\n"
      "v_mfma_f32_16x16x32_bf16 v[68:71], v[98:101], v[120:123], v[24:27]\n"
      "v_cvt_pk_bf16_f32 v112, v84, v85\n"
      "v_cvt_pk_bf16_f32 v113, v86, v87\n"
      "v_mfma_f32_16x16x32_bf16 v[72:75], v[96:99], v[120:123], v[24:27]\n"
      "v_cvt_pk_bf16_f32 v114, v88, v89\n"
      "v_cvt_pk_bf16_f32 v115, v90, v91\n"
      "v_mfma_f32_16x16x32_bf16 v[36:39], v[12:15], v[108:111], v[36:39]\n"
      "v_mfma_f32_16x16x32_bf16 v[84:87], v[98:101], v[124:127], v[28:31]\n"
      "v_cvt_pk_bf16_f32 v116, v48, v49\n"
      "v_cvt_pk_bf16_f32 v117, v50, v51\n"
      "v_mfma_f32_16x16x32_bf16 v[88:91], v[96:99], v[124:127], v[28:31]\n"
      "v_cvt_pk_bf16_f32 v118, v52, v53\n"
      "v_cvt_pk_bf16_f32 v119, v54, v55\n"
      "v_mfma_f32_16x16x32_bf16 v[32:35], v[12:15], v[112:115], v[32:35]\n"
      "v_mfma_f32_16x16x32_bf16 v[48:51], v[98:101], v[128:131], v[24:27]\n"
      "v_cvt_pk_bf16_f32 v120, v68, v69\n"
      "v_cvt_pk_bf16_f32 v121, v70, v71\n"
      "v_mfma_f32_16x16x32_bf16 v[52:55], v[96:99], v[128:131], v[24:27]\n"
      "v_cvt_pk_bf16_f32 v122, v72, v73\n"
      "v_cvt_pk_bf16_f32 v123, v74, v75\n"
      "v_mfma_f32_16x16x32_bf16 v[36:39], v[12:15], v[116:119], v[36:39]\n"
      "v_mfma_f32_16x16x32_bf16 v[68:71], v[98:101], v[132:135], v[28:31]\n"
      "v_cvt_pk_bf16_f32 v124, v84, v85\n"
      "v_cvt_pk_bf16_f32 v125, v86, v87\n"
      "v_mfma_f32_16x16x32_bf16 v[72:75], v[96:99], v[132:135], v[28:31]\n"
      "v_cvt_pk_bf16_f32 v126, v88, v89\n"
      "v_cvt_pk_bf16_f32 v127, v90, v91\n"
      "v_mfma_f32_16x16x32_bf16 v[32:35], v[12:15], v[120:123], v[32:35]\n"
      "v_mfma_f32_16x16x32_bf16 v[84:87], v[98:101], v[136:139], v[24:27]\n"
      "v_cvt_pk_bf16_f32 v128, v48, v49\n"
      "v_cvt_pk_bf16_f32 v129, v50, v51\n"
      "v_mfma_f32_16x16x32_bf16 v[88:91], v[96:99], v[136:139], v[24:27]\n"
      "v_cvt_pk_bf16_f32 v130, v52, v53\n"
      "v_cvt_pk_bf16_f32 v131, v54, v55\n"
      "v_mfma_f32_16x16x32_bf16 v[36:39], v[12:15], v[124:127], v[36:39]\n"
      "v_mfma_f32_16x16x32_bf16 v[48:51], v[98:101], v[140:143], v[28:31]\n"
      "v_cvt_pk_bf16_f32 v132, v68, v69\n"
      "v_cvt_pk_bf16_f32 v133, v70, v71\n"
      "v_mfma_f32_16x16x32_bf16 v[52:55], v[96:99], v[140:143], v[28:31]\n"
      "v_cvt_pk_bf16_f32 v134, v72, v73\n"
      "v_cvt_pk_bf16_f32 v135, v74, v75\n"
      "v_mfma_f32_16x16x32_bf16 v[32:35], v[12:15], v[128:131], v[32:35]\n"
      "v_mfma_f32_16x16x32_bf16 v[68:71], v[98:101], v[144:147], v[24:27]\n"
      "v_cvt_pk_bf16_f32 v136, v84, v85\n"
      "v_cvt_pk_bf16_f32 v137, v86, v87\n"
      "v_mfma_f32_16x16x32_bf16 v[72:75], v[96:99], v[144:147], v[24:27]\n"
      "v_cvt_pk_bf16_f32 v138, v88, v89\n"
      "v_cvt_pk_bf16_f32 v139, v90, v91\n"
      "v_mfma_f32_16x16x32_bf16 v[36:39], v[12:15], v[132:135], v[36:39]\n"
      "v_mfma_f32_16x16x32_bf16 v[84:87], v[98:101], v[148:151], v[28:31]\n"
      "v_cvt_pk_bf16_f32 v140, v48, v49\n"
      "v_cvt_pk_bf16_f32 v141, v50, v51\n"
      "v_mfma_f32_16x16x32_bf16 v[88:91], v[96:99], v[148:151], v[28:31]\n"
      "v_cvt_pk_bf16_f32 v142, v52, v53\n"
      "v_cvt_pk_bf16_f32 v143, v54, v55\n"
      "v_mfma_f32_16x16x32_bf16 v[32:35], v[12:15], v[136:139], v[32:35]\n"
      "v_mfma_f32_16x16x32_bf16 v[48:51], v[98:101], v[152:155], v[24:27]\n"
      "v_cvt_pk_bf16_f32 v144, v68, v69\n"
      "v_cvt_pk_bf16_f32 v145, v70, v71\n"
      "v_mfma_f32_16x16x32_bf16 v[52:55], v[96:99], v[152:155], v[24:27]\n"
      "v_cvt_pk_bf16_f32 v146, v72, v73\n"
      "v_cvt_pk_bf16_f32 v147, v74, v75\n"
      "v_mfma_f32_16x16x32_bf16 v[36:39], v[12:15], v[140:143], v[36:39]\n"
      "v_mfma_f32_16x16x32_bf16 v[68:71], v[98:101], v[156:159], v[28:31]\n"
      "v_cvt_pk_bf16_f32 v148, v84, v85\n"
      "v_cvt_pk_bf16_f32 v149, v86, v87\n"
      "v_mfma_f32_16x16x32_bf16 v[72:75], v[96:99], v[156:159], v[28:31]\n"
      "v_cvt_pk_bf16_f32 v150, v88, v89\n"
      "v_cvt_pk_bf16_f32 v151, v90, v91\n"
      "v_mfma_f32_16x16x32_bf16 v[32:35], v[12:15], v[144:147], v[32:35]\n"
      "v_mfma_f32_16x16x32_bf16 v[84:87], v[98:101], v[160:163], v[24:27]\n"
      "v_cvt_pk_bf16_f32 v152, v48, v49\n"
      "v_cvt_pk_bf16_f32 v153, v50, v51\n"
      "v_mfma_f32_16x16x32_bf16 v[88:91], v[96:99], v[160:163], v[24:27]\n"
      "v_cvt_pk_bf16_f32 v154, v52, v53\n"
      "v_cvt_pk_bf16_f32 v155, v54, v55\n"
      "v_mfma_f32_16x16x32_bf16 v[36:39], v[12:15], v[148:151], v[36:39]\n"
      "v_mfma_f32_16x16x32_bf16 v[48:51], v[98:101], v[164:167], v[28:31]\n"
      "v_cvt_pk_bf16_f32 v156, v68, v69\n"
      "v_cvt_pk_bf16_f32 v157, v70, v71\n"
      "v_mfma_f32_16x16x32_bf16 v[52:55], v[96:99], v[164:167], v[28:31]\n"
      "v_cvt_pk_bf16_f32 v158, v72, v73\n"
      "v_cvt_pk_bf16_f32 v159, v74, v75\n"
      "v_mfma_f32_16x16x32_bf16 v[32:35], v[12:15], v[152:155], v[32:35]\n"
      "v_mfma_f32_16x16x32_bf16 v[68:71], v[98:101], v[168:171], v[24:27]\n"
      "v_cvt_pk_bf16_f32 v160, v84, v85\n"
      "v_cvt_pk_bf16_f32 v161, v86, v87\n"
      "v_mfma_f32_16x16x32_bf16 v[72:75], v[96:99], v[168:171], v[24:27]\n"
      "v_cvt_pk_bf16_f32 v162, v88, v89\n"
      "v_cvt_pk_bf16_f32 v163, v90, v91\n"
      "v_mfma_f32_16x16x32_bf16 v[36:39], v[12:15], v[156:159], v[36:39]\n"
      "v_mfma_f32_16x16x32_bf16 v[84:87], v[98:101], v[172:175], v[28:31]\n"
      "v_cvt_pk_bf16_f32 v164, v48, v49\n"
      "v_cvt_pk_bf16_f32 v165, v50, v51\n"
      "v_mfma_f32_16x16x32_bf16 v[88:91], v[96:99], v[172:175], v[28:31]\n"
      "v_cvt_pk_bf16_f32 v166, v52, v53\n"
      "v_cvt_pk_bf16_f32 v167, v54, v55\n"
      "v_mfma_f32_16x16x32_bf16 v[32:35], v[12:15], v[160:163], v[32:35]\n"
      "v_mfma_f32_16x16x32_bf16 v[48:51], v[98:101], v[176:179], v[24:27]\n"
      "v_cvt_pk_bf16_f32 v168, v68, v69\n"
      "v_cvt_pk_bf16_f32 v169, v70, v71\n"
      "v_mfma_f32_16x16x32_bf16 v[52:55], v[96:99], v[176:179], v[24:27]\n"
      "v_cvt_pk_bf16_f32 v170, v72, v73\n"
      "v_cvt_pk_bf16_f32 v171, v74, v75\n"
      "v_mfma_f32_16x16x32_bf16 v[36:39], v[12:15], v[164:167], v[36:39]\n"
      "v_mfma_f32_16x16x32_bf16 v[68:71], v[98:101], v[180:183], v[28:31]\n"
      "v_cvt_pk_bf16_f32 v172, v84, v85\n"
      "v_cvt_pk_bf16_f32 v173, v86, v87\n"
      "v_mfma_f32_16x16x32_bf16 v[72:75], v[96:99], v[180:183], v[28:31]\n"
      "v_cvt_pk_bf16_f32 v174, v88, v89\n"
      "v_cvt_pk_bf16_f32 v175, v90, v91\n"
      "v_mfma_f32_16x16x32_bf16 v[32:35], v[12:15], v[168:171], v[32:35]\n"
      "v_mfma_f32_16x16x32_bf16 v[84:87], v[98:101], v[184:187], v[24:27]\n"
      "v_cvt_pk_bf16_f32 v176, v48, v49\n"
      "v_cvt_pk_bf16_f32 v177, v50, v51\n"
      "v_mfma_f32_16x16x32_bf16 v[88:91], v[96:99], v[184:187], v[24:27]\n"
      "v_cvt_pk_bf16_f32 v178, v52, v53\n"
      "v_cvt_pk_bf16_f32 v179, v54, v55\n"
      "v_mfma_f32_16x16x32_bf16 v[36:39], v[12:15], v[172:175], v[36:39]\n"
      "v_mfma_f32_16x16x32_bf16 v[48:51], v[98:101], v[188:191], v[28:31]\n"
      "v_cvt_pk_bf16_f32 v180, v68, v69\n"
      "v_cvt_pk_bf16_f32 v181, v70, v71\n"
      "v_mfma_f32_16x16x32_bf16 v[52:55], v[96:99], v[188:191], v[28:31]\n"
      "v_cvt_pk_bf16_f32 v182, v72, v73\n"
      "v_cvt_pk_bf16_f32 v183, v74, v75\n"
      "v_mfma_f32_16x16x32_bf16 v[32:35], v[12:15], v[176:179], v[32:35]\n"
      "v_mfma_f32_16x16x32_bf16 v[68:71], v[98:101], v[192:195], v[24:27]\n"
      "v_cvt_pk_bf16_f32 v184, v84, v85\n"
      "v_cvt_pk_bf16_f32 v185, v86, v87\n"
      "v_mfma_f32_16x16x32_bf16 v[72:75], v[96:99], v[192:195], v[24:27]\n"
      "v_cvt_pk_bf16_f32 v186, v88, v89\n"
      "v_cvt_pk_bf16_f32 v187, v90, v91\n"
      "v_mfma_f32_16x16x32_bf16 v[36:39], v[12:15], v[180:183], v[36:39]\n"
      "v_mfma_f32_16x16x32_bf16 v[84:87], v[98:101], v[196:199], v[28:31]\n"
      "v_cvt_pk_bf16_f32 v188, v48, v49\n"
      "v_cvt_pk_bf16_f32 v189, v50, v51\n"
      "v_mfma_f32_16x16x32_bf16 v[88:91], v[96:99], v[196:199], v[28:31]\n"
      "v_cvt_pk_bf16_f32 v190, v52, v53\n"
      "v_cvt_pk_bf16_f32 v191, v54, v55\n"
      "v_mfma_f32_16x16x32_bf16 v[32:35], v[12:15], v[184:187], v[32:35]\n"
      "v_mfma_f32_16x16x32_bf16 v[48:51], v[98:101], v[200:203], v[24:27]\n"
      "v_cvt_pk_bf16_f32 v192, v68, v69\n"
      "v_cvt_pk_bf16_f32 v193, v70, v71\n"
      "v_mfma_f32_16x16x32_bf16 v[52:55], v[96:99], v[200:203], v[24:27]\n"
      "v_cvt_pk_bf16_f32 v194, v72, v73\n"
      "v_cvt_pk_bf16_f32 v195, v74, v75\n"
      "v_mfma_f32_16x16x32_bf16 v[36:39], v[12:15], v[188:191], v[36:39]\n"
      "v_mfma_f32_16x16x32_bf16 v[68:71], v[98:101], v[204:207], v[28:31]\n"
      "v_cvt_pk_bf16_f32 v196, v84, v85\n"
      "v_cvt_pk_bf16_f32 v197, v86, v87\n"
      "v_mfma_f32_16x16x32_bf16 v[72:75], v[96:99], v[204:207], v[28:31]\n"
      "v_cvt_pk_bf16_f32 v198, v88, v89\n"
      "v_cvt_pk_bf16_f32 v199, v90, v91\n"
      "v_mfma_f32_16x16x32_bf16 v[32:35], v[12:15], v[192:195], v[32:35]\n"
      "v_mfma_f32_16x16x32_bf16 v[84:87], v[98:101], v[208:211], v[24:27]\n"
      "v_cvt_pk_bf16_f32 v200, v48, v49\n"
      "v_cvt_pk_bf16_f32 v201, v50, v51\n"
      "v_mfma_f32_16x16x32_bf16 v[88:91], v[96:99], v[208:211], v[24:27]\n"
      "v_cvt_pk_bf16_f32 v202, v52, v53\n"
      "v_cvt_pk_bf16_f32 v203, v54, v55\n"
      "v_mfma_f32_16x16x32_bf16 v[36:39], v[12:15], v[196:199], v[36:39]\n"
      "v_mfma_f32_16x16x32_bf16 v[48:51], v[98:101], v[212:215], v[28:31]\n"
      "v_cvt_pk_bf16_f32 v204, v68, v69\n"
      "v_cvt_pk_bf16_f32 v205, v70, v71\n"
      "v_mfma_f32_16x16x32_bf16 v[52:55], v[96:99], v[212:215], v[28:31]\n"
      "v_cvt_pk_bf16_f32 v206, v72, v73\n"
      "v_cvt_pk_bf16_f32 v207, v74, v75\n"
      "v_mfma_f32_16x16x32_bf16 v[32:35], v[12:15], v[200:203], v[32:35]\n"
      "s_nop 0\n"
      "v_mfma_f32_16x16x32_bf16 v[36:39], v[12:15], v[204:207], v[36:39]\n"
      "v_cvt_pk_bf16_f32 v208, v84, v85\n"
      "v_cvt_pk_bf16_f32 v209, v86, v87\n"
      "v_cvt_pk_bf16_f32 v210, v88, v89\n"
      "v_cvt_pk_bf16_f32 v211, v90, v91\n"
      "s_nop 1\n"
      "v_mfma_f32_16x16x32_bf16 v[32:35], v[12:15], v[208:211], v[32:35]\n"
      "v_cvt_pk_bf16_f32 v212, v48, v49\n"
      "v_cvt_pk_bf16_f32 v213, v50, v51\n"
      "v_cvt_pk_bf16_f32 v214, v52, v53\n"
      "v_cvt_pk_bf16_f32 v215, v54, v55\n"
      "s_nop 1\n"
      "v_mfma_f32_16x16x32_bf16 v[36:39], v[12:15], v[212:215], v[36:39]\n"
      "v_mfma_f32_16x16x32_bf16 v[24:27], v[12:15], v[216:219], v[32:35]\n"
      "v_mfma_f32_16x16x32_bf16 v[28:31], v[12:15], v[220:223], v[36:39]\n"
      "v_mfma_f32_16x16x32_bf16 v[24:27], v[12:15], v[224:227], v[24:27]\n"
      "v_mfma_f32_16x16x32_bf16 v[28:31], v[12:15], v[228:231], v[28:31]\n"
      "v_mfma_f32_16x16x32_bf16 v[24:27], v[12:15], v[232:235], v[24:27]\n"
      "v_mfma_f32_16x16x32_bf16 v[28:31], v[12:15], v[236:239], v[28:31]\n"
      "s_nop 6\n"
      "v_mul_f32 v24, 0xbc000000, v24\n"
      "v_mul_f32 v25, 0xbc000000, v25\n"
      "v_mul_f32 v26, 0xbc000000, v26\n"
      "v_mul_f32 v27, 0xbc000000, v27\n"
      "v_mul_f32 v28, 0xbc000000, v28\n"
      "v_mul_f32 v29, 0xbc000000, v29\n"
      "v_mul_f32 v30, 0xbc000000, v30\n"
      "v_mul_f32 v31, 0xbc000000, v31\n"
      "v_mfma_f32_16x16x32_bf16 v[68:71], v[98:101], v[216:219], v[24:27]\n"
      "v_mfma_f32_16x16x32_bf16 v[72:75], v[96:99], v[216:219], v[24:27]\n"
      "v_mfma_f32_16x16x32_bf16 v[84:87], v[98:101], v[220:223], v[28:31]\n"
      "v_mfma_f32_16x16x32_bf16 v[88:91], v[96:99], v[220:223], v[28:31]\n"
      "v_mfma_f32_16x16x32_bf16 v[48:51], v[98:101], v[224:227], v[24:27]\n"
      "s_nop 3\n"
      "v_cvt_pk_bf16_f32 v216, v68, v69\n"
      "v_cvt_pk_bf16_f32 v217, v70, v71\n"
      "v_mfma_f32_16x16x32_bf16 v[52:55], v[96:99], v[224:227], v[24:27]\n"
      "v_cvt_pk_bf16_f32 v218, v72, v73\n"
      "v_cvt_pk_bf16_f32 v219, v74, v75\n"
      "v_mfma_f32_16x16x32_bf16 v[68:71], v[98:101], v[228:231], v[28:31]\n"
      "v_cvt_pk_bf16_f32 v220, v84, v85\n"
      "v_cvt_pk_bf16_f32 v221, v86, v87\n"
      "v_mfma_f32_16x16x32_bf16 v[72:75], v[96:99], v[228:231], v[28:31]\n"
      "v_cvt_pk_bf16_f32 v222, v88, v89\n"
      "v_cvt_pk_bf16_f32 v223, v90, v91\n"
      "v_mfma_f32_16x16x32_bf16 v[32:35], v[12:15], v[216:219], v[32:35]\n"
      "v_mfma_f32_16x16x32_bf16 v[84:87], v[98:101], v[232:235], v[24:27]\n"
      "v_cvt_pk_bf16_f32 v224, v48, v49\n"
      "v_cvt_pk_bf16_f32 v225, v50, v51\n"
      "v_mfma_f32_16x16x32_bf16 v[88:91], v[96:99], v[232:235], v[24:27]\n"
      "v_cvt_pk_bf16_f32 v226, v52, v53\n"
      "v_cvt_pk_bf16_f32 v227, v54, v55\n"
      "v_mfma_f32_16x16x32_bf16 v[36:39], v[12:15], v[220:223], v[36:39]\n"
      "v_mfma_f32_16x16x32_bf16 v[48:51], v[98:101], v[236:239], v[28:31]\n"
      "v_cvt_pk_bf16_f32 v228, v68, v69\n"
      "v_cvt_pk_bf16_f32 v229, v70, v71\n"
      "v_mfma_f32_16x16x32_bf16 v[52:55], v[96:99], v[236:239], v[28:31]\n"
      "v_cvt_pk_bf16_f32 v230, v72, v73\n"
      "v_cvt_pk_bf16_f32 v231, v74, v75\n"
      "v_mfma_f32_16x16x32_bf16 v[32:35], v[12:15], v[224:227], v[32:35]\n"
      "s_nop 0\n"
      "v_mfma_f32_16x16x32_bf16 v[36:39], v[12:15], v[228:231], v[36:39]\n"
      "v_cvt_pk_bf16_f32 v232, v84, v85\n"
      "v_cvt_pk_bf16_f32 v233, v86, v87\n"
      "v_cvt_pk_bf16_f32 v234, v88, v89\n"
      "v_cvt_pk_bf16_f32 v235, v90, v91\n"
      "s_nop 1\n"
      "v_mfma_f32_16x16x32_bf16 v[32:35], v[12:15], v[232:235], v[32:35]\n"
      "v_cvt_pk_bf16_f32 v236, v48, v49\n"
      "v_cvt_pk_bf16_f32 v237, v50, v51\n"
      "v_cvt_pk_bf16_f32 v238, v52, v53\n"
      "v_cvt_pk_bf16_f32 v239, v54, v55\n"
      "s_nop 1\n"
      "v_mfma_f32_16x16x32_bf16 v[36:39], v[12:15], v[236:239], v[36:39]\n"
      "s_nop 0\n"
      "v_mul_f32 v24, 0xbd000000, v32\n"
      "v_mul_f32 v25, 0xbd000000, v33\n"
      "v_mul_f32 v26, 0xbd000000, v34\n"
      "v_mul_f32 v27, 0xbd000000, v35\n"
      "s_nop 2\n"
      "v_mul_f32 v28, 0xbd000000, v36\n"
      "v_mul_f32 v29, 0xbd000000, v37\n"
      "v_mul_f32 v30, 0xbd000000, v38\n"
      "v_mul_f32 v31, 0xbd000000, v39\n"
      "v_mfma_f32_16x16x32_bf16 v[68:71], v[98:101], v[240:243], v[24:27]\n"
      "v_mfma_f32_16x16x32_bf16 v[72:75], v[96:99], v[240:243], v[24:27]\n"
      "v_mfma_f32_16x16x32_bf16 v[84:87], v[98:101], v[244:247], v[28:31]\n"
      "v_mfma_f32_16x16x32_bf16 v[88:91], v[96:99], v[244:247], v[28:31]\n"
      "s_nop 4\n"
      "v_cvt_pk_bf16_f32 v240, v68, v69\n"
      "v_cvt_pk_bf16_f32 v241, v70, v71\n"
      "v_cvt_pk_bf16_f32 v242, v72, v73\n"
      "v_cvt_pk_bf16_f32 v243, v74, v75\n"
      "v_cvt_pk_bf16_f32 v244, v84, v85\n"
      "v_cvt_pk_bf16_f32 v245, v86, v87\n"
      "v_cvt_pk_bf16_f32 v246, v88, v89\n"
      "v_cvt_pk_bf16_f32 v247, v90, v91\n"
      "s_waitcnt vmcnt(0)\n"
      "v_cmp_ne_u64 vcc, s[98:99], v[44:45]\n"
      "s_cbranch_vccz .Lbk15\n"
      "s_mov_b32 s56, 0x20000\n"
      ".Lsp15:\n"
      "buffer_load_dwordx2 v[44:45], v5, s[36:39], 0 offen offset:8 sc1\n"
      "s_waitcnt vmcnt(0)\n"
      "v_cmp_ne_u64 vcc, s[98:99], v[44:45]\n"
      "s_cbranch_vccz .Lbk15\n"
      "s_sleep 4\n"
      "s_sub_u32 s56, s56, 1\n"
      "s_cbranch_scc0 .Lsp15\n"
      ".Lbk15:\n"
      "s_cmp_eq_u32 s28, 0\n"
      "s_cselect_b32 s50, 0, 0x40000000\n"
      "v_or_b32 v7, s50, v7\n"
      "s_mov_b32 s73, s79\n"
      "s_mov_b32 s56, 0x20000\n"
      ".Lsp16:\n"
      "v_cmp_gt_u32 vcc, 1, v6\n"
      "s_cbranch_vccz .Lbk16\n"
      "s_sleep 4\n"
      "buffer_load_dword v6, v5, s[36:39], 0 offen sc1\n"
      "s_waitcnt vmcnt(0)\n"
      "s_sub_u32 s56, s56, 1\n"
      "s_cbranch_scc0 .Lsp16\n"
      ".Lbk16:\n"
      "s_add_u32 m0, s73, 0\n"
      "s_nop 0\n"
      "buffer_load_dwordx4 v2, s[36:39], s69 offen sc1 lds\n"
      "buffer_load_dwordx4 v2, s[36:39], s69 offen offset:1024 sc1 lds\n"
      "buffer_load_dwordx4 v2, s[36:39], s69 offen offset:2048 sc1 lds\n"
      "buffer_load_dwordx4 v2, s[36:39], s69 offen offset:3072 sc1 lds\n"
      "s_add_u32 m0, s74, 0\n"
      "s_nop 0\n"
      "buffer_load_dwordx4 v4, s[36:39], s69 offen sc1 lds\n"
      "s_add_u32 s69, s69, 20480\n"
      "s_mov_b32 s56, 0x20000\n"
      ".Lsp17:\n"
      "v_cmp_gt_u32 vcc, 2, v6\n"
      "s_cbranch_vccz .Lbk17\n"
      "s_sleep 4\n"
      "buffer_load_dword v6, v5, s[36:39], 0 offen sc1\n"
      "s_waitcnt vmcnt(0)\n"
      "s_sub_u32 s56, s56, 1\n"
      "s_cbranch_scc0 .Lsp17\n"
      ".Lbk17:\n"
      "s_add_u32 m0, s73, 20480\n"
      "s_nop 0\n"
      "buffer_load_dwordx4 v2, s[36:39], s69 offen sc1 lds\n"
      "buffer_load_dwordx4 v2, s[36:39], s69 offen offset:1024 sc1 lds\n"
      "buffer_load_dwordx4 v2, s[36:39], s69 offen offset:2048 sc1 lds\n"
      "buffer_load_dwordx4 v2, s[36:39], s69 offen offset:3072 sc1 lds\n"
      "s_add_u32 m0, s74, 20480\n"
      "s_nop 0\n"
      "buffer_load_dwordx4 v4, s[36:39], s69 offen sc1 lds\n"
      "s_add_u32 s69, s69, 20480\n"
      "s_waitcnt vmcnt(5)\n"
      "s_barrier\n"
      "ds_read_b128 v[20:23], v3 offset:0\n"
      "ds_read_b128 v[24:27], v3 offset:1024\n"
      "ds_read_b128 v[28:31], v3 offset:2048\n"
      "buffer_load_dword v6, v5, s[36:39], 0 offen sc1\n"
      "s_mov_b32 s77, s76\n"
      "s_waitcnt lgkmcnt(0)\n"
      "s_branch .Lgu_in0\n"
      ".Lgu_loop0:\n"
      "v_mfma_f32_16x16x32_bf16 v[52:55], a[112:115], v[28:31], v[52:55]\n"
      "s_waitcnt lgkmcnt(1)\n"
      "ds_read_b128 v[92:95], v3 offset:37888\n"
      "v_add_u32 v9, s75, v80\n"
      "v_mfma_f32_16x16x32_bf16 v[52:55], a[120:123], v[32:35], v[52:55]\n"
      "v_add_u32 v3, s75, v2\n"
      "v_mul_f32 v84, v84, v72\n"
      "v_mfma_f32_16x16x32_bf16 v[56:59], v[216:219], v[28:31], v[56:59]\n"
      "v_rcp_f32 v74, v74\n"
      "v_mfma_f32_16x16x32_bf16 v[56:59], v[224:227], v[32:35], v[56:59]\n"
      "s_waitcnt vmcnt(6)\n"
      "v_mul_f32 v85, v85, v73\n"
      "s_barrier\n"
      "v_mfma_f32_16x16x32_bf16 v[60:63], a[116:119], v[28:31], v[60:63]\n"
      "ds_read_b128 v[20:23], v3 offset:0\n"
      "v_mul_f32 v86, v86, v74\n"
      "v_mfma_f32_16x16x32_bf16 v[60:63], a[124:127], v[32:35], v[60:63]\n"
      "buffer_store_dword v8, v7, s[36:39], 0 offen sc1\n"
      "v_mfma_f32_16x16x32_bf16 v[64:67], v[220:223], v[28:31], v[64:67]\n"
      "v_rcp_f32 v75, v75\n"
      "v_mfma_f32_16x16x32_bf16 v[64:67], v[228:231], v[32:35], v[64:67]\n"
      "s_waitcnt lgkmcnt(2)\n"
      "v_mul_f32 v87, v87, v75\n"
      "v_cvt_pk_bf16_f32 v78, v84, v85\n"
      "v_mfma_f32_16x16x32_bf16 v[52:55], a[128:131], v[88:91], v[52:55]\n"
      "s_waitcnt lgkmcnt(1)\n"
      "ds_read_b128 v[24:27], v3 offset:1024\n"
      "v_cvt_pk_bf16_f32 v79, v86, v87\n"
      "v_mfma_f32_16x16x32_bf16 v[52:55], a[136:139], v[92:95], v[52:55]\n"
      "buffer_store_dwordx4 v[76:79], v2, s[36:39], s71 offen sc1\n"
      "v_mfma_f32_16x16x32_bf16 v[56:59], v[232:235], v[88:91], v[56:59]\n"
      "ds_read_b64 v[10:11], v9 offset:18432\n"
      "ds_read_b64 v[12:13], v9 offset:18944\n"
      "v_mfma_f32_16x16x32_bf16 v[56:59], v[240:243], v[92:95], v[56:59]\n"
      "v_mfma_f32_16x16x32_bf16 v[60:63], a[132:135], v[88:91], v[60:63]\n"
      "ds_read_b128 v[28:31], v3 offset:2048\n"
      "v_mfma_f32_16x16x32_bf16 v[60:63], a[140:143], v[92:95], v[60:63]\n"
      "v_mfma_f32_16x16x32_bf16 v[64:67], v[236:239], v[88:91], v[64:67]\n"
      "v_mul_f32 v68, v52, v52\n"
      "v_mul_f32 v69, v53, v53\n"
      "v_mfma_f32_16x16x32_bf16 v[64:67], v[244:247], v[92:95], v[64:67]\n"
      "s_waitcnt lgkmcnt(1)\n"
      "v_add_f32 v10, v10, v12\n"
      "v_add_f32 v11, v11, v13\n"
      ".Lgu_in0:\n"
      "v_mfma_f32_16x16x32_bf16 v[36:39], a[0:3], v[20:23], 0\n"
      "ds_read_b128 v[32:35], v3 offset:3072\n"
      "v_permlane32_swap_b32 v10, v11\n"
      "v_mfma_f32_16x16x32_bf16 v[36:39], a[8:11], v[24:27], v[36:39]\n"
      "s_cmp_lt_u32 s66, 64\n"
      "v_add_f32 v10, v10, v11\n"
      "v_mov_b32 v12, v10\n"
      "v_mfma_f32_16x16x32_bf16 v[40:43], v[104:107], v[20:23], 0\n"
      "s_cselect_b32 s71, s70, 0x40000000\n"
      "v_permlane16_swap_b32 v10, v12\n"
      "v_mul_f32 v70, v54, v54\n"
      "v_mfma_f32_16x16x32_bf16 v[40:43], v[112:115], v[24:27], v[40:43]\n"
      "s_add_u32 s70, s70, 147456\n"
      "v_add_f32 v10, v10, v12\n"
      "v_mov_b32 v11, v10\n"
      "v_mfma_f32_16x16x32_bf16 v[44:47], a[4:7], v[20:23], 0\n"
      "s_max_i32 s72, s66, 0\n"
      "ds_read_b128 v[88:91], v3 offset:4096\n"
      "v_permlane32_swap_b32 v10, v11\n"
      "v_mfma_f32_16x16x32_bf16 v[44:47], a[12:15], v[24:27], v[44:47]\n"
      "s_add_u32 s67, s66, 4\n"
      "v_mul_f32 v10, 0x3ae38e39, v10\n"
      "v_fmaak_f32 v11, v81, v11, 0x3727c5ac\n"
      "v_mfma_f32_16x16x32_bf16 v[48:51], v[108:111], v[20:23], 0\n"
      "s_min_i32 s67, s67, 65\n"
      "v_fma_f32 v11, -v10, v10, v11\n"
      "v_max_f32 v14, 0x3727c5ac, v11\n"
      "v_mfma_f32_16x16x32_bf16 v[48:51], v[116:119], v[24:27], v[48:51]\n"
      "s_waitcnt lgkmcnt(2)\n"
      "v_rsq_f32 v12, v14\n"
      "v_mfma_f32_16x16x32_bf16 v[36:39], a[16:19], v[28:31], v[36:39]\n"
      "s_waitcnt lgkmcnt(1)\n"
      "ds_read_b128 v[92:95], v3 offset:5120\n"
      "v_mul_f32 v15, 0xc0135761, v12\n"
      "v_mfma_f32_16x16x32_bf16 v[36:39], a[24:27], v[32:35], v[36:39]\n"
      "s_cmp_le_i32 s66, 61\n"
      "v_mul_f32 v13, v12, v12\n"
      "v_mul_f32 v71, v55, v55\n"
      "v_mfma_f32_16x16x32_bf16 v[40:43], v[120:123], v[28:31], v[40:43]\n"
      "s_cselect_b32 s69, s68, 0x40000000\n"
      "v_mul_f32 v13, 0x3d372713, v13\n"
      "v_mul_f32 v16, v13, v15\n"
      "v_mfma_f32_16x16x32_bf16 v[40:43], v[128:131], v[32:35], v[40:43]\n"
      "s_add_u32 s68, s68, 20480\n"
      "v_fma_f32 v68, v68, v16, v15\n"
      "v_fma_f32 v69, v69, v16, v15\n"
      "v_mfma_f32_16x16x32_bf16 v[44:47], a[20:23], v[28:31], v[44:47]\n"
      "s_add_u32 s66, s66, 1\n"
      "ds_read_b128 v[20:23], v3 offset:6144\n"
      "v_fma_f32 v70, v70, v16, v15\n"
      "v_mfma_f32_16x16x32_bf16 v[44:47], a[28:31], v[32:35], v[44:47]\n"
      "s_xor_b32 s75, s75, 40960\n"
      "v_writelane_b32 v8, s72, 0\n"
      "v_fma_f32 v71, v71, v16, v15\n"
      "v_mfma_f32_16x16x32_bf16 v[48:51], v[124:127], v[28:31], v[48:51]\n"
      "s_xor_b32 s48, s75, 40960\n"
      "v_mul_f32 v68, v52, v68\n"
      "v_mul_f32 v69, v53, v69\n"
      "v_mfma_f32_16x16x32_bf16 v[48:51], v[132:135], v[32:35], v[48:51]\n"
      "s_waitcnt lgkmcnt(2)\n"
      "v_mul_f32 v70, v54, v70\n"
      "v_mul_f32 v71, v55, v71\n"
      "v_mfma_f32_16x16x32_bf16 v[36:39], a[32:35], v[88:91], v[36:39]\n"
      "s_waitcnt lgkmcnt(1)\n"
      "ds_read_b128 v[24:27], v3 offset:7168\n"
      "v_mul_f32 v56, v52, v56\n"
      "v_mfma_f32_16x16x32_bf16 v[36:39], a[40:43], v[92:95], v[36:39]\n"
      "s_sub_u32 s48, s75, s48\n"
      "v_exp_f32 v68, v68\n"
      "v_mfma_f32_16x16x32_bf16 v[40:43], v[136:139], v[88:91], v[40:43]\n"
      "s_add_u32 s73, s73, s48\n"
      "v_exp_f32 v69, v69\n"
      "v_mfma_f32_16x16x32_bf16 v[40:43], v[144:147], v[92:95], v[40:43]\n"
      "s_add_u32 s74, s74, s48\n"
      "v_mul_f32 v57, v53, v57\n"
      "v_mul_f32 v58, v54, v58\n"
      "v_mfma_f32_16x16x32_bf16 v[44:47], a[36:39], v[88:91], v[44:47]\n"
      "s_add_u32 m0, s73, 0\n"
      "ds_read_b128 v[28:31], v3 offset:8192\n"
      "v_mul_f32 v59, v55, v59\n"
      "v_mfma_f32_16x16x32_bf16 v[44:47], a[44:47], v[92:95], v[44:47]\n"
      "v_exp_f32 v70, v70\n"
      "v_mfma_f32_16x16x32_bf16 v[48:51], v[140:143], v[88:91], v[48:51]\n"
      "v_exp_f32 v71, v71\n"
      "v_mfma_f32_16x16x32_bf16 v[48:51], v[148:151], v[92:95], v[48:51]\n"
      "s_waitcnt lgkmcnt(2)\n"
      "v_fma_f32 v68, v68, v14, v14\n"
      "v_fma_f32 v69, v69, v14, v14\n"
      "v_mfma_f32_16x16x32_bf16 v[36:39], a[48:51], v[20:23], v[36:39]\n"
      "s_waitcnt lgkmcnt(1)\n"
      "ds_read_b128 v[32:35], v3 offset:9216\n"
      "v_fma_f32 v70, v70, v14, v14\n"
      "v_mfma_f32_16x16x32_bf16 v[36:39], a[56:59], v[24:27], v[36:39]\n"
      "v_fma_f32 v71, v71, v14, v14\n"
      "v_mul_f32 v72, v60, v60\n"
      "v_mfma_f32_16x16x32_bf16 v[40:43], v[152:155], v[20:23], v[40:43]\n"
      "v_rcp_f32 v68, v68\n"
      "v_mfma_f32_16x16x32_bf16 v[40:43], v[160:163], v[24:27], v[40:43]\n"
      "v_rcp_f32 v69, v69\n"
      "v_mfma_f32_16x16x32_bf16 v[44:47], a[52:55], v[20:23], v[44:47]\n"
      "ds_read_b128 v[88:91], v3 offset:10240\n"
      "v_mul_f32 v56, v56, v68\n"
      "v_mfma_f32_16x16x32_bf16 v[44:47], a[60:63], v[24:27], v[44:47]\n"
      "v_rcp_f32 v70, v70\n"
      "v_mfma_f32_16x16x32_bf16 v[48:51], v[156:159], v[20:23], v[48:51]\n"
      "v_rcp_f32 v71, v71\n"
      "v_mfma_f32_16x16x32_bf16 v[48:51], v[164:167], v[24:27], v[48:51]\n"
      "s_waitcnt lgkmcnt(2)\n"
      "v_mul_f32 v57, v57, v69\n"
      "v_mul_f32 v58, v58, v70\n"
      "v_mfma_f32_16x16x32_bf16 v[36:39], a[64:67], v[28:31], v[36:39]\n"
      "s_waitcnt lgkmcnt(1)\n"
      "ds_read_b128 v[92:95], v3 offset:11264\n"
      "v_mul_f32 v59, v59, v71\n"
      "v_mfma_f32_16x16x32_bf16 v[36:39], a[72:75], v[32:35], v[36:39]\n"
      "v_cvt_pk_bf16_f32 v76, v56, v57\n"
      "v_cvt_pk_bf16_f32 v77, v58, v59\n"
      "v_mfma_f32_16x16x32_bf16 v[40:43], v[168:171], v[28:31], v[40:43]\n"
      "v_mul_f32 v73, v61, v61\n"
      "v_mul_f32 v74, v62, v62\n"
      "v_mfma_f32_16x16x32_bf16 v[40:43], v[176:179], v[32:35], v[40:43]\n"
      "v_mul_f32 v75, v63, v63\n"
      "v_fma_f32 v72, v72, v16, v15\n"
      "v_mfma_f32_16x16x32_bf16 v[44:47], a[68:71], v[28:31], v[44:47]\n"
      "ds_read_b128 v[20:23], v3 offset:12288\n"
      "v_fma_f32 v73, v73, v16, v15\n"
      "v_mfma_f32_16x16x32_bf16 v[44:47], a[76:79], v[32:35], v[44:47]\n"
      "v_fma_f32 v74, v74, v16, v15\n"
      "v_fma_f32 v75, v75, v16, v15\n"
      "v_mfma_f32_16x16x32_bf16 v[48:51], v[172:175], v[28:31], v[48:51]\n"
      "v_mul_f32 v72, v60, v72\n"
      "v_mul_f32 v73, v61, v73\n"
      "v_mfma_f32_16x16x32_bf16 v[48:51], v[180:183], v[32:35], v[48:51]\n"
      "s_waitcnt vmcnt(7) lgkmcnt(2)\n"
      "v_cmp_gt_u32 vcc, s67, v6\n"
      "v_mul_f32 v74, v62, v74\n"
      "v_mfma_f32_16x16x32_bf16 v[36:39], a[80:83], v[88:91], v[36:39]\n"
      "s_waitcnt lgkmcnt(1)\n"
      "s_cbranch_vccnz .Lsl18\n"
      ".Lbk18:\n"
      "ds_read_b128 v[24:27], v3 offset:13312\n"
      "v_mfma_f32_16x16x32_bf16 v[36:39], a[88:91], v[92:95], v[36:39]\n"
      "buffer_load_dword v6, v5, s[36:39], 0 offen sc1\n"
      "v_mul_f32 v75, v63, v75\n"
      "v_mfma_f32_16x16x32_bf16 v[40:43], v[184:187], v[88:91], v[40:43]\n"
      "v_exp_f32 v72, v72\n"
      "v_mfma_f32_16x16x32_bf16 v[40:43], v[192:195], v[92:95], v[40:43]\n"
      "buffer_load_dwordx4 v2, s[36:39], s69 offen sc1 lds\n"
      "v_mul_f32 v84, v60, v64\n"
      "v_mfma_f32_16x16x32_bf16 v[44:47], a[84:87], v[88:91], v[44:47]\n"
      "ds_read_b128 v[28:31], v3 offset:14336\n"
      "v_mul_f32 v85, v61, v65\n"
      "v_mfma_f32_16x16x32_bf16 v[44:47], a[92:95], v[92:95], v[44:47]\n"
      "v_exp_f32 v73, v73\n"
      "v_mfma_f32_16x16x32_bf16 v[48:51], v[188:191], v[88:91], v[48:51]\n"
      "buffer_load_dwordx4 v2, s[36:39], s69 offen offset:1024 sc1 lds\n"
      "v_mul_f32 v86, v62, v66\n"
      "v_mfma_f32_16x16x32_bf16 v[48:51], v[196:199], v[92:95], v[48:51]\n"
      "s_waitcnt lgkmcnt(2)\n"
      "v_exp_f32 v74, v74\n"
      "v_mfma_f32_16x16x32_bf16 v[36:39], a[96:99], v[20:23], v[36:39]\n"
      "s_waitcnt lgkmcnt(1)\n"
      "ds_read_b128 v[32:35], v3 offset:15360\n"
      "v_mul_f32 v87, v63, v67\n"
      "v_mfma_f32_16x16x32_bf16 v[36:39], a[104:107], v[24:27], v[36:39]\n"
      "buffer_load_dwordx4 v2, s[36:39], s69 offen offset:2048 sc1 lds\n"
      "v_fma_f32 v72, v72, v14, v14\n"
      "v_mfma_f32_16x16x32_bf16 v[40:43], v[200:203], v[20:23], v[40:43]\n"
      "v_exp_f32 v75, v75\n"
      "v_mfma_f32_16x16x32_bf16 v[40:43], v[208:211], v[24:27], v[40:43]\n"
      "v_fma_f32 v73, v73, v14, v14\n"
      "v_fma_f32 v74, v74, v14, v14\n"
      "v_mfma_f32_16x16x32_bf16 v[44:47], a[100:103], v[20:23], v[44:47]\n"
      "buffer_load_dwordx4 v2, s[36:39], s69 offen offset:3072 sc1 lds\n"
      "ds_read_b128 v[88:91], v3 offset:16384\n"
      "v_mfma_f32_16x16x32_bf16 v[44:47], a[108:111], v[24:27], v[44:47]\n"
      "s_add_u32 m0, s74, 0\n"
      "v_fma_f32 v75, v75, v14, v14\n"
      "v_mfma_f32_16x16x32_bf16 v[48:51], v[204:207], v[20:23], v[48:51]\n"
      "v_rcp_f32 v72, v72\n"
      "v_mfma_f32_16x16x32_bf16 v[48:51], v[212:215], v[24:27], v[48:51]\n"
      "s_waitcnt lgkmcnt(2)\n"
      "buffer_load_dwordx4 v4, s[36:39], s69 offen sc1 lds\n"
      "v_rcp_f32 v73, v73\n"
      ".Lmg00:\n"
      "v_mfma_f32_16x16x32_bf16 v[36:39], a[112:115], v[28:31], v[36:39]\n"
      "s_waitcnt lgkmcnt(1)\n"
      "ds_read_b128 v[92:95], v3 offset:17408\n"
      "v_mul_f32 v84, v84, v72\n"
      "v_mfma_f32_16x16x32_bf16 v[36:39], a[120:123], v[32:35], v[36:39]\n"
      "v_rcp_f32 v74, v74\n"
      "v_mfma_f32_16x16x32_bf16 v[40:43], v[216:219], v[28:31], v[40:43]\n"
      "v_rcp_f32 v75, v75\n"
      "v_mfma_f32_16x16x32_bf16 v[40:43], v[224:227], v[32:35], v[40:43]\n"
      "s_waitcnt vmcnt(6)\n"
      "v_mul_f32 v85, v85, v73\n"
      "s_barrier\n"
      "v_mfma_f32_16x16x32_bf16 v[44:47], a[116:119], v[28:31], v[44:47]\n"
      "ds_read_b128 v[20:23], v3 offset:20480\n"
      "v_mul_f32 v86, v86, v74\n"
      "v_mfma_f32_16x16x32_bf16 v[44:47], a[124:127], v[32:35], v[44:47]\n"
      "buffer_store_dword v8, v7, s[36:39], 0 offen sc1\n"
      "v_mfma_f32_16x16x32_bf16 v[48:51], v[220:223], v[28:31], v[48:51]\n"
      "v_mul_f32 v87, v87, v75\n"
      "v_cvt_pk_bf16_f32 v78, v84, v85\n"
      "v_mfma_f32_16x16x32_bf16 v[48:51], v[228:231], v[32:35], v[48:51]\n"
      "s_waitcnt lgkmcnt(2)\n"
      "v_cvt_pk_bf16_f32 v79, v86, v87\n"
      "ds_read_b64 v[10:11], v9 offset:38912\n"
      "v_mfma_f32_16x16x32_bf16 v[36:39], a[128:131], v[88:91], v[36:39]\n"
      "s_waitcnt lgkmcnt(2)\n"
      "ds_read_b128 v[24:27], v3 offset:21504\n"
      "ds_read_b64 v[12:13], v9 offset:39424\n"
      "v_mfma_f32_16x16x32_bf16 v[36:39], a[136:139], v[92:95], v[36:39]\n"
      "buffer_store_dwordx4 v[76:79], v2, s[36:39], s71 offen sc1\n"
      "v_mfma_f32_16x16x32_bf16 v[40:43], v[232:235], v[88:91], v[40:43]\n"
      "v_mfma_f32_16x16x32_bf16 v[40:43], v[240:243], v[92:95], v[40:43]\n"
      "v_mfma_f32_16x16x32_bf16 v[44:47], a[132:135], v[88:91], v[44:47]\n"
      "ds_read_b128 v[28:31], v3 offset:22528\n"
      "v_mfma_f32_16x16x32_bf16 v[44:47], a[140:143], v[92:95], v[44:47]\n"
      "s_waitcnt lgkmcnt(1)\n"
      "v_add_f32 v10, v10, v12\n"
      "v_add_f32 v11, v11, v13\n"
      "v_mfma_f32_16x16x32_bf16 v[48:51], v[236:239], v[88:91], v[48:51]\n"
      "s_nop 0\n"
      "v_permlane32_swap_b32 v10, v11\n"
      "v_mul_f32 v68, v36, v36\n"
      "v_mfma_f32_16x16x32_bf16 v[48:51], v[244:247], v[92:95], v[48:51]\n"
      "v_add_f32 v10, v10, v11\n"
      "v_mov_b32 v12, v10\n"
      "v_mfma_f32_16x16x32_bf16 v[52:55], a[0:3], v[20:23], 0\n"
      "ds_read_b128 v[32:35], v3 offset:23552\n"
      "v_permlane16_swap_b32 v10, v12\n"
      "v_mfma_f32_16x16x32_bf16 v[52:55], a[8:11], v[24:27], v[52:55]\n"
      "s_cmp_lt_u32 s66, 64\n"
      "v_add_f32 v10, v10, v12\n"
      "v_mov_b32 v11, v10\n"
      "v_mfma_f32_16x16x32_bf16 v[56:59], v[104:107], v[20:23], 0\n"
      "s_cselect_b32 s71, s70, 0x40000000\n"
      "v_permlane32_swap_b32 v10, v11\n"
      "v_mul_f32 v69, v37, v37\n"
      "v_mfma_f32_16x16x32_bf16 v[56:59], v[112:115], v[24:27], v[56:59]\n"
      "s_add_u32 s70, s70, 147456\n"
      "v_mul_f32 v10, 0x3ae38e39, v10\n"
      "v_fmaak_f32 v11, v81, v11, 0x3727c5ac\n"
      "v_mfma_f32_16x16x32_bf16 v[60:63], a[4:7], v[20:23], 0\n"
      "s_max_i32 s72, s66, 0\n"
      "ds_read_b128 v[88:91], v3 offset:24576\n"
      "v_fma_f32 v11, -v10, v10, v11\n"
      "v_mfma_f32_16x16x32_bf16 v[60:63], a[12:15], v[24:27], v[60:63]\n"
      "s_add_u32 s67, s66, 4\n"
      "v_max_f32 v17, 0x3727c5ac, v11\n"
      "v_mul_f32 v70, v38, v38\n"
      "v_mfma_f32_16x16x32_bf16 v[64:67], v[108:111], v[20:23], 0\n"
      "s_min_i32 s67, s67, 65\n"
      "v_rsq_f32 v12, v17\n"
      "v_mfma_f32_16x16x32_bf16 v[64:67], v[116:119], v[24:27], v[64:67]\n"
      "s_waitcnt lgkmcnt(2)\n"
      "v_mul_f32 v18, 0xc0135761, v12\n"
      "v_mul_f32 v13, v12, v12\n"
      "v_mfma_f32_16x16x32_bf16 v[52:55], a[16:19], v[28:31], v[52:55]\n"
      "s_waitcnt lgkmcnt(1)\n"
      "ds_read_b128 v[92:95], v3 offset:25600\n"
      "v_mul_f32 v13, 0x3d372713, v13\n"
      "v_mfma_f32_16x16x32_bf16 v[52:55], a[24:27], v[32:35], v[52:55]\n"
      "s_cmp_le_i32 s66, 61\n"
      "v_mul_f32 v19, v13, v18\n"
      "v_mul_f32 v71, v39, v39\n"
      "v_mfma_f32_16x16x32_bf16 v[56:59], v[120:123], v[28:31], v[56:59]\n"
      "s_cselect_b32 s69, s68, 0x40000000\n"
      "v_fma_f32 v68, v68, v19, v18\n"
      "v_fma_f32 v69, v69, v19, v18\n"
      "v_mfma_f32_16x16x32_bf16 v[56:59], v[128:131], v[32:35], v[56:59]\n"
      "s_add_u32 s68, s68, 20480\n"
      "v_fma_f32 v70, v70, v19, v18\n"
      "v_fma_f32 v71, v71, v19, v18\n"
      "v_mfma_f32_16x16x32_bf16 v[60:63], a[20:23], v[28:31], v[60:63]\n"
      "s_add_u32 s66, s66, 1\n"
      "ds_read_b128 v[20:23], v3 offset:26624\n"
      "v_mul_f32 v68, v36, v68\n"
      "v_mfma_f32_16x16x32_bf16 v[60:63], a[28:31], v[32:35], v[60:63]\n"
      "s_add_u32 m0, s73, 20480\n"
      "v_writelane_b32 v8, s72, 0\n"
      "v_mul_f32 v69, v37, v69\n"
      "v_mfma_f32_16x16x32_bf16 v[64:67], v[124:127], v[28:31], v[64:67]\n"
      "v_mul_f32 v70, v38, v70\n"
      "v_mul_f32 v71, v39, v71\n"
      "v_mfma_f32_16x16x32_bf16 v[64:67], v[132:135], v[32:35], v[64:67]\n"
      "s_waitcnt lgkmcnt(2)\n"
      "v_exp_f32 v68, v68\n"
      "v_mfma_f32_16x16x32_bf16 v[52:55], a[32:35], v[88:91], v[52:55]\n"
      "s_waitcnt lgkmcnt(1)\n"
      "ds_read_b128 v[24:27], v3 offset:27648\n"
      "v_mul_f32 v40, v36, v40\n"
      "v_mfma_f32_16x16x32_bf16 v[52:55], a[40:43], v[92:95], v[52:55]\n"
      "v_exp_f32 v69, v69\n"
      "v_mfma_f32_16x16x32_bf16 v[56:59], v[136:139], v[88:91], v[56:59]\n"
      "v_mul_f32 v41, v37, v41\n"
      "v_mul_f32 v42, v38, v42\n"
      "v_mfma_f32_16x16x32_bf16 v[56:59], v[144:147], v[92:95], v[56:59]\n"
      "v_exp_f32 v70, v70\n"
      "v_mfma_f32_16x16x32_bf16 v[60:63], a[36:39], v[88:91], v[60:63]\n"
      "ds_read_b128 v[28:31], v3 offset:28672\n"
      "v_mul_f32 v43, v39, v43\n"
      "v_mfma_f32_16x16x32_bf16 v[60:63], a[44:47], v[92:95], v[60:63]\n"
      "v_exp_f32 v71, v71\n"
      "v_mfma_f32_16x16x32_bf16 v[64:67], v[140:143], v[88:91], v[64:67]\n"
      "v_fma_f32 v68, v68, v17, v17\n"
      "v_fma_f32 v69, v69, v17, v17\n"
      "v_mfma_f32_16x16x32_bf16 v[64:67], v[148:151], v[92:95], v[64:67]\n"
      "s_waitcnt lgkmcnt(2)\n"
      "v_fma_f32 v70, v70, v17, v17\n"
      "v_fma_f32 v71, v71, v17, v17\n"
      "v_mfma_f32_16x16x32_bf16 v[52:55], a[48:51], v[20:23], v[52:55]\n"
      "s_waitcnt lgkmcnt(1)\n"
      "ds_read_b128 v[32:35], v3 offset:29696\n"
      "v_mul_f32 v72, v44, v44\n"
      "v_mfma_f32_16x16x32_bf16 v[52:55], a[56:59], v[24:27], v[52:55]\n"
      "v_rcp_f32 v68, v68\n"
      "v_mfma_f32_16x16x32_bf16 v[56:59], v[152:155], v[20:23], v[56:59]\n"
      "v_rcp_f32 v69, v69\n"
      "v_mfma_f32_16x16x32_bf16 v[56:59], v[160:163], v[24:27], v[56:59]\n"
      "v_rcp_f32 v70, v70\n"
      "v_mfma_f32_16x16x32_bf16 v[60:63], a[52:55], v[20:23], v[60:63]\n"
      "ds_read_b128 v[88:91], v3 offset:30720\n"
      "v_mul_f32 v40, v40, v68\n"
      "v_mfma_f32_16x16x32_bf16 v[60:63], a[60:63], v[24:27], v[60:63]\n"
      "v_rcp_f32 v71, v71\n"
      "v_mfma_f32_16x16x32_bf16 v[64:67], v[156:159], v[20:23], v[64:67]\n"
      "v_mul_f32 v41, v41, v69\n"
      "v_mul_f32 v42, v42, v70\n"
      "v_mfma_f32_16x16x32_bf16 v[64:67], v[164:167], v[24:27], v[64:67]\n"
      "s_waitcnt lgkmcnt(2)\n"
      "v_mul_f32 v43, v43, v71\n"
      "v_cvt_pk_bf16_f32 v76, v40, v41\n"
      "v_mfma_f32_16x16x32_bf16 v[52:55], a[64:67], v[28:31], v[52:55]\n"
      "s_waitcnt lgkmcnt(1)\n"
      "ds_read_b128 v[92:95], v3 offset:31744\n"
      "v_cvt_pk_bf16_f32 v77, v42, v43\n"
      "v_mfma_f32_16x16x32_bf16 v[52:55], a[72:75], v[32:35], v[52:55]\n"
      "v_mul_f32 v73, v45, v45\n"
      "v_mul_f32 v74, v46, v46\n"
      "v_mfma_f32_16x16x32_bf16 v[56:59], v[168:171], v[28:31], v[56:59]\n"
      "v_mul_f32 v75, v47, v47\n"
      "v_fma_f32 v72, v72, v19, v18\n"
      "v_mfma_f32_16x16x32_bf16 v[56:59], v[176:179], v[32:35], v[56:59]\n"
      "v_fma_f32 v73, v73, v19, v18\n"
      "v_fma_f32 v74, v74, v19, v18\n"
      "v_mfma_f32_16x16x32_bf16 v[60:63], a[68:71], v[28:31], v[60:63]\n"
      "ds_read_b128 v[20:23], v3 offset:32768\n"
      "v_fma_f32 v75, v75, v19, v18\n"
      "v_mfma_f32_16x16x32_bf16 v[60:63], a[76:79], v[32:35], v[60:63]\n"
      "v_mul_f32 v72, v44, v72\n"
      "v_mul_f32 v73, v45, v73\n"
      "v_mfma_f32_16x16x32_bf16 v[64:67], v[172:175], v[28:31], v[64:67]\n"
      "v_mul_f32 v74, v46, v74\n"
      "v_mul_f32 v75, v47, v75\n"
      "v_mfma_f32_16x16x32_bf16 v[64:67], v[180:183], v[32:35], v[64:67]\n"
      "s_waitcnt vmcnt(7) lgkmcnt(2)\n"
      "v_cmp_gt_u32 vcc, s67, v6\n"
      "v_mul_f32 v84, v44, v48\n"
      "v_mfma_f32_16x16x32_bf16 v[52:55], a[80:83], v[88:91], v[52:55]\n"
      "s_waitcnt lgkmcnt(1)\n"
      "s_cbranch_vccnz .Lsl19\n"
      ".Lbk19:\n"
      "ds_read_b128 v[24:27], v3 offset:33792\n"
      "v_mfma_f32_16x16x32_bf16 v[52:55], a[88:91], v[92:95], v[52:55]\n"
      "buffer_load_dword v6, v5, s[36:39], 0 offen sc1\n"
      "v_mul_f32 v85, v45, v49\n"
      "v_mfma_f32_16x16x32_bf16 v[56:59], v[184:187], v[88:91], v[56:59]\n"
      "v_exp_f32 v72, v72\n"
      "v_mfma_f32_16x16x32_bf16 v[56:59], v[192:195], v[92:95], v[56:59]\n"
      "buffer_load_dwordx4 v2, s[36:39], s69 offen sc1 lds\n"
      "v_mul_f32 v86, v46, v50\n"
      "v_mfma_f32_16x16x32_bf16 v[60:63], a[84:87], v[88:91], v[60:63]\n"
      "ds_read_b128 v[28:31], v3 offset:34816\n"
      "v_mul_f32 v87, v47, v51\n"
      "v_mfma_f32_16x16x32_bf16 v[60:63], a[92:95], v[92:95], v[60:63]\n"
      "v_exp_f32 v73, v73\n"
      "v_mfma_f32_16x16x32_bf16 v[64:67], v[188:191], v[88:91], v[64:67]\n"
      "buffer_load_dwordx4 v2, s[36:39], s69 offen offset:1024 sc1 lds\n"
      "v_fma_f32 v72, v72, v17, v17\n"
      "v_mfma_f32_16x16x32_bf16 v[64:67], v[196:199], v[92:95], v[64:67]\n"
      "s_waitcnt lgkmcnt(2)\n"
      "v_exp_f32 v74, v74\n"
      "v_mfma_f32_16x16x32_bf16 v[52:55], a[96:99], v[20:23], v[52:55]\n"
      "s_waitcnt lgkmcnt(1)\n"
      "ds_read_b128 v[32:35], v3 offset:35840\n"
      "v_fma_f32 v73, v73, v17, v17\n"
      "v_mfma_f32_16x16x32_bf16 v[52:55], a[104:107], v[24:27], v[52:55]\n"
      "buffer_load_dwordx4 v2, s[36:39], s69 offen offset:2048 sc1 lds\n"
      "v_fma_f32 v74, v74, v17, v17\n"
      "v_mfma_f32_16x16x32_bf16 v[56:59], v[200:203], v[20:23], v[56:59]\n"
      "v_exp_f32 v75, v75\n"
      "v_mfma_f32_16x16x32_bf16 v[56:59], v[208:211], v[24:27], v[56:59]\n"
      "v_fma_f32 v75, v75, v17, v17\n"
      "v_mfma_f32_16x16x32_bf16 v[60:63], a[100:103], v[20:23], v[60:63]\n"
      "buffer_load_dwordx4 v2, s[36:39], s69 offen offset:3072 sc1 lds\n"
      "ds_read_b128 v[88:91], v3 offset:36864\n"
      "v_mfma_f32_16x16x32_bf16 v[60:63], a[108:111], v[24:27], v[60:63]\n"
      "s_add_u32 m0, s74, 20480\n"
      "v_rcp_f32 v72, v72\n"
      "v_mfma_f32_16x16x32_bf16 v[64:67], v[204:207], v[20:23], v[64:67]\n"
      "s_cmp_lt_i32 s66, 63\n"
      "v_rcp_f32 v73, v73\n"
      "v_mfma_f32_16x16x32_bf16 v[64:67], v[212:215], v[24:27], v[64:67]\n"
      "s_waitcnt lgkmcnt(2)\n"
      "buffer_load_dwordx4 v4, s[36:39], s69 offen sc1 lds\n"
      ".Lmg10:\n"
      "s_cbranch_scc1 .Lgu_loop0\n"
      ".Lgu_t:\n"
      "s_waitcnt lgkmcnt(0)\n"
      "v_mfma_f32_16x16x32_bf16 v[52:55], a[112:115], v[28:31], v[52:55]\n"
      "ds_read_b128 v[92:95], v3 offset:37888\n"
      "v_add_u32 v9, s75, v80\n"
      "v_mfma_f32_16x16x32_bf16 v[52:55], a[120:123], v[32:35], v[52:55]\n"
      "s_waitcnt vmcnt(6)\n"
      "s_barrier\n"
      "buffer_store_dword v8, v7, s[36:39], 0 offen sc1\n"
      "v_add_u32 v3, s75, v2\n"
      "v_mul_f32 v84, v84, v72\n"
      "v_mfma_f32_16x16x32_bf16 v[56:59], v[216:219], v[28:31], v[56:59]\n"
      "v_rcp_f32 v74, v74\n"
      "v_mfma_f32_16x16x32_bf16 v[56:59], v[224:227], v[32:35], v[56:59]\n"
      "v_rcp_f32 v75, v75\n"
      "v_mfma_f32_16x16x32_bf16 v[60:63], a[116:119], v[28:31], v[60:63]\n"
      "v_mul_f32 v85, v85, v73\n"
      "v_mul_f32 v86, v86, v74\n"
      "v_mfma_f32_16x16x32_bf16 v[60:63], a[124:127], v[32:35], v[60:63]\n"
      "v_mul_f32 v87, v87, v75\n"
      "v_cvt_pk_bf16_f32 v78, v84, v85\n"
      "v_mfma_f32_16x16x32_bf16 v[64:67], v[220:223], v[28:31], v[64:67]\n"
      "v_cvt_pk_bf16_f32 v79, v86, v87\n"
      "ds_read_b64 v[10:11], v9 offset:18432\n"
      "v_mfma_f32_16x16x32_bf16 v[64:67], v[228:231], v[32:35], v[64:67]\n"
      "buffer_store_dwordx4 v[76:79], v2, s[36:39], s71 offen sc1\n"
      "ds_read_b64 v[12:13], v9 offset:18944\n"
      "v_mfma_f32_16x16x32_bf16 v[52:55], a[128:131], v[88:91], v[52:55]\n"
      "s_waitcnt lgkmcnt(2)\n"
      "v_writelane_b32 v8, 63, 0\n"
      "v_mfma_f32_16x16x32_bf16 v[52:55], a[136:139], v[92:95], v[52:55]\n"
      "v_mfma_f32_16x16x32_bf16 v[56:59], v[232:235], v[88:91], v[56:59]\n"
      "v_mfma_f32_16x16x32_bf16 v[56:59], v[240:243], v[92:95], v[56:59]\n"
      "v_mfma_f32_16x16x32_bf16 v[60:63], a[132:135], v[88:91], v[60:63]\n"
      "s_waitcnt lgkmcnt(0)\n"
      "v_add_f32 v10, v10, v12\n"
      "v_add_f32 v11, v11, v13\n"
      "v_mfma_f32_16x16x32_bf16 v[60:63], a[140:143], v[92:95], v[60:63]\n"
      "s_nop 0\n"
      "v_permlane32_swap_b32 v10, v11\n"
      "v_mfma_f32_16x16x32_bf16 v[64:67], v[236:239], v[88:91], v[64:67]\n"
      "v_add_f32 v10, v10, v11\n"
      "v_mov_b32 v12, v10\n"
      "v_mfma_f32_16x16x32_bf16 v[64:67], v[244:247], v[92:95], v[64:67]\n"
      "s_nop 0\n"
      "v_permlane16_swap_b32 v10, v12\n"
      "v_mul_f32 v68, v52, v52\n"
      "v_add_f32 v10, v10, v12\n"
      "v_mov_b32 v11, v10\n"
      "v_mul_f32 v69, v53, v53\n"
      "v_mul_f32 v70, v54, v54\n"
      "v_permlane32_swap_b32 v10, v11\n"
      "v_mul_f32 v71, v55, v55\n"
      "v_mul_f32 v56, v52, v56\n"
      "v_mul_f32 v57, v53, v57\n"
      "v_mul_f32 v10, 0x3ae38e39, v10\n"
      "v_fmaak_f32 v11, v81, v11, 0x3727c5ac\n"
      "v_mul_f32 v58, v54, v58\n"
      "v_mul_f32 v59, v55, v59\n"
      "v_fma_f32 v11, -v10, v10, v11\n"
      "v_max_f32 v14, 0x3727c5ac, v11\n"
      "v_mul_f32 v72, v60, v60\n"
      "v_mul_f32 v73, v61, v61\n"
      "v_rsq_f32 v12, v14\n"
      "v_mul_f32 v74, v62, v62\n"
      "v_mul_f32 v75, v63, v63\n"
      "v_mul_f32 v15, 0xc0135761, v12\n"
      "v_mul_f32 v13, v12, v12\n"
      "v_mul_f32 v84, v60, v64\n"
      "v_mul_f32 v85, v61, v65\n"
      "v_mul_f32 v13, 0x3d372713, v13\n"
      "v_mul_f32 v16, v13, v15\n"
      "v_mul_f32 v86, v62, v66\n"
      "v_mul_f32 v87, v63, v67\n"
      "v_fma_f32 v68, v68, v16, v15\n"
      "v_fma_f32 v69, v69, v16, v15\n"
      "v_fma_f32 v70, v70, v16, v15\n"
      "v_fma_f32 v71, v71, v16, v15\n"
      "v_mul_f32 v68, v52, v68\n"
      "v_mul_f32 v69, v53, v69\n"
      "v_mul_f32 v70, v54, v70\n"
      "v_mul_f32 v71, v55, v71\n"
      "v_exp_f32 v68, v68\n"
      "v_exp_f32 v69, v69\n"
      "v_exp_f32 v70, v70\n"
      "v_exp_f32 v71, v71\n"
      "v_fma_f32 v68, v68, v14, v14\n"
      "v_fma_f32 v69, v69, v14, v14\n"
      "v_fma_f32 v70, v70, v14, v14\n"
      "v_fma_f32 v71, v71, v14, v14\n"
      "v_rcp_f32 v68, v68\n"
      "v_rcp_f32 v69, v69\n"
      "v_rcp_f32 v70, v70\n"
      "v_rcp_f32 v71, v71\n"
      "v_mul_f32 v56, v56, v68\n"
      "v_mul_f32 v57, v57, v69\n"
      "v_mul_f32 v58, v58, v70\n"
      "v_mul_f32 v59, v59, v71\n"
      "v_cvt_pk_bf16_f32 v76, v56, v57\n"
      "v_cvt_pk_bf16_f32 v77, v58, v59\n"
      "v_fma_f32 v72, v72, v16, v15\n"
      "v_fma_f32 v73, v73, v16, v15\n"
      "v_fma_f32 v74, v74, v16, v15\n"
      "v_fma_f32 v75, v75, v16, v15\n"
      "v_mul_f32 v72, v60, v72\n"
      "v_mul_f32 v73, v61, v73\n"
      "v_mul_f32 v74, v62, v74\n"
      "v_mul_f32 v75, v63, v75\n"
      "v_exp_f32 v72, v72\n"
      "v_exp_f32 v73, v73\n"
      "v_exp_f32 v74, v74\n"
      "v_exp_f32 v75, v75\n"
      "v_fma_f32 v72, v72, v14, v14\n"
      "v_fma_f32 v73, v73, v14, v14\n"
      "v_fma_f32 v74, v74, v14, v14\n"
      "v_fma_f32 v75, v75, v14, v14\n"
      "v_rcp_f32 v72, v72\n"
      "v_rcp_f32 v73, v73\n"
      "v_rcp_f32 v74, v74\n"
      "v_rcp_f32 v75, v75\n"
      "v_mul_f32 v84, v84, v72\n"
      "v_mul_f32 v85, v85, v73\n"
      "v_mul_f32 v86, v86, v74\n"
      "v_mul_f32 v87, v87, v75\n"
      "v_cvt_pk_bf16_f32 v78, v84, v85\n"
      "v_cvt_pk_bf16_f32 v79, v86, v87\n"
      "buffer_store_dwordx4 v[76:79], v2, s[36:39], s70 offen sc1\n"
      "s_waitcnt vmcnt(1)\n"
      "s_barrier\n"
      "buffer_store_dword v8, v7, s[36:39], 0 offen sc1\n"
      "v_mov_b32 v9, v8\n"
      "v_writelane_b32 v9, 64, 0\n"
      "s_waitcnt vmcnt(1)\n"
      "s_barrier\n"
      "buffer_store_dword v9, v7, s[36:39], 0 offen sc1\n"
      "s_branch .Lexit\n"
      ".Lwo:\n"
      "s_bfe_u32 s29, s48, 0x10001\n"
      "s_lshl_b32 s29, s29, 1\n"
      "s_lshr_b32 s50, s48, 2\n"
      "s_or_b32 s29, s29, s50\n"
      "s_sub_u32 s30, s49, 8\n"
      "s_and_b32 s31, s28, 1\n"
      "v_lshrrev_b32 v130, 4, v1\n"
      "v_and_b32 v131, 15, v1\n"
      "v_lshrrev_b32 v132, 3, v1\n"
      "v_and_b32 v133, 7, v1\n"
      "v_lshrrev_b32 v134, 2, v1\n"
      "v_and_b32 v134, 4, v134\n"
      "v_xor_b32 v133, v133, v134\n"
      "v_lshlrev_b32 v133, 4, v133\n"
      "v_mul_u32_u24 v140, 1152, v132\n"
      "v_add_u32 v140, v140, v133\n"
      "v_and_b32 v134, 3, v132\n"
      "v_lshrrev_b32 v135, 2, v132\n"
      "v_mul_u32_u24 v135, 36, v135\n"
      "v_add3_u32 v134, v134, v135, 16\n"
      "v_mul_u32_u24 v141, 1152, v134\n"
      "v_add_u32 v141, v141, v133\n"
      "v_bfe_u32 v132, v1, 1, 3\n"
      "v_and_b32 v133, 1, v1\n"
      "v_lshlrev_b32 v133, 4, v133\n"
      "v_lshrrev_b32 v134, 1, v130\n"
      "v_mul_u32_u24 v134, 20, v134\n"
      "v_and_b32 v135, 1, v130\n"
      "v_lshl_add_u32 v134, v135, 3, v134\n"
      "v_add_u32 v134, v134, v132\n"
      "v_mul_u32_u24 v142, 1152, v134\n"
      "v_add_u32 v142, v142, v133\n"
      "v_and_b32 v134, 3, v132\n"
      "v_lshrrev_b32 v135, 2, v132\n"
      "v_mul_u32_u24 v135, 36, v135\n"
      "v_add3_u32 v134, v134, v135, 16\n"
      "v_mul_u32_u24 v135, 72, v130\n"
      "v_add_u32 v134, v134, v135\n"
      "v_mul_u32_u24 v143, 1152, v134\n"
      "v_add_u32 v143, v143, v133\n"
      "v_bfe_u32 v132, v1, 2, 2\n"
      "v_lshl_add_u32 v133, v130, 3, v132\n"
      "v_and_b32 v134, 3, v1\n"
      "s_mul_i32 s74, s28, 40960\n"
      "v_lshlrev_b32 v149, 5, v133\n"
      "v_lshl_add_u32 v149, v134, 3, v149\n"
      "v_add_u32 v149, s74, v149\n"
      "v_lshlrev_b32 v133, 7, v133\n"
      "v_lshl_add_u32 v133, v134, 4, v133\n"
      "v_add_u32 v133, s74, v133\n"
      "v_lshrrev_b32 v132, 1, v132\n"
      "v_lshl_add_u32 v147, v132, 6, v133\n"
      "v_xor_b32 v148, 64, v147\n"
      "s_mul_i32 s79, s30, 576\n"
      "s_lshr_b32 s50, s28, 1\n"
      "s_lshl_b32 s50, s50, 5\n"
      "s_add_u32 s80, s79, s50\n"
      "s_add_u32 s80, s80, 512\n"
      "s_lshl_b32 s50, s28, 7\n"
      "s_add_u32 s79, s79, s50\n"
      "s_lshl_b32 s51, s29, 1\n"
      "s_add_u32 s51, s51, s30\n"
      "s_add_u32 s51, s51, 32\n"
      "s_lshl_b32 s51, s51, 2\n"
      "s_add_u32 s51, s51, s28\n"
      "s_lshl_b32 s51, s51, 7\n"
      "s_add_u32 s51, s51, 81911808\n"
      "v_lshlrev_b32 v6, 2, v1\n"
      "v_lshrrev_b32 v132, 5, v1\n"
      "v_lshl_or_b32 v6, v132, 30, v6\n"
      "v_add_u32 v6, s51, v6\n"
      "s_waitcnt lgkmcnt(0)\n"
      "s_mov_b32 s36, s20\n"
      "s_and_b32 s37, s21, 0xffff\n"
      "s_mov_b32 s38, 0x4eaf200\n"
      "s_mov_b32 s39, 0x20000\n"
      "s_mov_b32 s98, 82439168\n"
      "buffer_load_dwordx2 v[252:253], off, s[36:39], s98 sc1\n"
      "s_mov_b32 s40, s12\n"
      "s_and_b32 s41, s13, 0xffff\n"
      "s_mov_b32 s42, 0xa2000\n"
      "s_mov_b32 s43, 0x20000\n"
      "s_add_u32 m0, s74, 0\n"
      "s_add_u32 s50, s79, 0\n"
      "buffer_load_dwordx4 v140, s[40:43], s50 offen lds\n"
      "s_add_u32 m0, s74, 1024\n"
      "s_add_u32 s50, s79, 9216\n"
      "buffer_load_dwordx4 v140, s[40:43], s50 offen lds\n"
      "s_add_u32 m0, s74, 2048\n"
      "s_add_u32 s50, s79, 23040\n"
      "buffer_load_dwordx4 v140, s[40:43], s50 offen lds\n"
      "s_add_u32 m0, s74, 3072\n"
      "s_add_u32 s50, s79, 32256\n"
      "buffer_load_dwordx4 v140, s[40:43], s50 offen lds\n"
      "s_add_u32 m0, s74, 4096\n"
      "s_add_u32 s50, s80, 0\n"
      "buffer_load_dwordx4 v142, s[40:43], s50 offen lds\n"
      "s_add_u32 m0, s74, 5120\n"
      "s_add_u32 s50, s79, 41472\n"
      "buffer_load_dwordx4 v140, s[40:43], s50 offen lds\n"
      "s_add_u32 m0, s74, 6144\n"
      "s_add_u32 s50, s79, 50688\n"
      "buffer_load_dwordx4 v140, s[40:43], s50 offen lds\n"
      "s_add_u32 m0, s74, 7168\n"
      "s_add_u32 s50, s79, 64512\n"
      "buffer_load_dwordx4 v140, s[40:43], s50 offen lds\n"
      "s_add_u32 m0, s74, 8192\n"
      "s_add_u32 s50, s79, 73728\n"
      "buffer_load_dwordx4 v140, s[40:43], s50 offen lds\n"
      "s_add_u32 m0, s74, 9216\n"
      "s_add_u32 s50, s80, 41472\n"
      "buffer_load_dwordx4 v142, s[40:43], s50 offen lds\n"
      "s_add_u32 m0, s74, 10240\n"
      "s_add_u32 s50, s79, 82944\n"
      "buffer_load_dwordx4 v140, s[40:43], s50 offen lds\n"
      "s_add_u32 m0, s74, 11264\n"
      "s_add_u32 s50, s79, 92160\n"
      "buffer_load_dwordx4 v140, s[40:43], s50 offen lds\n"
      "s_add_u32 m0, s74, 12288\n"
      "s_add_u32 s50, s79, 105984\n"
      "buffer_load_dwordx4 v140, s[40:43], s50 offen lds\n"
      "s_add_u32 m0, s74, 13312\n"
      "s_add_u32 s50, s79, 115200\n"
      "buffer_load_dwordx4 v140, s[40:43], s50 offen lds\n"
      "s_add_u32 m0, s74, 14336\n"
      "s_add_u32 s50, s80, 82944\n"
      "buffer_load_dwordx4 v142, s[40:43], s50 offen lds\n"
      "s_add_u32 m0, s74, 15360\n"
      "s_add_u32 s50, s79, 124416\n"
      "buffer_load_dwordx4 v140, s[40:43], s50 offen lds\n"
      "s_add_u32 m0, s74, 16384\n"
      "s_add_u32 s50, s79, 133632\n"
      "buffer_load_dwordx4 v140, s[40:43], s50 offen lds\n"
      "s_add_u32 m0, s74, 17408\n"
      "s_add_u32 s50, s79, 147456\n"
      "buffer_load_dwordx4 v140, s[40:43], s50 offen lds\n"
      "s_add_u32 m0, s74, 18432\n"
      "s_add_u32 s50, s79, 156672\n"
      "buffer_load_dwordx4 v140, s[40:43], s50 offen lds\n"
      "s_add_u32 m0, s74, 19456\n"
      "s_add_u32 s50, s80, 124416\n"
      "buffer_load_dwordx4 v142, s[40:43], s50 offen lds\n"
      "s_add_u32 m0, s74, 20480\n"
      "s_add_u32 s50, s79, 165888\n"
      "buffer_load_dwordx4 v140, s[40:43], s50 offen lds\n"
      "s_add_u32 m0, s74, 21504\n"
      "s_add_u32 s50, s79, 175104\n"
      "buffer_load_dwordx4 v140, s[40:43], s50 offen lds\n"
      "s_add_u32 m0, s74, 22528\n"
      "s_add_u32 s50, s79, 188928\n"
      "buffer_load_dwordx4 v140, s[40:43], s50 offen lds\n"
      "s_add_u32 m0, s74, 23552\n"
      "s_add_u32 s50, s79, 198144\n"
      "buffer_load_dwordx4 v140, s[40:43], s50 offen lds\n"
      "s_add_u32 m0, s74, 24576\n"
      "s_add_u32 s50, s80, 165888\n"
      "buffer_load_dwordx4 v142, s[40:43], s50 offen lds\n"
      "s_add_u32 m0, s74, 25600\n"
      "s_add_u32 s50, s79, 207360\n"
      "buffer_load_dwordx4 v140, s[40:43], s50 offen lds\n"
      "s_add_u32 m0, s74, 26624\n"
      "s_add_u32 s50, s79, 216576\n"
      "buffer_load_dwordx4 v140, s[40:43], s50 offen lds\n"
      "s_add_u32 m0, s74, 27648\n"
      "s_add_u32 s50, s79, 230400\n"
      "buffer_load_dwordx4 v140, s[40:43], s50 offen lds\n"
      "s_add_u32 m0, s74, 28672\n"
      "s_add_u32 s50, s79, 239616\n"
      "buffer_load_dwordx4 v140, s[40:43], s50 offen lds\n"
      "s_add_u32 m0, s74, 29696\n"
      "s_add_u32 s50, s80, 207360\n"
      "buffer_load_dwordx4 v142, s[40:43], s50 offen lds\n"
      "s_add_u32 m0, s74, 30720\n"
      "s_add_u32 s50, s79, 248832\n"
      "buffer_load_dwordx4 v140, s[40:43], s50 offen lds\n"
      "s_add_u32 m0, s74, 31744\n"
      "s_add_u32 s50, s79, 258048\n"
      "buffer_load_dwordx4 v140, s[40:43], s50 offen lds\n"
      "s_add_u32 m0, s74, 32768\n"
      "s_add_u32 s50, s79, 271872\n"
      "buffer_load_dwordx4 v140, s[40:43], s50 offen lds\n"
      "s_add_u32 m0, s74, 33792\n"
      "s_add_u32 s50, s79, 281088\n"
      "buffer_load_dwordx4 v140, s[40:43], s50 offen lds\n"
      "s_add_u32 m0, s74, 34816\n"
      "s_add_u32 s50, s80, 248832\n"
      "buffer_load_dwordx4 v142, s[40:43], s50 offen lds\n"
      "s_add_u32 m0, s74, 35840\n"
      "s_add_u32 s50, s79, 290304\n"
      "buffer_load_dwordx4 v140, s[40:43], s50 offen lds\n"
      "s_add_u32 m0, s74, 36864\n"
      "s_add_u32 s50, s79, 299520\n"
      "buffer_load_dwordx4 v140, s[40:43], s50 offen lds\n"
      "s_add_u32 m0, s74, 37888\n"
      "s_add_u32 s50, s79, 313344\n"
      "buffer_load_dwordx4 v140, s[40:43], s50 offen lds\n"
      "s_add_u32 m0, s74, 38912\n"
      "s_add_u32 s50, s79, 322560\n"
      "buffer_load_dwordx4 v140, s[40:43], s50 offen lds\n"
      "s_add_u32 m0, s74, 39936\n"
      "s_add_u32 s50, s80, 290304\n"
      "buffer_load_dwordx4 v142, s[40:43], s50 offen lds\n"
      "s_waitcnt vmcnt(40)\n"
      "v_readfirstlane_b32 s98, v252\n"
      "v_readfirstlane_b32 s99, v253\n"
      "v_mov_b32 v7, 0\n"
      "s_add_u32 s98, s98, 1\n"
      "s_addc_u32 s99, s99, 0\n"
      "v_writelane_b32 v7, s98, 2\n"
      "v_writelane_b32 v7, s99, 3\n"
      "buffer_store_dword v7, v6, s[36:39], 0 offen sc1\n"
      "v_and_b32 v132, 4, v1\n"
      "v_lshlrev_b32 v132, 7, v132\n"
      "s_lshl_b32 s51, s29, 2\n"
      "s_add_u32 s51, s51, s28\n"
      "s_lshl_b32 s51, s51, 10\n"
      "s_add_u32 s51, s51, 81911808\n"
      "v_add_u32 v4, s51, v132\n"
      "s_lshl_b32 s51, s29, 5\n"
      "s_sub_u32 s51, 96, s51\n"
      "s_mul_i32 s53, s51, 1536\n"
      "s_add_u32 s53, s53, 82042880\n"
      "v_min_u32 v28, 11, v1\n"
      "v_lshlrev_b32 v28, 7, v28\n"
      "s_waitcnt vmcnt(36)\n"
      "ds_read_b64_tr_b16 a[0:1], v147 offset:0\n"
      "ds_read_b64_tr_b16 a[2:3], v147 offset:512\n"
      "ds_read_b64_tr_b16 a[4:5], v147 offset:8\n"
      "ds_read_b64_tr_b16 a[6:7], v147 offset:520\n"
      "ds_read_b64_tr_b16 a[8:9], v148 offset:0\n"
      "ds_read_b64_tr_b16 a[10:11], v148 offset:512\n"
      "ds_read_b64_tr_b16 a[12:13], v148 offset:8\n"
      "ds_read_b64_tr_b16 a[14:15], v148 offset:520\n"
      "ds_read_b64_tr_b16 a[16:17], v149 offset:4096\n"
      "ds_read_b64_tr_b16 a[18:19], v149 offset:4224\n"
      "s_waitcnt vmcnt(31)\n"
      "ds_read_b64_tr_b16 a[20:21], v147 offset:5120\n"
      "ds_read_b64_tr_b16 a[22:23], v147 offset:5632\n"
      "ds_read_b64_tr_b16 a[24:25], v147 offset:5128\n"
      "ds_read_b64_tr_b16 a[26:27], v147 offset:5640\n"
      "ds_read_b64_tr_b16 a[28:29], v148 offset:5120\n"
      "ds_read_b64_tr_b16 a[30:31], v148 offset:5632\n"
      "ds_read_b64_tr_b16 a[32:33], v148 offset:5128\n"
      "ds_read_b64_tr_b16 a[34:35], v148 offset:5640\n"
      "ds_read_b64_tr_b16 a[36:37], v149 offset:9216\n"
      "ds_read_b64_tr_b16 a[38:39], v149 offset:9344\n"
      "s_waitcnt lgkmcnt(10)\n"
      "s_add_u32 m0, s74, 0\n"
      "s_add_u32 s50, s79, 331776\n"
      "buffer_load_dwordx4 v140, s[40:43], s50 offen lds\n"
      "s_add_u32 m0, s74, 1024\n"
      "s_add_u32 s50, s79, 340992\n"
      "buffer_load_dwordx4 v140, s[40:43], s50 offen lds\n"
      "s_add_u32 m0, s74, 2048\n"
      "s_add_u32 s50, s79, 354816\n"
      "buffer_load_dwordx4 v140, s[40:43], s50 offen lds\n"
      "s_add_u32 m0, s74, 3072\n"
      "s_add_u32 s50, s79, 364032\n"
      "buffer_load_dwordx4 v140, s[40:43], s50 offen lds\n"
      "s_add_u32 m0, s74, 4096\n"
      "s_add_u32 s50, s80, 331776\n"
      "buffer_load_dwordx4 v142, s[40:43], s50 offen lds\n"
      "s_waitcnt vmcnt(31)\n"
      "ds_read_b64_tr_b16 a[40:41], v147 offset:10240\n"
      "ds_read_b64_tr_b16 a[42:43], v147 offset:10752\n"
      "ds_read_b64_tr_b16 a[44:45], v147 offset:10248\n"
      "ds_read_b64_tr_b16 a[46:47], v147 offset:10760\n"
      "ds_read_b64_tr_b16 a[48:49], v148 offset:10240\n"
      "ds_read_b64_tr_b16 a[50:51], v148 offset:10752\n"
      "ds_read_b64_tr_b16 a[52:53], v148 offset:10248\n"
      "ds_read_b64_tr_b16 a[54:55], v148 offset:10760\n"
      "ds_read_b64_tr_b16 a[56:57], v149 offset:14336\n"
      "ds_read_b64_tr_b16 a[58:59], v149 offset:14464\n"
      "s_waitcnt lgkmcnt(10)\n"
      "s_add_u32 m0, s74, 5120\n"
      "s_add_u32 s50, s79, 373248\n"
      "buffer_load_dwordx4 v140, s[40:43], s50 offen lds\n"
      "s_add_u32 m0, s74, 6144\n"
      "s_add_u32 s50, s79, 382464\n"
      "buffer_load_dwordx4 v140, s[40:43], s50 offen lds\n"
      "s_add_u32 m0, s74, 7168\n"
      "s_add_u32 s50, s79, 396288\n"
      "buffer_load_dwordx4 v140, s[40:43], s50 offen lds\n"
      "s_add_u32 m0, s74, 8192\n"
      "s_add_u32 s50, s79, 405504\n"
      "buffer_load_dwordx4 v140, s[40:43], s50 offen lds\n"
      "s_add_u32 m0, s74, 9216\n"
      "s_add_u32 s50, s80, 373248\n"
      "buffer_load_dwordx4 v142, s[40:43], s50 offen lds\n"
      "s_waitcnt vmcnt(31)\n"
      "ds_read_b64_tr_b16 a[60:61], v147 offset:15360\n"
      "ds_read_b64_tr_b16 a[62:63], v147 offset:15872\n"
      "ds_read_b64_tr_b16 a[64:65], v147 offset:15368\n"
      "ds_read_b64_tr_b16 a[66:67], v147 offset:15880\n"
      "ds_read_b64_tr_b16 a[68:69], v148 offset:15360\n"
      "ds_read_b64_tr_b16 a[70:71], v148 offset:15872\n"
      "ds_read_b64_tr_b16 a[72:73], v148 offset:15368\n"
      "ds_read_b64_tr_b16 a[74:75], v148 offset:15880\n"
      "ds_read_b64_tr_b16 a[76:77], v149 offset:19456\n"
      "ds_read_b64_tr_b16 a[78:79], v149 offset:19584\n"
      "s_waitcnt lgkmcnt(10)\n"
      "s_add_u32 m0, s74, 10240\n"
      "s_add_u32 s50, s79, 414720\n"
      "buffer_load_dwordx4 v140, s[40:43], s50 offen lds\n"
      "s_add_u32 m0, s74, 11264\n"
      "s_add_u32 s50, s79, 423936\n"
      "buffer_load_dwordx4 v140, s[40:43], s50 offen lds\n"
      "s_add_u32 m0, s74, 12288\n"
      "s_add_u32 s50, s79, 437760\n"
      "buffer_load_dwordx4 v140, s[40:43], s50 offen lds\n"
      "s_add_u32 m0, s74, 13312\n"
      "s_add_u32 s50, s79, 446976\n"
      "buffer_load_dwordx4 v140, s[40:43], s50 offen lds\n"
      "s_add_u32 m0, s74, 14336\n"
      "s_add_u32 s50, s80, 414720\n"
      "buffer_load_dwordx4 v142, s[40:43], s50 offen lds\n"
      "s_waitcnt vmcnt(31)\n"
      "ds_read_b64_tr_b16 a[80:81], v147 offset:20480\n"
      "ds_read_b64_tr_b16 a[82:83], v147 offset:20992\n"
      "ds_read_b64_tr_b16 a[84:85], v147 offset:20488\n"
      "ds_read_b64_tr_b16 a[86:87], v147 offset:21000\n"
      "ds_read_b64_tr_b16 a[88:89], v148 offset:20480\n"
      "ds_read_b64_tr_b16 a[90:91], v148 offset:20992\n"
      "ds_read_b64_tr_b16 a[92:93], v148 offset:20488\n"
      "ds_read_b64_tr_b16 a[94:95], v148 offset:21000\n"
      "ds_read_b64_tr_b16 a[96:97], v149 offset:24576\n"
      "ds_read_b64_tr_b16 a[98:99], v149 offset:24704\n"
      "s_waitcnt lgkmcnt(10)\n"
      "s_add_u32 m0, s74, 15360\n"
      "s_add_u32 s50, s79, 456192\n"
      "buffer_load_dwordx4 v140, s[40:43], s50 offen lds\n"
      "s_add_u32 m0, s74, 16384\n"
      "s_add_u32 s50, s79, 465408\n"
      "buffer_load_dwordx4 v140, s[40:43], s50 offen lds\n"
      "s_add_u32 m0, s74, 17408\n"
      "s_add_u32 s50, s79, 479232\n"
      "buffer_load_dwordx4 v140, s[40:43], s50 offen lds\n"
      "s_add_u32 m0, s74, 18432\n"
      "s_add_u32 s50, s79, 488448\n"
      "buffer_load_dwordx4 v140, s[40:43], s50 offen lds\n"
      "s_add_u32 m0, s74, 19456\n"
      "s_add_u32 s50, s80, 456192\n"
      "buffer_load_dwordx4 v142, s[40:43], s50 offen lds\n"
      "s_waitcnt vmcnt(31)\n"
      "ds_read_b64_tr_b16 a[100:101], v147 offset:25600\n"
      "ds_read_b64_tr_b16 a[102:103], v147 offset:26112\n"
      "ds_read_b64_tr_b16 a[104:105], v147 offset:25608\n"
      "ds_read_b64_tr_b16 a[106:107], v147 offset:26120\n"
      "ds_read_b64_tr_b16 a[108:109], v148 offset:25600\n"
      "ds_read_b64_tr_b16 a[110:111], v148 offset:26112\n"
      "ds_read_b64_tr_b16 a[112:113], v148 offset:25608\n"
      "ds_read_b64_tr_b16 a[114:115], v148 offset:26120\n"
      "ds_read_b64_tr_b16 a[116:117], v149 offset:29696\n"
      "ds_read_b64_tr_b16 a[118:119], v149 offset:29824\n"
      "s_waitcnt lgkmcnt(10)\n"
      "s_add_u32 m0, s74, 20480\n"
      "s_add_u32 s50, s79, 497664\n"
      "buffer_load_dwordx4 v140, s[40:43], s50 offen lds\n"
      "s_add_u32 m0, s74, 21504\n"
      "s_add_u32 s50, s79, 506880\n"
      "buffer_load_dwordx4 v140, s[40:43], s50 offen lds\n"
      "s_add_u32 m0, s74, 22528\n"
      "s_add_u32 s50, s79, 520704\n"
      "buffer_load_dwordx4 v140, s[40:43], s50 offen lds\n"
      "s_add_u32 m0, s74, 23552\n"
      "s_add_u32 s50, s79, 529920\n"
      "buffer_load_dwordx4 v140, s[40:43], s50 offen lds\n"
      "s_add_u32 m0, s74, 24576\n"
      "s_add_u32 s50, s80, 497664\n"
      "buffer_load_dwordx4 v142, s[40:43], s50 offen lds\n"
      "s_waitcnt vmcnt(31)\n"
      "ds_read_b64_tr_b16 a[120:121], v147 offset:30720\n"
      "ds_read_b64_tr_b16 a[122:123], v147 offset:31232\n"
      "ds_read_b64_tr_b16 a[124:125], v147 offset:30728\n"
      "ds_read_b64_tr_b16 a[126:127], v147 offset:31240\n"
      "ds_read_b64_tr_b16 a[128:129], v148 offset:30720\n"
      "ds_read_b64_tr_b16 a[130:131], v148 offset:31232\n"
      "ds_read_b64_tr_b16 a[132:133], v148 offset:30728\n"
      "ds_read_b64_tr_b16 a[134:135], v148 offset:31240\n"
      "ds_read_b64_tr_b16 a[136:137], v149 offset:34816\n"
      "ds_read_b64_tr_b16 a[138:139], v149 offset:34944\n"
      "s_waitcnt lgkmcnt(10)\n"
      "s_add_u32 m0, s74, 25600\n"
      "s_add_u32 s50, s79, 539136\n"
      "buffer_load_dwordx4 v140, s[40:43], s50 offen lds\n"
      "s_add_u32 m0, s74, 26624\n"
      "s_add_u32 s50, s79, 548352\n"
      "buffer_load_dwordx4 v140, s[40:43], s50 offen lds\n"
      "s_add_u32 m0, s74, 27648\n"
      "s_add_u32 s50, s79, 562176\n"
      "buffer_load_dwordx4 v140, s[40:43], s50 offen lds\n"
      "s_add_u32 m0, s74, 28672\n"
      "s_add_u32 s50, s79, 571392\n"
      "buffer_load_dwordx4 v140, s[40:43], s50 offen lds\n"
      "s_add_u32 m0, s74, 29696\n"
      "s_add_u32 s50, s80, 539136\n"
      "buffer_load_dwordx4 v142, s[40:43], s50 offen lds\n"
      "s_waitcnt vmcnt(31)\n"
      "ds_read_b64_tr_b16 a[140:141], v147 offset:35840\n"
      "ds_read_b64_tr_b16 a[142:143], v147 offset:36352\n"
      "ds_read_b64_tr_b16 a[144:145], v147 offset:35848\n"
      "ds_read_b64_tr_b16 a[146:147], v147 offset:36360\n"
      "ds_read_b64_tr_b16 a[148:149], v148 offset:35840\n"
      "ds_read_b64_tr_b16 a[150:151], v148 offset:36352\n"
      "ds_read_b64_tr_b16 a[152:153], v148 offset:35848\n"
      "ds_read_b64_tr_b16 a[154:155], v148 offset:36360\n"
      "ds_read_b64_tr_b16 a[156:157], v149 offset:39936\n"
      "ds_read_b64_tr_b16 a[158:159], v149 offset:40064\n"
      "s_waitcnt lgkmcnt(10)\n"
      "s_add_u32 m0, s74, 30720\n"
      "s_add_u32 s50, s79, 580608\n"
      "buffer_load_dwordx4 v140, s[40:43], s50 offen lds\n"
      "s_add_u32 m0, s74, 31744\n"
      "s_add_u32 s50, s79, 589824\n"
      "buffer_load_dwordx4 v140, s[40:43], s50 offen lds\n"
      "s_add_u32 m0, s74, 32768\n"
      "s_add_u32 s50, s79, 603648\n"
      "buffer_load_dwordx4 v140, s[40:43], s50 offen lds\n"
      "s_add_u32 m0, s74, 33792\n"
      "s_add_u32 s50, s79, 612864\n"
      "buffer_load_dwordx4 v140, s[40:43], s50 offen lds\n"
      "s_add_u32 m0, s74, 34816\n"
      "s_add_u32 s50, s80, 580608\n"
      "buffer_load_dwordx4 v142, s[40:43], s50 offen lds\n"
      "s_waitcnt vmcnt(30)\n"
      "ds_read_b64_tr_b16 a[160:161], v147 offset:0\n"
      "ds_read_b64_tr_b16 a[162:163], v147 offset:512\n"
      "ds_read_b64_tr_b16 a[164:165], v147 offset:8\n"
      "ds_read_b64_tr_b16 a[166:167], v147 offset:520\n"
      "ds_read_b64_tr_b16 a[168:169], v148 offset:0\n"
      "ds_read_b64_tr_b16 a[170:171], v148 offset:512\n"
      "ds_read_b64_tr_b16 a[172:173], v148 offset:8\n"
      "ds_read_b64_tr_b16 a[174:175], v148 offset:520\n"
      "ds_read_b64_tr_b16 a[176:177], v149 offset:4096\n"
      "ds_read_b64_tr_b16 a[178:179], v149 offset:4224\n"
      "s_waitcnt lgkmcnt(10)\n"
      "s_add_u32 m0, s74, 35840\n"
      "s_add_u32 s50, s79, 622080\n"
      "buffer_load_dwordx4 v140, s[40:43], s50 offen lds\n"
      "s_add_u32 m0, s74, 36864\n"
      "s_add_u32 s50, s79, 631296\n"
      "buffer_load_dwordx4 v140, s[40:43], s50 offen lds\n"
      "s_add_u32 m0, s74, 37888\n"
      "s_add_u32 s50, s79, 645120\n"
      "buffer_load_dwordx4 v140, s[40:43], s50 offen lds\n"
      "s_add_u32 m0, s74, 38912\n"
      "s_add_u32 s50, s79, 654336\n"
      "buffer_load_dwordx4 v140, s[40:43], s50 offen lds\n"
      "s_add_u32 m0, s74, 39936\n"
      "s_add_u32 s50, s80, 622080\n"
      "buffer_load_dwordx4 v142, s[40:43], s50 offen lds\n"
      "s_waitcnt vmcnt(30)\n"
      "ds_read_b64_tr_b16 a[180:181], v147 offset:5120\n"
      "ds_read_b64_tr_b16 a[182:183], v147 offset:5632\n"
      "ds_read_b64_tr_b16 a[184:185], v147 offset:5128\n"
      "ds_read_b64_tr_b16 a[186:187], v147 offset:5640\n"
      "ds_read_b64_tr_b16 a[188:189], v148 offset:5120\n"
      "ds_read_b64_tr_b16 a[190:191], v148 offset:5632\n"
      "ds_read_b64_tr_b16 a[192:193], v148 offset:5128\n"
      "ds_read_b64_tr_b16 a[194:195], v148 offset:5640\n"
      "ds_read_b64_tr_b16 a[196:197], v149 offset:9216\n"
      "ds_read_b64_tr_b16 a[198:199], v149 offset:9344\n"
      "s_waitcnt lgkmcnt(10)\n"
      "s_add_u32 m0, s74, 0\n"
      "s_add_u32 s50, s79, 0\n"
      "buffer_load_dwordx4 v141, s[40:43], s50 offen lds\n"
      "s_add_u32 m0, s74, 1024\n"
      "s_add_u32 s50, s79, 82944\n"
      "buffer_load_dwordx4 v141, s[40:43], s50 offen lds\n"
      "s_add_u32 m0, s74, 2048\n"
      "s_add_u32 s50, s79, 165888\n"
      "buffer_load_dwordx4 v141, s[40:43], s50 offen lds\n"
      "s_add_u32 m0, s74, 3072\n"
      "s_add_u32 s50, s79, 248832\n"
      "buffer_load_dwordx4 v141, s[40:43], s50 offen lds\n"
      "s_add_u32 m0, s74, 4096\n"
      "s_add_u32 s50, s80, 0\n"
      "buffer_load_dwordx4 v143, s[40:43], s50 offen lds\n"
      "s_waitcnt vmcnt(30)\n"
      "ds_read_b64_tr_b16 a[200:201], v147 offset:10240\n"
      "ds_read_b64_tr_b16 a[202:203], v147 offset:10752\n"
      "ds_read_b64_tr_b16 a[204:205], v147 offset:10248\n"
      "ds_read_b64_tr_b16 a[206:207], v147 offset:10760\n"
      "ds_read_b64_tr_b16 a[208:209], v148 offset:10240\n"
      "ds_read_b64_tr_b16 a[210:211], v148 offset:10752\n"
      "ds_read_b64_tr_b16 a[212:213], v148 offset:10248\n"
      "ds_read_b64_tr_b16 a[214:215], v148 offset:10760\n"
      "ds_read_b64_tr_b16 a[216:217], v149 offset:14336\n"
      "ds_read_b64_tr_b16 a[218:219], v149 offset:14464\n"
      "s_waitcnt lgkmcnt(10)\n"
      "s_add_u32 m0, s74, 5120\n"
      "s_add_u32 s50, s79, 331776\n"
      "buffer_load_dwordx4 v141, s[40:43], s50 offen lds\n"
      "s_add_u32 m0, s74, 6144\n"
      "s_add_u32 s50, s79, 414720\n"
      "buffer_load_dwordx4 v141, s[40:43], s50 offen lds\n"
      "s_add_u32 m0, s74, 7168\n"
      "s_add_u32 s50, s79, 497664\n"
      "buffer_load_dwordx4 v141, s[40:43], s50 offen lds\n"
      "s_add_u32 m0, s74, 8192\n"
      "s_add_u32 s50, s79, 580608\n"
      "buffer_load_dwordx4 v141, s[40:43], s50 offen lds\n"
      "s_add_u32 m0, s74, 9216\n"
      "s_add_u32 s50, s80, 331776\n"
      "buffer_load_dwordx4 v143, s[40:43], s50 offen lds\n"
      "s_waitcnt vmcnt(30)\n"
      "ds_read_b64_tr_b16 a[220:221], v147 offset:15360\n"
      "ds_read_b64_tr_b16 a[222:223], v147 offset:15872\n"
      "ds_read_b64_tr_b16 a[224:225], v147 offset:15368\n"
      "ds_read_b64_tr_b16 a[226:227], v147 offset:15880\n"
      "ds_read_b64_tr_b16 a[228:229], v148 offset:15360\n"
      "ds_read_b64_tr_b16 a[230:231], v148 offset:15872\n"
      "ds_read_b64_tr_b16 a[232:233], v148 offset:15368\n"
      "ds_read_b64_tr_b16 a[234:235], v148 offset:15880\n"
      "ds_read_b64_tr_b16 a[236:237], v149 offset:19456\n"
      "ds_read_b64_tr_b16 a[238:239], v149 offset:19584\n"
      "buffer_load_dwordx2 v[20:21], v28, s[36:39], s53 offen offset:8 sc1\n"
      "buffer_load_dwordx2 v[24:25], v4, s[36:39], 0 offen offset:8 sc1\n"
      "s_waitcnt vmcnt(27)\n"
      "ds_read_b64_tr_b16 a[240:241], v147 offset:20480\n"
      "ds_read_b64_tr_b16 a[242:243], v147 offset:20992\n"
      "ds_read_b64_tr_b16 a[244:245], v147 offset:20488\n"
      "ds_read_b64_tr_b16 a[246:247], v147 offset:21000\n"
      "ds_read_b64_tr_b16 a[248:249], v148 offset:20480\n"
      "ds_read_b64_tr_b16 a[250:251], v148 offset:20992\n"
      "ds_read_b64_tr_b16 a[252:253], v148 offset:20488\n"
      "ds_read_b64_tr_b16 a[254:255], v148 offset:21000\n"
      "ds_read_b64_tr_b16 v[150:151], v149 offset:24576\n"
      "ds_read_b64_tr_b16 v[152:153], v149 offset:24704\n"
      "s_waitcnt vmcnt(22)\n"
      "ds_read_b64_tr_b16 v[154:155], v147 offset:25600\n"
      "ds_read_b64_tr_b16 v[156:157], v147 offset:26112\n"
      "ds_read_b64_tr_b16 v[158:159], v147 offset:25608\n"
      "ds_read_b64_tr_b16 v[160:161], v147 offset:26120\n"
      "ds_read_b64_tr_b16 v[162:163], v148 offset:25600\n"
      "ds_read_b64_tr_b16 v[164:165], v148 offset:26112\n"
      "ds_read_b64_tr_b16 v[166:167], v148 offset:25608\n"
      "ds_read_b64_tr_b16 v[168:169], v148 offset:26120\n"
      "ds_read_b64_tr_b16 v[170:171], v149 offset:29696\n"
      "ds_read_b64_tr_b16 v[172:173], v149 offset:29824\n"
      "s_waitcnt vmcnt(17)\n"
      "ds_read_b64_tr_b16 v[174:175], v147 offset:30720\n"
      "ds_read_b64_tr_b16 v[176:177], v147 offset:31232\n"
      "ds_read_b64_tr_b16 v[178:179], v147 offset:30728\n"
      "ds_read_b64_tr_b16 v[180:181], v147 offset:31240\n"
      "ds_read_b64_tr_b16 v[182:183], v148 offset:30720\n"
      "ds_read_b64_tr_b16 v[184:185], v148 offset:31232\n"
      "ds_read_b64_tr_b16 v[186:187], v148 offset:30728\n"
      "ds_read_b64_tr_b16 v[188:189], v148 offset:31240\n"
      "ds_read_b64_tr_b16 v[190:191], v149 offset:34816\n"
      "ds_read_b64_tr_b16 v[192:193], v149 offset:34944\n"
      "s_waitcnt vmcnt(12)\n"
      "ds_read_b64_tr_b16 v[194:195], v147 offset:35840\n"
      "ds_read_b64_tr_b16 v[196:197], v147 offset:36352\n"
      "ds_read_b64_tr_b16 v[198:199], v147 offset:35848\n"
      "ds_read_b64_tr_b16 v[200:201], v147 offset:36360\n"
      "ds_read_b64_tr_b16 v[202:203], v148 offset:35840\n"
      "ds_read_b64_tr_b16 v[204:205], v148 offset:36352\n"
      "ds_read_b64_tr_b16 v[206:207], v148 offset:35848\n"
      "ds_read_b64_tr_b16 v[208:209], v148 offset:36360\n"
      "ds_read_b64_tr_b16 v[210:211], v149 offset:39936\n"
      "ds_read_b64_tr_b16 v[212:213], v149 offset:40064\n"
      "s_waitcnt vmcnt(7)\n"
      "ds_read_b64_tr_b16 v[214:215], v147 offset:0\n"
      "ds_read_b64_tr_b16 v[216:217], v147 offset:512\n"
      "ds_read_b64_tr_b16 v[218:219], v147 offset:8\n"
      "ds_read_b64_tr_b16 v[220:221], v147 offset:520\n"
      "ds_read_b64_tr_b16 v[222:223], v148 offset:0\n"
      "ds_read_b64_tr_b16 v[224:225], v148 offset:512\n"
      "ds_read_b64_tr_b16 v[226:227], v148 offset:8\n"
      "ds_read_b64_tr_b16 v[228:229], v148 offset:520\n"
      "ds_read_b64_tr_b16 v[230:231], v149 offset:4096\n"
      "ds_read_b64_tr_b16 v[232:233], v149 offset:4224\n"
      "s_waitcnt vmcnt(2)\n"
      "ds_read_b64_tr_b16 v[234:235], v147 offset:5120\n"
      "ds_read_b64_tr_b16 v[236:237], v147 offset:5632\n"
      "ds_read_b64_tr_b16 v[238:239], v147 offset:5128\n"
      "ds_read_b64_tr_b16 v[240:241], v147 offset:5640\n"
      "ds_read_b64_tr_b16 v[242:243], v148 offset:5120\n"
      "ds_read_b64_tr_b16 v[244:245], v148 offset:5632\n"
      "ds_read_b64_tr_b16 v[246:247], v148 offset:5128\n"
      "ds_read_b64_tr_b16 v[248:249], v148 offset:5640\n"
      "ds_read_b64_tr_b16 v[250:251], v149 offset:9216\n"
      "ds_read_b64_tr_b16 v[252:253], v149 offset:9344\n"
      "s_mov_b32 s66, -5\n"
      "s_mov_b32 s76, 0\n"
      "v_mov_b32 v134, 0x40000000\n"
      "s_mul_i32 s68, s29, 1310720\n"
      "s_lshl_b32 s50, s28, 12\n"
      "s_add_u32 s68, s68, s50\n"
      "s_add_u32 s68, s68, 5222400\n"
      "s_mov_b32 s67, 0\n"
      "s_mov_b32 s72, 40960\n"
      "s_mov_b32 s74, s50\n"
      "s_lshl_b32 s75, s28, 9\n"
      "s_add_u32 s75, s75, 16384\n"
      "v_and_b32 v132, 3, v1\n"
      "v_lshlrev_b32 v132, 7, v132\n"
      "v_and_b32 v133, 60, v1\n"
      "v_add_u32 v132, v132, v133\n"
      "s_mul_i32 s51, s28, 3072\n"
      "s_sub_u32 s51, 16384, s51\n"
      "v_add_u32 v8, s51, v132\n"
      "v_add_u32 v9, 256, v8\n"
      "s_lshl_b32 s51, s79, 4\n"
      "v_add_u32 v10, s51, v2\n"
      "v_lshrrev_b32 v132, 1, v130\n"
      "v_lshl_add_u32 v132, v132, 4, v131\n"
      "v_lshlrev_b32 v132, 4, v132\n"
      "v_and_b32 v133, 1, v130\n"
      "v_lshl_add_u32 v132, v133, 3, v132\n"
      "s_lshl_b32 s51, s80, 4\n"
      "v_add_u32 v11, s51, v132\n"
      "s_mul_i32 s70, s29, 1331200\n"
      "s_add_u32 s70, s70, 10424320\n"
      "s_lshl_b32 s51, s29, 5\n"
      "s_sub_u32 s51, 96, s51\n"
      "s_mul_i32 s78, s51, 20480\n"
      "v_mov_b32 v3, v2\n"
      "v_lshrrev_b32 v132, 5, v1\n"
      "v_lshlrev_b32 v132, 2, v132\n"
      "v_lshl_add_u32 v132, v131, 3, v132\n"
      "s_lshl_b32 s51, s30, 2\n"
      "s_add_u32 s51, s51, s28\n"
      "s_lshl_b32 s51, s51, 7\n"
      "s_add_u32 s51, s51, 38912\n"
      "v_add_u32 v12, s51, v132\n"
      "v_mov_b32 v5, 0\n"
      "s_mov_b32 s77, 0\n"
      "s_mov_b32 s79, 20480\n"
      "s_add_u32 s80, s78, 4294926336\n"
      "s_mov_b32 s78, 0x40000000\n"
      "s_waitcnt vmcnt(0)\n"
      "v_cmp_ne_u64 vcc, s[98:99], v[20:21]\n"
      "s_cbranch_vccz .Lbk20\n"
      "s_mov_b32 s56, 0x20000\n"
      ".Lsp20:\n"
      "buffer_load_dwordx2 v[20:21], v28, s[36:39], s53 offen offset:8 sc1\n"
      "s_waitcnt vmcnt(0)\n"
      "v_cmp_ne_u64 vcc, s[98:99], v[20:21]\n"
      "s_cbranch_vccz .Lbk20\n"
      "s_sleep 4\n"
      "s_sub_u32 s56, s56, 1\n"
      "s_cbranch_scc0 .Lsp20\n"
      ".Lbk20:\n"
      "v_cmp_ne_u64 vcc, s[98:99], v[24:25]\n"
      "s_cbranch_vccz .Lbk21\n"
      "s_mov_b32 s56, 0x20000\n"
      ".Lsp21:\n"
      "buffer_load_dwordx2 v[20:21], v4, s[36:39], 0 offen offset:8 sc1\n"
      "s_waitcnt vmcnt(0)\n"
      "v_cmp_ne_u64 vcc, s[98:99], v[20:21]\n"
      "s_cbranch_vccz .Lbk21\n"
      "s_sleep 4\n"
      "s_sub_u32 s56, s56, 1\n"
      "s_cbranch_scc0 .Lsp21\n"
      ".Lbk21:\n"
      "s_cmp_eq_u32 s28, 0\n"
      "s_cselect_b32 s50, 0, 0x40000000\n"
      "v_or_b32 v6, s50, v6\n"
      "s_waitcnt vmcnt(0) lgkmcnt(0)\n"
      "s_barrier\n"
      "s_cmp_eq_u32 s31, 1\n"
      "s_cbranch_scc1 .Lwo_e0\n"
      ".Lwo_loop0:\n"
      "v_mfma_f32_16x16x32_bf16 v[44:47], v[182:185], v[28:31], v[44:47]\n"
      "ds_read_b128 v[20:23], v3 offset:0\n"
      "v_mfma_f32_16x16x32_bf16 v[44:47], v[202:205], v[32:35], v[44:47]\n"
      "buffer_store_dword v7, v6, s[36:39], 0 offen sc1\n"
      "v_mfma_f32_16x16x32_bf16 v[48:51], v[186:189], v[28:31], v[48:51]\n"
      "s_cmp_lt_u32 s66, 64\n"
      "v_mfma_f32_16x16x32_bf16 v[48:51], v[206:209], v[32:35], v[48:51]\n"
      "s_waitcnt lgkmcnt(2)\n"
      "v_mfma_f32_16x16x32_bf16 v[36:39], v[214:217], v[14:17], v[36:39]\n"
      "s_waitcnt lgkmcnt(1)\n"
      "ds_read_b128 v[24:27], v3 offset:1024\n"
      "v_mfma_f32_16x16x32_bf16 v[36:39], v[234:237], v[72:75], v[36:39]\n"
      "s_cselect_b32 s71, s70, 0x40000000\n"
      "v_mfma_f32_16x16x32_bf16 v[40:43], v[218:221], v[14:17], v[40:43]\n"
      "s_add_u32 s70, s70, 20480\n"
      "v_mfma_f32_16x16x32_bf16 v[40:43], v[238:241], v[72:75], v[40:43]\n"
      "s_add_u32 s66, s66, 1\n"
      "v_mfma_f32_16x16x32_bf16 v[44:47], v[222:225], v[14:17], v[44:47]\n"
      "s_max_i32 s73, s66, 0\n"
      "ds_read_b128 v[28:31], v3 offset:2048\n"
      "v_mfma_f32_16x16x32_bf16 v[44:47], v[242:245], v[72:75], v[44:47]\n"
      "s_add_u32 s68, s68, 20480\n"
      "v_mfma_f32_16x16x32_bf16 v[48:51], v[226:229], v[14:17], v[48:51]\n"
      "s_add_u32 s48, s66, 2\n"
      "v_cvt_pk_bf16_f32 v106, v36, v37\n"
      "v_cvt_pk_bf16_f32 v107, v38, v39\n"
      "v_mfma_f32_16x16x32_bf16 v[48:51], v[246:249], v[72:75], v[48:51]\n"
      "s_waitcnt lgkmcnt(2)\n"
      "v_mul_f32 v122, v36, v36\n"
      "v_mul_f32 v123, v37, v37\n"
      "v_mfma_f32_16x16x32_bf16 v[52:55], a[0:3], v[20:23], v[76:79]\n"
      "s_waitcnt lgkmcnt(1)\n"
      "ds_read_b128 v[32:35], v3 offset:3072\n"
      "v_cvt_pk_bf16_f32 v108, v40, v41\n"
      "v_mfma_f32_16x16x32_bf16 v[52:55], a[20:23], v[24:27], v[52:55]\n"
      "s_cmp_lt_u32 s48, 64\n"
      "v_cvt_pk_bf16_f32 v109, v42, v43\n"
      "v_add_f32 v120, v36, v38\n"
      "v_mfma_f32_16x16x32_bf16 v[56:59], a[4:7], v[20:23], v[80:83]\n"
      "s_cselect_b32 s69, s68, 0x40000000\n"
      "v_cvt_pk_bf16_f32 v110, v44, v45\n"
      "v_cvt_pk_bf16_f32 v111, v46, v47\n"
      "v_mfma_f32_16x16x32_bf16 v[56:59], a[24:27], v[24:27], v[56:59]\n"
      "s_cselect_b32 s78, s80, 0x40000000\n"
      "v_fmac_f32 v122, v38, v38\n"
      "v_add_f32 v121, v37, v39\n"
      "v_mfma_f32_16x16x32_bf16 v[60:63], a[8:11], v[20:23], v[84:87]\n"
      "s_add_u32 m0, s74, s72\n"
      "buffer_store_dwordx4 v[106:109], v10, s[36:39], s71 offen sc1\n"
      "v_mfma_f32_16x16x32_bf16 v[60:63], a[28:31], v[24:27], v[60:63]\n"
      "ds_read_b128 v[14:17], v3 offset:4096\n"
      "v_cvt_pk_bf16_f32 v112, v48, v49\n"
      "v_mfma_f32_16x16x32_bf16 v[64:67], a[12:15], v[20:23], v[88:91]\n"
      "v_cvt_pk_bf16_f32 v113, v50, v51\n"
      "v_writelane_b32 v7, s73, 0\n"
      "v_mfma_f32_16x16x32_bf16 v[64:67], a[32:35], v[24:27], v[64:67]\n"
      "v_fmac_f32 v123, v39, v39\n"
      "v_add_f32 v120, v120, v40\n"
      "v_mfma_f32_16x16x32_bf16 v[68:71], a[16:19], v[20:23], v[92:95]\n"
      "v_fmac_f32 v122, v40, v40\n"
      "v_add_f32 v121, v121, v41\n"
      "v_mfma_f32_16x16x32_bf16 v[68:71], a[36:39], v[24:27], v[68:71]\n"
      "s_waitcnt lgkmcnt(2)\n"
      "v_fmac_f32 v123, v41, v41\n"
      "v_add_f32 v120, v120, v42\n"
      "v_mfma_f32_16x16x32_bf16 v[52:55], a[40:43], v[28:31], v[52:55]\n"
      "s_waitcnt lgkmcnt(1)\n"
      "ds_read_b128 v[72:75], v3 offset:5120\n"
      "v_fmac_f32 v122, v42, v42\n"
      "v_mfma_f32_16x16x32_bf16 v[52:55], a[60:63], v[32:35], v[52:55]\n"
      "buffer_store_dwordx4 v[110:113], v10, s[36:39], s71 offen offset:1024 sc1\n"
      "v_mfma_f32_16x16x32_bf16 v[56:59], a[44:47], v[28:31], v[56:59]\n"
      "v_add_f32 v121, v121, v43\n"
      "v_fmac_f32 v123, v43, v43\n"
      "v_mfma_f32_16x16x32_bf16 v[56:59], a[64:67], v[32:35], v[56:59]\n"
      "v_add_f32 v120, v120, v44\n"
      "v_fmac_f32 v122, v44, v44\n"
      "v_mfma_f32_16x16x32_bf16 v[60:63], a[48:51], v[28:31], v[60:63]\n"
      "v_add_f32 v121, v121, v45\n"
      "v_fmac_f32 v123, v45, v45\n"
      "v_mfma_f32_16x16x32_bf16 v[60:63], a[68:71], v[32:35], v[60:63]\n"
      "ds_read_b128 v[20:23], v3 offset:6144\n"
      "v_add_f32 v120, v120, v46\n"
      "v_mfma_f32_16x16x32_bf16 v[64:67], a[52:55], v[28:31], v[64:67]\n"
      "v_fmac_f32 v122, v46, v46\n"
      "v_add_f32 v121, v121, v47\n"
      "v_mfma_f32_16x16x32_bf16 v[64:67], a[72:75], v[32:35], v[64:67]\n"
      "buffer_load_dwordx4 v[96:99], v10, s[36:39], s78 offen sc1\n"
      "v_mfma_f32_16x16x32_bf16 v[68:71], a[56:59], v[28:31], v[68:71]\n"
      "v_fmac_f32 v123, v47, v47\n"
      "v_add_f32 v120, v120, v48\n"
      "v_mfma_f32_16x16x32_bf16 v[68:71], a[76:79], v[32:35], v[68:71]\n"
      "s_waitcnt lgkmcnt(2)\n"
      "v_fmac_f32 v122, v48, v48\n"
      "v_add_f32 v121, v121, v49\n"
      "v_mfma_f32_16x16x32_bf16 v[52:55], a[80:83], v[14:17], v[52:55]\n"
      "s_waitcnt lgkmcnt(1)\n"
      "ds_read_b128 v[24:27], v3 offset:7168\n"
      "v_fmac_f32 v123, v49, v49\n"
      "v_mfma_f32_16x16x32_bf16 v[52:55], a[100:103], v[72:75], v[52:55]\n"
      "v_add_f32 v120, v120, v50\n"
      "v_fmac_f32 v122, v50, v50\n"
      "v_mfma_f32_16x16x32_bf16 v[56:59], a[84:87], v[14:17], v[56:59]\n"
      "buffer_load_dwordx4 v[100:103], v10, s[36:39], s78 offen offset:1024 sc1\n"
      "v_mfma_f32_16x16x32_bf16 v[56:59], a[104:107], v[72:75], v[56:59]\n"
      "v_add_f32 v121, v121, v51\n"
      "v_fmac_f32 v123, v51, v51\n"
      "v_mfma_f32_16x16x32_bf16 v[60:63], a[88:91], v[14:17], v[60:63]\n"
      "v_add_f32 v120, v120, v121\n"
      "v_add_f32 v122, v122, v123\n"
      "v_mfma_f32_16x16x32_bf16 v[60:63], a[108:111], v[72:75], v[60:63]\n"
      "ds_read_b128 v[28:31], v3 offset:8192\n"
      "v_permlane32_swap_b32 v120, v122\n"
      "v_mfma_f32_16x16x32_bf16 v[64:67], a[92:95], v[14:17], v[64:67]\n"
      "v_add_f32 v120, v120, v122\n"
      "v_mfma_f32_16x16x32_bf16 v[64:67], a[112:115], v[72:75], v[64:67]\n"
      "buffer_load_dwordx2 v[104:105], v11, s[36:39], s78 offen sc1\n"
      "v_mfma_f32_16x16x32_bf16 v[68:71], a[96:99], v[14:17], v[68:71]\n"
      "v_mov_b32 v121, v120\n"
      "v_mfma_f32_16x16x32_bf16 v[68:71], a[116:119], v[72:75], v[68:71]\n"
      "s_waitcnt vmcnt(13) lgkmcnt(2)\n"
      "v_cmp_gt_u32 vcc, s67, v5\n"
      "v_permlane16_swap_b32 v120, v121\n"
      "v_mfma_f32_16x16x32_bf16 v[52:55], a[120:123], v[20:23], v[52:55]\n"
      "s_waitcnt lgkmcnt(1)\n"
      "ds_read_b128 v[32:35], v3 offset:9216\n"
      "v_add_f32 v120, v120, v121\n"
      "v_mfma_f32_16x16x32_bf16 v[52:55], a[140:143], v[24:27], v[52:55]\n"
      "buffer_store_dword v120, v12, s[36:39], s71 offen sc1\n"
      "v_mfma_f32_16x16x32_bf16 v[56:59], a[124:127], v[20:23], v[56:59]\n"
      "s_cbranch_vccnz .Lsl22\n"
      ".Lbk22:\n"
      "v_mfma_f32_16x16x32_bf16 v[56:59], a[144:147], v[24:27], v[56:59]\n"
      "s_cmp_eq_u32 s66, 29\n"
      "buffer_load_dword v5, v4, s[36:39], 0 offen sc1\n"
      "v_mfma_f32_16x16x32_bf16 v[60:63], a[128:131], v[20:23], v[60:63]\n"
      "s_cselect_b32 s48, 2007040, s79\n"
      "v_mfma_f32_16x16x32_bf16 v[60:63], a[148:151], v[24:27], v[60:63]\n"
      "s_add_u32 s80, s80, s48\n"
      "ds_read_b128 v[14:17], v3 offset:10240\n"
      "v_mfma_f32_16x16x32_bf16 v[64:67], a[132:135], v[20:23], v[64:67]\n"
      "s_add_u32 s67, s66, 4\n"
      "buffer_load_dwordx4 v2, s[36:39], s69 offen sc1 lds\n"
      "v_mfma_f32_16x16x32_bf16 v[64:67], a[152:155], v[24:27], v[64:67]\n"
      "s_min_i32 s67, s67, 64\n"
      "v_mfma_f32_16x16x32_bf16 v[68:71], a[136:139], v[20:23], v[68:71]\n"
      "v_lshlrev_b32 v128, 16, v116\n"
      "v_and_b32 v129, 0xffff0000, v116\n"
      "v_mfma_f32_16x16x32_bf16 v[68:71], a[156:159], v[24:27], v[68:71]\n"
      "s_waitcnt lgkmcnt(2)\n"
      "v_lshlrev_b32 v130, 16, v117\n"
      "v_and_b32 v131, 0xffff0000, v117\n"
      "v_mfma_f32_16x16x32_bf16 v[52:55], a[160:163], v[28:31], v[52:55]\n"
      "s_waitcnt lgkmcnt(1)\n"
      "ds_read_b128 v[72:75], v3 offset:11264\n"
      "v_lshlrev_b32 v132, 16, v118\n"
      "v_mfma_f32_16x16x32_bf16 v[52:55], a[180:183], v[32:35], v[52:55]\n"
      "s_max_i32 s67, s67, 0\n"
      "buffer_load_dwordx4 v2, s[36:39], s69 offen offset:1024 sc1 lds\n"
      "v_mfma_f32_16x16x32_bf16 v[56:59], a[164:167], v[28:31], v[56:59]\n"
      "s_mov_b32 s76, s77\n"
      "v_and_b32 v133, 0xffff0000, v118\n"
      "v_lshlrev_b32 v134, 16, v119\n"
      "v_mfma_f32_16x16x32_bf16 v[56:59], a[184:187], v[32:35], v[56:59]\n"
      "s_add_u32 s49, s75, s72\n"
      "v_and_b32 v135, 0xffff0000, v119\n"
      "v_lshlrev_b32 v136, 16, v124\n"
      "v_mfma_f32_16x16x32_bf16 v[60:63], a[168:171], v[28:31], v[60:63]\n"
      "s_mov_b32 s77, s72\n"
      "v_and_b32 v137, 0xffff0000, v124\n"
      "v_lshlrev_b32 v138, 16, v125\n"
      "v_mfma_f32_16x16x32_bf16 v[60:63], a[188:191], v[32:35], v[60:63]\n"
      "s_add_u32 s72, s72, 20480\n"
      "ds_read_b128 v[20:23], v3 offset:12288\n"
      "v_and_b32 v139, 0xffff0000, v125\n"
      "v_mfma_f32_16x16x32_bf16 v[64:67], a[172:175], v[28:31], v[64:67]\n"
      "s_cmp_lt_u32 s72, 81920\n"
      "buffer_load_dwordx4 v2, s[36:39], s69 offen offset:2048 sc1 lds\n"
      "v_mfma_f32_16x16x32_bf16 v[64:67], a[192:195], v[32:35], v[64:67]\n"
      "s_cselect_b32 s72, s72, 0\n"
      "v_lshlrev_b32 v140, 16, v126\n"
      "v_and_b32 v141, 0xffff0000, v126\n"
      "v_mfma_f32_16x16x32_bf16 v[68:71], a[176:179], v[28:31], v[68:71]\n"
      "v_lshlrev_b32 v142, 16, v127\n"
      "v_and_b32 v143, 0xffff0000, v127\n"
      "v_mfma_f32_16x16x32_bf16 v[68:71], a[196:199], v[32:35], v[68:71]\n"
      "s_waitcnt lgkmcnt(2)\n"
      "v_mfma_f32_16x16x32_bf16 v[52:55], a[200:203], v[14:17], v[52:55]\n"
      "s_waitcnt lgkmcnt(1)\n"
      "ds_read_b128 v[24:27], v3 offset:13312\n"
      "v_mfma_f32_16x16x32_bf16 v[52:55], a[220:223], v[72:75], v[52:55]\n"
      "buffer_load_dwordx4 v2, s[36:39], s69 offen offset:3072 sc1 lds\n"
      "v_mfma_f32_16x16x32_bf16 v[56:59], a[204:207], v[14:17], v[56:59]\n"
      "s_mov_b32 m0, s49\n"
      "v_mfma_f32_16x16x32_bf16 v[56:59], a[224:227], v[72:75], v[56:59]\n"
      "v_mfma_f32_16x16x32_bf16 v[60:63], a[208:211], v[14:17], v[60:63]\n"
      "v_mfma_f32_16x16x32_bf16 v[60:63], a[228:231], v[72:75], v[60:63]\n"
      "ds_read_b128 v[28:31], v3 offset:14336\n"
      "v_mfma_f32_16x16x32_bf16 v[64:67], a[212:215], v[14:17], v[64:67]\n"
      "buffer_load_dword v8, s[36:39], s69 offen sc1 lds\n"
      "v_mfma_f32_16x16x32_bf16 v[64:67], a[232:235], v[72:75], v[64:67]\n"
      "v_mfma_f32_16x16x32_bf16 v[68:71], a[216:219], v[14:17], v[68:71]\n"
      "v_mfma_f32_16x16x32_bf16 v[68:71], a[236:239], v[72:75], v[68:71]\n"
      "s_waitcnt lgkmcnt(2)\n"
      "buffer_load_dword v9, s[36:39], s69 offen offset:256 sc1 lds\n"
      "v_mfma_f32_16x16x32_bf16 v[52:55], a[240:243], v[20:23], v[52:55]\n"
      "s_waitcnt lgkmcnt(1)\n"
      "ds_read_b128 v[32:35], v3 offset:15360\n"
      "v_mfma_f32_16x16x32_bf16 v[52:55], v[154:157], v[24:27], v[52:55]\n"
      "s_cmp_lt_i32 s66, 63\n"
      "v_mfma_f32_16x16x32_bf16 v[56:59], a[244:247], v[20:23], v[56:59]\n"
      "v_mfma_f32_16x16x32_bf16 v[56:59], v[158:161], v[24:27], v[56:59]\n"
      "v_mfma_f32_16x16x32_bf16 v[60:63], a[248:251], v[20:23], v[60:63]\n"
      "v_mfma_f32_16x16x32_bf16 v[60:63], v[162:165], v[24:27], v[60:63]\n"
      "ds_read_b128 v[14:17], v3 offset:16384\n"
      "v_mfma_f32_16x16x32_bf16 v[64:67], a[252:255], v[20:23], v[64:67]\n"
      "s_waitcnt vmcnt(11)\n"
      "s_barrier\n"
      "v_mfma_f32_16x16x32_bf16 v[64:67], v[166:169], v[24:27], v[64:67]\n"
      "v_mfma_f32_16x16x32_bf16 v[68:71], v[150:153], v[20:23], v[68:71]\n"
      ".Lmw00:\n"
      "s_cbranch_scc0 .Lwo_t1\n"
      ".Lwo_e0:\n"
      "v_mfma_f32_16x16x32_bf16 v[68:71], v[170:173], v[24:27], v[68:71]\n"
      "s_waitcnt lgkmcnt(2)\n"
      "buffer_store_dword v7, v6, s[36:39], 0 offen sc1\n"
      "v_mfma_f32_16x16x32_bf16 v[52:55], v[174:177], v[28:31], v[52:55]\n"
      "s_waitcnt lgkmcnt(1)\n"
      "ds_read_b128 v[72:75], v3 offset:17408\n"
      "v_mfma_f32_16x16x32_bf16 v[52:55], v[194:197], v[32:35], v[52:55]\n"
      "s_cmp_lt_u32 s66, 64\n"
      "v_add_u32 v3, s76, v2\n"
      "v_mfma_f32_16x16x32_bf16 v[56:59], v[178:181], v[28:31], v[56:59]\n"
      "s_cselect_b32 s71, s70, 0x40000000\n"
      "v_mfma_f32_16x16x32_bf16 v[56:59], v[198:201], v[32:35], v[56:59]\n"
      "s_add_u32 s70, s70, 20480\n"
      "v_mfma_f32_16x16x32_bf16 v[60:63], v[182:185], v[28:31], v[60:63]\n"
      "s_add_u32 s66, s66, 1\n"
      "v_mfma_f32_16x16x32_bf16 v[60:63], v[202:205], v[32:35], v[60:63]\n"
      "s_max_i32 s73, s66, 0\n"
      "ds_read_b128 v[20:23], v3 offset:0\n"
      "v_mfma_f32_16x16x32_bf16 v[64:67], v[186:189], v[28:31], v[64:67]\n"
      "s_add_u32 s68, s68, 20480\n"
      "v_mfma_f32_16x16x32_bf16 v[64:67], v[206:209], v[32:35], v[64:67]\n"
      "s_add_u32 s48, s66, 2\n"
      "v_mfma_f32_16x16x32_bf16 v[68:71], v[190:193], v[28:31], v[68:71]\n"
      "s_cmp_lt_u32 s48, 64\n"
      "v_mfma_f32_16x16x32_bf16 v[68:71], v[210:213], v[32:35], v[68:71]\n"
      "s_waitcnt lgkmcnt(2)\n"
      "v_mfma_f32_16x16x32_bf16 v[52:55], v[214:217], v[14:17], v[52:55]\n"
      "s_waitcnt lgkmcnt(1)\n"
      "ds_read_b128 v[24:27], v3 offset:1024\n"
      "v_mfma_f32_16x16x32_bf16 v[52:55], v[234:237], v[72:75], v[52:55]\n"
      "s_cselect_b32 s69, s68, 0x40000000\n"
      "v_mfma_f32_16x16x32_bf16 v[56:59], v[218:221], v[14:17], v[56:59]\n"
      "s_cselect_b32 s78, s80, 0x40000000\n"
      "v_mfma_f32_16x16x32_bf16 v[56:59], v[238:241], v[72:75], v[56:59]\n"
      "s_add_u32 m0, s74, s72\n"
      "v_writelane_b32 v7, s73, 0\n"
      "v_mfma_f32_16x16x32_bf16 v[60:63], v[222:225], v[14:17], v[60:63]\n"
      "v_mfma_f32_16x16x32_bf16 v[60:63], v[242:245], v[72:75], v[60:63]\n"
      "ds_read_b128 v[28:31], v3 offset:2048\n"
      "v_mfma_f32_16x16x32_bf16 v[64:67], v[226:229], v[14:17], v[64:67]\n"
      "v_cvt_pk_bf16_f32 v106, v52, v53\n"
      "v_cvt_pk_bf16_f32 v107, v54, v55\n"
      "v_mfma_f32_16x16x32_bf16 v[64:67], v[246:249], v[72:75], v[64:67]\n"
      "v_mul_f32 v122, v52, v52\n"
      "v_mul_f32 v123, v53, v53\n"
      "v_mfma_f32_16x16x32_bf16 v[68:71], v[230:233], v[14:17], v[68:71]\n"
      "v_cvt_pk_bf16_f32 v108, v56, v57\n"
      "v_cvt_pk_bf16_f32 v109, v58, v59\n"
      "v_mfma_f32_16x16x32_bf16 v[68:71], v[250:253], v[72:75], v[68:71]\n"
      "s_waitcnt lgkmcnt(2)\n"
      "buffer_store_dwordx4 v[106:109], v10, s[36:39], s71 offen sc1\n"
      "v_mfma_f32_16x16x32_bf16 v[36:39], a[0:3], v[20:23], v[128:131]\n"
      "s_waitcnt lgkmcnt(1)\n"
      "ds_read_b128 v[32:35], v3 offset:3072\n"
      "v_cvt_pk_bf16_f32 v110, v60, v61\n"
      "v_mfma_f32_16x16x32_bf16 v[36:39], a[20:23], v[24:27], v[36:39]\n"
      "v_cvt_pk_bf16_f32 v111, v62, v63\n"
      "v_add_f32 v120, v52, v54\n"
      "v_mfma_f32_16x16x32_bf16 v[40:43], a[4:7], v[20:23], v[132:135]\n"
      "v_cvt_pk_bf16_f32 v112, v64, v65\n"
      "v_cvt_pk_bf16_f32 v113, v66, v67\n"
      "v_mfma_f32_16x16x32_bf16 v[40:43], a[24:27], v[24:27], v[40:43]\n"
      "v_cvt_pk_bf16_f32 v114, v68, v69\n"
      "v_cvt_pk_bf16_f32 v115, v70, v71\n"
      "v_mfma_f32_16x16x32_bf16 v[44:47], a[8:11], v[20:23], v[136:139]\n"
      "ds_read_b128 v[14:17], v3 offset:4096\n"
      "v_fmac_f32 v122, v54, v54\n"
      "v_mfma_f32_16x16x32_bf16 v[44:47], a[28:31], v[24:27], v[44:47]\n"
      "buffer_store_dwordx4 v[110:113], v10, s[36:39], s71 offen offset:1024 sc1\n"
      "v_mfma_f32_16x16x32_bf16 v[48:51], a[12:15], v[20:23], v[140:143]\n"
      "v_add_f32 v121, v53, v55\n"
      "v_fmac_f32 v123, v55, v55\n"
      "v_mfma_f32_16x16x32_bf16 v[48:51], a[32:35], v[24:27], v[48:51]\n"
      "s_waitcnt lgkmcnt(2)\n"
      "v_add_f32 v120, v120, v56\n"
      "v_fmac_f32 v122, v56, v56\n"
      "v_mfma_f32_16x16x32_bf16 v[36:39], a[40:43], v[28:31], v[36:39]\n"
      "s_waitcnt lgkmcnt(1)\n"
      "ds_read_b128 v[72:75], v3 offset:5120\n"
      "v_add_f32 v121, v121, v57\n"
      "v_mfma_f32_16x16x32_bf16 v[36:39], a[60:63], v[32:35], v[36:39]\n"
      "v_fmac_f32 v123, v57, v57\n"
      "v_add_f32 v120, v120, v58\n"
      "v_mfma_f32_16x16x32_bf16 v[40:43], a[44:47], v[28:31], v[40:43]\n"
      "v_fmac_f32 v122, v58, v58\n"
      "v_add_f32 v121, v121, v59\n"
      "v_mfma_f32_16x16x32_bf16 v[40:43], a[64:67], v[32:35], v[40:43]\n"
      "buffer_store_dwordx2 v[114:115], v11, s[36:39], s71 offen sc1\n"
      "v_mfma_f32_16x16x32_bf16 v[44:47], a[48:51], v[28:31], v[44:47]\n"
      "ds_read_b128 v[20:23], v3 offset:6144\n"
      "v_fmac_f32 v123, v59, v59\n"
      "v_mfma_f32_16x16x32_bf16 v[44:47], a[68:71], v[32:35], v[44:47]\n"
      "v_add_f32 v120, v120, v60\n"
      "v_fmac_f32 v122, v60, v60\n"
      "v_mfma_f32_16x16x32_bf16 v[48:51], a[52:55], v[28:31], v[48:51]\n"
      "v_add_f32 v121, v121, v61\n"
      "v_fmac_f32 v123, v61, v61\n"
      "v_mfma_f32_16x16x32_bf16 v[48:51], a[72:75], v[32:35], v[48:51]\n"
      "s_waitcnt lgkmcnt(2)\n"
      "buffer_load_dwordx4 v[116:119], v10, s[36:39], s78 offen sc1\n"
      "v_mfma_f32_16x16x32_bf16 v[36:39], a[80:83], v[14:17], v[36:39]\n"
      "s_waitcnt lgkmcnt(1)\n"
      "ds_read_b128 v[24:27], v3 offset:7168\n"
      "v_add_f32 v120, v120, v62\n"
      "v_mfma_f32_16x16x32_bf16 v[36:39], a[100:103], v[72:75], v[36:39]\n"
      "v_fmac_f32 v122, v62, v62\n"
      "v_add_f32 v121, v121, v63\n"
      "v_mfma_f32_16x16x32_bf16 v[40:43], a[84:87], v[14:17], v[40:43]\n"
      "v_fmac_f32 v123, v63, v63\n"
      "v_add_f32 v120, v120, v64\n"
      "v_mfma_f32_16x16x32_bf16 v[40:43], a[104:107], v[72:75], v[40:43]\n"
      "v_fmac_f32 v122, v64, v64\n"
      "v_add_f32 v121, v121, v65\n"
      "v_mfma_f32_16x16x32_bf16 v[44:47], a[88:91], v[14:17], v[44:47]\n"
      "ds_read_b128 v[28:31], v3 offset:8192\n"
      "v_fmac_f32 v123, v65, v65\n"
      "v_mfma_f32_16x16x32_bf16 v[44:47], a[108:111], v[72:75], v[44:47]\n"
      "buffer_load_dwordx4 v[124:127], v10, s[36:39], s78 offen offset:1024 sc1\n"
      "v_mfma_f32_16x16x32_bf16 v[48:51], a[92:95], v[14:17], v[48:51]\n"
      "v_add_f32 v120, v120, v66\n"
      "v_fmac_f32 v122, v66, v66\n"
      "v_mfma_f32_16x16x32_bf16 v[48:51], a[112:115], v[72:75], v[48:51]\n"
      "s_waitcnt vmcnt(12) lgkmcnt(2)\n"
      "v_cmp_gt_u32 vcc, s67, v5\n"
      "v_add_f32 v121, v121, v67\n"
      "v_mfma_f32_16x16x32_bf16 v[36:39], a[120:123], v[20:23], v[36:39]\n"
      "s_waitcnt lgkmcnt(1)\n"
      "ds_read_b128 v[32:35], v3 offset:9216\n"
      "v_fmac_f32 v123, v67, v67\n"
      "v_mfma_f32_16x16x32_bf16 v[36:39], a[140:143], v[24:27], v[36:39]\n"
      "v_add_f32 v120, v120, v68\n"
      "v_fmac_f32 v122, v68, v68\n"
      "v_mfma_f32_16x16x32_bf16 v[40:43], a[124:127], v[20:23], v[40:43]\n"
      "s_cbranch_vccnz .Lsl23\n"
      ".Lbk23:\n"
      "v_mfma_f32_16x16x32_bf16 v[40:43], a[144:147], v[24:27], v[40:43]\n"
      "s_cmp_eq_u32 s66, 29\n"
      "buffer_load_dword v5, v4, s[36:39], 0 offen sc1\n"
      "v_mfma_f32_16x16x32_bf16 v[44:47], a[128:131], v[20:23], v[44:47]\n"
      "s_cselect_b32 s48, 2007040, s79\n"
      "ds_read_b128 v[14:17], v3 offset:10240\n"
      "v_add_f32 v121, v121, v69\n"
      "v_mfma_f32_16x16x32_bf16 v[44:47], a[148:151], v[24:27], v[44:47]\n"
      "s_add_u32 s80, s80, s48\n"
      "buffer_load_dwordx4 v2, s[36:39], s69 offen sc1 lds\n"
      "v_mfma_f32_16x16x32_bf16 v[48:51], a[132:135], v[20:23], v[48:51]\n"
      "v_lshlrev_b32 v76, 16, v96\n"
      "v_and_b32 v77, 0xffff0000, v96\n"
      "v_mfma_f32_16x16x32_bf16 v[48:51], a[152:155], v[24:27], v[48:51]\n"
      "s_waitcnt lgkmcnt(2)\n"
      "v_lshlrev_b32 v78, 16, v97\n"
      "v_and_b32 v79, 0xffff0000, v97\n"
      "v_mfma_f32_16x16x32_bf16 v[36:39], a[160:163], v[28:31], v[36:39]\n"
      "s_waitcnt lgkmcnt(1)\n"
      "ds_read_b128 v[72:75], v3 offset:11264\n"
      "v_lshlrev_b32 v80, 16, v98\n"
      "v_mfma_f32_16x16x32_bf16 v[36:39], a[180:183], v[32:35], v[36:39]\n"
      "s_add_u32 s67, s66, 4\n"
      "v_and_b32 v81, 0xffff0000, v98\n"
      "v_lshlrev_b32 v82, 16, v99\n"
      "v_mfma_f32_16x16x32_bf16 v[40:43], a[164:167], v[28:31], v[40:43]\n"
      "s_min_i32 s67, s67, 64\n"
      "buffer_load_dwordx4 v2, s[36:39], s69 offen offset:1024 sc1 lds\n"
      "v_mfma_f32_16x16x32_bf16 v[40:43], a[184:187], v[32:35], v[40:43]\n"
      "s_max_i32 s67, s67, 0\n"
      "v_and_b32 v83, 0xffff0000, v99\n"
      "v_lshlrev_b32 v84, 16, v100\n"
      "v_mfma_f32_16x16x32_bf16 v[44:47], a[168:171], v[28:31], v[44:47]\n"
      "s_mov_b32 s76, s77\n"
      "ds_read_b128 v[20:23], v3 offset:12288\n"
      "v_and_b32 v85, 0xffff0000, v100\n"
      "v_mfma_f32_16x16x32_bf16 v[44:47], a[188:191], v[32:35], v[44:47]\n"
      "s_add_u32 s49, s75, s72\n"
      "v_lshlrev_b32 v86, 16, v101\n"
      "v_and_b32 v87, 0xffff0000, v101\n"
      "v_mfma_f32_16x16x32_bf16 v[48:51], a[172:175], v[28:31], v[48:51]\n"
      "s_mov_b32 s77, s72\n"
      "v_lshlrev_b32 v88, 16, v102\n"
      "v_and_b32 v89, 0xffff0000, v102\n"
      "v_mfma_f32_16x16x32_bf16 v[48:51], a[192:195], v[32:35], v[48:51]\n"
      "s_waitcnt lgkmcnt(2)\n"
      "buffer_load_dwordx4 v2, s[36:39], s69 offen offset:2048 sc1 lds\n"
      "v_mfma_f32_16x16x32_bf16 v[36:39], a[200:203], v[14:17], v[36:39]\n"
      "s_waitcnt lgkmcnt(1)\n"
      "ds_read_b128 v[24:27], v3 offset:13312\n"
      "v_lshlrev_b32 v90, 16, v103\n"
      "v_mfma_f32_16x16x32_bf16 v[36:39], a[220:223], v[72:75], v[36:39]\n"
      "s_add_u32 s72, s72, 20480\n"
      "v_and_b32 v91, 0xffff0000, v103\n"
      "v_lshlrev_b32 v92, 16, v104\n"
      "v_mfma_f32_16x16x32_bf16 v[40:43], a[204:207], v[14:17], v[40:43]\n"
      "s_cmp_lt_u32 s72, 81920\n"
      "v_and_b32 v93, 0xffff0000, v104\n"
      "v_lshlrev_b32 v94, 16, v105\n"
      "v_mfma_f32_16x16x32_bf16 v[40:43], a[224:227], v[72:75], v[40:43]\n"
      "s_cselect_b32 s72, s72, 0\n"
      "v_and_b32 v95, 0xffff0000, v105\n"
      "v_fmac_f32 v123, v69, v69\n"
      "v_mfma_f32_16x16x32_bf16 v[44:47], a[208:211], v[14:17], v[44:47]\n"
      "ds_read_b128 v[28:31], v3 offset:14336\n"
      "v_add_f32 v120, v120, v70\n"
      "v_mfma_f32_16x16x32_bf16 v[44:47], a[228:231], v[72:75], v[44:47]\n"
      "buffer_load_dwordx4 v2, s[36:39], s69 offen offset:3072 sc1 lds\n"
      "v_mfma_f32_16x16x32_bf16 v[48:51], a[212:215], v[14:17], v[48:51]\n"
      "s_mov_b32 m0, s49\n"
      "v_fmac_f32 v122, v70, v70\n"
      "v_add_f32 v121, v121, v71\n"
      "v_mfma_f32_16x16x32_bf16 v[48:51], a[232:235], v[72:75], v[48:51]\n"
      "s_waitcnt lgkmcnt(2)\n"
      "v_fmac_f32 v123, v71, v71\n"
      "v_add_f32 v120, v120, v121\n"
      "v_mfma_f32_16x16x32_bf16 v[36:39], a[240:243], v[20:23], v[36:39]\n"
      "s_waitcnt lgkmcnt(1)\n"
      "ds_read_b128 v[32:35], v3 offset:15360\n"
      "v_add_f32 v122, v122, v123\n"
      "v_mfma_f32_16x16x32_bf16 v[36:39], v[154:157], v[24:27], v[36:39]\n"
      "s_nop 0\n"
      "v_permlane32_swap_b32 v120, v122\n"
      "v_mfma_f32_16x16x32_bf16 v[40:43], a[244:247], v[20:23], v[40:43]\n"
      "buffer_load_dword v8, s[36:39], s69 offen sc1 lds\n"
      "v_mfma_f32_16x16x32_bf16 v[40:43], v[158:161], v[24:27], v[40:43]\n"
      "v_add_f32 v120, v120, v122\n"
      "v_mfma_f32_16x16x32_bf16 v[44:47], a[248:251], v[20:23], v[44:47]\n"
      "s_cmp_lt_i32 s66, 63\n"
      "ds_read_b128 v[14:17], v3 offset:16384\n"
      "v_mov_b32 v121, v120\n"
      "v_mfma_f32_16x16x32_bf16 v[44:47], v[162:165], v[24:27], v[44:47]\n"
      "buffer_load_dword v9, s[36:39], s69 offen offset:256 sc1 lds\n"
      "v_mfma_f32_16x16x32_bf16 v[48:51], a[252:255], v[20:23], v[48:51]\n"
      "v_permlane16_swap_b32 v120, v121\n"
      "v_mfma_f32_16x16x32_bf16 v[48:51], v[166:169], v[24:27], v[48:51]\n"
      "s_waitcnt lgkmcnt(2)\n"
      "v_add_f32 v120, v120, v121\n"
      "v_mfma_f32_16x16x32_bf16 v[36:39], v[174:177], v[28:31], v[36:39]\n"
      "s_waitcnt lgkmcnt(1)\n"
      "ds_read_b128 v[72:75], v3 offset:17408\n"
      "v_mfma_f32_16x16x32_bf16 v[36:39], v[194:197], v[32:35], v[36:39]\n"
      "s_waitcnt vmcnt(9)\n"
      "v_add_u32 v3, s76, v2\n"
      "s_barrier\n"
      "v_mfma_f32_16x16x32_bf16 v[40:43], v[178:181], v[28:31], v[40:43]\n"
      "buffer_store_dword v120, v12, s[36:39], s71 offen sc1\n"
      "v_mfma_f32_16x16x32_bf16 v[40:43], v[198:201], v[32:35], v[40:43]\n"
      ".Lmw10:\n"
      "s_cbranch_scc1 .Lwo_loop0\n"
      ".Lwo_t0:\n"
      "s_waitcnt lgkmcnt(0)\n"
      "v_mfma_f32_16x16x32_bf16 v[44:47], v[182:185], v[28:31], v[44:47]\n"
      "v_mfma_f32_16x16x32_bf16 v[44:47], v[202:205], v[32:35], v[44:47]\n"
      "v_mfma_f32_16x16x32_bf16 v[48:51], v[186:189], v[28:31], v[48:51]\n"
      "v_mfma_f32_16x16x32_bf16 v[48:51], v[206:209], v[32:35], v[48:51]\n"
      "v_mfma_f32_16x16x32_bf16 v[36:39], v[214:217], v[14:17], v[36:39]\n"
      "v_mfma_f32_16x16x32_bf16 v[36:39], v[234:237], v[72:75], v[36:39]\n"
      "v_mfma_f32_16x16x32_bf16 v[40:43], v[218:221], v[14:17], v[40:43]\n"
      "s_waitcnt vmcnt(10)\n"
      "buffer_store_dword v7, v6, s[36:39], 0 offen sc1\n"
      "v_mfma_f32_16x16x32_bf16 v[40:43], v[238:241], v[72:75], v[40:43]\n"
      "v_mfma_f32_16x16x32_bf16 v[44:47], v[222:225], v[14:17], v[44:47]\n"
      "v_mfma_f32_16x16x32_bf16 v[44:47], v[242:245], v[72:75], v[44:47]\n"
      "s_movk_i32 s73, 65\n"
      "v_writelane_b32 v7, s73, 0\n"
      "v_mfma_f32_16x16x32_bf16 v[48:51], v[226:229], v[14:17], v[48:51]\n"
      "v_cvt_pk_bf16_f32 v106, v36, v37\n"
      "v_cvt_pk_bf16_f32 v107, v38, v39\n"
      "v_mfma_f32_16x16x32_bf16 v[48:51], v[246:249], v[72:75], v[48:51]\n"
      "v_mul_f32 v122, v36, v36\n"
      "v_mul_f32 v123, v37, v37\n"
      "v_cvt_pk_bf16_f32 v108, v40, v41\n"
      "v_cvt_pk_bf16_f32 v109, v42, v43\n"
      "v_add_f32 v120, v36, v38\n"
      "v_fmac_f32 v122, v38, v38\n"
      "buffer_store_dwordx4 v[106:109], v10, s[36:39], s70 offen sc1\n"
      "v_add_f32 v121, v37, v39\n"
      "v_fmac_f32 v123, v39, v39\n"
      "v_add_f32 v120, v120, v40\n"
      "v_cvt_pk_bf16_f32 v110, v44, v45\n"
      "v_cvt_pk_bf16_f32 v111, v46, v47\n"
      "v_fmac_f32 v122, v40, v40\n"
      "v_add_f32 v121, v121, v41\n"
      "v_fmac_f32 v123, v41, v41\n"
      "v_add_f32 v120, v120, v42\n"
      "v_fmac_f32 v122, v42, v42\n"
      "v_add_f32 v121, v121, v43\n"
      "v_cvt_pk_bf16_f32 v112, v48, v49\n"
      "v_cvt_pk_bf16_f32 v113, v50, v51\n"
      "v_fmac_f32 v123, v43, v43\n"
      "v_add_f32 v120, v120, v44\n"
      "v_fmac_f32 v122, v44, v44\n"
      "v_add_f32 v121, v121, v45\n"
      "v_fmac_f32 v123, v45, v45\n"
      "v_add_f32 v120, v120, v46\n"
      "v_fmac_f32 v122, v46, v46\n"
      "v_add_f32 v121, v121, v47\n"
      "v_fmac_f32 v123, v47, v47\n"
      "v_add_f32 v120, v120, v48\n"
      "buffer_store_dwordx4 v[110:113], v10, s[36:39], s70 offen offset:1024 sc1\n"
      "v_fmac_f32 v122, v48, v48\n"
      "v_add_f32 v121, v121, v49\n"
      "v_fmac_f32 v123, v49, v49\n"
      "v_add_f32 v120, v120, v50\n"
      "v_fmac_f32 v122, v50, v50\n"
      "v_add_f32 v121, v121, v51\n"
      "v_fmac_f32 v123, v51, v51\n"
      "v_add_f32 v120, v120, v121\n"
      "v_add_f32 v122, v122, v123\n"
      "s_nop 1\n"
      "v_permlane32_swap_b32 v120, v122\n"
      "v_add_f32 v120, v120, v122\n"
      "v_mov_b32 v121, v120\n"
      "s_nop 1\n"
      "v_permlane16_swap_b32 v120, v121\n"
      "v_add_f32 v120, v120, v121\n"
      "buffer_store_dword v120, v12, s[36:39], s70 offen sc1\n"
      "s_waitcnt vmcnt(0) lgkmcnt(0)\n"
      "s_barrier\n"
      "buffer_store_dword v7, v6, s[36:39], 0 offen sc1\n"
      "s_branch .Lexit\n"
      ".Lwo_t1:\n"
      "s_waitcnt lgkmcnt(0)\n"
      "v_mfma_f32_16x16x32_bf16 v[68:71], v[170:173], v[24:27], v[68:71]\n"
      "v_mfma_f32_16x16x32_bf16 v[52:55], v[174:177], v[28:31], v[52:55]\n"
      "s_waitcnt vmcnt(11)\n"
      "buffer_store_dword v7, v6, s[36:39], 0 offen sc1\n"
      "ds_read_b128 v[72:75], v3 offset:17408\n"
      "v_mfma_f32_16x16x32_bf16 v[52:55], v[194:197], v[32:35], v[52:55]\n"
      "v_mfma_f32_16x16x32_bf16 v[56:59], v[178:181], v[28:31], v[56:59]\n"
      "v_mfma_f32_16x16x32_bf16 v[56:59], v[198:201], v[32:35], v[56:59]\n"
      "v_mfma_f32_16x16x32_bf16 v[60:63], v[182:185], v[28:31], v[60:63]\n"
      "v_mfma_f32_16x16x32_bf16 v[60:63], v[202:205], v[32:35], v[60:63]\n"
      "v_mfma_f32_16x16x32_bf16 v[64:67], v[186:189], v[28:31], v[64:67]\n"
      "v_mfma_f32_16x16x32_bf16 v[64:67], v[206:209], v[32:35], v[64:67]\n"
      "v_mfma_f32_16x16x32_bf16 v[68:71], v[190:193], v[28:31], v[68:71]\n"
      "s_movk_i32 s73, 65\n"
      "v_writelane_b32 v7, s73, 0\n"
      "v_mfma_f32_16x16x32_bf16 v[68:71], v[210:213], v[32:35], v[68:71]\n"
      "v_mfma_f32_16x16x32_bf16 v[52:55], v[214:217], v[14:17], v[52:55]\n"
      "s_waitcnt lgkmcnt(0)\n"
      "v_mfma_f32_16x16x32_bf16 v[52:55], v[234:237], v[72:75], v[52:55]\n"
      "v_mfma_f32_16x16x32_bf16 v[56:59], v[218:221], v[14:17], v[56:59]\n"
      "v_mfma_f32_16x16x32_bf16 v[56:59], v[238:241], v[72:75], v[56:59]\n"
      "v_mfma_f32_16x16x32_bf16 v[60:63], v[222:225], v[14:17], v[60:63]\n"
      "v_mfma_f32_16x16x32_bf16 v[60:63], v[242:245], v[72:75], v[60:63]\n"
      "v_mfma_f32_16x16x32_bf16 v[64:67], v[226:229], v[14:17], v[64:67]\n"
      "s_nop 2\n"
      "v_cvt_pk_bf16_f32 v106, v52, v53\n"
      "v_cvt_pk_bf16_f32 v107, v54, v55\n"
      "v_mfma_f32_16x16x32_bf16 v[64:67], v[246:249], v[72:75], v[64:67]\n"
      "v_mul_f32 v122, v52, v52\n"
      "v_mul_f32 v123, v53, v53\n"
      "v_mfma_f32_16x16x32_bf16 v[68:71], v[230:233], v[14:17], v[68:71]\n"
      "v_cvt_pk_bf16_f32 v108, v56, v57\n"
      "v_cvt_pk_bf16_f32 v109, v58, v59\n"
      "v_mfma_f32_16x16x32_bf16 v[68:71], v[250:253], v[72:75], v[68:71]\n"
      "buffer_store_dwordx4 v[106:109], v10, s[36:39], s70 offen sc1\n"
      "v_add_f32 v120, v52, v54\n"
      "v_cvt_pk_bf16_f32 v110, v60, v61\n"
      "v_cvt_pk_bf16_f32 v111, v62, v63\n"
      "v_fmac_f32 v122, v54, v54\n"
      "v_add_f32 v121, v53, v55\n"
      "v_fmac_f32 v123, v55, v55\n"
      "v_add_f32 v120, v120, v56\n"
      "v_fmac_f32 v122, v56, v56\n"
      "v_add_f32 v121, v121, v57\n"
      "v_cvt_pk_bf16_f32 v112, v64, v65\n"
      "v_cvt_pk_bf16_f32 v113, v66, v67\n"
      "v_fmac_f32 v123, v57, v57\n"
      "v_add_f32 v120, v120, v58\n"
      "v_cvt_pk_bf16_f32 v114, v68, v69\n"
      "v_cvt_pk_bf16_f32 v115, v70, v71\n"
      "v_fmac_f32 v122, v58, v58\n"
      "v_add_f32 v121, v121, v59\n"
      "v_fmac_f32 v123, v59, v59\n"
      "v_add_f32 v120, v120, v60\n"
      "v_fmac_f32 v122, v60, v60\n"
      "v_add_f32 v121, v121, v61\n"
      "buffer_store_dwordx4 v[110:113], v10, s[36:39], s70 offen offset:1024 sc1\n"
      "v_fmac_f32 v123, v61, v61\n"
      "v_add_f32 v120, v120, v62\n"
      "v_fmac_f32 v122, v62, v62\n"
      "v_add_f32 v121, v121, v63\n"
      "v_fmac_f32 v123, v63, v63\n"
      "v_add_f32 v120, v120, v64\n"
      "v_fmac_f32 v122, v64, v64\n"
      "v_add_f32 v121, v121, v65\n"
      "v_fmac_f32 v123, v65, v65\n"
      "v_add_f32 v120, v120, v66\n"
      "v_fmac_f32 v122, v66, v66\n"
      "v_add_f32 v121, v121, v67\n"
      "v_fmac_f32 v123, v67, v67\n"
      "v_add_f32 v120, v120, v68\n"
      "v_fmac_f32 v122, v68, v68\n"
      "v_add_f32 v121, v121, v69\n"
      "v_fmac_f32 v123, v69, v69\n"
      "v_add_f32 v120, v120, v70\n"
      "v_fmac_f32 v122, v70, v70\n"
      "v_add_f32 v121, v121, v71\n"
      "v_fmac_f32 v123, v71, v71\n"
      "buffer_store_dwordx2 v[114:115], v11, s[36:39], s70 offen sc1\n"
      "v_add_f32 v120, v120, v121\n"
      "v_add_f32 v122, v122, v123\n"
      "s_nop 1\n"
      "v_permlane32_swap_b32 v120, v122\n"
      "v_add_f32 v120, v120, v122\n"
      "v_mov_b32 v121, v120\n"
      "s_nop 1\n"
      "v_permlane16_swap_b32 v120, v121\n"
      "v_add_f32 v120, v120, v121\n"
      "buffer_store_dword v120, v12, s[36:39], s70 offen sc1\n"
      "s_waitcnt vmcnt(0) lgkmcnt(0)\n"
      "s_barrier\n"
      "buffer_store_dword v7, v6, s[36:39], 0 offen sc1\n"
      "s_branch .Lexit\n"
      ".Lexit:\n"
      ".Lend:\n"
      "s_endpgm\n"
      ".Lsl0:\n"
      "s_mov_b32 s56, 0x20000\n"
      ".Lsp0:\n"
      "buffer_load_dwordx2 v[18:19], v102, s[36:39], s71 offen offset:8 sc1\n"
      "s_waitcnt vmcnt(0)\n"
      "v_cmp_ne_u64 vcc, s[98:99], v[18:19]\n"
      "s_cbranch_vccz .Lbk0\n"
      "s_sleep 4\n"
      "s_sub_u32 s56, s56, 1\n"
      "s_cbranch_scc0 .Lsp0\n"
      "s_branch .Lbk0\n"
      ".Lsl1:\n"
      "s_mov_b32 s56, 0x20000\n"
      ".Lsp1:\n"
      "buffer_load_dword v18, v5, s[36:39], s71 offen sc1\n"
      "s_waitcnt vmcnt(0)\n"
      "v_cmp_ne_u32 vcc, v16, v18\n"
      "s_cbranch_vccz .Lbk1\n"
      "s_sleep 4\n"
      "s_sub_u32 s56, s56, 1\n"
      "s_cbranch_scc0 .Lsp1\n"
      "s_branch .Lbk1\n"
      ".Ltg0:\n"
      "v_swap_b32 v108, v112\n"
      "v_swap_b32 v109, v113\n"
      "v_swap_b32 v110, v114\n"
      "v_swap_b32 v111, v115\n"
      "s_add_u32 s80, s80, s81\n"
      "s_xor_b32 s81, s81, 33\n"
      "s_branch .Ltb0\n"
      ".Lsl2:\n"
      "s_mov_b32 s56, 0x20000\n"
      ".Lsp2:\n"
      "buffer_load_dword v18, v5, s[36:39], s71 offen sc1\n"
      "s_waitcnt vmcnt(0)\n"
      "v_cmp_ne_u32 vcc, v16, v18\n"
      "s_cbranch_vccz .Lbk2\n"
      "s_sleep 4\n"
      "s_sub_u32 s56, s56, 1\n"
      "s_cbranch_scc0 .Lsp2\n"
      "s_branch .Lbk2\n"
      ".Ltg1:\n"
      "v_swap_b32 v108, v112\n"
      "v_swap_b32 v109, v113\n"
      "v_swap_b32 v110, v114\n"
      "v_swap_b32 v111, v115\n"
      "s_add_u32 s80, s80, s81\n"
      "s_xor_b32 s81, s81, 33\n"
      "s_branch .Ltb1\n"
      ".Lsl7:\n"
      "s_mov_b32 s56, 0x20000\n"
      ".Lsp7:\n"
      "buffer_load_dword v6, v5, s[36:39], 0 offen sc1\n"
      "s_waitcnt vmcnt(0)\n"
      "v_cmp_gt_u32 vcc, s67, v6\n"
      "s_cbranch_vccz .Lbk7\n"
      "s_sleep 4\n"
      "s_sub_u32 s56, s56, 1\n"
      "s_cbranch_scc0 .Lsp7\n"
      "s_branch .Lbk7\n"
      ".Lsl8:\n"
      "s_mov_b32 s56, 0x20000\n"
      ".Lsp8:\n"
      "buffer_load_dword v6, v5, s[36:39], 0 offen sc1\n"
      "s_waitcnt vmcnt(0)\n"
      "v_cmp_gt_u32 vcc, s67, v6\n"
      "s_cbranch_vccz .Lbk8\n"
      "s_sleep 4\n"
      "s_sub_u32 s56, s56, 1\n"
      "s_cbranch_scc0 .Lsp8\n"
      "s_branch .Lbk8\n"
      ".Lsl9:\n"
      "s_mov_b32 s56, 0x20000\n"
      ".Lsp9:\n"
      "buffer_load_dword v6, v5, s[36:39], 0 offen sc1\n"
      "s_waitcnt vmcnt(0)\n"
      "v_cmp_gt_u32 vcc, s67, v6\n"
      "s_cbranch_vccz .Lbk9\n"
      "s_sleep 4\n"
      "s_sub_u32 s56, s56, 1\n"
      "s_cbranch_scc0 .Lsp9\n"
      "s_branch .Lbk9\n"
      ".Lsl10:\n"
      "s_mov_b32 s56, 0x20000\n"
      ".Lsp10:\n"
      "buffer_load_dword v6, v5, s[36:39], 0 offen sc1\n"
      "s_waitcnt vmcnt(0)\n"
      "v_cmp_gt_u32 vcc, s67, v6\n"
      "s_cbranch_vccz .Lbk10\n"
      "s_sleep 4\n"
      "s_sub_u32 s56, s56, 1\n"
      "s_cbranch_scc0 .Lsp10\n"
      "s_branch .Lbk10\n"
      ".Lsl11:\n"
      "s_mov_b32 s56, 0x20000\n"
      ".Lsp11:\n"
      "buffer_load_dword v6, v5, s[36:39], 0 offen sc1\n"
      "s_waitcnt vmcnt(0)\n"
      "v_cmp_gt_u32 vcc, s67, v6\n"
      "s_cbranch_vccz .Lbk11\n"
      "s_sleep 4\n"
      "s_sub_u32 s56, s56, 1\n"
      "s_cbranch_scc0 .Lsp11\n"
      "s_branch .Lbk11\n"
      ".Lsl12:\n"
      "s_mov_b32 s56, 0x20000\n"
      ".Lsp12:\n"
      "buffer_load_dword v6, v5, s[36:39], 0 offen sc1\n"
      "s_waitcnt vmcnt(0)\n"
      "v_cmp_gt_u32 vcc, s67, v6\n"
      "s_cbranch_vccz .Lbk12\n"
      "s_sleep 4\n"
      "s_sub_u32 s56, s56, 1\n"
      "s_cbranch_scc0 .Lsp12\n"
      "s_branch .Lbk12\n"
      ".Lsl13:\n"
      "s_mov_b32 s56, 0x20000\n"
      ".Lsp13:\n"
      "buffer_load_dword v10, v9, s[36:39], 0 offen sc1\n"
      "s_waitcnt vmcnt(0)\n"
      "v_cmp_gt_u32 vcc, s74, v10\n"
      "s_cbranch_vccz .Lbk13\n"
      "s_sleep 4\n"
      "s_sub_u32 s56, s56, 1\n"
      "s_cbranch_scc0 .Lsp13\n"
      "s_branch .Lbk13\n"
      ".Lsl14:\n"
      "s_mov_b32 s56, 0x20000\n"
      ".Lsp14:\n"
      "buffer_load_dword v10, v9, s[36:39], 0 offen sc1\n"
      "s_waitcnt vmcnt(0)\n"
      "v_cmp_gt_u32 vcc, s74, v10\n"
      "s_cbranch_vccz .Lbk14\n"
      "s_sleep 4\n"
      "s_sub_u32 s56, s56, 1\n"
      "s_cbranch_scc0 .Lsp14\n"
      "s_branch .Lbk14\n"
      ".Lsl18:\n"
      "s_mov_b32 s56, 0x20000\n"
      ".Lsp18:\n"
      "buffer_load_dword v6, v5, s[36:39], 0 offen sc1\n"
      "s_waitcnt vmcnt(0)\n"
      "v_cmp_gt_u32 vcc, s67, v6\n"
      "s_cbranch_vccz .Lbk18\n"
      "s_sleep 4\n"
      "s_sub_u32 s56, s56, 1\n"
      "s_cbranch_scc0 .Lsp18\n"
      "s_branch .Lbk18\n"
      ".Lsl19:\n"
      "s_mov_b32 s56, 0x20000\n"
      ".Lsp19:\n"
      "buffer_load_dword v6, v5, s[36:39], 0 offen sc1\n"
      "s_waitcnt vmcnt(0)\n"
      "v_cmp_gt_u32 vcc, s67, v6\n"
      "s_cbranch_vccz .Lbk19\n"
      "s_sleep 4\n"
      "s_sub_u32 s56, s56, 1\n"
      "s_cbranch_scc0 .Lsp19\n"
      "s_branch .Lbk19\n"
      ".Lsl22:\n"
      "s_mov_b32 s56, 0x20000\n"
      ".Lsp22:\n"
      "buffer_load_dword v5, v4, s[36:39], 0 offen sc1\n"
      "s_waitcnt vmcnt(0)\n"
      "v_cmp_gt_u32 vcc, s67, v5\n"
      "s_cbranch_vccz .Lbk22\n"
      "s_sleep 4\n"
      "s_sub_u32 s56, s56, 1\n"
      "s_cbranch_scc0 .Lsp22\n"
      "s_branch .Lbk22\n"
      ".Lsl23:\n"
      "s_mov_b32 s56, 0x20000\n"
      ".Lsp23:\n"
      "buffer_load_dword v5, v4, s[36:39], 0 offen sc1\n"
      "s_waitcnt vmcnt(0)\n"
      "v_cmp_gt_u32 vcc, s67, v5\n"
      "s_cbranch_vccz .Lbk23\n"
      "s_sleep 4\n"
      "s_sub_u32 s56, s56, 1\n"
      "s_cbranch_scc0 .Lsp23\n"
      "s_branch .Lbk23\n"
      "s_endpgm\n"
      ".Lfin:\n"
      : "+v"(tid)
      : "s"(blockIdx.x), "s"(__builtin_amdgcn_kernarg_segment_ptr())
      : "v1", "v2", "v3", "v4", "v5", "v6", "v7", "v8", "v9", "v10", "v11", "v12", "v13", "v14", "v15", "v16", "v17", "v18", "v19", "v20", "v21", "v22", "v23", "v24", "v25", "v26", "v27", "v28", "v29", "v30", "v31", "v32", "v33", "v34", "v35", "v36", "v37", "v38", "v39", "v40", "v41", "v42", "v43", "v44", "v45", "v46", "v47", "v48", "v49", "v50", "v51", "v52", "v53", "v54", "v55", "v56", "v57", "v58", "v59", "v60", "v61", "v62", "v63", "v64", "v65", "v66", "v67", "v68", "v69", "v70", "v71", "v72", "v73", "v74", "v75", "v76", "v77", "v78", "v79", "v80", "v81", "v82", "v83", "v84", "v85", "v86", "v87", "v88", "v89", "v90", "v91", "v92", "v93", "v94", "v95", "v96", "v97", "v98", "v99", "v100", "v101", "v102", "v103", "v104", "v105", "v106", "v107", "v108", "v109", "v110", "v111", "v112", "v113", "v114", "v115", "v116", "v117", "v118", "v119", "v120", "v121", "v122", "v123", "v124", "v125", "v126", "v127", "v128", "v129", "v130", "v131", "v132", "v133", "v134", "v135", "v136", "v137", "v138", "v139", "v140", "v141", "v142", "v143", "v144", "v145", "v146", "v147", "v148", "v149", "v150", "v151", "v152", "v153", "v154", "v155", "v156", "v157", "v158", "v159", "v160", "v161", "v162", "v163", "v164", "v165", "v166", "v167", "v168", "v169", "v170", "v171", "v172", "v173", "v174", "v175", "v176", "v177", "v178", "v179", "v180", "v181", "v182", "v183", "v184", "v185", "v186", "v187", "v188", "v189", "v190", "v191", "v192", "v193", "v194", "v195", "v196", "v197", "v198", "v199", "v200", "v201", "v202", "v203", "v204", "v205", "v206", "v207", "v208", "v209", "v210", "v211", "v212", "v213", "v214", "v215", "v216", "v217", "v218", "v219", "v220", "v221", "v222", "v223", "v224", "v225", "v226", "v227", "v228", "v229", "v230", "v231", "v232", "v233", "v234", "v235", "v236", "v237", "v238", "v239", "v240", "v241", "v242", "v243", "v244", "v245", "v246", "v247", "v248", "v249", "v250", "v251", "v252", "v253", "v254", "v255", "a0", "a1", "a2", "a3", "a4", "a5", "a6", "a7", "a8", "a9", "a10", "a11", "a12", "a13", "a14", "a15", "a16", "a17", "a18", "a19", "a20", "a21", "a22", "a23", "a24", "a25", "a26", "a27", "a28", "a29", "a30", "a31", "a32", "a33", "a34", "a35", "a36", "a37", "a38", "a39", "a40", "a41", "a42", "a43", "a44", "a45", "a46", "a47", "a48", "a49", "a50", "a51", "a52", "a53", "a54", "a55", "a56", "a57", "a58", "a59", "a60", "a61", "a62", "a63", "a64", "a65", "a66", "a67", "a68", "a69", "a70", "a71", "a72", "a73", "a74", "a75", "a76", "a77", "a78", "a79", "a80", "a81", "a82", "a83", "a84", "a85", "a86", "a87", "a88", "a89", "a90", "a91", "a92", "a93", "a94", "a95", "a96", "a97", "a98", "a99", "a100", "a101", "a102", "a103", "a104", "a105", "a106", "a107", "a108", "a109", "a110", "a111", "a112", "a113", "a114", "a115", "a116", "a117", "a118", "a119", "a120", "a121", "a122", "a123", "a124", "a125", "a126", "a127", "a128", "a129", "a130", "a131", "a132", "a133", "a134", "a135", "a136", "a137", "a138", "a139", "a140", "a141", "a142", "a143", "a144", "a145", "a146", "a147", "a148", "a149", "a150", "a151", "a152", "a153", "a154", "a155", "a156", "a157", "a158", "a159", "a160", "a161", "a162", "a163", "a164", "a165", "a166", "a167", "a168", "a169", "a170", "a171", "a172", "a173", "a174", "a175", "a176", "a177", "a178", "a179", "a180", "a181", "a182", "a183", "a184", "a185", "a186", "a187", "a188", "a189", "a190", "a191", "a192", "a193", "a194", "a195", "a196", "a197", "a198", "a199", "a200", "a201", "a202", "a203", "a204", "a205", "a206", "a207", "a208", "a209", "a210", "a211", "a212", "a213", "a214", "a215", "a216", "a217", "a218", "a219", "a220", "a221", "a222", "a223", "a224", "a225", "a226", "a227", "a228", "a229", "a230", "a231", "a232", "a233", "a234", "a235", "a236", "a237", "a238", "a239", "a240", "a241", "a242", "a243", "a244", "a245", "a246", "a247", "a248", "a249", "a250", "a251", "a252", "a253", "a254", "a255", "s8", "s9", "s10", "s11", "s12", "s13", "s14", "s15", "s16", "s17", "s18", "s19", "s20", "s21", "s22", "s23", "s24", "s25", "s26", "s27", "s28", "s29", "s30", "s31", "s34", "s35", "s36", "s37", "s38", "s39", "s40", "s41", "s42", "s43", "s44", "s45", "s46", "s47", "s48", "s49", "s50", "s51", "s52", "s53", "s54", "s55", "s56", "s57", "s58", "s59", "s60", "s61", "s62", "s63", "s64", "s65", "s66", "s67", "s68", "s69", "s70", "s71", "s72", "s73", "s74", "s75", "s76", "s77", "s78", "s79", "s80", "s81", "s82", "s83", "s84", "s85", "s86", "s87", "s88", "s89", "s90", "s91", "s92", "s93", "s94", "s95", "s96", "s97", "s98", "s99", "vcc", "scc", "memory"
  );
}

extern "C" void kernel_launch(void* const* d_in, const int* in_sizes, int n_in, void* d_out, int out_size, void* d_ws,
                              size_t ws_size, hipStream_t stream) {
  char* ws = (char*)d_ws + ((256 - ((size_t)d_ws & 255)) & 255);
  (void)hipFuncSetAttribute((const void*)k_sp, hipFuncAttributeMaxDynamicSharedMemorySize, SP_LDS);
  k_sp<<<256, 256, SP_LDS, stream>>>((const u16*)d_in[0], (const u16*)d_in[1], (const u16*)d_in[2], (const u16*)d_in[4],
                                     (const u16*)d_in[5], (const u16*)d_in[6], ws, (u16*)d_out
  );
}
```
